```python
import torch

def _softmax(x, axis=-1):
    return torch.softmax(x, dim=axis)
def _gelu(x):
    return torch.nn.functional.gelu(x, approximate="tanh")
def _silu(x):
    return torch.nn.functional.silu(x)
def _sigmoid(x):
    return torch.sigmoid(x)
def _layernorm(x, axis=-1, eps=1e-5):
    m = x.mean(dim=axis, keepdim=True)
    v = x.var(dim=axis, keepdim=True, unbiased=False)
    return (x - m) / torch.sqrt(v + eps)
def _scatter_add(tgt, idx, src, axis=0):
    return tgt.clone().index_add_(axis, idx, src)
def _segment_sum(src, seg, n):
    out = torch.zeros((n,) + src.shape[1:], dtype=src.dtype)
    return out.index_add_(0, seg, src)
def _pad(x, pw):
    flat = tuple(v for pair in reversed(pw) for v in pair)
    return torch.nn.functional.pad(x, flat)

kernel_name = 'standalone__gqa_block_cached__near_ridge'

def setup_inputs():

    _w = (lambda a: (lambda t: t.to(torch.bfloat16) if t.is_floating_point() else t)(torch.as_tensor(a)))
    B, Sq, D = 16, 240, 512
    Hq, Hkv, Sk, Dh = 32, 32, 512, 128
    assert Hq % Hkv == 0, (Hq, Hkv)
    return {
        "x": _w((lambda *s: torch.randn(*s, dtype=torch.bfloat16) * 0.8)(B, Sq, D)),
        "k_cache": _w((lambda *s: torch.randn(*s, dtype=torch.bfloat16) * 0.8)(B, Hkv, Sk, Dh)),
        "v_cache": _w((lambda *s: torch.randn(*s, dtype=torch.bfloat16) * 0.05)(B, Hkv, Sk, Dh)),
        "wq": _w((lambda *s: torch.randn(*s, dtype=torch.bfloat16) * 0.8)(D, Hq * Dh) * (D ** -0.5)),
        "wo": _w((lambda *s: torch.randn(*s, dtype=torch.bfloat16) * 0.05)(Hq * Dh, D) * ((Hq * Dh) ** -0.5)),
    }

def reference(x, k_cache, v_cache, wq, wo):
    B, Sq, D = x.shape
    Hq, Dh = 32, 128
    Hkv = k_cache.shape[1]
    rep = Hq // Hkv
    q = (lambda x, *d: x.permute(*d))((x @ wq).reshape(B, Sq, Hq, Dh), 0, 2, 1, 3)
    k = (lambda x, n, axis: torch.repeat_interleave(x, n, dim=axis))(k_cache, rep, axis=1)
    v = (lambda x, n, axis: torch.repeat_interleave(x, n, dim=axis))(v_cache, rep, axis=1)
    scores = torch.einsum("bhqd,bhkd->bhqk", q, k) * (Dh ** -0.5)
    p = _softmax(scores, axis=-1)
    o = torch.einsum("bhqk,bhkd->bhqd", p, v)
    return (lambda x, *d: x.permute(*d))(o, 0, 2, 1, 3).reshape(B, Sq, Hq * Dh) @ wo
```

```cpp
#include <hip/hip_runtime.h>
#include <hip/hip_bf16.h>
#include <stdint.h>

typedef float    f32x16 __attribute__((ext_vector_type(16)));
typedef float    f32x4  __attribute__((ext_vector_type(4)));
typedef float    f32x2  __attribute__((ext_vector_type(2)));
typedef short    s16x8  __attribute__((ext_vector_type(8)));
typedef short    s16x4  __attribute__((ext_vector_type(4)));
typedef __bf16   bf16x8 __attribute__((ext_vector_type(8)));
typedef __bf16   bf16x2 __attribute__((ext_vector_type(2)));
typedef unsigned u32x4  __attribute__((ext_vector_type(4)));
typedef unsigned u32x2  __attribute__((ext_vector_type(2)));
#define LDSP(T) __attribute__((address_space(3))) T*
#define GLBP(T) const __attribute__((address_space(1))) T*
#define DI __device__ __forceinline__

constexpr int B = 16, SQ = 240, DM = 512, H = 32, SK = 512, DH = 128;

DI f32x16 mfma(bf16x8 a, bf16x8 b, f32x16 c) { return __builtin_amdgcn_mfma_f32_32x32x16_bf16(a, b, c, 0, 0, 0); }
DI unsigned cvtpk(float lo, float hi) { f32x2 v = {lo, hi}; bf16x2 b = __builtin_convertvector(v, bf16x2); return __builtin_bit_cast(unsigned, b); }
DI bf16x8 pack8(const f32x16& x, int s, float sc) {
  u32x4 w = {cvtpk(x[8*s+0]*sc, x[8*s+1]*sc), cvtpk(x[8*s+2]*sc, x[8*s+3]*sc), cvtpk(x[8*s+4]*sc, x[8*s+5]*sc), cvtpk(x[8*s+6]*sc, x[8*s+7]*sc)};
  return __builtin_bit_cast(bf16x8, w);
}
DI bf16x8 lds_b128(unsigned addr) { return *(LDSP(bf16x8))(uintptr_t)addr; }
DI bf16x8 lds_tr(unsigned a0, unsigned a1) {
  s16x4 lo = __builtin_amdgcn_ds_read_tr16_b64_v4i16((LDSP(s16x4))(uintptr_t)a0);
  s16x4 hi = __builtin_amdgcn_ds_read_tr16_b64_v4i16((LDSP(s16x4))(uintptr_t)a1);
  s16x8 r = __builtin_shufflevector(lo, hi, 0, 1, 2, 3, 4, 5, 6, 7);
  return __builtin_bit_cast(bf16x8, r);
}
DI void glds(const void* g, unsigned ldsaddr) {
  __builtin_amdgcn_global_load_lds((GLBP(unsigned))g, (LDSP(unsigned))(uintptr_t)ldsaddr, 16, 0, 0);
}

__global__ __launch_bounds__(256, 1)
void k1_qattn(const unsigned short* __restrict__ x, const unsigned short* __restrict__ kc, const unsigned short* __restrict__ vc,
              const unsigned short* __restrict__ wq, unsigned short* __restrict__ of) {
  extern __shared__ __attribute__((aligned(16))) char smem[];
  const unsigned ldsb = (unsigned)(uintptr_t)(LDSP(char))smem;
  const int tid = threadIdx.x, lane = tid & 63, w = __builtin_amdgcn_readfirstlane(tid >> 6), r = lane & 31, h = lane >> 5;
  const int bid = blockIdx.x, xcd = bid & 7, idx = bid >> 3;
  const int b = (xcd >> 1) * 4 + (idx & 3), head = (xcd & 1) * 16 + (idx >> 2);
  const int drow = (lane >> 2) & 7, dsub = lane >> 5, dslot = lane & 3;
  const char* xg = (const char*)x + (size_t)b * SQ * DM * 2;
  const char* wg = (const char*)wq + (size_t)head * DH * 2;
  const char* kg = (const char*)kc + ((size_t)(b * H + head)) * SK * DH * 2;
  const char* vg = (const char*)vc + ((size_t)(b * H + head)) * SK * DH * 2;
  auto stage_q = [&](int kt, unsigned buf) {
#pragma unroll
    for (int g = 0; g < 8; ++g) {
      int row = 64 * w + 8 * g + drow; int swz = (row >> 2) & 3; if (row > SQ - 1) row = SQ - 1;
      glds(xg + (size_t)row * (DM * 2) + kt * 128 + dsub * 64 + ((dslot ^ swz) << 4), buf + 8192 * w + 1024 * g);
    }
#pragma unroll
    for (int i = 0; i < 4; ++i) {
      int row = kt * 64 + 8 * (2 * w + (i >> 1)) + drow;
      glds(wg + (size_t)row * (H * DH * 2) + (i & 1) * 128 + dsub * 64 + (dslot << 4), buf + 32768 + 4096 * w + 1024 * i);
    }
  };
  auto stage_kv = [&](int t, unsigned buf) {
#pragma unroll
    for (int i = 0; i < 4; ++i) {
      int rl = 8 * (2 * w + (i >> 1)) + drow; int swz = (rl >> 2) & 3;
      glds(kg + (size_t)(t * 64 + rl) * 256 + (i & 1) * 128 + dsub * 64 + ((dslot ^ swz) << 4), buf + 4096 * w + 1024 * i);
      glds(vg + (size_t)(t * 64 + rl) * 256 + (i & 1) * 128 + dsub * 64 + (dslot << 4), buf + 16384 + 4096 * w + 1024 * i);
    }
  };
  const unsigned rowb0 = 64 * (r & 7) + 16 * ((0 + h) ^ ((r >> 2) & 3)), rowb1 = rowb0 ^ 32;
  const unsigned xb[2] = {8192u * w + 1024u * (r >> 3) + rowb0, 8192u * w + 1024u * (r >> 3) + rowb1};
  const unsigned kb_[2] = {2048u * (r >> 3) + rowb0, 2048u * (r >> 3) + rowb1};
  const int i16 = lane & 15, q4 = i16 >> 2, p4 = i16 & 3, chalf = (lane >> 4) & 1;
  const int p4s = ((p4 & 1) << 1) | (p4 >> 1);
  const unsigned wb = 2048u * h + 64u * q4 + 32u * chalf + 8u * p4s;
  const unsigned vb = 64u * (4 * h + q4) + 32u * chalf + 8u * p4;

  f32x16 acc[4][2];
#pragma unroll
  for (int i = 0; i < 4; ++i) { acc[i][0] = f32x16{}; acc[i][1] = f32x16{}; }
  stage_q(0, ldsb);
  for (int kt = 0; kt < 8; ++kt) {
    const unsigned buf = ldsb + (kt & 1) * 49152;
    __syncthreads();
    if (kt + 1 < 8) stage_q(kt + 1, ldsb + ((kt + 1) & 1) * 49152);
    else stage_kv(0, ldsb + 98304);
#pragma unroll
    for (int s = 0; s < 4; ++s) {
      bf16x8 xf[2];
#pragma unroll
      for (int qb = 0; qb < 2; ++qb) xf[qb] = lds_b128(buf + xb[s & 1] + 4096 * qb + 512 * (s >> 1));
#pragma unroll
      for (int dt = 0; dt < 4; ++dt) {
        const unsigned a = buf + 32768 + wb + 4096 * s + 512 * dt;
        bf16x8 wf = lds_tr(a, a + 256);
        acc[dt][0] = mfma(wf, xf[0], acc[dt][0]);
        acc[dt][1] = mfma(wf, xf[1], acc[dt][1]);
      }
    }
  }
  const float C = 0.12751743082f;
  bf16x8 qf[2][8];
#pragma unroll
  for (int qb = 0; qb < 2; ++qb)
#pragma unroll
    for (int dt = 0; dt < 4; ++dt) { qf[qb][2 * dt] = pack8(acc[dt][qb], 0, C); qf[qb][2 * dt + 1] = pack8(acc[dt][qb], 1, C); }
#pragma unroll
  for (int i = 0; i < 4; ++i) { acc[i][0] = f32x16{}; acc[i][1] = f32x16{}; }
  float m[2] = {-1e30f, -1e30f}, l[2] = {0.f, 0.f};
  for (int t = 0; t < 8; ++t) {
    const unsigned buf = ldsb + ((t & 1) ? 0 : 98304);
    __syncthreads();
    if (t + 1 < 8) stage_kv(t + 1, ldsb + ((t & 1) ? 98304 : 0));
    f32x16 s[2][2];
#pragma unroll
    for (int kb = 0; kb < 2; ++kb) {
      s[kb][0] = f32x16{}; s[kb][1] = f32x16{};
#pragma unroll
      for (int d0 = 0; d0 < 8; ++d0) {
        bf16x8 kf = lds_b128(buf + kb_[d0 & 1] + 8192 * kb + 512 * (d0 >> 1));
        s[kb][0] = mfma(kf, qf[0][d0], s[kb][0]);
        s[kb][1] = mfma(kf, qf[1][d0], s[kb][1]);
      }
    }
    bf16x8 pf[2][4];
#pragma unroll
    for (int qb = 0; qb < 2; ++qb) {
      float mx = s[0][qb][0];
#pragma unroll
      for (int i = 1; i < 16; ++i) mx = fmaxf(mx, s[0][qb][i]);
#pragma unroll
      for (int i = 0; i < 16; ++i) mx = fmaxf(mx, s[1][qb][i]);
      mx = fmaxf(mx, __shfl_xor(mx, 32));
      const float mn = fmaxf(m[qb], mx), al = __builtin_amdgcn_exp2f(m[qb] - mn);
      m[qb] = mn;
      float rs = 0.f;
#pragma unroll
      for (int kb = 0; kb < 2; ++kb)
#pragma unroll
        for (int i = 0; i < 16; ++i) { float p = __builtin_amdgcn_exp2f(s[kb][qb][i] - mn); s[kb][qb][i] = p; rs += p; }
      l[qb] = l[qb] * al + rs;
#pragma unroll
      for (int db = 0; db < 4; ++db) acc[db][qb] *= al;
#pragma unroll
      for (int ks = 0; ks < 4; ++ks) pf[qb][ks] = pack8(s[ks >> 1][qb], ks & 1, 1.f);
    }
#pragma unroll
    for (int ks = 0; ks < 4; ++ks)
#pragma unroll
      for (int db = 0; db < 4; ++db) {
        const unsigned a = buf + 16384 + vb + 4096 * ks + 512 * db;
        bf16x8 vf = lds_tr(a, a + 2048);
        acc[db][0] = mfma(vf, pf[0][ks], acc[db][0]);
        acc[db][1] = mfma(vf, pf[1][ks], acc[db][1]);
      }
  }
#pragma unroll
  for (int qb = 0; qb < 2; ++qb) {
    const float lt = l[qb] + __shfl_xor(l[qb], 32);
    const float inv = 1.f / lt;
    char* o = (char*)of + ((((size_t)b * 8 + 2 * w + qb) * H + head) * 8) * 1024 + lane * 16;
#pragma unroll
    for (int db = 0; db < 4; ++db)
#pragma unroll
      for (int s = 0; s < 2; ++s) *(bf16x8*)(o + (db * 2 + s) * 1024) = pack8(acc[db][qb], s, inv);
  }
}

__global__ __launch_bounds__(256, 1)
void k2_oproj(const unsigned short* __restrict__ of, const unsigned short* __restrict__ wo, float* __restrict__ part) {
  extern __shared__ __attribute__((aligned(16))) char smem[];
  const unsigned ldsb = (unsigned)(uintptr_t)(LDSP(char))smem;
  const int tid = threadIdx.x, lane = tid & 63, w = __builtin_amdgcn_readfirstlane(tid >> 6), h = lane >> 5;
  const int bid = blockIdx.x, xcd = bid & 7, idx = bid >> 3;
  const int ksp = xcd & 3, b = (xcd >> 2) * 8 + (idx >> 2), nt = idx & 3;
  const int drow = (lane >> 2) & 7, dsub = lane >> 5, dslot = lane & 3;
  const char* wg = (const char*)wo + (size_t)ksp * 1024 * DM * 2 + nt * 256;
  auto stage = [&](int kt, unsigned buf) {
#pragma unroll
    for (int i = 0; i < 4; ++i) {
      int row = kt * 64 + 8 * (2 * w + (i >> 1)) + drow;
      glds(wg + (size_t)row * (DM * 2) + (i & 1) * 128 + dsub * 64 + (dslot << 4), buf + 4096 * w + 1024 * i);
    }
  };
  const int i16 = lane & 15, q4 = i16 >> 2, p4 = i16 & 3, chalf = (lane >> 4) & 1;
  const unsigned vb = 64u * (4 * h + q4) + 32u * chalf + 8u * p4;
  f32x16 acc[4][2];
#pragma unroll
  for (int i = 0; i < 4; ++i) { acc[i][0] = f32x16{}; acc[i][1] = f32x16{}; }
  const char* ob[2];
#pragma unroll
  for (int qb = 0; qb < 2; ++qb) ob[qb] = (const char*)of + ((((size_t)b * 8 + 2 * w + qb) * H + ksp * 8) * 8) * 1024 + lane * 16;
  stage(0, ldsb);
  bf16x8 of_[2][4];
#pragma unroll
  for (int qb = 0; qb < 2; ++qb)
#pragma unroll
    for (int ks = 0; ks < 4; ++ks) of_[qb][ks] = *(const bf16x8*)(ob[qb] + ks * 1024);
  for (int kt = 0; kt < 16; ++kt) {
    const unsigned buf = ldsb + (kt & 1) * 16384;
    __syncthreads();
    bf16x8 cur[2][4];
#pragma unroll
    for (int qb = 0; qb < 2; ++qb)
#pragma unroll
      for (int ks = 0; ks < 4; ++ks) cur[qb][ks] = of_[qb][ks];
    if (kt + 1 < 16) {
      stage(kt + 1, ldsb + ((kt + 1) & 1) * 16384);
#pragma unroll
      for (int qb = 0; qb < 2; ++qb)
#pragma unroll
        for (int ks = 0; ks < 4; ++ks) of_[qb][ks] = *(const bf16x8*)(ob[qb] + ((kt + 1) * 4 + ks) * 1024);
    }
#pragma unroll
    for (int ks = 0; ks < 4; ++ks)
#pragma unroll
      for (int db = 0; db < 4; ++db) {
        const unsigned a = buf + vb + 4096 * ks + 512 * db;
        bf16x8 vf = lds_tr(a, a + 2048);
        acc[db][0] = mfma(vf, cur[0][ks], acc[db][0]);
        acc[db][1] = mfma(vf, cur[1][ks], acc[db][1]);
      }
  }
  float* pp = part + ((((size_t)ksp * 16 + b) * 4 + nt) * 4 + w) * (2 * 4 * 4 * 64 * 4) + lane * 4;
#pragma unroll
  for (int qb = 0; qb < 2; ++qb)
#pragma unroll
    for (int db = 0; db < 4; ++db)
#pragma unroll
      for (int g = 0; g < 4; ++g) {
        f32x4 v = {acc[db][qb][4 * g], acc[db][qb][4 * g + 1], acc[db][qb][4 * g + 2], acc[db][qb][4 * g + 3]};
        *(f32x4*)(pp + ((qb * 4 + db) * 4 + g) * 256) = v;
      }
}
__global__ __launch_bounds__(256) void k3_reduce(const float* __restrict__ part, unsigned short* __restrict__ out) {
  const int i = blockIdx.x * 256 + threadIdx.x;
  const int lane = i & 63, g = (i >> 6) & 3, db = (i >> 8) & 3, qb = (i >> 10) & 1, w = (i >> 11) & 3, nt = (i >> 13) & 3, b = i >> 15;
  f32x4 v = *(const f32x4*)(part + (size_t)i * 4);
#pragma unroll
  for (int k = 1; k < 4; ++k) v += *(const f32x4*)(part + ((size_t)k * 524288 + i) * 4);
  const int q = 64 * w + 32 * qb + (lane & 31), n = nt * 128 + db * 32 + 8 * g + 4 * (lane >> 5);
  if (q < SQ) { u32x2 o = {cvtpk(v[0], v[1]), cvtpk(v[2], v[3])}; *(u32x2*)(out + ((size_t)b * SQ + q) * DM + n) = o; }
}

extern "C" void kernel_launch(void* const* d_in, const int* in_sizes, int n_in, void* d_out, int out_size, void* d_ws, size_t ws_size, hipStream_t stream) {
  const unsigned short* x = (const unsigned short*)d_in[0];
  const unsigned short* kc = (const unsigned short*)d_in[1];
  const unsigned short* vc = (const unsigned short*)d_in[2];
  const unsigned short* wq = (const unsigned short*)d_in[3];
  const unsigned short* wo = (const unsigned short*)d_in[4];
  unsigned short* of = (unsigned short*)d_ws;
  float* part = (float*)((char*)d_ws + (size_t)32 * 1024 * 1024);
  (void)hipFuncSetAttribute((const void*)k1_qattn, hipFuncAttributeMaxDynamicSharedMemorySize, 131072);
  hipLaunchKernelGGL(k1_qattn, dim3(512), dim3(256), 131072, stream, x, kc, vc, wq, of);
  hipLaunchKernelGGL(k2_oproj, dim3(256), dim3(256), 32768, stream, of, wo, part);
  hipLaunchKernelGGL(k3_reduce, dim3(2048), dim3(256), 0, stream, part, (unsigned short*)d_out);
}
```

```cpp
#include <hip/hip_runtime.h>
#include <hip/hip_bf16.h>
#include <stdint.h>

__global__ __launch_bounds__(256, 1) __attribute__((aligned(4096)))
void gqa_block_fused(const unsigned short* __restrict__ x, const unsigned short* __restrict__ kc, const unsigned short* __restrict__ vc,
                     const unsigned short* __restrict__ wq, const unsigned short* __restrict__ wo, unsigned short* __restrict__ out,
                     char* __restrict__ ws) {
  extern __shared__ __attribute__((aligned(16))) char smem[];
  const unsigned ldsb = (unsigned)(uintptr_t)(__attribute__((address_space(3))) char*)smem;
  const unsigned w = __builtin_amdgcn_readfirstlane(threadIdx.x >> 6);
  asm volatile(R"ASM(
s_load_dwordx2 s[8:9], %0, 0
s_load_dwordx2 s[12:13], %0, 24
s_load_dwordx2 s[16:17], %0, 8
s_load_dwordx2 s[20:21], %0, 16
s_load_dwordx2 s[28:29], %0, 32
s_load_dwordx2 s[36:37], %0, 40
s_load_dwordx2 s[24:25], %0, 48
v_mbcnt_lo_u32_b32 v0, -1, 0
v_mbcnt_hi_u32_b32 v0, -1, v0
s_lshr_b32 s88, %1, 3
s_getpc_b64 s[76:77]
L_pc_%=:
v_lshlrev_b32 v10, 7, v0
s_and_b32 s77, s77, 0xffff
s_mov_b32 s78, L_end_%=-L_pc_%=
s_mov_b32 s79, 0x20000
s_lshl_b32 s87, s88, 2
s_add_u32 s87, s87, %2
s_lshl_b32 s87, s87, 13
buffer_load_dword v11, v10, s[76:79], s87 offen
s_bfe_u32 s89, %1, 0x20001
s_lshl_b32 s89, s89, 2
s_and_b32 s90, s88, 3
s_add_u32 s89, s89, s90
s_lshr_b32 s91, s88, 2
s_and_b32 s92, %1, 1
s_lshl_b32 s54, s92, 4
s_add_u32 s54, s54, s91
s_lshl_b32 s43, s54, 8
s_mul_i32 s85, s89, 245760
s_mov_b32 s64, 0
s_mov_b32 s65, 128
s_mov_b32 s66, 256
s_lshl_b32 s40, %2, 12
s_add_u32 s40, s40, %3
v_bfe_u32 v1, v0, 2, 3
v_lshrrev_b32 v2, 5, v0
v_lshlrev_b32 v2, 6, v2
v_and_b32 v3, 3, v0
v_lshrrev_b32 v4, 2, v1
v_xor_b32 v5, v3, v4
v_lshl_add_u32 v5, v5, 4, v2
s_and_b32 s84, %2, 1
s_lshl_b32 s84, s84, 1
v_xor_b32 v8, s84, v3
v_lshl_add_u32 v8, v8, 4, v2
s_lshl_b32 s84, %2, 17
v_lshl_add_u32 v7, v1, 13, s84
v_add_u32 v213, v7, v8
v_add_u32 v214, 63488, v213
s_lshl_b32 s84, %2, 16
v_lshl_add_u32 v7, v1, 10, s84
v_add_u32 v215, v7, v5
v_xor_b32 v216, 48, v215
v_add_u32 v216, 7168, v216
s_mov_b32 s10, 245760
s_mov_b32 s11, 0x20000
s_mov_b32 s14, 4194304
s_mov_b32 s15, 0x20000
s_waitcnt lgkmcnt(0)
s_and_b32 s13, s13, 0xffff
s_add_u32 s8, s8, s85
s_addc_u32 s9, s9, 0
s_and_b32 s9, s9, 0xffff
s_add_u32 m0, s40, 0
s_lshl_b32 s56, s64, 12
s_add_u32 s56, s56, s43
buffer_load_dwordx4 v213, s[12:15], s56 offen lds
buffer_load_dwordx4 v214, s[12:15], s56 offen offset:2048 lds
s_add_u32 m0, s40, 1024
s_add_u32 s57, s56, 128
buffer_load_dwordx4 v213, s[12:15], s57 offen lds
buffer_load_dwordx4 v214, s[12:15], s57 offen offset:2048 lds
s_add_u32 m0, s40, 16384
s_add_u32 s58, s64, 0
buffer_load_dwordx4 v215, s[8:11], s58 offen lds
buffer_load_dwordx4 v216, s[8:11], s58 offen offset:1024 lds
s_add_u32 m0, s40, 18432
s_add_u32 s59, s64, 16384
buffer_load_dwordx4 v215, s[8:11], s59 offen lds
buffer_load_dwordx4 v216, s[8:11], s59 offen offset:1024 lds
s_add_u32 m0, s40, 32768
s_add_u32 s56, s64, 32768
buffer_load_dwordx4 v215, s[8:11], s56 offen lds
buffer_load_dwordx4 v216, s[8:11], s56 offen offset:1024 lds
s_add_u32 m0, s40, 34816
s_add_u32 s57, s64, 49152
buffer_load_dwordx4 v215, s[8:11], s57 offen lds
buffer_load_dwordx4 v216, s[8:11], s57 offen offset:1024 lds
s_mov_b64 exec, 0
s_cmp_eq_u32 %2, 0
s_cbranch_scc1 L_tch1_%=
s_cmp_eq_u32 %2, 1
s_cbranch_scc1 L_tch2_%=
s_cmp_eq_u32 %2, 2
s_cbranch_scc1 L_tch3_%=
s_cmp_eq_u32 %2, 3
s_cbranch_scc1 L_tch4_%=
L_tb0_%=:
s_mov_b64 exec, -1
s_nop 1
s_lshl_b32 s84, s90, 1
s_add_u32 s84, s84, s91
s_sub_u32 s85, s84, 5
s_cselect_b32 s84, s84, s85
s_sub_u32 s85, s84, 5
s_cselect_b32 s84, s84, s85
s_lshl_b32 s84, s84, 7
s_add_u32 s67, s84, 384
v_bfe_u32 v12, v0, 4, 1
v_lshrrev_b32 v13, 5, v0
v_lshl_add_u32 v14, v12, 1, v13
v_bfe_u32 v15, v0, 2, 2
v_lshrrev_b32 v16, 1, v15
v_xor_b32 v16, v16, v15
v_xor_b32 v16, v16, v14
v_lshlrev_b32 v16, 4, v16
v_and_b32 v17, 7, v0
v_lshl_add_u32 v16, v17, 6, v16
v_bfe_u32 v17, v0, 3, 1
v_lshl_add_u32 v195, v17, 10, v16
v_add_u32 v195, s40, v195
v_lshlrev_b32 v18, 3, v3
v_lshl_add_u32 v18, v15, 6, v18
v_lshl_add_u32 v18, v12, 5, v18
v_add_u32 v18, %3, v18
v_lshl_add_u32 v204, v14, 11, v18
v_xor_b32 v207, 32, v204
s_add_u32 m0, s40, 49152
s_lshl_b32 s58, s65, 12
s_add_u32 s58, s58, s43
buffer_load_dwordx4 v213, s[12:15], s58 offen lds
buffer_load_dwordx4 v214, s[12:15], s58 offen offset:2048 lds
s_add_u32 m0, s40, 50176
s_add_u32 s59, s58, 128
buffer_load_dwordx4 v213, s[12:15], s59 offen lds
buffer_load_dwordx4 v214, s[12:15], s59 offen offset:2048 lds
s_add_u32 m0, s40, 65536
s_add_u32 s56, s65, 0
buffer_load_dwordx4 v215, s[8:11], s56 offen lds
buffer_load_dwordx4 v216, s[8:11], s56 offen offset:1024 lds
s_add_u32 m0, s40, 67584
s_add_u32 s57, s65, 16384
buffer_load_dwordx4 v215, s[8:11], s57 offen lds
buffer_load_dwordx4 v216, s[8:11], s57 offen offset:1024 lds
s_add_u32 m0, s40, 81920
s_add_u32 s58, s65, 32768
buffer_load_dwordx4 v215, s[8:11], s58 offen lds
buffer_load_dwordx4 v216, s[8:11], s58 offen offset:1024 lds
s_add_u32 m0, s40, 83968
s_add_u32 s59, s65, 49152
buffer_load_dwordx4 v215, s[8:11], s59 offen lds
buffer_load_dwordx4 v216, s[8:11], s59 offen offset:1024 lds
s_waitcnt vmcnt(20)
s_barrier
s_waitcnt vmcnt(16)
s_waitcnt vmcnt(12)
ds_read_b128 a[128:131], v195 offset:16384
ds_read_b128 a[132:135], v195 offset:16896
ds_read_b64_tr_b16 a[136:137], v204 offset:0
ds_read_b64_tr_b16 a[138:139], v204 offset:256
ds_read_b128 a[140:143], v195 offset:18432
ds_read_b128 a[144:147], v195 offset:18944
ds_read_b64_tr_b16 a[148:149], v204 offset:8192
ds_read_b64_tr_b16 a[150:151], v204 offset:8448
ds_read_b128 a[152:155], v195 offset:32768
ds_read_b128 a[156:159], v195 offset:33280
ds_read_b128 a[160:163], v195 offset:34816
ds_read_b128 a[164:167], v195 offset:35328
ds_read_b64_tr_b16 a[168:169], v207 offset:0
ds_read_b64_tr_b16 a[170:171], v207 offset:256
ds_read_b64_tr_b16 a[172:173], v207 offset:8192
ds_read_b64_tr_b16 a[174:175], v207 offset:8448
ds_read_b64_tr_b16 a[176:177], v204 offset:512
ds_read_b64_tr_b16 a[178:179], v204 offset:768
ds_read_b64_tr_b16 a[180:181], v204 offset:8704
ds_read_b64_tr_b16 a[182:183], v204 offset:8960
s_waitcnt lgkmcnt(15)
v_mfma_f32_16x16x32_bf16 v[64:67], a[136:139], a[128:131], 0
s_waitcnt lgkmcnt(11)
v_add_u32 v196, 65536, v195
v_add_u32 v205, 65536, v204
v_mfma_f32_16x16x32_bf16 v[64:67], a[148:151], a[132:135], v[64:67]
s_lshl_b32 s56, s66, 12
v_xor_b32 v208, 32, v205
v_add_u32 v197, 131072, v195
v_mfma_f32_16x16x32_bf16 v[72:75], a[136:139], a[140:143], 0
s_add_u32 m0, s40, 98304
v_add_u32 v206, 131072, v204
v_xor_b32 v209, 32, v206
v_mfma_f32_16x16x32_bf16 v[72:75], a[148:151], a[144:147], v[72:75]
s_add_u32 s56, s56, s43
buffer_load_dwordx4 v213, s[12:15], s56 offen lds
v_mfma_f32_16x16x32_bf16 v[80:83], a[136:139], a[152:155], 0
s_waitcnt lgkmcnt(6)
ds_read_b64_tr_b16 a[184:185], v207 offset:512
ds_read_b64_tr_b16 a[186:187], v207 offset:768
v_mfma_f32_16x16x32_bf16 v[80:83], a[148:151], a[156:159], v[80:83]
ds_read_b64_tr_b16 a[188:189], v207 offset:8704
ds_read_b64_tr_b16 a[190:191], v207 offset:8960
v_mfma_f32_16x16x32_bf16 v[88:91], a[136:139], a[160:163], 0
buffer_load_dwordx4 v214, s[12:15], s56 offen offset:2048 lds
v_mfma_f32_16x16x32_bf16 v[88:91], a[148:151], a[164:167], v[88:91]
s_add_u32 s68, s67, 128
s_cmp_eq_u32 s68, 1024
s_cselect_b32 s68, 384, s68
v_mfma_f32_16x16x32_bf16 v[68:71], a[168:171], a[128:131], 0
s_waitcnt lgkmcnt(8)
v_mfma_f32_16x16x32_bf16 v[68:71], a[172:175], a[132:135], v[68:71]
s_add_u32 s69, s68, 128
s_cmp_eq_u32 s69, 1024
s_cselect_b32 s69, 384, s69
v_mfma_f32_16x16x32_bf16 v[76:79], a[168:171], a[140:143], 0
s_add_u32 m0, s40, 99328
v_mfma_f32_16x16x32_bf16 v[76:79], a[172:175], a[144:147], v[76:79]
s_add_u32 s57, s56, 128
buffer_load_dwordx4 v213, s[12:15], s57 offen lds
v_mfma_f32_16x16x32_bf16 v[84:87], a[168:171], a[152:155], 0
s_waitcnt lgkmcnt(6)
ds_read_b64_tr_b16 a[136:137], v204 offset:1024
ds_read_b64_tr_b16 a[138:139], v204 offset:1280
v_mfma_f32_16x16x32_bf16 v[84:87], a[172:175], a[156:159], v[84:87]
ds_read_b64_tr_b16 a[148:149], v204 offset:9216
ds_read_b64_tr_b16 a[150:151], v204 offset:9472
v_mfma_f32_16x16x32_bf16 v[92:95], a[168:171], a[160:163], 0
buffer_load_dwordx4 v214, s[12:15], s57 offen offset:2048 lds
v_mfma_f32_16x16x32_bf16 v[92:95], a[172:175], a[164:167], v[92:95]
s_add_u32 s70, s69, 128
s_cmp_eq_u32 s70, 1024
s_cselect_b32 s70, 384, s70
v_mfma_f32_16x16x32_bf16 v[96:99], a[176:179], a[128:131], 0
s_waitcnt lgkmcnt(8)
v_mfma_f32_16x16x32_bf16 v[96:99], a[180:183], a[132:135], v[96:99]
s_lshl_b32 s58, s67, 12
v_mfma_f32_16x16x32_bf16 v[104:107], a[176:179], a[140:143], 0
s_add_u32 m0, s40, 114688
v_mfma_f32_16x16x32_bf16 v[104:107], a[180:183], a[144:147], v[104:107]
s_add_u32 s58, s58, s43
buffer_load_dwordx4 v213, s[12:15], s58 offen lds
v_mfma_f32_16x16x32_bf16 v[112:115], a[176:179], a[152:155], 0
s_waitcnt lgkmcnt(6)
ds_read_b64_tr_b16 a[168:169], v207 offset:1024
ds_read_b64_tr_b16 a[170:171], v207 offset:1280
v_mfma_f32_16x16x32_bf16 v[112:115], a[180:183], a[156:159], v[112:115]
ds_read_b64_tr_b16 a[172:173], v207 offset:9216
ds_read_b64_tr_b16 a[174:175], v207 offset:9472
v_mfma_f32_16x16x32_bf16 v[120:123], a[176:179], a[160:163], 0
buffer_load_dwordx4 v214, s[12:15], s58 offen offset:2048 lds
v_mfma_f32_16x16x32_bf16 v[120:123], a[180:183], a[164:167], v[120:123]
s_add_u32 s71, s70, 128
s_cmp_eq_u32 s71, 1024
s_cselect_b32 s71, 384, s71
v_mfma_f32_16x16x32_bf16 v[100:103], a[184:187], a[128:131], 0
s_waitcnt lgkmcnt(8)
s_mov_b32 s61, %1
s_mov_b32 s62, %2
v_mfma_f32_16x16x32_bf16 v[100:103], a[188:191], a[132:135], v[100:103]
s_mov_b32 s63, %3
s_and_b32 s25, s25, 0xffff
s_mov_b32 s26, 50331648
v_mfma_f32_16x16x32_bf16 v[108:111], a[184:187], a[140:143], 0
s_add_u32 m0, s40, 115712
s_mov_b32 s27, 0x20000
s_and_b32 s17, s17, 0xffff
v_mfma_f32_16x16x32_bf16 v[108:111], a[188:191], a[144:147], v[108:111]
s_add_u32 s59, s58, 128
buffer_load_dwordx4 v213, s[12:15], s59 offen lds
v_mfma_f32_16x16x32_bf16 v[116:119], a[184:187], a[152:155], 0
s_waitcnt lgkmcnt(6)
ds_read_b64_tr_b16 a[176:177], v204 offset:1536
ds_read_b64_tr_b16 a[178:179], v204 offset:1792
v_mfma_f32_16x16x32_bf16 v[116:119], a[188:191], a[156:159], v[116:119]
s_mov_b32 s18, 67108864
ds_read_b64_tr_b16 a[180:181], v204 offset:9728
ds_read_b64_tr_b16 a[182:183], v204 offset:9984
v_mfma_f32_16x16x32_bf16 v[124:127], a[184:187], a[160:163], 0
s_mov_b32 s19, 0x20000
ds_read_b64_tr_b16 a[192:193], v207 offset:9728
ds_read_b64_tr_b16 a[194:195], v207 offset:9984
v_mfma_f32_16x16x32_bf16 v[124:127], a[188:191], a[164:167], v[124:127]
s_and_b32 s21, s21, 0xffff
ds_read_b64_tr_b16 a[184:185], v207 offset:1536
ds_read_b64_tr_b16 a[186:187], v207 offset:1792
v_mfma_f32_16x16x32_bf16 v[128:131], a[136:139], a[128:131], 0
s_waitcnt lgkmcnt(12)
buffer_load_dwordx4 v214, s[12:15], s59 offen offset:2048 lds
v_mfma_f32_16x16x32_bf16 v[128:131], a[148:151], a[132:135], v[128:131]
s_mov_b32 s22, 67108864
s_mov_b32 s23, 0x20000
s_and_b32 s29, s29, 0xffff
v_mfma_f32_16x16x32_bf16 v[136:139], a[136:139], a[140:143], 0
s_add_u32 m0, s40, 131072
s_mov_b32 s30, 4194304
s_mov_b32 s31, 0x20000
v_mfma_f32_16x16x32_bf16 v[136:139], a[148:151], a[144:147], v[136:139]
s_add_u32 s56, s66, 0
buffer_load_dwordx4 v215, s[8:11], s56 offen lds
v_mfma_f32_16x16x32_bf16 v[144:147], a[136:139], a[152:155], 0
s_waitcnt lgkmcnt(10)
v_mov_b32 v9, 0
s_mov_b32 s84, 33619968
v_mfma_f32_16x16x32_bf16 v[144:147], a[148:151], a[156:159], v[144:147]
buffer_load_dwordx4 v216, s[8:11], s56 offen offset:1024 lds
v_mfma_f32_16x16x32_bf16 v[152:155], a[136:139], a[160:163], 0
buffer_load_dwordx2 v[254:255], v9, s[24:27], s84 offen sc1
v_mfma_f32_16x16x32_bf16 v[152:155], a[148:151], a[164:167], v[152:155]
s_add_u32 s44, s43, 2048
s_lshl_b32 s41, s89, 5
s_add_u32 s41, s41, s54
v_mfma_f32_16x16x32_bf16 v[132:135], a[168:171], a[128:131], 0
s_waitcnt vmcnt(15) lgkmcnt(0)
s_barrier
v_mfma_f32_16x16x32_bf16 v[132:135], a[172:175], a[132:135], v[132:135]
s_lshl_b32 s41, s41, 17
s_add_u32 s84, s88, s61
s_and_b32 s84, s84, 7
v_mfma_f32_16x16x32_bf16 v[140:143], a[168:171], a[140:143], 0
s_lshl_b32 s84, s84, 14
ds_read_b128 a[196:199], v196 offset:2048
ds_read_b128 a[200:203], v196 offset:2560
v_mfma_f32_16x16x32_bf16 v[140:143], a[172:175], a[144:147], v[140:143]
s_or_b32 s41, s41, s84
ds_read_b64_tr_b16 a[188:189], v204 offset:49152
ds_read_b64_tr_b16 a[190:191], v204 offset:49408
v_mfma_f32_16x16x32_bf16 v[148:151], a[168:171], a[152:155], 0
s_waitcnt vmcnt(11)
ds_read_b128 a[136:139], v196 offset:0
ds_read_b128 a[148:151], v196 offset:512
v_mfma_f32_16x16x32_bf16 v[148:151], a[172:175], a[156:159], v[148:151]
s_add_u32 m0, s40, 133120
ds_read_b64_tr_b16 a[204:205], v204 offset:57344
ds_read_b64_tr_b16 a[206:207], v204 offset:57600
v_mfma_f32_16x16x32_bf16 v[156:159], a[168:171], a[160:163], 0
s_add_u32 s57, s66, 16384
buffer_load_dwordx4 v215, s[8:11], s57 offen lds
v_mfma_f32_16x16x32_bf16 v[156:159], a[172:175], a[164:167], v[156:159]
s_add_u32 s42, s41, 1048576
ds_read_b128 a[208:211], v196 offset:18432
ds_read_b128 a[212:215], v196 offset:18944
v_mfma_f32_16x16x32_bf16 v[160:163], a[176:179], a[128:131], 0
ds_read_b128 a[168:171], v196 offset:16384
ds_read_b128 a[172:175], v196 offset:16896
v_mfma_f32_16x16x32_bf16 v[160:163], a[180:183], a[132:135], v[160:163]
s_lshl_b32 s55, s89, 2
buffer_load_dwordx4 v216, s[8:11], s57 offen offset:1024 lds
v_mfma_f32_16x16x32_bf16 v[168:171], a[176:179], a[140:143], 0
s_add_u32 s55, s55, s62
s_lshl_b32 s45, s55, 5
s_add_u32 s45, s45, s54
v_mfma_f32_16x16x32_bf16 v[168:171], a[180:183], a[144:147], v[168:171]
s_lshl_b32 s45, s45, 14
s_and_b32 s84, s91, 3
s_lshl_b32 s84, s84, 2
v_mfma_f32_16x16x32_bf16 v[176:179], a[176:179], a[152:155], 0
ds_read_b64_tr_b16 a[216:217], v207 offset:49152
ds_read_b64_tr_b16 a[218:219], v207 offset:49408
v_mfma_f32_16x16x32_bf16 v[176:179], a[180:183], a[156:159], v[176:179]
s_add_u32 m0, s40, 147456
ds_read_b64_tr_b16 a[220:221], v207 offset:57344
ds_read_b64_tr_b16 a[222:223], v207 offset:57600
v_mfma_f32_16x16x32_bf16 v[184:187], a[176:179], a[160:163], 0
s_add_u32 s58, s66, 32768
buffer_load_dwordx4 v215, s[8:11], s58 offen lds
v_mfma_f32_16x16x32_bf16 v[184:187], a[180:183], a[164:167], v[184:187]
s_add_u32 s84, s84, s62
s_lshl_b32 s84, s84, 10
s_or_b32 s45, s45, s84
v_mfma_f32_16x16x32_bf16 v[164:167], a[184:187], a[128:131], 0
buffer_load_dwordx4 v216, s[8:11], s58 offen offset:1024 lds
v_mfma_f32_16x16x32_bf16 v[164:167], a[192:195], a[132:135], v[164:167]
s_add_u32 s46, s45, 131072
s_lshl_b32 s49, s55, 1
s_add_u32 s49, s49, s92
v_mfma_f32_16x16x32_bf16 v[172:175], a[184:187], a[140:143], 0
s_lshl_b32 s49, s49, 3
s_add_u32 s49, s49, s91
s_lshl_b32 s49, s49, 3
v_mfma_f32_16x16x32_bf16 v[172:175], a[192:195], a[144:147], v[172:175]
s_add_u32 s49, s49, 33554432
s_bfe_u32 s93, s61, 0x20001
s_lshl_b32 s93, s93, 3
v_mfma_f32_16x16x32_bf16 v[180:183], a[184:187], a[152:155], 0
s_waitcnt lgkmcnt(11)
ds_read_b64_tr_b16 a[128:129], v204 offset:49664
ds_read_b64_tr_b16 a[130:131], v204 offset:49920
v_mfma_f32_16x16x32_bf16 v[180:183], a[192:195], a[156:159], v[180:183]
s_add_u32 m0, s40, 149504
ds_read_b64_tr_b16 a[132:133], v204 offset:57856
ds_read_b64_tr_b16 a[134:135], v204 offset:58112
v_mfma_f32_16x16x32_bf16 v[188:191], a[184:187], a[160:163], 0
s_add_u32 s59, s66, 49152
buffer_load_dwordx4 v215, s[8:11], s59 offen lds
v_mfma_f32_16x16x32_bf16 v[188:191], a[192:195], a[164:167], v[188:191]
s_add_u32 s93, s93, s91
s_lshl_b32 s93, s93, 1
s_lshl_b32 s50, s93, 7
v_mfma_f32_16x16x32_bf16 v[64:67], a[188:191], a[136:139], v[64:67]
s_waitcnt lgkmcnt(9)
buffer_load_dwordx4 v216, s[8:11], s59 offen offset:1024 lds
v_mfma_f32_16x16x32_bf16 v[64:67], a[204:207], a[148:151], v[64:67]
s_lshl_b32 s84, s92, 6
s_add_u32 s50, s50, s84
s_add_u32 s50, s50, 33554432
v_mfma_f32_16x16x32_bf16 v[72:75], a[188:191], a[196:199], v[72:75]
s_lshr_b32 s84, s62, 1
s_add_u32 s93, s93, s84
s_lshl_b32 s60, s93, 19
v_mfma_f32_16x16x32_bf16 v[72:75], a[204:207], a[200:203], v[72:75]
s_lshl_b32 s84, s92, 18
s_or_b32 s60, s60, s84
s_and_b32 s84, s93, 3
v_mfma_f32_16x16x32_bf16 v[80:83], a[188:191], a[168:171], v[80:83]
s_waitcnt lgkmcnt(6)
ds_read_b64_tr_b16 a[140:141], v207 offset:49664
ds_read_b64_tr_b16 a[142:143], v207 offset:49920
v_mfma_f32_16x16x32_bf16 v[80:83], a[204:207], a[172:175], v[80:83]
s_add_u32 m0, s40, 16384
ds_read_b64_tr_b16 a[144:145], v207 offset:57856
ds_read_b64_tr_b16 a[146:147], v207 offset:58112
v_mfma_f32_16x16x32_bf16 v[88:91], a[188:191], a[208:211], v[88:91]
s_add_u32 s56, s67, 0
buffer_load_dwordx4 v215, s[8:11], s56 offen lds
v_mfma_f32_16x16x32_bf16 v[88:91], a[204:207], a[212:215], v[88:91]
s_and_b32 s85, s62, 1
s_xor_b32 s84, s84, s85
s_lshl_b32 s84, s84, 10
v_mfma_f32_16x16x32_bf16 v[68:71], a[216:219], a[136:139], v[68:71]
s_waitcnt lgkmcnt(8)
buffer_load_dwordx4 v216, s[8:11], s56 offen offset:1024 lds
v_mfma_f32_16x16x32_bf16 v[68:71], a[220:223], a[148:151], v[68:71]
s_or_b32 s60, s60, s84
s_lshl_b32 s90, s90, 8
s_and_b32 s51, s93, 3
v_mfma_f32_16x16x32_bf16 v[76:79], a[216:219], a[196:199], v[76:79]
s_lshl_b32 s51, s51, 16
s_add_u32 s51, s51, s90
s_lshl_b32 s84, s85, 7
v_mfma_f32_16x16x32_bf16 v[76:79], a[220:223], a[200:203], v[76:79]
s_add_u32 s51, s51, s84
s_lshl_b32 s84, s92, 6
s_add_u32 s51, s51, s84
v_mfma_f32_16x16x32_bf16 v[84:87], a[216:219], a[168:171], v[84:87]
s_waitcnt lgkmcnt(6)
ds_read_b64_tr_b16 a[152:153], v204 offset:50176
ds_read_b64_tr_b16 a[154:155], v204 offset:50432
v_mfma_f32_16x16x32_bf16 v[84:87], a[220:223], a[172:175], v[84:87]
s_add_u32 m0, s40, 18432
ds_read_b64_tr_b16 a[156:157], v204 offset:58368
ds_read_b64_tr_b16 a[158:159], v204 offset:58624
v_mfma_f32_16x16x32_bf16 v[92:95], a[216:219], a[208:211], v[92:95]
s_add_u32 s57, s67, 16384
buffer_load_dwordx4 v215, s[8:11], s57 offen lds
v_mfma_f32_16x16x32_bf16 v[92:95], a[220:223], a[212:215], v[92:95]
s_lshl_b32 s84, s92, 21
s_add_u32 s90, s90, s84
s_lshl_b32 s98, s61, 2
v_mfma_f32_16x16x32_bf16 v[96:99], a[128:131], a[136:139], v[96:99]
s_waitcnt lgkmcnt(8)
buffer_load_dwordx4 v216, s[8:11], s57 offen offset:1024 lds
v_mfma_f32_16x16x32_bf16 v[96:99], a[132:135], a[148:151], v[96:99]
s_add_u32 s98, s98, s62
s_lshl_b32 s98, s98, 13
s_add_u32 s98, s98, 41943040
v_mfma_f32_16x16x32_bf16 v[104:107], a[128:131], a[196:199], v[104:107]
s_lshl_b32 s84, s62, 1
s_add_u32 s84, s84, s88
s_and_b32 s84, s84, 7
v_mfma_f32_16x16x32_bf16 v[104:107], a[132:135], a[200:203], v[104:107]
s_lshl_b32 s84, s84, 10
s_or_b32 s98, s98, s84
s_lshr_b32 s92, s93, 2
v_mfma_f32_16x16x32_bf16 v[112:115], a[128:131], a[168:171], v[112:115]
s_waitcnt lgkmcnt(6)
ds_read_b64_tr_b16 a[160:161], v207 offset:50176
ds_read_b64_tr_b16 a[162:163], v207 offset:50432
v_mfma_f32_16x16x32_bf16 v[112:115], a[132:135], a[172:175], v[112:115]
s_add_u32 m0, s40, 32768
ds_read_b64_tr_b16 a[164:165], v207 offset:58368
ds_read_b64_tr_b16 a[166:167], v207 offset:58624
v_mfma_f32_16x16x32_bf16 v[120:123], a[128:131], a[208:211], v[120:123]
s_add_u32 s58, s67, 32768
buffer_load_dwordx4 v215, s[8:11], s58 offen lds
v_mfma_f32_16x16x32_bf16 v[120:123], a[132:135], a[212:215], v[120:123]
s_mul_i32 s92, s92, 245760
s_add_u32 s36, s36, s92
s_addc_u32 s37, s37, 0
v_mfma_f32_16x16x32_bf16 v[100:103], a[140:143], a[136:139], v[100:103]
s_waitcnt lgkmcnt(8)
buffer_load_dwordx4 v216, s[8:11], s58 offen offset:1024 lds
v_mfma_f32_16x16x32_bf16 v[100:103], a[144:147], a[148:151], v[100:103]
s_and_b32 s37, s37, 0xffff
s_mov_b32 s38, 245760
s_mov_b32 s39, 0x20000
v_mfma_f32_16x16x32_bf16 v[108:111], a[140:143], a[196:199], v[108:111]
s_add_u32 s86, s88, s91
s_add_u32 s84, s86, 0
s_and_b32 s84, s84, 1
v_mfma_f32_16x16x32_bf16 v[108:111], a[144:147], a[200:203], v[108:111]
s_lshl_b32 s72, s84, 14
s_add_u32 s72, s72, s60
s_lshl_b32 s85, s84, 12
v_mfma_f32_16x16x32_bf16 v[116:119], a[140:143], a[168:171], v[116:119]
s_waitcnt lgkmcnt(6)
ds_read_b64_tr_b16 a[128:129], v204 offset:50688
ds_read_b64_tr_b16 a[130:131], v204 offset:50944
v_mfma_f32_16x16x32_bf16 v[116:119], a[144:147], a[172:175], v[116:119]
s_xor_b32 s72, s72, s85
ds_read_b64_tr_b16 a[132:133], v204 offset:58880
ds_read_b64_tr_b16 a[134:135], v204 offset:59136
v_mfma_f32_16x16x32_bf16 v[124:127], a[140:143], a[208:211], v[124:127]
s_add_u32 s59, s67, 49152
ds_read_b64_tr_b16 a[176:177], v207 offset:58880
ds_read_b64_tr_b16 a[178:179], v207 offset:59136
v_mfma_f32_16x16x32_bf16 v[124:127], a[144:147], a[212:215], v[124:127]
s_add_u32 m0, s40, 34816
ds_read_b64_tr_b16 a[140:141], v207 offset:50688
ds_read_b64_tr_b16 a[142:143], v207 offset:50944
v_mfma_f32_16x16x32_bf16 v[128:131], a[152:155], a[136:139], v[128:131]
s_waitcnt lgkmcnt(12)
buffer_load_dwordx4 v215, s[8:11], s59 offen lds
v_mfma_f32_16x16x32_bf16 v[128:131], a[156:159], a[148:151], v[128:131]
s_lshl_b32 s80, s84, 17
s_add_u32 s80, s80, s90
s_add_u32 s84, s86, 1
v_mfma_f32_16x16x32_bf16 v[136:139], a[152:155], a[196:199], v[136:139]
s_and_b32 s84, s84, 1
buffer_load_dwordx4 v216, s[8:11], s59 offen offset:1024 lds
v_mfma_f32_16x16x32_bf16 v[136:139], a[156:159], a[200:203], v[136:139]
s_lshl_b32 s73, s84, 14
s_add_u32 s73, s73, s60
s_lshl_b32 s85, s84, 12
v_mfma_f32_16x16x32_bf16 v[144:147], a[152:155], a[168:171], v[144:147]
s_waitcnt lgkmcnt(10)
s_xor_b32 s73, s73, s85
s_lshl_b32 s81, s84, 17
v_mfma_f32_16x16x32_bf16 v[144:147], a[156:159], a[172:175], v[144:147]
s_add_u32 s81, s81, s90
s_mov_b32 s47, 0x3e0293ee
s_mov_b32 s48, 0x5f800000
v_mfma_f32_16x16x32_bf16 v[152:155], a[152:155], a[208:211], v[152:155]
s_lshl_b32 s56, s68, 12
v_mfma_f32_16x16x32_bf16 v[152:155], a[156:159], a[212:215], v[152:155]
s_add_u32 m0, s40, 0
v_mfma_f32_16x16x32_bf16 v[132:135], a[160:163], a[136:139], v[132:135]
s_waitcnt vmcnt(12) lgkmcnt(0)
s_barrier
v_mfma_f32_16x16x32_bf16 v[132:135], a[164:167], a[148:151], v[132:135]
s_add_u32 s56, s56, s43
buffer_load_dwordx4 v213, s[12:15], s56 offen lds
v_mfma_f32_16x16x32_bf16 v[140:143], a[160:163], a[196:199], v[140:143]
ds_read_b128 a[180:183], v197 offset:2048
ds_read_b128 a[184:187], v197 offset:2560
v_mfma_f32_16x16x32_bf16 v[140:143], a[164:167], a[200:203], v[140:143]
ds_read_b64_tr_b16 a[156:157], v205 offset:32768
ds_read_b64_tr_b16 a[158:159], v205 offset:33024
v_mfma_f32_16x16x32_bf16 v[148:151], a[160:163], a[168:171], v[148:151]
s_waitcnt vmcnt(9)
ds_read_b128 a[144:147], v197 offset:0
ds_read_b128 a[152:155], v197 offset:512
v_mfma_f32_16x16x32_bf16 v[148:151], a[164:167], a[172:175], v[148:151]
ds_read_b64_tr_b16 a[188:189], v205 offset:40960
ds_read_b64_tr_b16 a[190:191], v205 offset:41216
v_mfma_f32_16x16x32_bf16 v[156:159], a[160:163], a[208:211], v[156:159]
buffer_load_dwordx4 v214, s[12:15], s56 offen offset:2048 lds
v_mfma_f32_16x16x32_bf16 v[156:159], a[164:167], a[212:215], v[156:159]
s_add_u32 m0, s40, 1024
ds_read_b128 a[192:195], v197 offset:18432
ds_read_b128 a[204:207], v197 offset:18944
v_mfma_f32_16x16x32_bf16 v[160:163], a[128:131], a[136:139], v[160:163]
ds_read_b128 a[160:163], v197 offset:16384
ds_read_b128 a[164:167], v197 offset:16896
v_mfma_f32_16x16x32_bf16 v[160:163], a[132:135], a[148:151], v[160:163]
s_add_u32 s57, s56, 128
buffer_load_dwordx4 v213, s[12:15], s57 offen lds
v_mfma_f32_16x16x32_bf16 v[168:171], a[128:131], a[196:199], v[168:171]
s_cmp_eq_u32 s62, 3
s_cselect_b32 s99, 0x7149f2ca, 0
s_cselect_b32 s85, 0, 0x7f800000
v_mfma_f32_16x16x32_bf16 v[168:171], a[132:135], a[200:203], v[168:171]
buffer_load_dwordx4 v214, s[12:15], s57 offen offset:2048 lds
v_mfma_f32_16x16x32_bf16 v[176:179], a[128:131], a[168:171], v[176:179]
ds_read_b64_tr_b16 a[216:217], v208 offset:32768
ds_read_b64_tr_b16 a[218:219], v208 offset:33024
v_mfma_f32_16x16x32_bf16 v[176:179], a[132:135], a[172:175], v[176:179]
ds_read_b64_tr_b16 a[220:221], v208 offset:40960
ds_read_b64_tr_b16 a[222:223], v208 offset:41216
v_mfma_f32_16x16x32_bf16 v[184:187], a[128:131], a[208:211], v[184:187]
s_lshl_b32 s58, s69, 12
v_lshlrev_b32 v217, 4, v0
v_lshlrev_b32 v4, 1, v4
v_mfma_f32_16x16x32_bf16 v[184:187], a[132:135], a[212:215], v[184:187]
s_add_u32 m0, s40, 65536
v_xor_b32 v6, v3, v4
v_lshl_add_u32 v6, v6, 4, v2
v_mfma_f32_16x16x32_bf16 v[164:167], a[140:143], a[136:139], v[164:167]
s_lshl_b32 s84, s62, 12
v_lshl_add_u32 v7, v1, 8, s84
v_mfma_f32_16x16x32_bf16 v[164:167], a[176:179], a[148:151], v[164:167]
s_add_u32 s58, s58, s43
buffer_load_dwordx4 v213, s[12:15], s58 offen lds
v_mfma_f32_16x16x32_bf16 v[172:175], a[140:143], a[196:199], v[172:175]
v_add_u32 v210, v7, v5
v_xor_b32 v211, 48, v210
v_mfma_f32_16x16x32_bf16 v[172:175], a[176:179], a[200:203], v[172:175]
buffer_load_dwordx4 v214, s[12:15], s58 offen offset:2048 lds
v_mfma_f32_16x16x32_bf16 v[180:183], a[140:143], a[168:171], v[180:183]
s_waitcnt lgkmcnt(11)
ds_read_b64_tr_b16 a[128:129], v205 offset:33280
ds_read_b64_tr_b16 a[130:131], v205 offset:33536
v_mfma_f32_16x16x32_bf16 v[180:183], a[176:179], a[172:175], v[180:183]
ds_read_b64_tr_b16 a[132:133], v205 offset:41472
ds_read_b64_tr_b16 a[134:135], v205 offset:41728
v_mfma_f32_16x16x32_bf16 v[188:191], a[140:143], a[208:211], v[188:191]
L_tch1_%=:
s_cbranch_execz L_tch5_%=
v_add_u32 v212, v7, v6
v_lshlrev_b32 v252, 26, v0
v_mfma_f32_16x16x32_bf16 v[188:191], a[176:179], a[212:215], v[188:191]
s_add_u32 m0, s40, 66560
v_lshl_add_u32 v7, v17, 11, v16
v_add_u32 v7, s63, v7
v_mfma_f32_16x16x32_bf16 v[64:67], a[156:159], a[144:147], v[64:67]
s_waitcnt lgkmcnt(9)
v_add_u32 v192, 0, v7
v_add_u32 v193, 65536, v7
v_mfma_f32_16x16x32_bf16 v[64:67], a[188:191], a[152:155], v[64:67]
s_add_u32 s59, s58, 128
buffer_load_dwordx4 v213, s[12:15], s59 offen lds
v_mfma_f32_16x16x32_bf16 v[72:75], a[156:159], a[180:183], v[72:75]
v_add_u32 v194, 131072, v7
v_lshl_add_u32 v7, v13, 11, v18
v_mfma_f32_16x16x32_bf16 v[72:75], a[188:191], a[184:187], v[72:75]
buffer_load_dwordx4 v214, s[12:15], s59 offen offset:2048 lds
v_mfma_f32_16x16x32_bf16 v[80:83], a[156:159], a[160:163], v[80:83]
s_waitcnt lgkmcnt(6)
ds_read_b64_tr_b16 a[136:137], v208 offset:33280
ds_read_b64_tr_b16 a[138:139], v208 offset:33536
v_mfma_f32_16x16x32_bf16 v[80:83], a[188:191], a[164:167], v[80:83]
ds_read_b64_tr_b16 a[140:141], v208 offset:41472
ds_read_b64_tr_b16 a[142:143], v208 offset:41728
v_mfma_f32_16x16x32_bf16 v[88:91], a[156:159], a[192:195], v[88:91]
v_lshl_add_u32 v7, v12, 8, v7
v_add_u32 v198, 0, v7
v_mfma_f32_16x16x32_bf16 v[88:91], a[188:191], a[204:207], v[88:91]
s_add_u32 m0, s40, 81920
v_xor_b32 v201, 32, v198
v_add_u32 v199, 65536, v7
v_mfma_f32_16x16x32_bf16 v[68:71], a[216:219], a[144:147], v[68:71]
s_waitcnt lgkmcnt(8)
v_xor_b32 v202, 32, v199
v_add_u32 v200, 131072, v7
v_mfma_f32_16x16x32_bf16 v[68:71], a[220:223], a[152:155], v[68:71]
s_add_u32 s56, s68, 0
buffer_load_dwordx4 v215, s[8:11], s56 offen lds
v_mfma_f32_16x16x32_bf16 v[76:79], a[216:219], a[180:183], v[76:79]
v_xor_b32 v203, 32, v200
v_cmp_eq_u32 vcc, 0, v3
v_mfma_f32_16x16x32_bf16 v[76:79], a[220:223], a[184:187], v[76:79]
buffer_load_dwordx4 v216, s[8:11], s56 offen offset:1024 lds
v_mfma_f32_16x16x32_bf16 v[84:87], a[216:219], a[160:163], v[84:87]
s_waitcnt lgkmcnt(6)
ds_read_b64_tr_b16 a[148:149], v205 offset:33792
ds_read_b64_tr_b16 a[150:151], v205 offset:34048
v_mfma_f32_16x16x32_bf16 v[84:87], a[220:223], a[164:167], v[84:87]
ds_read_b64_tr_b16 a[156:157], v205 offset:41984
ds_read_b64_tr_b16 a[158:159], v205 offset:42240
v_mfma_f32_16x16x32_bf16 v[92:95], a[216:219], a[192:195], v[92:95]
v_mov_b32 v6, 0x3f803f80
v_mfma_f32_16x16x32_bf16 v[92:95], a[220:223], a[204:207], v[92:95]
s_add_u32 m0, s40, 83968
v_mfma_f32_16x16x32_bf16 v[96:99], a[128:131], a[144:147], v[96:99]
s_waitcnt lgkmcnt(8)
v_cndmask_b32 v232, 0, v6, vcc
v_cndmask_b32 v233, 0, v6, vcc
v_mfma_f32_16x16x32_bf16 v[96:99], a[132:135], a[152:155], v[96:99]
s_add_u32 s57, s68, 16384
buffer_load_dwordx4 v215, s[8:11], s57 offen lds
v_mfma_f32_16x16x32_bf16 v[104:107], a[128:131], a[180:183], v[104:107]
v_cndmask_b32 v234, 0, v6, vcc
v_cndmask_b32 v235, 0, v6, vcc
v_mfma_f32_16x16x32_bf16 v[104:107], a[132:135], a[184:187], v[104:107]
buffer_load_dwordx4 v216, s[8:11], s57 offen offset:1024 lds
v_mfma_f32_16x16x32_bf16 v[112:115], a[128:131], a[160:163], v[112:115]
s_waitcnt lgkmcnt(6)
ds_read_b64_tr_b16 a[168:169], v208 offset:33792
ds_read_b64_tr_b16 a[170:171], v208 offset:34048
v_mfma_f32_16x16x32_bf16 v[112:115], a[132:135], a[164:167], v[112:115]
ds_read_b64_tr_b16 a[172:173], v208 offset:41984
ds_read_b64_tr_b16 a[174:175], v208 offset:42240
v_mfma_f32_16x16x32_bf16 v[120:123], a[128:131], a[192:195], v[120:123]
v_mfma_f32_16x16x32_bf16 v[120:123], a[132:135], a[204:207], v[120:123]
s_add_u32 m0, s40, 49152
v_mfma_f32_16x16x32_bf16 v[100:103], a[136:139], a[144:147], v[100:103]
s_waitcnt lgkmcnt(8)
v_mfma_f32_16x16x32_bf16 v[100:103], a[140:143], a[152:155], v[100:103]
s_add_u32 s58, s68, 32768
buffer_load_dwordx4 v215, s[8:11], s58 offen lds
v_mfma_f32_16x16x32_bf16 v[108:111], a[136:139], a[180:183], v[108:111]
v_mfma_f32_16x16x32_bf16 v[108:111], a[140:143], a[184:187], v[108:111]
buffer_load_dwordx4 v216, s[8:11], s58 offen offset:1024 lds
v_mfma_f32_16x16x32_bf16 v[116:119], a[136:139], a[160:163], v[116:119]
s_waitcnt lgkmcnt(6)
ds_read_b64_tr_b16 a[128:129], v205 offset:34304
ds_read_b64_tr_b16 a[130:131], v205 offset:34560
v_mfma_f32_16x16x32_bf16 v[116:119], a[140:143], a[164:167], v[116:119]
ds_read_b64_tr_b16 a[132:133], v205 offset:42496
ds_read_b64_tr_b16 a[134:135], v205 offset:42752
v_mfma_f32_16x16x32_bf16 v[124:127], a[136:139], a[192:195], v[124:127]
v_mfma_f32_16x16x32_bf16 v[124:127], a[140:143], a[204:207], v[124:127]
s_add_u32 m0, s40, 51200
v_mfma_f32_16x16x32_bf16 v[128:131], a[148:151], a[144:147], v[128:131]
s_waitcnt lgkmcnt(8)
v_mfma_f32_16x16x32_bf16 v[128:131], a[156:159], a[152:155], v[128:131]
s_add_u32 s59, s68, 49152
buffer_load_dwordx4 v215, s[8:11], s59 offen lds
v_mfma_f32_16x16x32_bf16 v[136:139], a[148:151], a[180:183], v[136:139]
v_mfma_f32_16x16x32_bf16 v[136:139], a[156:159], a[184:187], v[136:139]
buffer_load_dwordx4 v216, s[8:11], s59 offen offset:1024 lds
v_mfma_f32_16x16x32_bf16 v[144:147], a[148:151], a[160:163], v[144:147]
s_waitcnt lgkmcnt(6)
ds_read_b64_tr_b16 a[136:137], v208 offset:34304
ds_read_b64_tr_b16 a[138:139], v208 offset:34560
v_mfma_f32_16x16x32_bf16 v[144:147], a[156:159], a[164:167], v[144:147]
ds_read_b64_tr_b16 a[140:141], v208 offset:42496
ds_read_b64_tr_b16 a[142:143], v208 offset:42752
v_mfma_f32_16x16x32_bf16 v[152:155], a[148:151], a[192:195], v[152:155]
v_mfma_f32_16x16x32_bf16 v[152:155], a[156:159], a[204:207], v[152:155]
s_add_u32 m0, s40, 131072
v_mfma_f32_16x16x32_bf16 v[132:135], a[168:171], a[144:147], v[132:135]
s_waitcnt vmcnt(20) lgkmcnt(8)
v_mfma_f32_16x16x32_bf16 v[132:135], a[172:175], a[152:155], v[132:135]
s_add_u32 s56, s69, 0
buffer_load_dwordx4 v215, s[8:11], s56 offen lds
v_mfma_f32_16x16x32_bf16 v[140:143], a[168:171], a[180:183], v[140:143]
ds_read_b128 a[188:191], v195 offset:18432
ds_read_b128 a[196:199], v195 offset:18944
v_mfma_f32_16x16x32_bf16 v[140:143], a[172:175], a[184:187], v[140:143]
ds_read_b64_tr_b16 a[176:177], v205 offset:49152
ds_read_b64_tr_b16 a[178:179], v205 offset:49408
v_mfma_f32_16x16x32_bf16 v[148:151], a[168:171], a[160:163], v[148:151]
s_waitcnt vmcnt(17) lgkmcnt(10)
ds_read_b128 a[148:151], v195 offset:16384
ds_read_b128 a[156:159], v195 offset:16896
v_mfma_f32_16x16x32_bf16 v[148:151], a[172:175], a[164:167], v[148:151]
ds_read_b64_tr_b16 a[200:201], v205 offset:57344
ds_read_b64_tr_b16 a[202:203], v205 offset:57600
v_mfma_f32_16x16x32_bf16 v[156:159], a[168:171], a[192:195], v[156:159]
buffer_load_dwordx4 v216, s[8:11], s56 offen offset:1024 lds
v_mfma_f32_16x16x32_bf16 v[156:159], a[172:175], a[204:207], v[156:159]
s_add_u32 m0, s40, 133120
ds_read_b128 a[208:211], v195 offset:34816
ds_read_b128 a[212:215], v195 offset:35328
v_mfma_f32_16x16x32_bf16 v[160:163], a[128:131], a[144:147], v[160:163]
s_waitcnt lgkmcnt(14)
ds_read_b128 a[168:171], v195 offset:32768
ds_read_b128 a[172:175], v195 offset:33280
v_mfma_f32_16x16x32_bf16 v[160:163], a[132:135], a[152:155], v[160:163]
s_add_u32 s57, s69, 16384
buffer_load_dwordx4 v215, s[8:11], s57 offen lds
v_mfma_f32_16x16x32_bf16 v[168:171], a[128:131], a[180:183], v[168:171]
v_mfma_f32_16x16x32_bf16 v[168:171], a[132:135], a[184:187], v[168:171]
buffer_load_dwordx4 v216, s[8:11], s57 offen offset:1024 lds
v_mfma_f32_16x16x32_bf16 v[176:179], a[128:131], a[160:163], v[176:179]
s_waitcnt lgkmcnt(14)
ds_read_b64_tr_b16 a[216:217], v208 offset:49152
ds_read_b64_tr_b16 a[218:219], v208 offset:49408
v_mfma_f32_16x16x32_bf16 v[176:179], a[132:135], a[164:167], v[176:179]
ds_read_b64_tr_b16 a[220:221], v208 offset:57344
ds_read_b64_tr_b16 a[222:223], v208 offset:57600
v_mfma_f32_16x16x32_bf16 v[184:187], a[128:131], a[192:195], v[184:187]
v_mfma_f32_16x16x32_bf16 v[184:187], a[132:135], a[204:207], v[184:187]
s_add_u32 m0, s40, 147456
v_mfma_f32_16x16x32_bf16 v[164:167], a[136:139], a[144:147], v[164:167]
s_waitcnt lgkmcnt(15)
v_mfma_f32_16x16x32_bf16 v[164:167], a[140:143], a[152:155], v[164:167]
s_add_u32 s58, s69, 32768
buffer_load_dwordx4 v215, s[8:11], s58 offen lds
v_mfma_f32_16x16x32_bf16 v[172:175], a[136:139], a[180:183], v[172:175]
v_mfma_f32_16x16x32_bf16 v[172:175], a[140:143], a[184:187], v[172:175]
buffer_load_dwordx4 v216, s[8:11], s58 offen offset:1024 lds
v_mfma_f32_16x16x32_bf16 v[180:183], a[136:139], a[160:163], v[180:183]
s_waitcnt lgkmcnt(11)
ds_read_b64_tr_b16 a[128:129], v205 offset:49664
ds_read_b64_tr_b16 a[130:131], v205 offset:49920
v_mfma_f32_16x16x32_bf16 v[180:183], a[140:143], a[164:167], v[180:183]
ds_read_b64_tr_b16 a[132:133], v205 offset:57856
ds_read_b64_tr_b16 a[134:135], v205 offset:58112
v_mfma_f32_16x16x32_bf16 v[188:191], a[136:139], a[192:195], v[188:191]
v_mfma_f32_16x16x32_bf16 v[188:191], a[140:143], a[204:207], v[188:191]
s_add_u32 m0, s40, 149504
v_mfma_f32_16x16x32_bf16 v[64:67], a[176:179], a[148:151], v[64:67]
s_waitcnt lgkmcnt(9)
v_mfma_f32_16x16x32_bf16 v[64:67], a[200:203], a[156:159], v[64:67]
s_add_u32 s59, s69, 49152
buffer_load_dwordx4 v215, s[8:11], s59 offen lds
v_mfma_f32_16x16x32_bf16 v[72:75], a[176:179], a[188:191], v[72:75]
v_mfma_f32_16x16x32_bf16 v[72:75], a[200:203], a[196:199], v[72:75]
buffer_load_dwordx4 v216, s[8:11], s59 offen offset:1024 lds
v_mfma_f32_16x16x32_bf16 v[80:83], a[176:179], a[168:171], v[80:83]
s_waitcnt lgkmcnt(6)
ds_read_b64_tr_b16 a[136:137], v208 offset:49664
ds_read_b64_tr_b16 a[138:139], v208 offset:49920
v_mfma_f32_16x16x32_bf16 v[80:83], a[200:203], a[172:175], v[80:83]
ds_read_b64_tr_b16 a[140:141], v208 offset:57856
ds_read_b64_tr_b16 a[142:143], v208 offset:58112
v_mfma_f32_16x16x32_bf16 v[88:91], a[176:179], a[208:211], v[88:91]
v_mfma_f32_16x16x32_bf16 v[88:91], a[200:203], a[212:215], v[88:91]
s_add_u32 m0, s40, 16384
v_mfma_f32_16x16x32_bf16 v[68:71], a[216:219], a[148:151], v[68:71]
s_waitcnt lgkmcnt(8)
v_mfma_f32_16x16x32_bf16 v[68:71], a[220:223], a[156:159], v[68:71]
s_add_u32 s56, s70, 0
buffer_load_dwordx4 v215, s[8:11], s56 offen lds
v_mfma_f32_16x16x32_bf16 v[76:79], a[216:219], a[188:191], v[76:79]
v_mfma_f32_16x16x32_bf16 v[76:79], a[220:223], a[196:199], v[76:79]
buffer_load_dwordx4 v216, s[8:11], s56 offen offset:1024 lds
v_mfma_f32_16x16x32_bf16 v[84:87], a[216:219], a[168:171], v[84:87]
s_waitcnt lgkmcnt(6)
ds_read_b64_tr_b16 a[144:145], v205 offset:50176
ds_read_b64_tr_b16 a[146:147], v205 offset:50432
v_mfma_f32_16x16x32_bf16 v[84:87], a[220:223], a[172:175], v[84:87]
ds_read_b64_tr_b16 a[152:153], v205 offset:58368
ds_read_b64_tr_b16 a[154:155], v205 offset:58624
v_mfma_f32_16x16x32_bf16 v[92:95], a[216:219], a[208:211], v[92:95]
v_mfma_f32_16x16x32_bf16 v[92:95], a[220:223], a[212:215], v[92:95]
s_add_u32 m0, s40, 18432
v_mfma_f32_16x16x32_bf16 v[96:99], a[128:131], a[148:151], v[96:99]
s_waitcnt lgkmcnt(8)
v_mfma_f32_16x16x32_bf16 v[96:99], a[132:135], a[156:159], v[96:99]
s_add_u32 s57, s70, 16384
buffer_load_dwordx4 v215, s[8:11], s57 offen lds
v_mfma_f32_16x16x32_bf16 v[104:107], a[128:131], a[188:191], v[104:107]
v_mfma_f32_16x16x32_bf16 v[104:107], a[132:135], a[196:199], v[104:107]
buffer_load_dwordx4 v216, s[8:11], s57 offen offset:1024 lds
v_mfma_f32_16x16x32_bf16 v[112:115], a[128:131], a[168:171], v[112:115]
s_waitcnt lgkmcnt(6)
ds_read_b64_tr_b16 a[160:161], v208 offset:50176
ds_read_b64_tr_b16 a[162:163], v208 offset:50432
v_mfma_f32_16x16x32_bf16 v[112:115], a[132:135], a[172:175], v[112:115]
ds_read_b64_tr_b16 a[164:165], v208 offset:58368
ds_read_b64_tr_b16 a[166:167], v208 offset:58624
v_mfma_f32_16x16x32_bf16 v[120:123], a[128:131], a[208:211], v[120:123]
v_mfma_f32_16x16x32_bf16 v[120:123], a[132:135], a[212:215], v[120:123]
s_add_u32 m0, s40, 32768
v_mfma_f32_16x16x32_bf16 v[100:103], a[136:139], a[148:151], v[100:103]
s_waitcnt lgkmcnt(8)
v_mfma_f32_16x16x32_bf16 v[100:103], a[140:143], a[156:159], v[100:103]
s_add_u32 s58, s70, 32768
buffer_load_dwordx4 v215, s[8:11], s58 offen lds
v_mfma_f32_16x16x32_bf16 v[108:111], a[136:139], a[188:191], v[108:111]
v_mfma_f32_16x16x32_bf16 v[108:111], a[140:143], a[196:199], v[108:111]
buffer_load_dwordx4 v216, s[8:11], s58 offen offset:1024 lds
v_mfma_f32_16x16x32_bf16 v[116:119], a[136:139], a[168:171], v[116:119]
s_waitcnt lgkmcnt(6)
ds_read_b64_tr_b16 a[128:129], v205 offset:50688
ds_read_b64_tr_b16 a[130:131], v205 offset:50944
v_mfma_f32_16x16x32_bf16 v[116:119], a[140:143], a[172:175], v[116:119]
ds_read_b64_tr_b16 a[132:133], v205 offset:58880
ds_read_b64_tr_b16 a[134:135], v205 offset:59136
v_mfma_f32_16x16x32_bf16 v[124:127], a[136:139], a[208:211], v[124:127]
ds_read_b64_tr_b16 a[176:177], v208 offset:58880
ds_read_b64_tr_b16 a[178:179], v208 offset:59136
v_mfma_f32_16x16x32_bf16 v[124:127], a[140:143], a[212:215], v[124:127]
s_add_u32 m0, s40, 34816
ds_read_b64_tr_b16 a[136:137], v208 offset:50688
ds_read_b64_tr_b16 a[138:139], v208 offset:50944
v_mfma_f32_16x16x32_bf16 v[128:131], a[144:147], a[148:151], v[128:131]
s_waitcnt lgkmcnt(12)
v_mfma_f32_16x16x32_bf16 v[128:131], a[152:155], a[156:159], v[128:131]
s_add_u32 s59, s70, 49152
buffer_load_dwordx4 v215, s[8:11], s59 offen lds
v_mfma_f32_16x16x32_bf16 v[136:139], a[144:147], a[188:191], v[136:139]
v_mfma_f32_16x16x32_bf16 v[136:139], a[152:155], a[196:199], v[136:139]
buffer_load_dwordx4 v216, s[8:11], s59 offen offset:1024 lds
v_mfma_f32_16x16x32_bf16 v[144:147], a[144:147], a[168:171], v[144:147]
s_waitcnt lgkmcnt(10)
v_mfma_f32_16x16x32_bf16 v[144:147], a[152:155], a[172:175], v[144:147]
v_mfma_f32_16x16x32_bf16 v[152:155], a[144:147], a[208:211], v[152:155]
v_mfma_f32_16x16x32_bf16 v[152:155], a[152:155], a[212:215], v[152:155]
s_add_u32 m0, s40, 98304
v_mfma_f32_16x16x32_bf16 v[132:135], a[160:163], a[148:151], v[132:135]
s_waitcnt vmcnt(20) lgkmcnt(0)
s_barrier
v_mfma_f32_16x16x32_bf16 v[132:135], a[164:167], a[156:159], v[132:135]
s_add_u32 s56, s71, 0
buffer_load_dwordx4 v215, s[8:11], s56 offen lds
v_mfma_f32_16x16x32_bf16 v[140:143], a[160:163], a[188:191], v[140:143]
ds_read_b128 a[180:183], v196 offset:18432
ds_read_b128 a[184:187], v196 offset:18944
v_mfma_f32_16x16x32_bf16 v[140:143], a[164:167], a[196:199], v[140:143]
ds_read_b64_tr_b16 a[152:153], v204 offset:0
ds_read_b64_tr_b16 a[154:155], v204 offset:256
v_mfma_f32_16x16x32_bf16 v[148:151], a[160:163], a[168:171], v[148:151]
s_waitcnt vmcnt(17)
ds_read_b128 a[140:143], v196 offset:16384
ds_read_b128 a[144:147], v196 offset:16896
v_mfma_f32_16x16x32_bf16 v[148:151], a[164:167], a[172:175], v[148:151]
ds_read_b64_tr_b16 a[192:193], v204 offset:8192
ds_read_b64_tr_b16 a[194:195], v204 offset:8448
v_mfma_f32_16x16x32_bf16 v[156:159], a[160:163], a[208:211], v[156:159]
buffer_load_dwordx4 v216, s[8:11], s56 offen offset:1024 lds
v_mfma_f32_16x16x32_bf16 v[156:159], a[164:167], a[212:215], v[156:159]
s_add_u32 m0, s40, 100352
ds_read_b128 a[200:203], v195 offset:51200
ds_read_b128 a[204:207], v195 offset:51712
v_mfma_f32_16x16x32_bf16 v[160:163], a[128:131], a[148:151], v[160:163]
ds_read_b128 a[160:163], v195 offset:49152
ds_read_b128 a[164:167], v195 offset:49664
v_mfma_f32_16x16x32_bf16 v[160:163], a[132:135], a[156:159], v[160:163]
s_add_u32 s57, s71, 16384
buffer_load_dwordx4 v215, s[8:11], s57 offen lds
v_mfma_f32_16x16x32_bf16 v[168:171], a[128:131], a[188:191], v[168:171]
v_readfirstlane_b32 s52, v254
v_readfirstlane_b32 s53, v255
v_mfma_f32_16x16x32_bf16 v[168:171], a[132:135], a[196:199], v[168:171]
buffer_load_dwordx4 v216, s[8:11], s57 offen offset:1024 lds
v_mfma_f32_16x16x32_bf16 v[176:179], a[128:131], a[168:171], v[176:179]
ds_read_b64_tr_b16 a[216:217], v207 offset:0
ds_read_b64_tr_b16 a[218:219], v207 offset:256
v_mfma_f32_16x16x32_bf16 v[176:179], a[132:135], a[172:175], v[176:179]
s_add_u32 s52, s52, 0x9e3779b9
ds_read_b64_tr_b16 a[220:221], v207 offset:8192
ds_read_b64_tr_b16 a[222:223], v207 offset:8448
v_mfma_f32_16x16x32_bf16 v[184:187], a[128:131], a[208:211], v[184:187]
s_lshl_b32 s58, s70, 12
s_mul_i32 s54, s52, 0x85ebca6b
s_xor_b32 s53, s53, s54
v_mfma_f32_16x16x32_bf16 v[184:187], a[132:135], a[212:215], v[184:187]
s_add_u32 m0, s40, 114688
s_add_u32 s53, s53, 0x7f4a7c15
v_mov_b32 v254, s52
v_mfma_f32_16x16x32_bf16 v[164:167], a[136:139], a[148:151], v[164:167]
v_mov_b32 v255, s53
v_mfma_f32_16x16x32_bf16 v[164:167], a[176:179], a[156:159], v[164:167]
s_add_u32 s58, s58, s43
buffer_load_dwordx4 v213, s[12:15], s58 offen lds
v_mfma_f32_16x16x32_bf16 v[172:175], a[136:139], a[188:191], v[172:175]
v_mfma_f32_16x16x32_bf16 v[172:175], a[176:179], a[196:199], v[172:175]
buffer_load_dwordx4 v214, s[12:15], s58 offen offset:2048 lds
v_mfma_f32_16x16x32_bf16 v[180:183], a[136:139], a[168:171], v[180:183]
s_waitcnt lgkmcnt(11)
ds_read_b64_tr_b16 a[128:129], v204 offset:512
ds_read_b64_tr_b16 a[130:131], v204 offset:768
v_mfma_f32_16x16x32_bf16 v[180:183], a[176:179], a[172:175], v[180:183]
ds_read_b64_tr_b16 a[132:133], v204 offset:8704
ds_read_b64_tr_b16 a[134:135], v204 offset:8960
v_mfma_f32_16x16x32_bf16 v[188:191], a[136:139], a[208:211], v[188:191]
v_mfma_f32_16x16x32_bf16 v[188:191], a[176:179], a[212:215], v[188:191]
s_add_u32 m0, s40, 115712
v_mfma_f32_16x16x32_bf16 v[64:67], a[152:155], a[140:143], v[64:67]
s_waitcnt lgkmcnt(9)
v_mfma_f32_16x16x32_bf16 v[64:67], a[192:195], a[144:147], v[64:67]
s_add_u32 s59, s58, 128
buffer_load_dwordx4 v213, s[12:15], s59 offen lds
v_mfma_f32_16x16x32_bf16 v[72:75], a[152:155], a[180:183], v[72:75]
v_mfma_f32_16x16x32_bf16 v[72:75], a[192:195], a[184:187], v[72:75]
buffer_load_dwordx4 v214, s[12:15], s59 offen offset:2048 lds
v_mfma_f32_16x16x32_bf16 v[80:83], a[152:155], a[160:163], v[80:83]
s_waitcnt lgkmcnt(6)
ds_read_b64_tr_b16 a[136:137], v207 offset:512
ds_read_b64_tr_b16 a[138:139], v207 offset:768
v_mfma_f32_16x16x32_bf16 v[80:83], a[192:195], a[164:167], v[80:83]
ds_read_b64_tr_b16 a[148:149], v207 offset:8704
ds_read_b64_tr_b16 a[150:151], v207 offset:8960
v_mfma_f32_16x16x32_bf16 v[88:91], a[152:155], a[200:203], v[88:91]
s_lshl_b32 s56, s71, 12
v_mfma_f32_16x16x32_bf16 v[88:91], a[192:195], a[204:207], v[88:91]
s_add_u32 m0, s40, 81920
v_mfma_f32_16x16x32_bf16 v[68:71], a[216:219], a[140:143], v[68:71]
s_waitcnt lgkmcnt(8)
v_mfma_f32_16x16x32_bf16 v[68:71], a[220:223], a[144:147], v[68:71]
s_add_u32 s56, s56, s43
buffer_load_dwordx4 v213, s[12:15], s56 offen lds
v_mfma_f32_16x16x32_bf16 v[76:79], a[216:219], a[180:183], v[76:79]
v_mfma_f32_16x16x32_bf16 v[76:79], a[220:223], a[184:187], v[76:79]
buffer_load_dwordx4 v214, s[12:15], s56 offen offset:2048 lds
v_mfma_f32_16x16x32_bf16 v[84:87], a[216:219], a[160:163], v[84:87]
s_waitcnt lgkmcnt(6)
ds_read_b64_tr_b16 a[152:153], v204 offset:1024
ds_read_b64_tr_b16 a[154:155], v204 offset:1280
v_mfma_f32_16x16x32_bf16 v[84:87], a[220:223], a[164:167], v[84:87]
ds_read_b64_tr_b16 a[156:157], v204 offset:9216
ds_read_b64_tr_b16 a[158:159], v204 offset:9472
v_mfma_f32_16x16x32_bf16 v[92:95], a[216:219], a[200:203], v[92:95]
v_mfma_f32_16x16x32_bf16 v[92:95], a[220:223], a[204:207], v[92:95]
s_add_u32 m0, s40, 82944
v_mfma_f32_16x16x32_bf16 v[96:99], a[128:131], a[140:143], v[96:99]
s_waitcnt lgkmcnt(8)
v_mfma_f32_16x16x32_bf16 v[96:99], a[132:135], a[144:147], v[96:99]
s_add_u32 s57, s56, 128
buffer_load_dwordx4 v213, s[12:15], s57 offen lds
v_mfma_f32_16x16x32_bf16 v[104:107], a[128:131], a[180:183], v[104:107]
v_mfma_f32_16x16x32_bf16 v[104:107], a[132:135], a[184:187], v[104:107]
buffer_load_dwordx4 v214, s[12:15], s57 offen offset:2048 lds
v_mfma_f32_16x16x32_bf16 v[112:115], a[128:131], a[160:163], v[112:115]
s_waitcnt lgkmcnt(6)
ds_read_b64_tr_b16 a[168:169], v207 offset:1024
ds_read_b64_tr_b16 a[170:171], v207 offset:1280
v_mfma_f32_16x16x32_bf16 v[112:115], a[132:135], a[164:167], v[112:115]
ds_read_b64_tr_b16 a[172:173], v207 offset:9216
ds_read_b64_tr_b16 a[174:175], v207 offset:9472
v_mfma_f32_16x16x32_bf16 v[120:123], a[128:131], a[200:203], v[120:123]
v_mfma_f32_16x16x32_bf16 v[120:123], a[132:135], a[204:207], v[120:123]
s_add_u32 m0, s40, 49152
v_mfma_f32_16x16x32_bf16 v[100:103], a[136:139], a[140:143], v[100:103]
s_waitcnt lgkmcnt(8)
v_mfma_f32_16x16x32_bf16 v[100:103], a[148:151], a[144:147], v[100:103]
s_add_u32 s58, s71, 32768
buffer_load_dwordx4 v215, s[8:11], s58 offen lds
v_mfma_f32_16x16x32_bf16 v[108:111], a[136:139], a[180:183], v[108:111]
v_mfma_f32_16x16x32_bf16 v[108:111], a[148:151], a[184:187], v[108:111]
buffer_load_dwordx4 v216, s[8:11], s58 offen offset:1024 lds
v_mfma_f32_16x16x32_bf16 v[116:119], a[136:139], a[160:163], v[116:119]
s_waitcnt lgkmcnt(6)
ds_read_b64_tr_b16 a[128:129], v204 offset:1536
ds_read_b64_tr_b16 a[130:131], v204 offset:1792
v_mfma_f32_16x16x32_bf16 v[116:119], a[148:151], a[164:167], v[116:119]
ds_read_b64_tr_b16 a[132:133], v204 offset:9728
ds_read_b64_tr_b16 a[134:135], v204 offset:9984
v_mfma_f32_16x16x32_bf16 v[124:127], a[136:139], a[200:203], v[124:127]
v_mfma_f32_16x16x32_bf16 v[124:127], a[148:151], a[204:207], v[124:127]
s_add_u32 m0, s40, 51200
v_mfma_f32_16x16x32_bf16 v[128:131], a[152:155], a[140:143], v[128:131]
s_waitcnt lgkmcnt(8)
v_mfma_f32_16x16x32_bf16 v[128:131], a[156:159], a[144:147], v[128:131]
s_add_u32 s59, s71, 49152
buffer_load_dwordx4 v215, s[8:11], s59 offen lds
v_mfma_f32_16x16x32_bf16 v[136:139], a[152:155], a[180:183], v[136:139]
v_mfma_f32_16x16x32_bf16 v[136:139], a[156:159], a[184:187], v[136:139]
buffer_load_dwordx4 v216, s[8:11], s59 offen offset:1024 lds
v_mfma_f32_16x16x32_bf16 v[144:147], a[152:155], a[160:163], v[144:147]
s_waitcnt lgkmcnt(6)
ds_read_b64_tr_b16 a[136:137], v207 offset:1536
ds_read_b64_tr_b16 a[138:139], v207 offset:1792
v_mfma_f32_16x16x32_bf16 v[144:147], a[156:159], a[164:167], v[144:147]
ds_read_b64_tr_b16 a[148:149], v207 offset:9728
ds_read_b64_tr_b16 a[150:151], v207 offset:9984
v_mfma_f32_16x16x32_bf16 v[152:155], a[152:155], a[200:203], v[152:155]
v_mfma_f32_16x16x32_bf16 v[152:155], a[156:159], a[204:207], v[152:155]
v_mfma_f32_16x16x32_bf16 v[132:135], a[168:171], a[140:143], v[132:135]
s_waitcnt vmcnt(28) lgkmcnt(8)
v_mfma_f32_16x16x32_bf16 v[132:135], a[172:175], a[144:147], v[132:135]
v_mfma_f32_16x16x32_bf16 v[140:143], a[168:171], a[180:183], v[140:143]
ds_read_b128 a[188:191], v197 offset:2048
ds_read_b128 a[192:195], v197 offset:2560
v_mfma_f32_16x16x32_bf16 v[140:143], a[172:175], a[184:187], v[140:143]
ds_read_b64_tr_b16 a[176:177], v205 offset:0
ds_read_b64_tr_b16 a[178:179], v205 offset:256
v_mfma_f32_16x16x32_bf16 v[148:151], a[168:171], a[160:163], v[148:151]
s_waitcnt vmcnt(24) lgkmcnt(10)
ds_read_b128 a[152:155], v197 offset:0
ds_read_b128 a[156:159], v197 offset:512
v_mfma_f32_16x16x32_bf16 v[148:151], a[172:175], a[164:167], v[148:151]
ds_read_b64_tr_b16 a[196:197], v205 offset:8192
ds_read_b64_tr_b16 a[198:199], v205 offset:8448
v_mfma_f32_16x16x32_bf16 v[156:159], a[168:171], a[200:203], v[156:159]
v_mfma_f32_16x16x32_bf16 v[156:159], a[172:175], a[204:207], v[156:159]
ds_read_b128 a[208:211], v197 offset:18432
ds_read_b128 a[212:215], v197 offset:18944
v_mfma_f32_16x16x32_bf16 v[160:163], a[128:131], a[140:143], v[160:163]
s_waitcnt vmcnt(20) lgkmcnt(14)
ds_read_b128 a[168:171], v197 offset:16384
ds_read_b128 a[172:175], v197 offset:16896
v_mfma_f32_16x16x32_bf16 v[160:163], a[132:135], a[144:147], v[160:163]
v_mfma_f32_16x16x32_bf16 v[168:171], a[128:131], a[180:183], v[168:171]
v_mfma_f32_16x16x32_bf16 v[168:171], a[132:135], a[184:187], v[168:171]
ds_read_b128 a[228:231], v195 offset:18432
ds_read_b128 a[232:235], v195 offset:18944
v_mfma_f32_16x16x32_bf16 v[176:179], a[128:131], a[160:163], v[176:179]
s_waitcnt lgkmcnt(15)
ds_read_b64_tr_b16 a[216:217], v208 offset:0
ds_read_b64_tr_b16 a[218:219], v208 offset:256
v_mfma_f32_16x16x32_bf16 v[176:179], a[132:135], a[164:167], v[176:179]
ds_read_b64_tr_b16 a[220:221], v208 offset:8192
ds_read_b64_tr_b16 a[222:223], v208 offset:8448
v_mfma_f32_16x16x32_bf16 v[184:187], a[128:131], a[200:203], v[184:187]
s_add_u32 m0, s40, 131072
ds_read_b128 a[128:131], v195 offset:16384
ds_read_b128 a[224:227], v195 offset:16896
v_mfma_f32_16x16x32_bf16 v[184:187], a[132:135], a[204:207], v[184:187]
s_waitcnt lgkmcnt(14)
s_xor_b32 s78, s41, 0
buffer_load_dwordx4 v210, s[16:19], s78 offen nt lds
v_mfma_f32_16x16x32_bf16 v[164:167], a[136:139], a[140:143], v[164:167]
v_mfma_f32_16x16x32_bf16 v[164:167], a[148:151], a[144:147], v[164:167]
buffer_load_dwordx4 v211, s[16:19], s78 offen offset:2048 nt lds
v_mfma_f32_16x16x32_bf16 v[172:175], a[136:139], a[180:183], v[172:175]
v_mfma_f32_16x16x32_bf16 v[172:175], a[148:151], a[184:187], v[172:175]
v_mfma_f32_16x16x32_bf16 v[180:183], a[136:139], a[160:163], v[180:183]
ds_read_b64_tr_b16 a[132:133], v205 offset:512
ds_read_b64_tr_b16 a[134:135], v205 offset:768
v_mfma_f32_16x16x32_bf16 v[180:183], a[148:151], a[164:167], v[180:183]
ds_read_b64_tr_b16 a[140:141], v205 offset:8704
ds_read_b64_tr_b16 a[142:143], v205 offset:8960
v_mfma_f32_16x16x32_bf16 v[188:191], a[136:139], a[200:203], v[188:191]
v_mfma_f32_16x16x32_bf16 v[188:191], a[148:151], a[204:207], v[188:191]
v_mfma_f32_16x16x32_bf16 v[64:67], a[176:179], a[152:155], v[64:67]
s_waitcnt lgkmcnt(13)
v_mfma_f32_16x16x32_bf16 v[64:67], a[196:199], a[156:159], v[64:67]
v_mfma_f32_16x16x32_bf16 v[72:75], a[176:179], a[188:191], v[72:75]
v_mfma_f32_16x16x32_bf16 v[72:75], a[196:199], a[192:195], v[72:75]
v_mfma_f32_16x16x32_bf16 v[80:83], a[176:179], a[168:171], v[80:83]
s_waitcnt lgkmcnt(8)
ds_read_b64_tr_b16 a[136:137], v208 offset:512
ds_read_b64_tr_b16 a[138:139], v208 offset:768
v_mfma_f32_16x16x32_bf16 v[80:83], a[196:199], a[172:175], v[80:83]
ds_read_b64_tr_b16 a[144:145], v208 offset:8704
ds_read_b64_tr_b16 a[146:147], v208 offset:8960
v_mfma_f32_16x16x32_bf16 v[88:91], a[176:179], a[208:211], v[88:91]
s_add_u32 m0, s40, 132096
v_mfma_f32_16x16x32_bf16 v[88:91], a[196:199], a[212:215], v[88:91]
s_xor_b32 s79, s41, 128
buffer_load_dwordx4 v210, s[16:19], s79 offen nt lds
v_mfma_f32_16x16x32_bf16 v[68:71], a[216:219], a[152:155], v[68:71]
s_waitcnt vmcnt(19) lgkmcnt(10)
v_mfma_f32_16x16x32_bf16 v[68:71], a[220:223], a[156:159], v[68:71]
ds_read_b128 a[164:167], v195 offset:34816
ds_read_b128 a[176:179], v195 offset:35328
v_mfma_f32_16x16x32_bf16 v[76:79], a[216:219], a[188:191], v[76:79]
ds_read_b128 a[148:151], v195 offset:32768
ds_read_b128 a[160:163], v195 offset:33280
v_mfma_f32_16x16x32_bf16 v[76:79], a[220:223], a[192:195], v[76:79]
buffer_load_dwordx4 v211, s[16:19], s79 offen offset:2048 nt lds
v_mfma_f32_16x16x32_bf16 v[84:87], a[216:219], a[168:171], v[84:87]
s_waitcnt lgkmcnt(10)
ds_read_b64_tr_b16 a[180:181], v205 offset:1024
ds_read_b64_tr_b16 a[182:183], v205 offset:1280
v_mfma_f32_16x16x32_bf16 v[84:87], a[220:223], a[172:175], v[84:87]
ds_read_b64_tr_b16 a[184:185], v205 offset:9216
ds_read_b64_tr_b16 a[186:187], v205 offset:9472
v_mfma_f32_16x16x32_bf16 v[92:95], a[216:219], a[208:211], v[92:95]
v_mfma_f32_16x16x32_bf16 v[92:95], a[220:223], a[212:215], v[92:95]
v_mfma_f32_16x16x32_bf16 v[96:99], a[132:135], a[152:155], v[96:99]
s_waitcnt lgkmcnt(12)
v_mfma_f32_16x16x32_bf16 v[96:99], a[140:143], a[156:159], v[96:99]
v_mfma_f32_16x16x32_bf16 v[104:107], a[132:135], a[188:191], v[104:107]
v_mfma_f32_16x16x32_bf16 v[104:107], a[140:143], a[192:195], v[104:107]
v_mfma_f32_16x16x32_bf16 v[112:115], a[132:135], a[168:171], v[112:115]
s_waitcnt lgkmcnt(10)
ds_read_b64_tr_b16 a[196:197], v208 offset:1024
ds_read_b64_tr_b16 a[198:199], v208 offset:1280
v_mfma_f32_16x16x32_bf16 v[112:115], a[140:143], a[172:175], v[112:115]
ds_read_b64_tr_b16 a[200:201], v208 offset:9216
ds_read_b64_tr_b16 a[202:203], v208 offset:9472
v_mfma_f32_16x16x32_bf16 v[120:123], a[132:135], a[208:211], v[120:123]
s_add_u32 m0, s40, 147456
v_mfma_f32_16x16x32_bf16 v[120:123], a[140:143], a[212:215], v[120:123]
s_xor_b32 s78, s41, 16384
buffer_load_dwordx4 v210, s[16:19], s78 offen nt lds
v_mfma_f32_16x16x32_bf16 v[100:103], a[136:139], a[152:155], v[100:103]
s_waitcnt vmcnt(17) lgkmcnt(12)
v_mfma_f32_16x16x32_bf16 v[100:103], a[144:147], a[156:159], v[100:103]
buffer_load_dwordx4 v211, s[16:19], s78 offen offset:2048 nt lds
v_mfma_f32_16x16x32_bf16 v[108:111], a[136:139], a[188:191], v[108:111]
ds_read_b64_tr_b16 a[236:237], v208 offset:9728
ds_read_b64_tr_b16 a[238:239], v208 offset:9984
v_mfma_f32_16x16x32_bf16 v[108:111], a[144:147], a[192:195], v[108:111]
ds_read_b128 a[216:219], v196 offset:34816
ds_read_b128 a[220:223], v196 offset:35328
v_mfma_f32_16x16x32_bf16 v[116:119], a[136:139], a[168:171], v[116:119]
s_waitcnt lgkmcnt(10)
ds_read_b64_tr_b16 a[132:133], v205 offset:1536
ds_read_b64_tr_b16 a[134:135], v205 offset:1792
v_mfma_f32_16x16x32_bf16 v[116:119], a[144:147], a[172:175], v[116:119]
ds_read_b64_tr_b16 a[140:141], v205 offset:9728
ds_read_b64_tr_b16 a[142:143], v205 offset:9984
v_mfma_f32_16x16x32_bf16 v[124:127], a[136:139], a[208:211], v[124:127]
L_tch2_%=:
s_cbranch_execz L_tch6_%=
ds_read_b128 a[136:139], v196 offset:32768
ds_read_b128 a[204:207], v196 offset:33280
v_mfma_f32_16x16x32_bf16 v[124:127], a[144:147], a[212:215], v[124:127]
ds_read_b64_tr_b16 a[144:145], v208 offset:1536
ds_read_b64_tr_b16 a[146:147], v208 offset:1792
v_mfma_f32_16x16x32_bf16 v[128:131], a[180:183], a[152:155], v[128:131]
s_waitcnt lgkmcnt(15)
v_mfma_f32_16x16x32_bf16 v[128:131], a[184:187], a[156:159], v[128:131]
v_mfma_f32_16x16x32_bf16 v[136:139], a[180:183], a[188:191], v[136:139]
v_mfma_f32_16x16x32_bf16 v[136:139], a[184:187], a[192:195], v[136:139]
v_mfma_f32_16x16x32_bf16 v[144:147], a[180:183], a[168:171], v[144:147]
s_waitcnt lgkmcnt(14)
v_mfma_f32_16x16x32_bf16 v[144:147], a[184:187], a[172:175], v[144:147]
v_mfma_f32_16x16x32_bf16 v[152:155], a[180:183], a[208:211], v[152:155]
s_add_u32 m0, s40, 148480
v_mfma_f32_16x16x32_bf16 v[152:155], a[184:187], a[212:215], v[152:155]
s_xor_b32 s79, s41, 16512
buffer_load_dwordx4 v210, s[16:19], s79 offen nt lds
v_mfma_f32_16x16x32_bf16 v[132:135], a[196:199], a[152:155], v[132:135]
s_waitcnt vmcnt(11) lgkmcnt(0)
s_barrier
v_mfma_f32_16x16x32_bf16 v[132:135], a[200:203], a[156:159], v[132:135]
buffer_load_dwordx4 v211, s[16:19], s79 offen offset:2048 nt lds
v_mfma_f32_16x16x32_bf16 v[140:143], a[196:199], a[188:191], v[140:143]
v_mfma_f32_16x16x32_bf16 v[140:143], a[200:203], a[192:195], v[140:143]
v_mfma_f32_16x16x32_bf16 v[148:151], a[196:199], a[168:171], v[148:151]
ds_read_b64_tr_b16 a[180:181], v205 offset:49152
ds_read_b64_tr_b16 a[182:183], v205 offset:49408
v_mfma_f32_16x16x32_bf16 v[148:151], a[200:203], a[172:175], v[148:151]
ds_read_b64_tr_b16 a[184:185], v205 offset:57344
ds_read_b64_tr_b16 a[186:187], v205 offset:57600
v_mfma_f32_16x16x32_bf16 v[156:159], a[196:199], a[208:211], v[156:159]
ds_read_b64_tr_b16 a[196:197], v205 offset:16384
ds_read_b64_tr_b16 a[198:199], v205 offset:16640
v_mfma_f32_16x16x32_bf16 v[156:159], a[200:203], a[212:215], v[156:159]
ds_read_b64_tr_b16 a[200:201], v205 offset:24576
ds_read_b64_tr_b16 a[202:203], v205 offset:24832
v_mfma_f32_16x16x32_bf16 v[160:163], a[132:135], a[152:155], v[160:163]
ds_read_b64_tr_b16 a[240:241], v208 offset:49152
ds_read_b64_tr_b16 a[242:243], v208 offset:49408
v_mfma_f32_16x16x32_bf16 v[160:163], a[140:143], a[156:159], v[160:163]
ds_read_b64_tr_b16 a[244:245], v208 offset:57344
ds_read_b64_tr_b16 a[246:247], v208 offset:57600
v_mfma_f32_16x16x32_bf16 v[168:171], a[132:135], a[188:191], v[168:171]
ds_read_b64_tr_b16 a[248:249], v208 offset:16384
ds_read_b64_tr_b16 a[250:251], v208 offset:16640
v_mfma_f32_16x16x32_bf16 v[168:171], a[140:143], a[192:195], v[168:171]
ds_read_b64_tr_b16 a[0:1], v208 offset:24576
ds_read_b64_tr_b16 a[2:3], v208 offset:24832
v_mfma_f32_16x16x32_bf16 v[176:179], a[132:135], a[168:171], v[176:179]
s_waitcnt vmcnt(8)
v_mfma_f32_16x16x32_bf16 v[176:179], a[140:143], a[172:175], v[176:179]
v_mfma_f32_16x16x32_bf16 v[184:187], a[132:135], a[208:211], v[184:187]
s_add_u32 m0, s40, 16384
ds_read_b128 a[132:135], v195 offset:49152
ds_read_b128 a[252:255], v195 offset:49664
v_mfma_f32_16x16x32_bf16 v[184:187], a[140:143], a[212:215], v[184:187]
s_xor_b32 s78, s41, 0
buffer_load_dwordx4 v212, s[20:23], s78 offen nt lds
v_mfma_f32_16x16x32_bf16 v[164:167], a[144:147], a[152:155], v[164:167]
ds_read_b128 a[140:143], v195 offset:51200
ds_read_b128 a[152:155], v195 offset:51712
v_mfma_f32_16x16x32_bf16 v[164:167], a[236:239], a[156:159], v[164:167]
buffer_load_dwordx4 v212, s[20:23], s78 offen offset:2048 nt lds
v_mfma_f32_16x16x32_bf16 v[172:175], a[144:147], a[188:191], v[172:175]
v_mfma_f32_16x16x32_bf16 v[172:175], a[236:239], a[192:195], v[172:175]
v_mfma_f32_16x16x32_bf16 v[180:183], a[144:147], a[168:171], v[180:183]
s_waitcnt lgkmcnt(15)
v_mfma_f32_16x16x32_bf16 v[180:183], a[236:239], a[172:175], v[180:183]
v_mfma_f32_16x16x32_bf16 v[188:191], a[144:147], a[208:211], v[188:191]
v_mfma_f32_16x16x32_bf16 v[188:191], a[236:239], a[212:215], v[188:191]
v_mfma_f32_16x16x32_bf16 v[64:67], a[180:183], a[128:131], v[64:67]
s_waitcnt lgkmcnt(10)
v_mfma_f32_16x16x32_bf16 v[64:67], a[184:187], a[224:227], v[64:67]
v_mfma_f32_16x16x32_bf16 v[64:67], a[196:199], a[136:139], v[64:67]
v_mfma_f32_16x16x32_bf16 v[64:67], a[200:203], a[204:207], v[64:67]
v_mfma_f32_16x16x32_bf16 v[68:71], a[240:243], a[128:131], v[68:71]
s_waitcnt lgkmcnt(4)
v_mfma_f32_16x16x32_bf16 v[68:71], a[244:247], a[224:227], v[68:71]
v_mfma_f32_16x16x32_bf16 v[68:71], a[248:251], a[136:139], v[68:71]
s_add_u32 m0, s40, 17408
v_mfma_f32_16x16x32_bf16 v[68:71], a[0:3], a[204:207], v[68:71]
s_xor_b32 s79, s41, 128
buffer_load_dwordx4 v212, s[20:23], s79 offen nt lds
v_mfma_f32_16x16x32_bf16 v[72:75], a[180:183], a[228:231], v[72:75]
v_mfma_f32_16x16x32_bf16 v[72:75], a[184:187], a[232:235], v[72:75]
buffer_load_dwordx4 v212, s[20:23], s79 offen offset:2048 nt lds
v_mfma_f32_16x16x32_bf16 v[72:75], a[196:199], a[216:219], v[72:75]
v_mul_f32 v64, s47, v64
v_mul_f32 v65, s47, v65
v_mfma_f32_16x16x32_bf16 v[72:75], a[200:203], a[220:223], v[72:75]
v_mul_f32 v66, s47, v66
v_mul_f32 v67, s47, v67
v_mfma_f32_16x16x32_bf16 v[76:79], a[240:243], a[228:231], v[76:79]
ds_read_b64_tr_b16 a[144:145], v205 offset:49664
ds_read_b64_tr_b16 a[146:147], v205 offset:49920
v_mfma_f32_16x16x32_bf16 v[76:79], a[244:247], a[232:235], v[76:79]
ds_read_b64_tr_b16 a[156:157], v205 offset:57856
ds_read_b64_tr_b16 a[158:159], v205 offset:58112
v_mfma_f32_16x16x32_bf16 v[76:79], a[248:251], a[216:219], v[76:79]
ds_read_b64_tr_b16 a[168:169], v205 offset:16896
ds_read_b64_tr_b16 a[170:171], v205 offset:17152
v_mfma_f32_16x16x32_bf16 v[76:79], a[0:3], a[220:223], v[76:79]
ds_read_b64_tr_b16 a[172:173], v205 offset:25088
ds_read_b64_tr_b16 a[174:175], v205 offset:25344
v_mfma_f32_16x16x32_bf16 v[80:83], a[180:183], a[148:151], v[80:83]
s_waitcnt lgkmcnt(10)
ds_read_b64_tr_b16 a[188:189], v208 offset:49664
ds_read_b64_tr_b16 a[190:191], v208 offset:49920
v_mfma_f32_16x16x32_bf16 v[80:83], a[184:187], a[160:163], v[80:83]
ds_read_b64_tr_b16 a[192:193], v208 offset:57856
ds_read_b64_tr_b16 a[194:195], v208 offset:58112
v_mfma_f32_16x16x32_bf16 v[80:83], a[196:199], a[132:135], v[80:83]
ds_read_b64_tr_b16 a[208:209], v208 offset:16896
ds_read_b64_tr_b16 a[210:211], v208 offset:17152
v_mfma_f32_16x16x32_bf16 v[80:83], a[200:203], a[252:255], v[80:83]
ds_read_b64_tr_b16 a[212:213], v208 offset:25088
ds_read_b64_tr_b16 a[214:215], v208 offset:25344
v_mfma_f32_16x16x32_bf16 v[84:87], a[240:243], a[148:151], v[84:87]
v_mul_f32 v68, s47, v68
v_mul_f32 v69, s47, v69
v_mfma_f32_16x16x32_bf16 v[84:87], a[244:247], a[160:163], v[84:87]
v_mul_f32 v70, s47, v70
v_mul_f32 v71, s47, v71
v_mfma_f32_16x16x32_bf16 v[84:87], a[248:251], a[132:135], v[84:87]
s_add_u32 m0, s40, 32768
v_cvt_pk_bf16_f32 v0, v64, v65
v_cvt_pk_bf16_f32 v1, v66, v67
v_mfma_f32_16x16x32_bf16 v[84:87], a[0:3], a[252:255], v[84:87]
s_xor_b32 s78, s41, 32768
buffer_load_dwordx4 v210, s[16:19], s78 offen nt lds
v_mfma_f32_16x16x32_bf16 v[88:91], a[180:183], a[164:167], v[88:91]
s_waitcnt lgkmcnt(15)
v_cvt_pk_bf16_f32 v2, v68, v69
v_cvt_pk_bf16_f32 v3, v70, v71
v_mfma_f32_16x16x32_bf16 v[88:91], a[184:187], a[176:179], v[88:91]
buffer_load_dwordx4 v211, s[16:19], s78 offen offset:2048 nt lds
v_mfma_f32_16x16x32_bf16 v[88:91], a[196:199], a[140:143], v[88:91]
v_permlane16_swap_b32 v0, v2
v_permlane16_swap_b32 v1, v3
v_mfma_f32_16x16x32_bf16 v[88:91], a[200:203], a[152:155], v[88:91]
v_mul_f32 v72, s47, v72
v_mul_f32 v73, s47, v73
v_mfma_f32_16x16x32_bf16 v[92:95], a[240:243], a[164:167], v[92:95]
s_waitcnt lgkmcnt(14)
v_mul_f32 v74, s47, v74
v_mul_f32 v75, s47, v75
v_mfma_f32_16x16x32_bf16 v[92:95], a[244:247], a[176:179], v[92:95]
v_mul_f32 v76, s47, v76
v_mul_f32 v77, s47, v77
v_mfma_f32_16x16x32_bf16 v[92:95], a[248:251], a[140:143], v[92:95]
v_mul_f32 v78, s47, v78
v_mul_f32 v79, s47, v79
v_mfma_f32_16x16x32_bf16 v[92:95], a[0:3], a[152:155], v[92:95]
v_cvt_pk_bf16_f32 v16, v72, v73
v_cvt_pk_bf16_f32 v17, v74, v75
v_mfma_f32_16x16x32_bf16 v[96:99], a[144:147], a[128:131], v[96:99]
s_waitcnt lgkmcnt(6)
v_cvt_pk_bf16_f32 v18, v76, v77
v_cvt_pk_bf16_f32 v19, v78, v79
v_mfma_f32_16x16x32_bf16 v[96:99], a[156:159], a[224:227], v[96:99]
v_permlane16_swap_b32 v16, v18
v_permlane16_swap_b32 v17, v19
v_mfma_f32_16x16x32_bf16 v[96:99], a[168:171], a[136:139], v[96:99]
v_mul_f32 v80, s47, v80
v_mul_f32 v81, s47, v81
v_mfma_f32_16x16x32_bf16 v[96:99], a[172:175], a[204:207], v[96:99]
v_mul_f32 v82, s47, v82
v_mul_f32 v83, s47, v83
v_mfma_f32_16x16x32_bf16 v[100:103], a[188:191], a[128:131], v[100:103]
s_waitcnt lgkmcnt(0)
v_mul_f32 v84, s47, v84
v_mul_f32 v85, s47, v85
v_mfma_f32_16x16x32_bf16 v[100:103], a[192:195], a[224:227], v[100:103]
v_mul_f32 v86, s47, v86
v_mul_f32 v87, s47, v87
v_mfma_f32_16x16x32_bf16 v[100:103], a[208:211], a[136:139], v[100:103]
s_add_u32 m0, s40, 33792
v_cvt_pk_bf16_f32 v32, v80, v81
v_cvt_pk_bf16_f32 v33, v82, v83
v_mfma_f32_16x16x32_bf16 v[100:103], a[212:215], a[204:207], v[100:103]
s_xor_b32 s79, s41, 32896
buffer_load_dwordx4 v210, s[16:19], s79 offen nt lds
v_mfma_f32_16x16x32_bf16 v[104:107], a[144:147], a[228:231], v[104:107]
v_cvt_pk_bf16_f32 v34, v84, v85
v_cvt_pk_bf16_f32 v35, v86, v87
v_mfma_f32_16x16x32_bf16 v[104:107], a[156:159], a[232:235], v[104:107]
buffer_load_dwordx4 v211, s[16:19], s79 offen offset:2048 nt lds
v_mfma_f32_16x16x32_bf16 v[104:107], a[168:171], a[216:219], v[104:107]
v_permlane16_swap_b32 v32, v34
v_permlane16_swap_b32 v33, v35
v_mfma_f32_16x16x32_bf16 v[104:107], a[172:175], a[220:223], v[104:107]
v_mul_f32 v88, s47, v88
v_mul_f32 v89, s47, v89
v_mfma_f32_16x16x32_bf16 v[108:111], a[188:191], a[228:231], v[108:111]
ds_read_b64_tr_b16 a[180:181], v205 offset:50176
ds_read_b64_tr_b16 a[182:183], v205 offset:50432
v_mfma_f32_16x16x32_bf16 v[108:111], a[192:195], a[232:235], v[108:111]
ds_read_b64_tr_b16 a[184:185], v205 offset:58368
ds_read_b64_tr_b16 a[186:187], v205 offset:58624
v_mfma_f32_16x16x32_bf16 v[108:111], a[208:211], a[216:219], v[108:111]
ds_read_b64_tr_b16 a[196:197], v205 offset:17408
ds_read_b64_tr_b16 a[198:199], v205 offset:17664
v_mfma_f32_16x16x32_bf16 v[108:111], a[212:215], a[220:223], v[108:111]
ds_read_b64_tr_b16 a[200:201], v205 offset:25600
ds_read_b64_tr_b16 a[202:203], v205 offset:25856
v_mfma_f32_16x16x32_bf16 v[112:115], a[144:147], a[148:151], v[112:115]
ds_read_b64_tr_b16 a[236:237], v208 offset:50176
ds_read_b64_tr_b16 a[238:239], v208 offset:50432
v_mfma_f32_16x16x32_bf16 v[112:115], a[156:159], a[160:163], v[112:115]
ds_read_b64_tr_b16 a[240:241], v208 offset:58368
ds_read_b64_tr_b16 a[242:243], v208 offset:58624
v_mfma_f32_16x16x32_bf16 v[112:115], a[168:171], a[132:135], v[112:115]
ds_read_b64_tr_b16 a[244:245], v208 offset:17408
ds_read_b64_tr_b16 a[246:247], v208 offset:17664
v_mfma_f32_16x16x32_bf16 v[112:115], a[172:175], a[252:255], v[112:115]
ds_read_b64_tr_b16 a[248:249], v208 offset:25600
ds_read_b64_tr_b16 a[250:251], v208 offset:25856
v_mfma_f32_16x16x32_bf16 v[116:119], a[188:191], a[148:151], v[116:119]
v_mul_f32 v90, s47, v90
v_mul_f32 v91, s47, v91
v_mfma_f32_16x16x32_bf16 v[116:119], a[192:195], a[160:163], v[116:119]
v_mul_f32 v92, s47, v92
v_mul_f32 v93, s47, v93
v_mfma_f32_16x16x32_bf16 v[116:119], a[208:211], a[132:135], v[116:119]
v_mul_f32 v94, s47, v94
v_mul_f32 v95, s47, v95
v_mfma_f32_16x16x32_bf16 v[116:119], a[212:215], a[252:255], v[116:119]
v_cvt_pk_bf16_f32 v48, v88, v89
v_cvt_pk_bf16_f32 v49, v90, v91
v_mfma_f32_16x16x32_bf16 v[120:123], a[144:147], a[164:167], v[120:123]
v_cvt_pk_bf16_f32 v50, v92, v93
v_cvt_pk_bf16_f32 v51, v94, v95
v_mfma_f32_16x16x32_bf16 v[120:123], a[156:159], a[176:179], v[120:123]
v_permlane16_swap_b32 v48, v50
v_permlane16_swap_b32 v49, v51
v_mfma_f32_16x16x32_bf16 v[120:123], a[168:171], a[140:143], v[120:123]
v_mul_f32 v96, s47, v96
v_mul_f32 v97, s47, v97
v_mfma_f32_16x16x32_bf16 v[120:123], a[172:175], a[152:155], v[120:123]
v_mul_f32 v98, s47, v98
v_mul_f32 v99, s47, v99
v_mfma_f32_16x16x32_bf16 v[124:127], a[188:191], a[164:167], v[124:127]
s_waitcnt lgkmcnt(14)
v_mul_f32 v100, s47, v100
v_mul_f32 v101, s47, v101
v_mfma_f32_16x16x32_bf16 v[124:127], a[192:195], a[176:179], v[124:127]
v_mul_f32 v102, s47, v102
v_mul_f32 v103, s47, v103
v_mfma_f32_16x16x32_bf16 v[124:127], a[208:211], a[140:143], v[124:127]
v_cvt_pk_bf16_f32 v4, v96, v97
v_cvt_pk_bf16_f32 v5, v98, v99
v_mfma_f32_16x16x32_bf16 v[124:127], a[212:215], a[152:155], v[124:127]
v_cvt_pk_bf16_f32 v6, v100, v101
v_cvt_pk_bf16_f32 v7, v102, v103
v_mfma_f32_16x16x32_bf16 v[128:131], a[180:183], a[128:131], v[128:131]
s_waitcnt lgkmcnt(6)
v_permlane16_swap_b32 v4, v6
v_permlane16_swap_b32 v5, v7
v_mfma_f32_16x16x32_bf16 v[128:131], a[184:187], a[224:227], v[128:131]
v_mul_f32 v104, s47, v104
v_mul_f32 v105, s47, v105
v_mfma_f32_16x16x32_bf16 v[128:131], a[196:199], a[136:139], v[128:131]
v_mul_f32 v106, s47, v106
v_mul_f32 v107, s47, v107
v_mfma_f32_16x16x32_bf16 v[128:131], a[200:203], a[204:207], v[128:131]
v_mul_f32 v108, s47, v108
v_mul_f32 v109, s47, v109
v_mfma_f32_16x16x32_bf16 v[132:135], a[236:239], a[128:131], v[132:135]
s_waitcnt lgkmcnt(0)
v_mul_f32 v110, s47, v110
v_mul_f32 v111, s47, v111
v_mfma_f32_16x16x32_bf16 v[132:135], a[240:243], a[224:227], v[132:135]
v_cvt_pk_bf16_f32 v20, v104, v105
v_cvt_pk_bf16_f32 v21, v106, v107
v_mfma_f32_16x16x32_bf16 v[132:135], a[244:247], a[136:139], v[132:135]
v_cvt_pk_bf16_f32 v22, v108, v109
v_cvt_pk_bf16_f32 v23, v110, v111
v_mfma_f32_16x16x32_bf16 v[132:135], a[248:251], a[204:207], v[132:135]
s_add_u32 m0, s40, 98304
v_permlane16_swap_b32 v20, v22
v_permlane16_swap_b32 v21, v23
v_mfma_f32_16x16x32_bf16 v[136:139], a[180:183], a[228:231], v[136:139]
v_mul_f32 v112, s47, v112
v_mul_f32 v113, s47, v113
v_mfma_f32_16x16x32_bf16 v[136:139], a[184:187], a[232:235], v[136:139]
s_xor_b32 s78, s41, 16384
buffer_load_dwordx4 v212, s[20:23], s78 offen nt lds
v_mfma_f32_16x16x32_bf16 v[136:139], a[196:199], a[216:219], v[136:139]
v_mul_f32 v114, s47, v114
v_mul_f32 v115, s47, v115
v_mfma_f32_16x16x32_bf16 v[136:139], a[200:203], a[220:223], v[136:139]
buffer_load_dwordx4 v212, s[20:23], s78 offen offset:2048 nt lds
v_mfma_f32_16x16x32_bf16 v[140:143], a[236:239], a[228:231], v[140:143]
ds_read_b64_tr_b16 a[144:145], v205 offset:50688
ds_read_b64_tr_b16 a[146:147], v205 offset:50944
v_mfma_f32_16x16x32_bf16 v[140:143], a[240:243], a[232:235], v[140:143]
ds_read_b64_tr_b16 a[156:157], v205 offset:58880
ds_read_b64_tr_b16 a[158:159], v205 offset:59136
v_mfma_f32_16x16x32_bf16 v[140:143], a[244:247], a[216:219], v[140:143]
ds_read_b64_tr_b16 a[168:169], v205 offset:17920
ds_read_b64_tr_b16 a[170:171], v205 offset:18176
v_mfma_f32_16x16x32_bf16 v[140:143], a[248:251], a[220:223], v[140:143]
ds_read_b64_tr_b16 a[172:173], v205 offset:26112
ds_read_b64_tr_b16 a[174:175], v205 offset:26368
v_mfma_f32_16x16x32_bf16 v[144:147], a[180:183], a[148:151], v[144:147]
ds_read_b64_tr_b16 a[188:189], v208 offset:50688
ds_read_b64_tr_b16 a[190:191], v208 offset:50944
v_mfma_f32_16x16x32_bf16 v[144:147], a[184:187], a[160:163], v[144:147]
ds_read_b64_tr_b16 a[192:193], v208 offset:58880
ds_read_b64_tr_b16 a[194:195], v208 offset:59136
v_mfma_f32_16x16x32_bf16 v[144:147], a[196:199], a[132:135], v[144:147]
ds_read_b64_tr_b16 a[208:209], v208 offset:17920
ds_read_b64_tr_b16 a[210:211], v208 offset:18176
v_mfma_f32_16x16x32_bf16 v[144:147], a[200:203], a[252:255], v[144:147]
ds_read_b64_tr_b16 a[212:213], v208 offset:26112
ds_read_b64_tr_b16 a[214:215], v208 offset:26368
v_mfma_f32_16x16x32_bf16 v[148:151], a[236:239], a[148:151], v[148:151]
v_mul_f32 v116, s47, v116
v_mul_f32 v117, s47, v117
v_mfma_f32_16x16x32_bf16 v[148:151], a[240:243], a[160:163], v[148:151]
v_mul_f32 v118, s47, v118
v_mul_f32 v119, s47, v119
v_mfma_f32_16x16x32_bf16 v[148:151], a[244:247], a[132:135], v[148:151]
v_cvt_pk_bf16_f32 v36, v112, v113
v_cvt_pk_bf16_f32 v37, v114, v115
v_mfma_f32_16x16x32_bf16 v[148:151], a[248:251], a[252:255], v[148:151]
v_cvt_pk_bf16_f32 v38, v116, v117
v_cvt_pk_bf16_f32 v39, v118, v119
v_mfma_f32_16x16x32_bf16 v[152:155], a[180:183], a[164:167], v[152:155]
v_permlane16_swap_b32 v36, v38
v_permlane16_swap_b32 v37, v39
v_mfma_f32_16x16x32_bf16 v[152:155], a[184:187], a[176:179], v[152:155]
v_mul_f32 v120, s47, v120
v_mul_f32 v121, s47, v121
v_mfma_f32_16x16x32_bf16 v[152:155], a[196:199], a[140:143], v[152:155]
v_mul_f32 v122, s47, v122
v_mul_f32 v123, s47, v123
v_mfma_f32_16x16x32_bf16 v[152:155], a[200:203], a[152:155], v[152:155]
v_mul_f32 v124, s47, v124
v_mul_f32 v125, s47, v125
v_mfma_f32_16x16x32_bf16 v[156:159], a[236:239], a[164:167], v[156:159]
s_waitcnt lgkmcnt(14)
v_mul_f32 v126, s47, v126
v_mul_f32 v127, s47, v127
v_mfma_f32_16x16x32_bf16 v[156:159], a[240:243], a[176:179], v[156:159]
v_cvt_pk_bf16_f32 v52, v120, v121
v_cvt_pk_bf16_f32 v53, v122, v123
v_mfma_f32_16x16x32_bf16 v[156:159], a[244:247], a[140:143], v[156:159]
v_cvt_pk_bf16_f32 v54, v124, v125
v_cvt_pk_bf16_f32 v55, v126, v127
v_mfma_f32_16x16x32_bf16 v[156:159], a[248:251], a[152:155], v[156:159]
v_permlane16_swap_b32 v52, v54
v_permlane16_swap_b32 v53, v55
v_mfma_f32_16x16x32_bf16 v[160:163], a[144:147], a[128:131], v[160:163]
s_waitcnt lgkmcnt(6)
v_mul_f32 v128, s47, v128
v_mul_f32 v129, s47, v129
v_mfma_f32_16x16x32_bf16 v[160:163], a[156:159], a[224:227], v[160:163]
v_mul_f32 v130, s47, v130
v_mul_f32 v131, s47, v131
v_mfma_f32_16x16x32_bf16 v[160:163], a[168:171], a[136:139], v[160:163]
v_mul_f32 v132, s47, v132
v_mul_f32 v133, s47, v133
v_mfma_f32_16x16x32_bf16 v[160:163], a[172:175], a[204:207], v[160:163]
v_mul_f32 v134, s47, v134
v_mul_f32 v135, s47, v135
v_mfma_f32_16x16x32_bf16 v[164:167], a[188:191], a[128:131], v[164:167]
s_waitcnt lgkmcnt(0)
v_cvt_pk_bf16_f32 v8, v128, v129
v_cvt_pk_bf16_f32 v9, v130, v131
v_mfma_f32_16x16x32_bf16 v[164:167], a[192:195], a[224:227], v[164:167]
v_cvt_pk_bf16_f32 v10, v132, v133
v_cvt_pk_bf16_f32 v11, v134, v135
v_mfma_f32_16x16x32_bf16 v[164:167], a[208:211], a[136:139], v[164:167]
v_permlane16_swap_b32 v8, v10
v_permlane16_swap_b32 v9, v11
v_mfma_f32_16x16x32_bf16 v[164:167], a[212:215], a[204:207], v[164:167]
v_mul_f32 v136, s47, v136
v_mul_f32 v137, s47, v137
v_mfma_f32_16x16x32_bf16 v[168:171], a[144:147], a[228:231], v[168:171]
s_waitcnt vmcnt(14)
s_barrier
v_mfma_f32_16x16x32_bf16 v[168:171], a[156:159], a[232:235], v[168:171]
v_mul_f32 v138, s47, v138
v_mul_f32 v139, s47, v139
v_mfma_f32_16x16x32_bf16 v[168:171], a[168:171], a[216:219], v[168:171]
s_add_u32 m0, s40, 99328
v_mul_f32 v140, s47, v140
v_mul_f32 v141, s47, v141
v_mfma_f32_16x16x32_bf16 v[168:171], a[172:175], a[220:223], v[168:171]
s_xor_b32 s79, s41, 16512
buffer_load_dwordx4 v212, s[20:23], s79 offen nt lds
v_mfma_f32_16x16x32_bf16 v[172:175], a[188:191], a[228:231], v[172:175]
ds_read_b128 a[180:183], v194 offset:0
ds_read_b128 a[184:187], v194 offset:512
v_mfma_f32_16x16x32_bf16 v[172:175], a[192:195], a[232:235], v[172:175]
buffer_load_dwordx4 v212, s[20:23], s79 offen offset:2048 nt lds
v_mfma_f32_16x16x32_bf16 v[172:175], a[208:211], a[216:219], v[172:175]
v_mul_f32 v142, s47, v142
v_mul_f32 v143, s47, v143
v_mfma_f32_16x16x32_bf16 v[172:175], a[212:215], a[220:223], v[172:175]
v_cvt_pk_bf16_f32 v24, v136, v137
v_cvt_pk_bf16_f32 v25, v138, v139
v_mfma_f32_16x16x32_bf16 v[176:179], a[144:147], a[148:151], v[176:179]
ds_read_b128 a[196:199], v194 offset:4096
ds_read_b128 a[200:203], v194 offset:4608
v_mfma_f32_16x16x32_bf16 v[176:179], a[156:159], a[160:163], v[176:179]
v_cvt_pk_bf16_f32 v26, v140, v141
v_cvt_pk_bf16_f32 v27, v142, v143
v_mfma_f32_16x16x32_bf16 v[176:179], a[168:171], a[132:135], v[176:179]
v_permlane16_swap_b32 v24, v26
v_permlane16_swap_b32 v25, v27
v_mfma_f32_16x16x32_bf16 v[176:179], a[172:175], a[252:255], v[176:179]
v_mul_f32 v144, s47, v144
v_mul_f32 v145, s47, v145
v_mfma_f32_16x16x32_bf16 v[180:183], a[188:191], a[148:151], v[180:183]
v_mul_f32 v146, s47, v146
v_mul_f32 v147, s47, v147
v_mfma_f32_16x16x32_bf16 v[180:183], a[192:195], a[160:163], v[180:183]
v_mul_f32 v148, s47, v148
v_mul_f32 v149, s47, v149
v_mfma_f32_16x16x32_bf16 v[180:183], a[208:211], a[132:135], v[180:183]
v_mul_f32 v150, s47, v150
v_mul_f32 v151, s47, v151
v_mfma_f32_16x16x32_bf16 v[180:183], a[212:215], a[252:255], v[180:183]
v_cvt_pk_bf16_f32 v40, v144, v145
v_cvt_pk_bf16_f32 v41, v146, v147
v_mfma_f32_16x16x32_bf16 v[184:187], a[144:147], a[164:167], v[184:187]
v_cvt_pk_bf16_f32 v42, v148, v149
v_cvt_pk_bf16_f32 v43, v150, v151
v_mfma_f32_16x16x32_bf16 v[184:187], a[156:159], a[176:179], v[184:187]
v_permlane16_swap_b32 v40, v42
v_permlane16_swap_b32 v41, v43
v_mfma_f32_16x16x32_bf16 v[184:187], a[168:171], a[140:143], v[184:187]
v_mul_f32 v152, s47, v152
v_mul_f32 v153, s47, v153
v_mfma_f32_16x16x32_bf16 v[184:187], a[172:175], a[152:155], v[184:187]
v_mul_f32 v154, s47, v154
v_mul_f32 v155, s47, v155
v_mfma_f32_16x16x32_bf16 v[188:191], a[188:191], a[164:167], v[188:191]
s_waitcnt lgkmcnt(3)
ds_read_b128 a[144:147], v194 offset:1024
ds_read_b128 a[156:159], v194 offset:1536
v_mfma_f32_16x16x32_bf16 v[188:191], a[192:195], a[176:179], v[188:191]
v_mul_f32 v156, s47, v156
v_mul_f32 v157, s47, v157
v_mfma_f32_16x16x32_bf16 v[188:191], a[208:211], a[140:143], v[188:191]
v_mul_f32 v158, s47, v158
v_mul_f32 v159, s47, v159
v_mfma_f32_16x16x32_bf16 v[188:191], a[212:215], a[152:155], v[188:191]
v_cvt_pk_bf16_f32 v56, v152, v153
v_cvt_pk_bf16_f32 v57, v154, v155
v_mfma_f32_16x16x32_bf16 v[64:67], a[180:183], v[0:3], 0
s_waitcnt lgkmcnt(3)
ds_read_b128 a[168:171], v194 offset:5120
ds_read_b128 a[172:175], v194 offset:5632
v_mfma_f32_16x16x32_bf16 v[72:75], a[180:183], v[16:19], 0
v_cvt_pk_bf16_f32 v58, v156, v157
v_cvt_pk_bf16_f32 v59, v158, v159
v_mfma_f32_16x16x32_bf16 v[80:83], a[180:183], v[32:35], 0
v_permlane16_swap_b32 v56, v58
v_permlane16_swap_b32 v57, v59
v_mfma_f32_16x16x32_bf16 v[88:91], a[180:183], v[48:51], 0
v_mul_f32 v160, s47, v160
v_mul_f32 v161, s47, v161
v_mfma_f32_16x16x32_bf16 v[68:71], a[196:199], v[0:3], 0
v_mul_f32 v162, s47, v162
v_mul_f32 v163, s47, v163
v_mfma_f32_16x16x32_bf16 v[76:79], a[196:199], v[16:19], 0
v_mul_f32 v164, s47, v164
v_mul_f32 v165, s47, v165
v_mfma_f32_16x16x32_bf16 v[84:87], a[196:199], v[32:35], 0
v_mul_f32 v166, s47, v166
v_mul_f32 v167, s47, v167
v_mfma_f32_16x16x32_bf16 v[92:95], a[196:199], v[48:51], 0
v_cvt_pk_bf16_f32 v12, v160, v161
v_cvt_pk_bf16_f32 v13, v162, v163
v_mfma_f32_16x16x32_bf16 v[64:67], a[184:187], v[4:7], v[64:67]
s_waitcnt lgkmcnt(4)
v_cvt_pk_bf16_f32 v14, v164, v165
v_cvt_pk_bf16_f32 v15, v166, v167
v_mfma_f32_16x16x32_bf16 v[72:75], a[184:187], v[20:23], v[72:75]
v_permlane16_swap_b32 v12, v14
v_permlane16_swap_b32 v13, v15
v_mfma_f32_16x16x32_bf16 v[80:83], a[184:187], v[36:39], v[80:83]
v_mul_f32 v168, s47, v168
v_mul_f32 v169, s47, v169
v_mfma_f32_16x16x32_bf16 v[88:91], a[184:187], v[52:55], v[88:91]
s_add_u32 m0, s40, 0
v_mul_f32 v170, s47, v170
v_mul_f32 v171, s47, v171
v_mfma_f32_16x16x32_bf16 v[68:71], a[200:203], v[4:7], v[68:71]
s_waitcnt lgkmcnt(3)
ds_read_b128 a[180:183], v194 offset:8192
ds_read_b128 a[184:187], v194 offset:8704
v_mfma_f32_16x16x32_bf16 v[76:79], a[200:203], v[20:23], v[76:79]
s_xor_b32 s78, s41, 49152
buffer_load_dwordx4 v210, s[16:19], s78 offen nt lds
v_mfma_f32_16x16x32_bf16 v[84:87], a[200:203], v[36:39], v[84:87]
v_mul_f32 v172, s47, v172
v_mul_f32 v173, s47, v173
v_mfma_f32_16x16x32_bf16 v[92:95], a[200:203], v[52:55], v[92:95]
buffer_load_dwordx4 v211, s[16:19], s78 offen offset:2048 nt lds
v_mfma_f32_16x16x32_bf16 v[64:67], a[144:147], v[8:11], v[64:67]
s_waitcnt lgkmcnt(3)
v_mul_f32 v174, s47, v174
v_mul_f32 v175, s47, v175
v_mfma_f32_16x16x32_bf16 v[72:75], a[144:147], v[24:27], v[72:75]
v_cvt_pk_bf16_f32 v28, v168, v169
v_cvt_pk_bf16_f32 v29, v170, v171
v_mfma_f32_16x16x32_bf16 v[80:83], a[144:147], v[40:43], v[80:83]
v_cvt_pk_bf16_f32 v30, v172, v173
v_cvt_pk_bf16_f32 v31, v174, v175
v_mfma_f32_16x16x32_bf16 v[88:91], a[144:147], v[56:59], v[88:91]
v_permlane16_swap_b32 v28, v30
v_permlane16_swap_b32 v29, v31
v_mfma_f32_16x16x32_bf16 v[68:71], a[168:171], v[8:11], v[68:71]
ds_read_b128 a[144:147], v194 offset:9216
ds_read_b128 a[188:191], v194 offset:9728
v_mfma_f32_16x16x32_bf16 v[76:79], a[168:171], v[24:27], v[76:79]
v_mul_f32 v176, s47, v176
v_mul_f32 v177, s47, v177
v_mfma_f32_16x16x32_bf16 v[84:87], a[168:171], v[40:43], v[84:87]
v_mul_f32 v178, s47, v178
v_mul_f32 v179, s47, v179
v_mfma_f32_16x16x32_bf16 v[92:95], a[168:171], v[56:59], v[92:95]
v_mul_f32 v180, s47, v180
v_mul_f32 v181, s47, v181
v_mfma_f32_16x16x32_bf16 v[64:67], a[156:159], v[12:15], v[64:67]
s_waitcnt lgkmcnt(4)
v_mul_f32 v182, s47, v182
v_mul_f32 v183, s47, v183
v_cvt_pk_bf16_f32 v44, v176, v177
v_cvt_pk_bf16_f32 v45, v178, v179
v_cvt_pk_bf16_f32 v46, v180, v181
v_cvt_pk_bf16_f32 v47, v182, v183
s_nop 0
v_permlane16_swap_b32 v44, v46
v_permlane16_swap_b32 v45, v47
v_mfma_f32_16x16x32_bf16 v[72:75], a[156:159], v[28:31], v[72:75]
v_mul_f32 v184, s47, v184
v_mul_f32 v185, s47, v185
v_mul_f32 v186, s47, v186
v_mul_f32 v187, s47, v187
v_mul_f32 v188, s47, v188
v_mul_f32 v189, s47, v189
v_mul_f32 v190, s47, v190
v_mul_f32 v191, s47, v191
v_cvt_pk_bf16_f32 v60, v184, v185
v_cvt_pk_bf16_f32 v61, v186, v187
v_cvt_pk_bf16_f32 v62, v188, v189
v_cvt_pk_bf16_f32 v63, v190, v191
s_nop 0
v_permlane16_swap_b32 v60, v62
v_permlane16_swap_b32 v61, v63
v_mfma_f32_16x16x32_bf16 v[80:83], a[156:159], v[44:47], v[80:83]
s_nop 0
v_mfma_f32_16x16x32_bf16 v[88:91], a[156:159], v[60:63], v[88:91]
v_mfma_f32_16x16x32_bf16 v[68:71], a[172:175], v[12:15], v[68:71]
s_waitcnt lgkmcnt(3)
ds_read_b128 a[156:159], v194 offset:12288
ds_read_b128 a[168:171], v194 offset:12800
v_mfma_f32_16x16x32_bf16 v[76:79], a[172:175], v[28:31], v[76:79]
v_mfma_f32_16x16x32_bf16 v[84:87], a[172:175], v[44:47], v[84:87]
v_mfma_f32_16x16x32_bf16 v[92:95], a[172:175], v[60:63], v[92:95]
ds_read_b128 a[172:175], v194 offset:13312
ds_read_b128 a[192:195], v194 offset:13824
v_mfma_f32_16x16x32_bf16 v[96:99], a[180:183], v[0:3], 0
s_waitcnt lgkmcnt(6)
v_mfma_f32_16x16x32_bf16 v[96:99], a[184:187], v[4:7], v[96:99]
v_mfma_f32_16x16x32_bf16 v[104:107], a[180:183], v[16:19], 0
v_mfma_f32_16x16x32_bf16 v[104:107], a[184:187], v[20:23], v[104:107]
v_max3_f32 v218, v64, v65, v66
v_max3_f32 v218, v218, v67, v68
v_mfma_f32_16x16x32_bf16 v[112:115], a[180:183], v[32:35], 0
s_waitcnt lgkmcnt(5)
v_max3_f32 v218, v218, v69, v70
v_max_f32 v218, v218, v71
v_mfma_f32_16x16x32_bf16 v[112:115], a[184:187], v[36:39], v[112:115]
v_max3_f32 v219, v72, v73, v74
v_max3_f32 v219, v219, v75, v76
v_mfma_f32_16x16x32_bf16 v[120:123], a[180:183], v[48:51], 0
v_max3_f32 v219, v219, v77, v78
v_max_f32 v219, v219, v79
v_mfma_f32_16x16x32_bf16 v[120:123], a[184:187], v[52:55], v[120:123]
v_max3_f32 v220, v80, v81, v82
v_max3_f32 v220, v220, v83, v84
v_mfma_f32_16x16x32_bf16 v[96:99], a[144:147], v[8:11], v[96:99]
s_waitcnt vmcnt(10) lgkmcnt(0)
s_barrier
v_mfma_f32_16x16x32_bf16 v[96:99], a[188:191], v[12:15], v[96:99]
v_max3_f32 v220, v220, v85, v86
v_max_f32 v220, v220, v87
v_mfma_f32_16x16x32_bf16 v[104:107], a[144:147], v[24:27], v[104:107]
v_max3_f32 v221, v88, v89, v90
v_max3_f32 v221, v221, v91, v92
v_mfma_f32_16x16x32_bf16 v[104:107], a[188:191], v[28:31], v[104:107]
v_max3_f32 v221, v221, v93, v94
v_max_f32 v221, v221, v95
v_mfma_f32_16x16x32_bf16 v[112:115], a[144:147], v[40:43], v[112:115]
ds_read_b128 a[180:183], v194 offset:16384
ds_read_b128 a[184:187], v194 offset:16896
v_mfma_f32_16x16x32_bf16 v[112:115], a[188:191], v[44:47], v[112:115]
v_mov_b32 v226, v218
v_mov_b32 v227, v219
v_mfma_f32_16x16x32_bf16 v[120:123], a[144:147], v[56:59], v[120:123]
s_add_u32 m0, s40, 1024
v_mov_b32 v228, v220
v_mov_b32 v229, v221
v_mfma_f32_16x16x32_bf16 v[120:123], a[188:191], v[60:63], v[120:123]
s_xor_b32 s79, s41, 49280
buffer_load_dwordx4 v210, s[16:19], s79 offen nt lds
v_mfma_f32_16x16x32_bf16 v[100:103], a[156:159], v[0:3], 0
L_tch3_%=:
s_cbranch_execz L_tch7_%=
v_permlane16_swap_b32 v218, v226
v_permlane16_swap_b32 v219, v227
v_mfma_f32_16x16x32_bf16 v[100:103], a[168:171], v[4:7], v[100:103]
buffer_load_dwordx4 v211, s[16:19], s79 offen offset:2048 nt lds
v_mfma_f32_16x16x32_bf16 v[108:111], a[156:159], v[16:19], 0
v_permlane16_swap_b32 v220, v228
v_permlane16_swap_b32 v221, v229
v_mfma_f32_16x16x32_bf16 v[108:111], a[168:171], v[20:23], v[108:111]
v_max_f32 v218, v218, v226
v_max_f32 v219, v219, v227
v_mfma_f32_16x16x32_bf16 v[116:119], a[156:159], v[32:35], 0
ds_read_b128 a[144:147], v194 offset:17408
ds_read_b128 a[188:191], v194 offset:17920
v_mfma_f32_16x16x32_bf16 v[116:119], a[168:171], v[36:39], v[116:119]
v_max_f32 v220, v220, v228
v_max_f32 v221, v221, v229
v_mfma_f32_16x16x32_bf16 v[124:127], a[156:159], v[48:51], 0
v_mov_b32 v226, v218
v_mov_b32 v227, v219
v_mfma_f32_16x16x32_bf16 v[124:127], a[168:171], v[52:55], v[124:127]
v_mov_b32 v228, v220
v_mov_b32 v229, v221
v_mfma_f32_16x16x32_bf16 v[100:103], a[172:175], v[8:11], v[100:103]
v_permlane32_swap_b32 v218, v226
v_permlane32_swap_b32 v219, v227
v_mfma_f32_16x16x32_bf16 v[100:103], a[192:195], v[12:15], v[100:103]
v_permlane32_swap_b32 v220, v228
v_permlane32_swap_b32 v221, v229
v_mfma_f32_16x16x32_bf16 v[108:111], a[172:175], v[24:27], v[108:111]
v_max_f32 v218, v218, v226
v_max_f32 v219, v219, v227
v_mfma_f32_16x16x32_bf16 v[108:111], a[192:195], v[28:31], v[108:111]
v_max_f32 v220, v220, v228
v_max_f32 v221, v221, v229
v_mfma_f32_16x16x32_bf16 v[116:119], a[172:175], v[40:43], v[116:119]
s_waitcnt lgkmcnt(3)
ds_read_b128 a[156:159], v194 offset:20480
ds_read_b128 a[168:171], v194 offset:20992
v_mfma_f32_16x16x32_bf16 v[116:119], a[192:195], v[44:47], v[116:119]
v_add_f32 v221, s99, v221
v_sub_f32 v160, 0, v218
v_mfma_f32_16x16x32_bf16 v[124:127], a[172:175], v[56:59], v[124:127]
v_sub_f32 v161, 0, v218
v_sub_f32 v162, 0, v218
v_mfma_f32_16x16x32_bf16 v[124:127], a[192:195], v[60:63], v[124:127]
v_sub_f32 v163, 0, v218
v_sub_f32 v164, 0, v219
v_sub_f32 v165, 0, v219
v_sub_f32 v166, 0, v219
v_sub_f32 v167, 0, v219
v_sub_f32 v168, 0, v220
v_sub_f32 v169, 0, v220
v_sub_f32 v170, 0, v220
v_sub_f32 v171, 0, v220
v_sub_f32 v172, 0, v221
v_sub_f32 v173, 0, v221
v_sub_f32 v174, 0, v221
v_sub_f32 v175, 0, v221
v_sub_f32 v64, v64, v218
v_sub_f32 v65, v65, v218
v_sub_f32 v66, v66, v218
v_sub_f32 v67, v67, v218
v_exp_f32 v64, v64
v_exp_f32 v65, v65
v_exp_f32 v66, v66
v_exp_f32 v67, v67
v_sub_f32 v68, v68, v218
v_sub_f32 v69, v69, v218
v_sub_f32 v70, v70, v218
v_sub_f32 v71, v71, v218
v_exp_f32 v68, v68
v_cvt_pk_bf16_f32 v128, v64, v65
v_cvt_pk_bf16_f32 v129, v66, v67
v_exp_f32 v69, v69
v_exp_f32 v70, v70
v_exp_f32 v71, v71
v_sub_f32 v72, v72, v219
v_sub_f32 v73, v73, v219
v_sub_f32 v74, v74, v219
v_sub_f32 v75, v75, v219
v_exp_f32 v72, v72
v_cvt_pk_bf16_f32 v130, v68, v69
v_cvt_pk_bf16_f32 v131, v70, v71
v_exp_f32 v73, v73
v_exp_f32 v74, v74
v_exp_f32 v75, v75
v_sub_f32 v76, v76, v219
v_sub_f32 v77, v77, v219
v_sub_f32 v78, v78, v219
v_sub_f32 v79, v79, v219
v_exp_f32 v76, v76
v_cvt_pk_bf16_f32 v132, v72, v73
v_cvt_pk_bf16_f32 v133, v74, v75
v_exp_f32 v77, v77
v_exp_f32 v78, v78
v_exp_f32 v79, v79
v_sub_f32 v80, v80, v220
v_sub_f32 v81, v81, v220
v_sub_f32 v82, v82, v220
v_sub_f32 v83, v83, v220
v_exp_f32 v80, v80
v_cvt_pk_bf16_f32 v134, v76, v77
v_cvt_pk_bf16_f32 v135, v78, v79
v_exp_f32 v81, v81
v_exp_f32 v82, v82
v_exp_f32 v83, v83
v_sub_f32 v84, v84, v220
v_sub_f32 v85, v85, v220
v_sub_f32 v86, v86, v220
v_sub_f32 v87, v87, v220
v_exp_f32 v84, v84
v_cvt_pk_bf16_f32 v136, v80, v81
v_cvt_pk_bf16_f32 v137, v82, v83
v_exp_f32 v85, v85
v_exp_f32 v86, v86
v_exp_f32 v87, v87
v_sub_f32 v88, v88, v221
v_sub_f32 v89, v89, v221
v_sub_f32 v90, v90, v221
v_sub_f32 v91, v91, v221
v_exp_f32 v88, v88
v_cvt_pk_bf16_f32 v138, v84, v85
v_cvt_pk_bf16_f32 v139, v86, v87
v_exp_f32 v89, v89
v_exp_f32 v90, v90
v_exp_f32 v91, v91
v_sub_f32 v92, v92, v221
v_sub_f32 v93, v93, v221
v_sub_f32 v94, v94, v221
v_sub_f32 v95, v95, v221
v_exp_f32 v92, v92
v_cvt_pk_bf16_f32 v140, v88, v89
v_cvt_pk_bf16_f32 v141, v90, v91
v_exp_f32 v93, v93
v_exp_f32 v94, v94
v_exp_f32 v95, v95
v_cvt_pk_bf16_f32 v142, v92, v93
v_cvt_pk_bf16_f32 v143, v94, v95
v_mfma_f32_16x16x32_bf16 v[64:67], a[180:183], v[0:3], v[160:163]
s_waitcnt lgkmcnt(4)
v_sub_f32 v96, v96, v218
v_sub_f32 v97, v97, v218
v_mfma_f32_16x16x32_bf16 v[64:67], a[184:187], v[4:7], v[64:67]
v_sub_f32 v98, v98, v218
v_sub_f32 v99, v99, v218
v_mfma_f32_16x16x32_bf16 v[72:75], a[180:183], v[16:19], v[164:167]
v_exp_f32 v96, v96
v_mfma_f32_16x16x32_bf16 v[72:75], a[184:187], v[20:23], v[72:75]
v_exp_f32 v97, v97
v_mfma_f32_16x16x32_bf16 v[80:83], a[180:183], v[32:35], v[168:171]
s_waitcnt lgkmcnt(3)
ds_read_b128 a[172:175], v194 offset:21504
ds_read_b128 a[192:195], v194 offset:22016
v_mfma_f32_16x16x32_bf16 v[80:83], a[184:187], v[36:39], v[80:83]
v_exp_f32 v98, v98
v_mfma_f32_16x16x32_bf16 v[88:91], a[180:183], v[48:51], v[172:175]
v_exp_f32 v99, v99
v_mfma_f32_16x16x32_bf16 v[88:91], a[184:187], v[52:55], v[88:91]
v_sub_f32 v100, v100, v218
v_sub_f32 v101, v101, v218
v_mfma_f32_16x16x32_bf16 v[64:67], a[144:147], v[8:11], v[64:67]
s_waitcnt lgkmcnt(4)
v_sub_f32 v102, v102, v218
v_sub_f32 v103, v103, v218
v_mfma_f32_16x16x32_bf16 v[64:67], a[188:191], v[12:15], v[64:67]
v_exp_f32 v100, v100
v_mfma_f32_16x16x32_bf16 v[72:75], a[144:147], v[24:27], v[72:75]
v_cvt_pk_bf16_f32 v144, v96, v97
v_cvt_pk_bf16_f32 v145, v98, v99
v_mfma_f32_16x16x32_bf16 v[72:75], a[188:191], v[28:31], v[72:75]
v_exp_f32 v101, v101
v_mfma_f32_16x16x32_bf16 v[80:83], a[144:147], v[40:43], v[80:83]
s_waitcnt lgkmcnt(3)
v_exp_f32 v102, v102
v_mfma_f32_16x16x32_bf16 v[80:83], a[188:191], v[44:47], v[80:83]
v_exp_f32 v103, v103
v_mfma_f32_16x16x32_bf16 v[88:91], a[144:147], v[56:59], v[88:91]
v_sub_f32 v104, v104, v219
v_sub_f32 v105, v105, v219
v_mfma_f32_16x16x32_bf16 v[88:91], a[188:191], v[60:63], v[88:91]
s_add_u32 m0, s40, 65536
v_sub_f32 v106, v106, v219
v_sub_f32 v107, v107, v219
v_mfma_f32_16x16x32_bf16 v[68:71], a[156:159], v[0:3], v[160:163]
s_waitcnt lgkmcnt(2)
ds_read_b64_tr_b16 a[144:145], v198 offset:16384
ds_read_b64_tr_b16 a[146:147], v198 offset:20480
v_mfma_f32_16x16x32_bf16 v[68:71], a[168:171], v[4:7], v[68:71]
s_xor_b32 s78, s41, 32768
buffer_load_dwordx4 v212, s[20:23], s78 offen nt lds
v_mfma_f32_16x16x32_bf16 v[76:79], a[156:159], v[16:19], v[164:167]
v_exp_f32 v104, v104
v_mfma_f32_16x16x32_bf16 v[76:79], a[168:171], v[20:23], v[76:79]
buffer_load_dwordx4 v212, s[20:23], s78 offen offset:2048 nt lds
v_mfma_f32_16x16x32_bf16 v[84:87], a[156:159], v[32:35], v[168:171]
s_waitcnt lgkmcnt(3)
ds_read_b64_tr_b16 a[180:181], v201 offset:16384
ds_read_b64_tr_b16 a[182:183], v201 offset:20480
v_mfma_f32_16x16x32_bf16 v[84:87], a[168:171], v[36:39], v[84:87]
v_cvt_pk_bf16_f32 v146, v100, v101
v_cvt_pk_bf16_f32 v147, v102, v103
v_mfma_f32_16x16x32_bf16 v[92:95], a[156:159], v[48:51], v[172:175]
v_exp_f32 v105, v105
v_mfma_f32_16x16x32_bf16 v[92:95], a[168:171], v[52:55], v[92:95]
v_exp_f32 v106, v106
v_mfma_f32_16x16x32_bf16 v[68:71], a[172:175], v[8:11], v[68:71]
s_waitcnt lgkmcnt(4)
ds_read_b64_tr_b16 a[156:157], v198 offset:16896
ds_read_b64_tr_b16 a[158:159], v198 offset:20992
v_mfma_f32_16x16x32_bf16 v[68:71], a[192:195], v[12:15], v[68:71]
v_exp_f32 v107, v107
v_mfma_f32_16x16x32_bf16 v[76:79], a[172:175], v[24:27], v[76:79]
v_sub_f32 v108, v108, v219
v_sub_f32 v109, v109, v219
v_mfma_f32_16x16x32_bf16 v[76:79], a[192:195], v[28:31], v[76:79]
v_sub_f32 v110, v110, v219
v_sub_f32 v111, v111, v219
v_mfma_f32_16x16x32_bf16 v[84:87], a[172:175], v[40:43], v[84:87]
ds_read_b64_tr_b16 a[168:169], v201 offset:16896
ds_read_b64_tr_b16 a[170:171], v201 offset:20992
v_mfma_f32_16x16x32_bf16 v[84:87], a[192:195], v[44:47], v[84:87]
v_exp_f32 v108, v108
v_mfma_f32_16x16x32_bf16 v[92:95], a[172:175], v[56:59], v[92:95]
v_cvt_pk_bf16_f32 v148, v104, v105
v_cvt_pk_bf16_f32 v149, v106, v107
v_mfma_f32_16x16x32_bf16 v[92:95], a[192:195], v[60:63], v[92:95]
v_exp_f32 v109, v109
v_mfma_f32_16x16x32_bf16 v[176:179], v[232:235], v[128:131], 0
s_waitcnt lgkmcnt(6)
ds_read_b64_tr_b16 a[172:173], v198 offset:17408
ds_read_b64_tr_b16 a[174:175], v198 offset:21504
v_mfma_f32_16x16x32_bf16 v[180:183], v[232:235], v[132:135], 0
v_exp_f32 v110, v110
v_mfma_f32_16x16x32_bf16 v[184:187], v[232:235], v[136:139], 0
v_exp_f32 v111, v111
v_mfma_f32_16x16x32_bf16 v[188:191], v[232:235], v[140:143], 0
v_sub_f32 v112, v112, v220
v_sub_f32 v113, v113, v220
v_mfma_f32_16x16x32_bf16 a[0:3], a[144:147], v[128:131], 0
s_waitcnt lgkmcnt(6)
ds_read_b64_tr_b16 a[184:185], v201 offset:17408
ds_read_b64_tr_b16 a[186:187], v201 offset:21504
v_mfma_f32_16x16x32_bf16 a[4:7], a[144:147], v[132:135], 0
v_sub_f32 v114, v114, v220
v_sub_f32 v115, v115, v220
v_mfma_f32_16x16x32_bf16 a[8:11], a[144:147], v[136:139], 0
v_exp_f32 v112, v112
v_mfma_f32_16x16x32_bf16 a[12:15], a[144:147], v[140:143], 0
v_cvt_pk_bf16_f32 v150, v108, v109
v_cvt_pk_bf16_f32 v151, v110, v111
v_mfma_f32_16x16x32_bf16 a[16:19], a[180:183], v[128:131], 0
s_waitcnt lgkmcnt(6)
ds_read_b64_tr_b16 a[144:145], v198 offset:17920
ds_read_b64_tr_b16 a[146:147], v198 offset:22016
v_mfma_f32_16x16x32_bf16 a[20:23], a[180:183], v[132:135], 0
v_exp_f32 v113, v113
v_mfma_f32_16x16x32_bf16 a[24:27], a[180:183], v[136:139], 0
v_exp_f32 v114, v114
v_mfma_f32_16x16x32_bf16 a[28:31], a[180:183], v[140:143], 0
v_exp_f32 v115, v115
v_mfma_f32_16x16x32_bf16 a[32:35], a[156:159], v[128:131], 0
s_waitcnt lgkmcnt(6)
ds_read_b64_tr_b16 a[180:181], v201 offset:17920
ds_read_b64_tr_b16 a[182:183], v201 offset:22016
v_mfma_f32_16x16x32_bf16 a[36:39], a[156:159], v[132:135], 0
v_sub_f32 v116, v116, v220
v_sub_f32 v117, v117, v220
v_mfma_f32_16x16x32_bf16 a[40:43], a[156:159], v[136:139], 0
v_sub_f32 v118, v118, v220
v_sub_f32 v119, v119, v220
v_mfma_f32_16x16x32_bf16 a[44:47], a[156:159], v[140:143], 0
v_exp_f32 v116, v116
v_mfma_f32_16x16x32_bf16 a[48:51], a[168:171], v[128:131], 0
s_waitcnt lgkmcnt(6)
ds_read_b128 a[156:159], v194 offset:24576
ds_read_b128 a[188:191], v194 offset:25088
v_mfma_f32_16x16x32_bf16 a[52:55], a[168:171], v[132:135], 0
v_cvt_pk_bf16_f32 v152, v112, v113
v_cvt_pk_bf16_f32 v153, v114, v115
v_mfma_f32_16x16x32_bf16 a[56:59], a[168:171], v[136:139], 0
s_add_u32 m0, s40, 66560
v_exp_f32 v117, v117
v_mfma_f32_16x16x32_bf16 a[60:63], a[168:171], v[140:143], 0
s_xor_b32 s79, s41, 32896
buffer_load_dwordx4 v212, s[20:23], s79 offen nt lds
v_mfma_f32_16x16x32_bf16 a[64:67], a[172:175], v[128:131], 0
s_waitcnt lgkmcnt(6)
v_exp_f32 v118, v118
v_mfma_f32_16x16x32_bf16 a[68:71], a[172:175], v[132:135], 0
buffer_load_dwordx4 v212, s[20:23], s79 offen offset:2048 nt lds
v_mfma_f32_16x16x32_bf16 a[72:75], a[172:175], v[136:139], 0
v_exp_f32 v119, v119
v_mfma_f32_16x16x32_bf16 a[76:79], a[172:175], v[140:143], 0
v_sub_f32 v120, v120, v221
v_sub_f32 v121, v121, v221
v_mfma_f32_16x16x32_bf16 a[80:83], a[184:187], v[128:131], 0
s_waitcnt lgkmcnt(4)
ds_read_b128 a[168:171], v194 offset:25600
ds_read_b128 a[172:175], v194 offset:26112
v_mfma_f32_16x16x32_bf16 a[84:87], a[184:187], v[132:135], 0
v_sub_f32 v122, v122, v221
v_sub_f32 v123, v123, v221
v_mfma_f32_16x16x32_bf16 a[88:91], a[184:187], v[136:139], 0
v_exp_f32 v120, v120
v_mfma_f32_16x16x32_bf16 a[92:95], a[184:187], v[140:143], 0
v_cvt_pk_bf16_f32 v154, v116, v117
v_cvt_pk_bf16_f32 v155, v118, v119
v_mfma_f32_16x16x32_bf16 a[96:99], a[144:147], v[128:131], 0
s_waitcnt lgkmcnt(4)
v_exp_f32 v121, v121
v_mfma_f32_16x16x32_bf16 a[100:103], a[144:147], v[132:135], 0
v_exp_f32 v122, v122
v_mfma_f32_16x16x32_bf16 a[104:107], a[144:147], v[136:139], 0
v_exp_f32 v123, v123
v_mfma_f32_16x16x32_bf16 a[108:111], a[144:147], v[140:143], 0
v_sub_f32 v124, v124, v221
v_sub_f32 v125, v125, v221
v_mfma_f32_16x16x32_bf16 a[112:115], a[180:183], v[128:131], 0
s_waitcnt lgkmcnt(3)
ds_read_b128 a[144:147], v194 offset:28672
ds_read_b128 a[184:187], v194 offset:29184
v_mfma_f32_16x16x32_bf16 a[116:119], a[180:183], v[132:135], 0
v_sub_f32 v126, v126, v221
v_sub_f32 v127, v127, v221
v_mfma_f32_16x16x32_bf16 a[120:123], a[180:183], v[136:139], 0
v_exp_f32 v124, v124
v_mfma_f32_16x16x32_bf16 a[124:127], a[180:183], v[140:143], 0
v_cvt_pk_bf16_f32 v156, v120, v121
v_cvt_pk_bf16_f32 v157, v122, v123
v_exp_f32 v125, v125
v_exp_f32 v126, v126
v_exp_f32 v127, v127
v_cvt_pk_bf16_f32 v158, v124, v125
v_cvt_pk_bf16_f32 v159, v126, v127
v_mfma_f32_16x16x32_bf16 v[96:99], a[156:159], v[0:3], v[160:163]
s_waitcnt lgkmcnt(4)
v_exp_f32 v64, v64
v_mfma_f32_16x16x32_bf16 v[96:99], a[188:191], v[4:7], v[96:99]
v_mfma_f32_16x16x32_bf16 v[104:107], a[156:159], v[16:19], v[164:167]
v_exp_f32 v65, v65
v_mfma_f32_16x16x32_bf16 v[104:107], a[188:191], v[20:23], v[104:107]
v_exp_f32 v66, v66
v_mfma_f32_16x16x32_bf16 v[112:115], a[156:159], v[32:35], v[168:171]
s_waitcnt lgkmcnt(3)
ds_read_b128 a[180:183], v194 offset:29696
ds_read_b128 a[192:195], v194 offset:30208
v_mfma_f32_16x16x32_bf16 v[112:115], a[188:191], v[36:39], v[112:115]
v_exp_f32 v67, v67
v_mfma_f32_16x16x32_bf16 v[120:123], a[156:159], v[48:51], v[172:175]
v_mfma_f32_16x16x32_bf16 v[120:123], a[188:191], v[52:55], v[120:123]
v_exp_f32 v68, v68
v_mfma_f32_16x16x32_bf16 v[96:99], a[168:171], v[8:11], v[96:99]
s_waitcnt lgkmcnt(4)
v_cvt_pk_bf16_f32 v128, v64, v65
v_cvt_pk_bf16_f32 v129, v66, v67
v_mfma_f32_16x16x32_bf16 v[96:99], a[172:175], v[12:15], v[96:99]
v_exp_f32 v69, v69
v_mfma_f32_16x16x32_bf16 v[104:107], a[168:171], v[24:27], v[104:107]
v_exp_f32 v70, v70
v_mfma_f32_16x16x32_bf16 v[104:107], a[172:175], v[28:31], v[104:107]
v_mfma_f32_16x16x32_bf16 v[112:115], a[168:171], v[40:43], v[112:115]
s_waitcnt lgkmcnt(3)
v_exp_f32 v71, v71
v_mfma_f32_16x16x32_bf16 v[112:115], a[172:175], v[44:47], v[112:115]
v_exp_f32 v72, v72
v_mfma_f32_16x16x32_bf16 v[120:123], a[168:171], v[56:59], v[120:123]
v_cvt_pk_bf16_f32 v130, v68, v69
v_cvt_pk_bf16_f32 v131, v70, v71
v_mfma_f32_16x16x32_bf16 v[120:123], a[172:175], v[60:63], v[120:123]
s_add_u32 m0, s40, 49152
v_exp_f32 v73, v73
v_mfma_f32_16x16x32_bf16 v[100:103], a[144:147], v[0:3], v[160:163]
s_waitcnt lgkmcnt(2)
ds_read_b64_tr_b16 a[156:157], v198 offset:24576
ds_read_b64_tr_b16 a[158:159], v198 offset:28672
v_mfma_f32_16x16x32_bf16 v[100:103], a[184:187], v[4:7], v[100:103]
s_xor_b32 s78, s41, 65536
buffer_load_dwordx4 v210, s[16:19], s78 offen nt lds
v_mfma_f32_16x16x32_bf16 v[108:111], a[144:147], v[16:19], v[164:167]
v_mfma_f32_16x16x32_bf16 v[108:111], a[184:187], v[20:23], v[108:111]
buffer_load_dwordx4 v211, s[16:19], s78 offen offset:2048 nt lds
v_mfma_f32_16x16x32_bf16 v[116:119], a[144:147], v[32:35], v[168:171]
s_waitcnt lgkmcnt(3)
ds_read_b64_tr_b16 a[168:169], v201 offset:24576
ds_read_b64_tr_b16 a[170:171], v201 offset:28672
v_mfma_f32_16x16x32_bf16 v[116:119], a[184:187], v[36:39], v[116:119]
v_exp_f32 v74, v74
v_mfma_f32_16x16x32_bf16 v[124:127], a[144:147], v[48:51], v[172:175]
v_exp_f32 v75, v75
v_mfma_f32_16x16x32_bf16 v[124:127], a[184:187], v[52:55], v[124:127]
v_exp_f32 v76, v76
v_mfma_f32_16x16x32_bf16 v[100:103], a[180:183], v[8:11], v[100:103]
s_waitcnt lgkmcnt(4)
ds_read_b64_tr_b16 a[144:145], v198 offset:25088
ds_read_b64_tr_b16 a[146:147], v198 offset:29184
v_mfma_f32_16x16x32_bf16 v[100:103], a[192:195], v[12:15], v[100:103]
v_cvt_pk_bf16_f32 v132, v72, v73
v_mfma_f32_16x16x32_bf16 v[108:111], a[180:183], v[24:27], v[108:111]
v_cvt_pk_bf16_f32 v133, v74, v75
v_mfma_f32_16x16x32_bf16 v[108:111], a[192:195], v[28:31], v[108:111]
v_exp_f32 v77, v77
v_mfma_f32_16x16x32_bf16 v[116:119], a[180:183], v[40:43], v[116:119]
ds_read_b64_tr_b16 a[172:173], v201 offset:25088
ds_read_b64_tr_b16 a[174:175], v201 offset:29184
v_mfma_f32_16x16x32_bf16 v[116:119], a[192:195], v[44:47], v[116:119]
v_exp_f32 v78, v78
v_mfma_f32_16x16x32_bf16 v[124:127], a[180:183], v[56:59], v[124:127]
v_exp_f32 v79, v79
v_mfma_f32_16x16x32_bf16 v[124:127], a[192:195], v[60:63], v[124:127]
v_mfma_f32_16x16x32_bf16 v[176:179], v[232:235], v[144:147], v[176:179]
s_waitcnt lgkmcnt(6)
ds_read_b64_tr_b16 a[180:181], v198 offset:25600
ds_read_b64_tr_b16 a[182:183], v198 offset:29696
v_mfma_f32_16x16x32_bf16 v[180:183], v[232:235], v[148:151], v[180:183]
ds_read_b64_tr_b16 a[192:193], v201 offset:26112
ds_read_b64_tr_b16 a[194:195], v201 offset:30208
v_mfma_f32_16x16x32_bf16 v[184:187], v[232:235], v[152:155], v[184:187]
ds_read_b64_tr_b16 a[188:189], v198 offset:26112
ds_read_b64_tr_b16 a[190:191], v198 offset:30208
v_mfma_f32_16x16x32_bf16 v[188:191], v[232:235], v[156:159], v[188:191]
ds_read_b64_tr_b16 a[184:185], v201 offset:25600
ds_read_b64_tr_b16 a[186:187], v201 offset:29696
v_mfma_f32_16x16x32_bf16 a[0:3], a[156:159], v[144:147], a[0:3]
s_waitcnt lgkmcnt(12)
v_exp_f32 v80, v80
v_mfma_f32_16x16x32_bf16 a[4:7], a[156:159], v[148:151], a[4:7]
v_cvt_pk_bf16_f32 v134, v76, v77
v_cvt_pk_bf16_f32 v135, v78, v79
v_mfma_f32_16x16x32_bf16 a[8:11], a[156:159], v[152:155], a[8:11]
v_exp_f32 v81, v81
v_mfma_f32_16x16x32_bf16 a[12:15], a[156:159], v[156:159], a[12:15]
v_and_b32 v248, 0x1f0, v217
v_lshlrev_b32 v248, 3, v248
v_mfma_f32_16x16x32_bf16 a[16:19], a[168:171], v[144:147], a[16:19]
s_waitcnt lgkmcnt(10)
v_exp_f32 v82, v82
v_mfma_f32_16x16x32_bf16 a[20:23], a[168:171], v[148:151], a[20:23]
s_lshl_b32 s86, s61, 2
v_exp_f32 v83, v83
v_mfma_f32_16x16x32_bf16 a[24:27], a[168:171], v[152:155], a[24:27]
s_add_u32 s86, s86, s62
v_exp_f32 v84, v84
v_mfma_f32_16x16x32_bf16 a[28:31], a[168:171], v[156:159], a[28:31]
s_lshl_b32 s86, s86, 12
v_cvt_pk_bf16_f32 v136, v80, v81
v_cvt_pk_bf16_f32 v137, v82, v83
v_mfma_f32_16x16x32_bf16 a[32:35], a[144:147], v[144:147], a[32:35]
s_waitcnt vmcnt(10) lgkmcnt(0)
s_barrier
v_mfma_f32_16x16x32_bf16 a[36:39], a[144:147], v[148:151], a[36:39]
v_and_b32 v249, 0x7ff, v248
s_add_u32 s87, s86, 2048
v_mfma_f32_16x16x32_bf16 a[40:43], a[144:147], v[152:155], a[40:43]
v_exp_f32 v85, v85
v_mfma_f32_16x16x32_bf16 a[44:47], a[144:147], v[156:159], a[44:47]
v_exp_f32 v86, v86
v_mfma_f32_16x16x32_bf16 a[48:51], a[172:175], v[144:147], a[48:51]
ds_read_b128 a[144:147], v192 offset:32768
ds_read_b128 a[156:159], v192 offset:33280
v_mfma_f32_16x16x32_bf16 a[52:55], a[172:175], v[148:151], a[52:55]
v_exp_f32 v87, v87
v_mfma_f32_16x16x32_bf16 a[56:59], a[172:175], v[152:155], a[56:59]
s_add_u32 m0, s40, 50176
v_exp_f32 v88, v88
v_mfma_f32_16x16x32_bf16 a[60:63], a[172:175], v[156:159], a[60:63]
s_xor_b32 s79, s41, 65664
buffer_load_dwordx4 v210, s[16:19], s79 offen nt lds
v_mfma_f32_16x16x32_bf16 a[64:67], a[180:183], v[144:147], a[64:67]
v_cvt_pk_bf16_f32 v138, v84, v85
v_mfma_f32_16x16x32_bf16 a[68:71], a[180:183], v[148:151], a[68:71]
buffer_load_dwordx4 v211, s[16:19], s79 offen offset:2048 nt lds
v_mfma_f32_16x16x32_bf16 a[72:75], a[180:183], v[152:155], a[72:75]
v_cvt_pk_bf16_f32 v139, v86, v87
v_mfma_f32_16x16x32_bf16 a[76:79], a[180:183], v[156:159], a[76:79]
v_exp_f32 v89, v89
v_mfma_f32_16x16x32_bf16 a[80:83], a[184:187], v[144:147], a[80:83]
ds_read_b128 a[168:171], v192 offset:33792
ds_read_b128 a[172:175], v192 offset:34304
v_mfma_f32_16x16x32_bf16 a[84:87], a[184:187], v[148:151], a[84:87]
v_exp_f32 v90, v90
v_mfma_f32_16x16x32_bf16 a[88:91], a[184:187], v[152:155], a[88:91]
buffer_load_dword v250, v249, s[12:15], s87 offen
v_mfma_f32_16x16x32_bf16 a[92:95], a[184:187], v[156:159], a[92:95]
v_exp_f32 v91, v91
v_mfma_f32_16x16x32_bf16 a[96:99], a[188:191], v[144:147], a[96:99]
v_exp_f32 v92, v92
v_mfma_f32_16x16x32_bf16 a[100:103], a[188:191], v[148:151], a[100:103]
v_cvt_pk_bf16_f32 v140, v88, v89
v_cvt_pk_bf16_f32 v141, v90, v91
v_mfma_f32_16x16x32_bf16 a[104:107], a[188:191], v[152:155], a[104:107]
v_exp_f32 v93, v93
v_mfma_f32_16x16x32_bf16 a[108:111], a[188:191], v[156:159], a[108:111]
buffer_load_dword v251, v248, s[28:31], s86 offen
v_mfma_f32_16x16x32_bf16 a[112:115], a[192:195], v[144:147], a[112:115]
s_waitcnt lgkmcnt(3)
ds_read_b128 a[180:183], v192 offset:36864
ds_read_b128 a[184:187], v192 offset:37376
v_mfma_f32_16x16x32_bf16 a[116:119], a[192:195], v[148:151], a[116:119]
v_exp_f32 v94, v94
v_mfma_f32_16x16x32_bf16 a[120:123], a[192:195], v[152:155], a[120:123]
v_exp_f32 v95, v95
v_mfma_f32_16x16x32_bf16 a[124:127], a[192:195], v[156:159], a[124:127]
v_cvt_pk_bf16_f32 v142, v92, v93
v_cvt_pk_bf16_f32 v143, v94, v95
v_mfma_f32_16x16x32_bf16 v[64:67], a[144:147], v[0:3], v[160:163]
s_waitcnt lgkmcnt(4)
v_exp_f32 v96, v96
v_mfma_f32_16x16x32_bf16 v[64:67], a[156:159], v[4:7], v[64:67]
v_mfma_f32_16x16x32_bf16 v[72:75], a[144:147], v[16:19], v[164:167]
v_exp_f32 v97, v97
v_mfma_f32_16x16x32_bf16 v[72:75], a[156:159], v[20:23], v[72:75]
v_exp_f32 v98, v98
v_mfma_f32_16x16x32_bf16 v[80:83], a[144:147], v[32:35], v[168:171]
s_waitcnt lgkmcnt(3)
ds_read_b128 a[188:191], v192 offset:37888
ds_read_b128 a[192:195], v192 offset:38400
v_mfma_f32_16x16x32_bf16 v[80:83], a[156:159], v[36:39], v[80:83]
v_exp_f32 v99, v99
v_mfma_f32_16x16x32_bf16 v[88:91], a[144:147], v[48:51], v[172:175]
v_mfma_f32_16x16x32_bf16 v[88:91], a[156:159], v[52:55], v[88:91]
v_exp_f32 v100, v100
v_mfma_f32_16x16x32_bf16 v[64:67], a[168:171], v[8:11], v[64:67]
s_waitcnt lgkmcnt(4)
v_cvt_pk_bf16_f32 v144, v96, v97
v_cvt_pk_bf16_f32 v145, v98, v99
v_mfma_f32_16x16x32_bf16 v[64:67], a[172:175], v[12:15], v[64:67]
v_exp_f32 v101, v101
v_mfma_f32_16x16x32_bf16 v[72:75], a[168:171], v[24:27], v[72:75]
v_mfma_f32_16x16x32_bf16 v[72:75], a[172:175], v[28:31], v[72:75]
v_exp_f32 v102, v102
v_mfma_f32_16x16x32_bf16 v[80:83], a[168:171], v[40:43], v[80:83]
s_waitcnt lgkmcnt(3)
v_exp_f32 v103, v103
v_mfma_f32_16x16x32_bf16 v[80:83], a[172:175], v[44:47], v[80:83]
v_exp_f32 v104, v104
v_mfma_f32_16x16x32_bf16 v[88:91], a[168:171], v[56:59], v[88:91]
v_cvt_pk_bf16_f32 v146, v100, v101
v_cvt_pk_bf16_f32 v147, v102, v103
v_mfma_f32_16x16x32_bf16 v[88:91], a[172:175], v[60:63], v[88:91]
s_add_u32 m0, s40, 81920
v_mfma_f32_16x16x32_bf16 v[68:71], a[180:183], v[0:3], v[160:163]
s_waitcnt lgkmcnt(2)
ds_read_b64_tr_b16 a[144:145], v199 offset:32768
ds_read_b64_tr_b16 a[146:147], v199 offset:36864
v_mfma_f32_16x16x32_bf16 v[68:71], a[184:187], v[4:7], v[68:71]
s_xor_b32 s78, s41, 49152
buffer_load_dwordx4 v212, s[20:23], s78 offen nt lds
v_mfma_f32_16x16x32_bf16 v[76:79], a[180:183], v[16:19], v[164:167]
v_exp_f32 v105, v105
v_mfma_f32_16x16x32_bf16 v[76:79], a[184:187], v[20:23], v[76:79]
buffer_load_dwordx4 v212, s[20:23], s78 offen offset:2048 nt lds
v_mfma_f32_16x16x32_bf16 v[84:87], a[180:183], v[32:35], v[168:171]
s_waitcnt lgkmcnt(3)
ds_read_b64_tr_b16 a[156:157], v202 offset:32768
ds_read_b64_tr_b16 a[158:159], v202 offset:36864
v_mfma_f32_16x16x32_bf16 v[84:87], a[184:187], v[36:39], v[84:87]
v_exp_f32 v106, v106
v_mfma_f32_16x16x32_bf16 v[92:95], a[180:183], v[48:51], v[172:175]
v_exp_f32 v107, v107
v_mfma_f32_16x16x32_bf16 v[92:95], a[184:187], v[52:55], v[92:95]
v_mfma_f32_16x16x32_bf16 v[68:71], a[188:191], v[8:11], v[68:71]
s_waitcnt lgkmcnt(4)
ds_read_b64_tr_b16 a[168:169], v199 offset:33280
ds_read_b64_tr_b16 a[170:171], v199 offset:37376
v_mfma_f32_16x16x32_bf16 v[68:71], a[192:195], v[12:15], v[68:71]
v_exp_f32 v108, v108
v_mfma_f32_16x16x32_bf16 v[76:79], a[188:191], v[24:27], v[76:79]
v_cvt_pk_bf16_f32 v148, v104, v105
v_cvt_pk_bf16_f32 v149, v106, v107
v_mfma_f32_16x16x32_bf16 v[76:79], a[192:195], v[28:31], v[76:79]
v_exp_f32 v109, v109
v_mfma_f32_16x16x32_bf16 v[84:87], a[188:191], v[40:43], v[84:87]
ds_read_b64_tr_b16 a[172:173], v202 offset:33280
ds_read_b64_tr_b16 a[174:175], v202 offset:37376
v_mfma_f32_16x16x32_bf16 v[84:87], a[192:195], v[44:47], v[84:87]
v_mfma_f32_16x16x32_bf16 v[92:95], a[188:191], v[56:59], v[92:95]
v_exp_f32 v110, v110
v_mfma_f32_16x16x32_bf16 v[92:95], a[192:195], v[60:63], v[92:95]
v_exp_f32 v111, v111
v_mfma_f32_16x16x32_bf16 v[176:179], v[232:235], v[128:131], v[176:179]
s_waitcnt lgkmcnt(6)
ds_read_b64_tr_b16 a[180:181], v199 offset:33792
ds_read_b64_tr_b16 a[182:183], v199 offset:37888
v_mfma_f32_16x16x32_bf16 v[180:183], v[232:235], v[132:135], v[180:183]
v_exp_f32 v112, v112
v_mfma_f32_16x16x32_bf16 v[184:187], v[232:235], v[136:139], v[184:187]
v_cvt_pk_bf16_f32 v150, v108, v109
v_cvt_pk_bf16_f32 v151, v110, v111
v_mfma_f32_16x16x32_bf16 v[188:191], v[232:235], v[140:143], v[188:191]
v_mfma_f32_16x16x32_bf16 a[0:3], a[144:147], v[128:131], a[0:3]
s_waitcnt lgkmcnt(6)
ds_read_b64_tr_b16 a[184:185], v202 offset:33792
ds_read_b64_tr_b16 a[186:187], v202 offset:37888
v_mfma_f32_16x16x32_bf16 a[4:7], a[144:147], v[132:135], a[4:7]
v_exp_f32 v113, v113
v_mfma_f32_16x16x32_bf16 a[8:11], a[144:147], v[136:139], a[8:11]
v_exp_f32 v114, v114
v_mfma_f32_16x16x32_bf16 a[12:15], a[144:147], v[140:143], a[12:15]
v_exp_f32 v115, v115
v_mfma_f32_16x16x32_bf16 a[16:19], a[156:159], v[128:131], a[16:19]
s_waitcnt lgkmcnt(6)
ds_read_b64_tr_b16 a[144:145], v199 offset:34304
ds_read_b64_tr_b16 a[146:147], v199 offset:38400
v_mfma_f32_16x16x32_bf16 a[20:23], a[156:159], v[132:135], a[20:23]
v_mfma_f32_16x16x32_bf16 a[24:27], a[156:159], v[136:139], a[24:27]
v_exp_f32 v116, v116
v_mfma_f32_16x16x32_bf16 a[28:31], a[156:159], v[140:143], a[28:31]
v_cvt_pk_bf16_f32 v152, v112, v113
v_cvt_pk_bf16_f32 v153, v114, v115
v_mfma_f32_16x16x32_bf16 a[32:35], a[168:171], v[128:131], a[32:35]
s_waitcnt lgkmcnt(6)
ds_read_b64_tr_b16 a[156:157], v202 offset:34304
ds_read_b64_tr_b16 a[158:159], v202 offset:38400
v_mfma_f32_16x16x32_bf16 a[36:39], a[168:171], v[132:135], a[36:39]
v_exp_f32 v117, v117
v_mfma_f32_16x16x32_bf16 a[40:43], a[168:171], v[136:139], a[40:43]
v_mfma_f32_16x16x32_bf16 a[44:47], a[168:171], v[140:143], a[44:47]
v_exp_f32 v118, v118
v_mfma_f32_16x16x32_bf16 a[48:51], a[172:175], v[128:131], a[48:51]
s_waitcnt lgkmcnt(6)
ds_read_b128 a[168:171], v192 offset:40960
ds_read_b128 a[188:191], v192 offset:41472
v_mfma_f32_16x16x32_bf16 a[52:55], a[172:175], v[132:135], a[52:55]
v_exp_f32 v119, v119
v_mfma_f32_16x16x32_bf16 a[56:59], a[172:175], v[136:139], a[56:59]
s_add_u32 m0, s40, 82944
v_exp_f32 v120, v120
v_mfma_f32_16x16x32_bf16 a[60:63], a[172:175], v[140:143], a[60:63]
s_xor_b32 s79, s41, 49280
buffer_load_dwordx4 v212, s[20:23], s79 offen nt lds
v_mfma_f32_16x16x32_bf16 a[64:67], a[180:183], v[128:131], a[64:67]
s_waitcnt lgkmcnt(6)
v_cvt_pk_bf16_f32 v154, v116, v117
v_cvt_pk_bf16_f32 v155, v118, v119
v_mfma_f32_16x16x32_bf16 a[68:71], a[180:183], v[132:135], a[68:71]
buffer_load_dwordx4 v212, s[20:23], s79 offen offset:2048 nt lds
v_mfma_f32_16x16x32_bf16 a[72:75], a[180:183], v[136:139], a[72:75]
v_mfma_f32_16x16x32_bf16 a[76:79], a[180:183], v[140:143], a[76:79]
v_exp_f32 v121, v121
v_mfma_f32_16x16x32_bf16 a[80:83], a[184:187], v[128:131], a[80:83]
s_waitcnt lgkmcnt(4)
ds_read_b128 a[172:175], v192 offset:41984
ds_read_b128 a[180:183], v192 offset:42496
v_mfma_f32_16x16x32_bf16 a[84:87], a[184:187], v[132:135], a[84:87]
v_exp_f32 v122, v122
v_mfma_f32_16x16x32_bf16 a[88:91], a[184:187], v[136:139], a[88:91]
v_exp_f32 v123, v123
v_mfma_f32_16x16x32_bf16 a[92:95], a[184:187], v[140:143], a[92:95]
L_tch4_%=:
s_cbranch_execz L_tch8_%=
v_mfma_f32_16x16x32_bf16 a[96:99], a[144:147], v[128:131], a[96:99]
s_waitcnt lgkmcnt(4)
v_exp_f32 v124, v124
v_mfma_f32_16x16x32_bf16 a[100:103], a[144:147], v[132:135], a[100:103]
v_cvt_pk_bf16_f32 v156, v120, v121
v_cvt_pk_bf16_f32 v157, v122, v123
v_mfma_f32_16x16x32_bf16 a[104:107], a[144:147], v[136:139], a[104:107]
v_exp_f32 v125, v125
v_mfma_f32_16x16x32_bf16 a[108:111], a[144:147], v[140:143], a[108:111]
v_mfma_f32_16x16x32_bf16 a[112:115], a[156:159], v[128:131], a[112:115]
s_waitcnt lgkmcnt(3)
ds_read_b128 a[144:147], v192 offset:45056
ds_read_b128 a[184:187], v192 offset:45568
v_mfma_f32_16x16x32_bf16 a[116:119], a[156:159], v[132:135], a[116:119]
v_exp_f32 v126, v126
v_mfma_f32_16x16x32_bf16 a[120:123], a[156:159], v[136:139], a[120:123]
v_exp_f32 v127, v127
v_mfma_f32_16x16x32_bf16 a[124:127], a[156:159], v[140:143], a[124:127]
v_cvt_pk_bf16_f32 v158, v124, v125
v_cvt_pk_bf16_f32 v159, v126, v127
v_mfma_f32_16x16x32_bf16 v[96:99], a[168:171], v[0:3], v[160:163]
s_waitcnt lgkmcnt(4)
v_exp_f32 v64, v64
v_mfma_f32_16x16x32_bf16 v[96:99], a[188:191], v[4:7], v[96:99]
v_mfma_f32_16x16x32_bf16 v[104:107], a[168:171], v[16:19], v[164:167]
v_exp_f32 v65, v65
v_mfma_f32_16x16x32_bf16 v[104:107], a[188:191], v[20:23], v[104:107]
v_exp_f32 v66, v66
v_mfma_f32_16x16x32_bf16 v[112:115], a[168:171], v[32:35], v[168:171]
s_waitcnt lgkmcnt(3)
ds_read_b128 a[156:159], v192 offset:46080
ds_read_b128 a[192:195], v192 offset:46592
v_mfma_f32_16x16x32_bf16 v[112:115], a[188:191], v[36:39], v[112:115]
v_exp_f32 v67, v67
v_mfma_f32_16x16x32_bf16 v[120:123], a[168:171], v[48:51], v[172:175]
v_mfma_f32_16x16x32_bf16 v[120:123], a[188:191], v[52:55], v[120:123]
v_exp_f32 v68, v68
v_mfma_f32_16x16x32_bf16 v[96:99], a[172:175], v[8:11], v[96:99]
s_waitcnt lgkmcnt(4)
v_cvt_pk_bf16_f32 v128, v64, v65
v_cvt_pk_bf16_f32 v129, v66, v67
v_mfma_f32_16x16x32_bf16 v[96:99], a[180:183], v[12:15], v[96:99]
v_exp_f32 v69, v69
v_mfma_f32_16x16x32_bf16 v[104:107], a[172:175], v[24:27], v[104:107]
v_exp_f32 v70, v70
v_mfma_f32_16x16x32_bf16 v[104:107], a[180:183], v[28:31], v[104:107]
v_mfma_f32_16x16x32_bf16 v[112:115], a[172:175], v[40:43], v[112:115]
s_waitcnt lgkmcnt(3)
v_exp_f32 v71, v71
v_mfma_f32_16x16x32_bf16 v[112:115], a[180:183], v[44:47], v[112:115]
v_exp_f32 v72, v72
v_mfma_f32_16x16x32_bf16 v[120:123], a[172:175], v[56:59], v[120:123]
v_cvt_pk_bf16_f32 v130, v68, v69
v_cvt_pk_bf16_f32 v131, v70, v71
v_mfma_f32_16x16x32_bf16 v[120:123], a[180:183], v[60:63], v[120:123]
s_add_u32 m0, s40, 114688
v_exp_f32 v73, v73
v_mfma_f32_16x16x32_bf16 v[100:103], a[144:147], v[0:3], v[160:163]
s_waitcnt lgkmcnt(2)
ds_read_b64_tr_b16 a[168:169], v199 offset:40960
ds_read_b64_tr_b16 a[170:171], v199 offset:45056
v_mfma_f32_16x16x32_bf16 v[100:103], a[184:187], v[4:7], v[100:103]
s_xor_b32 s78, s41, 81920
buffer_load_dwordx4 v210, s[16:19], s78 offen nt lds
v_mfma_f32_16x16x32_bf16 v[108:111], a[144:147], v[16:19], v[164:167]
v_mfma_f32_16x16x32_bf16 v[108:111], a[184:187], v[20:23], v[108:111]
buffer_load_dwordx4 v211, s[16:19], s78 offen offset:2048 nt lds
v_mfma_f32_16x16x32_bf16 v[116:119], a[144:147], v[32:35], v[168:171]
s_waitcnt lgkmcnt(3)
ds_read_b64_tr_b16 a[172:173], v202 offset:40960
ds_read_b64_tr_b16 a[174:175], v202 offset:45056
v_mfma_f32_16x16x32_bf16 v[116:119], a[184:187], v[36:39], v[116:119]
v_exp_f32 v74, v74
v_mfma_f32_16x16x32_bf16 v[124:127], a[144:147], v[48:51], v[172:175]
v_exp_f32 v75, v75
v_mfma_f32_16x16x32_bf16 v[124:127], a[184:187], v[52:55], v[124:127]
v_exp_f32 v76, v76
v_mfma_f32_16x16x32_bf16 v[100:103], a[156:159], v[8:11], v[100:103]
s_waitcnt lgkmcnt(4)
ds_read_b64_tr_b16 a[144:145], v199 offset:41472
ds_read_b64_tr_b16 a[146:147], v199 offset:45568
v_mfma_f32_16x16x32_bf16 v[100:103], a[192:195], v[12:15], v[100:103]
v_cvt_pk_bf16_f32 v132, v72, v73
v_mfma_f32_16x16x32_bf16 v[108:111], a[156:159], v[24:27], v[108:111]
v_cvt_pk_bf16_f32 v133, v74, v75
v_mfma_f32_16x16x32_bf16 v[108:111], a[192:195], v[28:31], v[108:111]
v_exp_f32 v77, v77
v_mfma_f32_16x16x32_bf16 v[116:119], a[156:159], v[40:43], v[116:119]
ds_read_b64_tr_b16 a[180:181], v202 offset:41472
ds_read_b64_tr_b16 a[182:183], v202 offset:45568
v_mfma_f32_16x16x32_bf16 v[116:119], a[192:195], v[44:47], v[116:119]
v_exp_f32 v78, v78
v_mfma_f32_16x16x32_bf16 v[124:127], a[156:159], v[56:59], v[124:127]
v_exp_f32 v79, v79
v_mfma_f32_16x16x32_bf16 v[124:127], a[192:195], v[60:63], v[124:127]
v_mfma_f32_16x16x32_bf16 v[176:179], v[232:235], v[144:147], v[176:179]
s_waitcnt lgkmcnt(6)
ds_read_b64_tr_b16 a[156:157], v199 offset:41984
ds_read_b64_tr_b16 a[158:159], v199 offset:46080
v_mfma_f32_16x16x32_bf16 v[180:183], v[232:235], v[148:151], v[180:183]
ds_read_b64_tr_b16 a[192:193], v202 offset:42496
ds_read_b64_tr_b16 a[194:195], v202 offset:46592
v_mfma_f32_16x16x32_bf16 v[184:187], v[232:235], v[152:155], v[184:187]
ds_read_b64_tr_b16 a[188:189], v199 offset:42496
ds_read_b64_tr_b16 a[190:191], v199 offset:46592
v_mfma_f32_16x16x32_bf16 v[188:191], v[232:235], v[156:159], v[188:191]
ds_read_b64_tr_b16 a[184:185], v202 offset:41984
ds_read_b64_tr_b16 a[186:187], v202 offset:46080
v_mfma_f32_16x16x32_bf16 a[0:3], a[168:171], v[144:147], a[0:3]
s_waitcnt lgkmcnt(12)
v_exp_f32 v80, v80
v_mfma_f32_16x16x32_bf16 a[4:7], a[168:171], v[148:151], a[4:7]
v_cvt_pk_bf16_f32 v134, v76, v77
v_cvt_pk_bf16_f32 v135, v78, v79
v_mfma_f32_16x16x32_bf16 a[8:11], a[168:171], v[152:155], a[8:11]
v_exp_f32 v81, v81
v_mfma_f32_16x16x32_bf16 a[12:15], a[168:171], v[156:159], a[12:15]
v_mfma_f32_16x16x32_bf16 a[16:19], a[172:175], v[144:147], a[16:19]
s_waitcnt lgkmcnt(10)
v_exp_f32 v82, v82
v_mfma_f32_16x16x32_bf16 a[20:23], a[172:175], v[148:151], a[20:23]
v_exp_f32 v83, v83
v_mfma_f32_16x16x32_bf16 a[24:27], a[172:175], v[152:155], a[24:27]
v_exp_f32 v84, v84
v_mfma_f32_16x16x32_bf16 a[28:31], a[172:175], v[156:159], a[28:31]
v_cvt_pk_bf16_f32 v136, v80, v81
v_cvt_pk_bf16_f32 v137, v82, v83
v_mfma_f32_16x16x32_bf16 a[32:35], a[144:147], v[144:147], a[32:35]
s_waitcnt vmcnt(12) lgkmcnt(0)
s_barrier
v_mfma_f32_16x16x32_bf16 a[36:39], a[144:147], v[148:151], a[36:39]
v_mfma_f32_16x16x32_bf16 a[40:43], a[144:147], v[152:155], a[40:43]
v_exp_f32 v85, v85
v_mfma_f32_16x16x32_bf16 a[44:47], a[144:147], v[156:159], a[44:47]
v_exp_f32 v86, v86
v_mfma_f32_16x16x32_bf16 a[48:51], a[180:183], v[144:147], a[48:51]
ds_read_b128 a[144:147], v192 offset:0
ds_read_b128 a[168:171], v192 offset:512
v_mfma_f32_16x16x32_bf16 a[52:55], a[180:183], v[148:151], a[52:55]
v_exp_f32 v87, v87
v_mfma_f32_16x16x32_bf16 a[56:59], a[180:183], v[152:155], a[56:59]
s_add_u32 m0, s40, 115712
v_exp_f32 v88, v88
v_mfma_f32_16x16x32_bf16 a[60:63], a[180:183], v[156:159], a[60:63]
s_xor_b32 s79, s41, 82048
buffer_load_dwordx4 v210, s[16:19], s79 offen nt lds
v_mfma_f32_16x16x32_bf16 a[64:67], a[156:159], v[144:147], a[64:67]
v_cvt_pk_bf16_f32 v138, v84, v85
v_mfma_f32_16x16x32_bf16 a[68:71], a[156:159], v[148:151], a[68:71]
buffer_load_dwordx4 v211, s[16:19], s79 offen offset:2048 nt lds
v_mfma_f32_16x16x32_bf16 a[72:75], a[156:159], v[152:155], a[72:75]
v_cvt_pk_bf16_f32 v139, v86, v87
v_mfma_f32_16x16x32_bf16 a[76:79], a[156:159], v[156:159], a[76:79]
v_exp_f32 v89, v89
v_mfma_f32_16x16x32_bf16 a[80:83], a[184:187], v[144:147], a[80:83]
ds_read_b128 a[156:159], v192 offset:1024
ds_read_b128 a[172:175], v192 offset:1536
v_mfma_f32_16x16x32_bf16 a[84:87], a[184:187], v[148:151], a[84:87]
v_exp_f32 v90, v90
v_mfma_f32_16x16x32_bf16 a[88:91], a[184:187], v[152:155], a[88:91]
v_mfma_f32_16x16x32_bf16 a[92:95], a[184:187], v[156:159], a[92:95]
v_exp_f32 v91, v91
v_mfma_f32_16x16x32_bf16 a[96:99], a[188:191], v[144:147], a[96:99]
v_exp_f32 v92, v92
v_mfma_f32_16x16x32_bf16 a[100:103], a[188:191], v[148:151], a[100:103]
v_cvt_pk_bf16_f32 v140, v88, v89
v_cvt_pk_bf16_f32 v141, v90, v91
v_mfma_f32_16x16x32_bf16 a[104:107], a[188:191], v[152:155], a[104:107]
v_exp_f32 v93, v93
v_mfma_f32_16x16x32_bf16 a[108:111], a[188:191], v[156:159], a[108:111]
v_mfma_f32_16x16x32_bf16 a[112:115], a[192:195], v[144:147], a[112:115]
s_waitcnt lgkmcnt(3)
ds_read_b128 a[180:183], v192 offset:4096
ds_read_b128 a[184:187], v192 offset:4608
v_mfma_f32_16x16x32_bf16 a[116:119], a[192:195], v[148:151], a[116:119]
v_exp_f32 v94, v94
v_mfma_f32_16x16x32_bf16 a[120:123], a[192:195], v[152:155], a[120:123]
v_exp_f32 v95, v95
v_mfma_f32_16x16x32_bf16 a[124:127], a[192:195], v[156:159], a[124:127]
v_cvt_pk_bf16_f32 v142, v92, v93
v_cvt_pk_bf16_f32 v143, v94, v95
v_mfma_f32_16x16x32_bf16 v[64:67], a[144:147], v[0:3], v[160:163]
s_waitcnt lgkmcnt(4)
v_exp_f32 v96, v96
v_mfma_f32_16x16x32_bf16 v[64:67], a[168:171], v[4:7], v[64:67]
v_mfma_f32_16x16x32_bf16 v[72:75], a[144:147], v[16:19], v[164:167]
v_exp_f32 v97, v97
v_mfma_f32_16x16x32_bf16 v[72:75], a[168:171], v[20:23], v[72:75]
v_exp_f32 v98, v98
v_mfma_f32_16x16x32_bf16 v[80:83], a[144:147], v[32:35], v[168:171]
s_waitcnt lgkmcnt(3)
ds_read_b128 a[188:191], v192 offset:5120
ds_read_b128 a[192:195], v192 offset:5632
v_mfma_f32_16x16x32_bf16 v[80:83], a[168:171], v[36:39], v[80:83]
v_exp_f32 v99, v99
v_mfma_f32_16x16x32_bf16 v[88:91], a[144:147], v[48:51], v[172:175]
v_mfma_f32_16x16x32_bf16 v[88:91], a[168:171], v[52:55], v[88:91]
v_exp_f32 v100, v100
v_mfma_f32_16x16x32_bf16 v[64:67], a[156:159], v[8:11], v[64:67]
s_waitcnt lgkmcnt(4)
v_cvt_pk_bf16_f32 v144, v96, v97
v_cvt_pk_bf16_f32 v145, v98, v99
v_mfma_f32_16x16x32_bf16 v[64:67], a[172:175], v[12:15], v[64:67]
v_exp_f32 v101, v101
v_mfma_f32_16x16x32_bf16 v[72:75], a[156:159], v[24:27], v[72:75]
v_mfma_f32_16x16x32_bf16 v[72:75], a[172:175], v[28:31], v[72:75]
v_exp_f32 v102, v102
v_mfma_f32_16x16x32_bf16 v[80:83], a[156:159], v[40:43], v[80:83]
s_waitcnt lgkmcnt(3)
v_exp_f32 v103, v103
v_mfma_f32_16x16x32_bf16 v[80:83], a[172:175], v[44:47], v[80:83]
v_exp_f32 v104, v104
v_mfma_f32_16x16x32_bf16 v[88:91], a[156:159], v[56:59], v[88:91]
v_cvt_pk_bf16_f32 v146, v100, v101
v_cvt_pk_bf16_f32 v147, v102, v103
v_mfma_f32_16x16x32_bf16 v[88:91], a[172:175], v[60:63], v[88:91]
s_add_u32 m0, s40, 131072
v_mfma_f32_16x16x32_bf16 v[68:71], a[180:183], v[0:3], v[160:163]
s_waitcnt lgkmcnt(2)
ds_read_b64_tr_b16 a[144:145], v199 offset:0
ds_read_b64_tr_b16 a[146:147], v199 offset:4096
v_mfma_f32_16x16x32_bf16 v[68:71], a[184:187], v[4:7], v[68:71]
s_xor_b32 s78, s41, 65536
buffer_load_dwordx4 v212, s[20:23], s78 offen nt lds
v_mfma_f32_16x16x32_bf16 v[76:79], a[180:183], v[16:19], v[164:167]
v_exp_f32 v105, v105
v_mfma_f32_16x16x32_bf16 v[76:79], a[184:187], v[20:23], v[76:79]
buffer_load_dwordx4 v212, s[20:23], s78 offen offset:2048 nt lds
v_mfma_f32_16x16x32_bf16 v[84:87], a[180:183], v[32:35], v[168:171]
s_waitcnt lgkmcnt(3)
ds_read_b64_tr_b16 a[156:157], v202 offset:0
ds_read_b64_tr_b16 a[158:159], v202 offset:4096
v_mfma_f32_16x16x32_bf16 v[84:87], a[184:187], v[36:39], v[84:87]
v_exp_f32 v106, v106
v_mfma_f32_16x16x32_bf16 v[92:95], a[180:183], v[48:51], v[172:175]
v_exp_f32 v107, v107
v_mfma_f32_16x16x32_bf16 v[92:95], a[184:187], v[52:55], v[92:95]
v_mfma_f32_16x16x32_bf16 v[68:71], a[188:191], v[8:11], v[68:71]
s_waitcnt lgkmcnt(4)
ds_read_b64_tr_b16 a[168:169], v199 offset:512
ds_read_b64_tr_b16 a[170:171], v199 offset:4608
v_mfma_f32_16x16x32_bf16 v[68:71], a[192:195], v[12:15], v[68:71]
v_exp_f32 v108, v108
v_mfma_f32_16x16x32_bf16 v[76:79], a[188:191], v[24:27], v[76:79]
v_cvt_pk_bf16_f32 v148, v104, v105
v_cvt_pk_bf16_f32 v149, v106, v107
v_mfma_f32_16x16x32_bf16 v[76:79], a[192:195], v[28:31], v[76:79]
v_exp_f32 v109, v109
v_mfma_f32_16x16x32_bf16 v[84:87], a[188:191], v[40:43], v[84:87]
ds_read_b64_tr_b16 a[172:173], v202 offset:512
ds_read_b64_tr_b16 a[174:175], v202 offset:4608
v_mfma_f32_16x16x32_bf16 v[84:87], a[192:195], v[44:47], v[84:87]
v_mfma_f32_16x16x32_bf16 v[92:95], a[188:191], v[56:59], v[92:95]
v_exp_f32 v110, v110
v_mfma_f32_16x16x32_bf16 v[92:95], a[192:195], v[60:63], v[92:95]
v_exp_f32 v111, v111
v_mfma_f32_16x16x32_bf16 v[176:179], v[232:235], v[128:131], v[176:179]
s_waitcnt lgkmcnt(6)
ds_read_b64_tr_b16 a[180:181], v199 offset:1024
ds_read_b64_tr_b16 a[182:183], v199 offset:5120
v_mfma_f32_16x16x32_bf16 v[180:183], v[232:235], v[132:135], v[180:183]
v_exp_f32 v112, v112
v_mfma_f32_16x16x32_bf16 v[184:187], v[232:235], v[136:139], v[184:187]
v_cvt_pk_bf16_f32 v150, v108, v109
v_cvt_pk_bf16_f32 v151, v110, v111
v_mfma_f32_16x16x32_bf16 v[188:191], v[232:235], v[140:143], v[188:191]
v_mfma_f32_16x16x32_bf16 a[0:3], a[144:147], v[128:131], a[0:3]
s_waitcnt lgkmcnt(6)
ds_read_b64_tr_b16 a[184:185], v202 offset:1024
ds_read_b64_tr_b16 a[186:187], v202 offset:5120
v_mfma_f32_16x16x32_bf16 a[4:7], a[144:147], v[132:135], a[4:7]
v_exp_f32 v113, v113
v_mfma_f32_16x16x32_bf16 a[8:11], a[144:147], v[136:139], a[8:11]
v_exp_f32 v114, v114
v_mfma_f32_16x16x32_bf16 a[12:15], a[144:147], v[140:143], a[12:15]
v_exp_f32 v115, v115
v_mfma_f32_16x16x32_bf16 a[16:19], a[156:159], v[128:131], a[16:19]
s_waitcnt lgkmcnt(6)
ds_read_b64_tr_b16 a[144:145], v199 offset:1536
ds_read_b64_tr_b16 a[146:147], v199 offset:5632
v_mfma_f32_16x16x32_bf16 a[20:23], a[156:159], v[132:135], a[20:23]
v_mfma_f32_16x16x32_bf16 a[24:27], a[156:159], v[136:139], a[24:27]
v_exp_f32 v116, v116
v_mfma_f32_16x16x32_bf16 a[28:31], a[156:159], v[140:143], a[28:31]
v_cvt_pk_bf16_f32 v152, v112, v113
v_cvt_pk_bf16_f32 v153, v114, v115
v_mfma_f32_16x16x32_bf16 a[32:35], a[168:171], v[128:131], a[32:35]
s_waitcnt lgkmcnt(6)
ds_read_b64_tr_b16 a[156:157], v202 offset:1536
ds_read_b64_tr_b16 a[158:159], v202 offset:5632
v_mfma_f32_16x16x32_bf16 a[36:39], a[168:171], v[132:135], a[36:39]
v_exp_f32 v117, v117
v_mfma_f32_16x16x32_bf16 a[40:43], a[168:171], v[136:139], a[40:43]
v_mfma_f32_16x16x32_bf16 a[44:47], a[168:171], v[140:143], a[44:47]
v_exp_f32 v118, v118
v_mfma_f32_16x16x32_bf16 a[48:51], a[172:175], v[128:131], a[48:51]
s_waitcnt lgkmcnt(6)
ds_read_b128 a[168:171], v192 offset:8192
ds_read_b128 a[188:191], v192 offset:8704
v_mfma_f32_16x16x32_bf16 a[52:55], a[172:175], v[132:135], a[52:55]
v_exp_f32 v119, v119
v_mfma_f32_16x16x32_bf16 a[56:59], a[172:175], v[136:139], a[56:59]
s_add_u32 m0, s40, 132096
v_exp_f32 v120, v120
v_mfma_f32_16x16x32_bf16 a[60:63], a[172:175], v[140:143], a[60:63]
s_xor_b32 s79, s41, 65664
buffer_load_dwordx4 v212, s[20:23], s79 offen nt lds
v_mfma_f32_16x16x32_bf16 a[64:67], a[180:183], v[128:131], a[64:67]
s_waitcnt lgkmcnt(6)
v_cvt_pk_bf16_f32 v154, v116, v117
v_cvt_pk_bf16_f32 v155, v118, v119
v_mfma_f32_16x16x32_bf16 a[68:71], a[180:183], v[132:135], a[68:71]
buffer_load_dwordx4 v212, s[20:23], s79 offen offset:2048 nt lds
v_mfma_f32_16x16x32_bf16 a[72:75], a[180:183], v[136:139], a[72:75]
v_mfma_f32_16x16x32_bf16 a[76:79], a[180:183], v[140:143], a[76:79]
v_exp_f32 v121, v121
v_mfma_f32_16x16x32_bf16 a[80:83], a[184:187], v[128:131], a[80:83]
s_waitcnt lgkmcnt(4)
ds_read_b128 a[172:175], v192 offset:9216
ds_read_b128 a[180:183], v192 offset:9728
v_mfma_f32_16x16x32_bf16 a[84:87], a[184:187], v[132:135], a[84:87]
v_exp_f32 v122, v122
v_mfma_f32_16x16x32_bf16 a[88:91], a[184:187], v[136:139], a[88:91]
v_exp_f32 v123, v123
v_mfma_f32_16x16x32_bf16 a[92:95], a[184:187], v[140:143], a[92:95]
v_mfma_f32_16x16x32_bf16 a[96:99], a[144:147], v[128:131], a[96:99]
s_waitcnt lgkmcnt(4)
v_exp_f32 v124, v124
v_mfma_f32_16x16x32_bf16 a[100:103], a[144:147], v[132:135], a[100:103]
v_cvt_pk_bf16_f32 v156, v120, v121
v_cvt_pk_bf16_f32 v157, v122, v123
v_mfma_f32_16x16x32_bf16 a[104:107], a[144:147], v[136:139], a[104:107]
v_exp_f32 v125, v125
v_mfma_f32_16x16x32_bf16 a[108:111], a[144:147], v[140:143], a[108:111]
v_mfma_f32_16x16x32_bf16 a[112:115], a[156:159], v[128:131], a[112:115]
s_waitcnt lgkmcnt(3)
ds_read_b128 a[144:147], v192 offset:12288
ds_read_b128 a[184:187], v192 offset:12800
v_mfma_f32_16x16x32_bf16 a[116:119], a[156:159], v[132:135], a[116:119]
v_exp_f32 v126, v126
v_mfma_f32_16x16x32_bf16 a[120:123], a[156:159], v[136:139], a[120:123]
v_exp_f32 v127, v127
v_mfma_f32_16x16x32_bf16 a[124:127], a[156:159], v[140:143], a[124:127]
v_cvt_pk_bf16_f32 v158, v124, v125
v_cvt_pk_bf16_f32 v159, v126, v127
v_mfma_f32_16x16x32_bf16 v[96:99], a[168:171], v[0:3], v[160:163]
s_waitcnt lgkmcnt(4)
v_exp_f32 v64, v64
v_mfma_f32_16x16x32_bf16 v[96:99], a[188:191], v[4:7], v[96:99]
v_mfma_f32_16x16x32_bf16 v[104:107], a[168:171], v[16:19], v[164:167]
v_exp_f32 v65, v65
v_mfma_f32_16x16x32_bf16 v[104:107], a[188:191], v[20:23], v[104:107]
v_exp_f32 v66, v66
v_mfma_f32_16x16x32_bf16 v[112:115], a[168:171], v[32:35], v[168:171]
s_waitcnt lgkmcnt(3)
ds_read_b128 a[156:159], v192 offset:13312
ds_read_b128 a[192:195], v192 offset:13824
v_mfma_f32_16x16x32_bf16 v[112:115], a[188:191], v[36:39], v[112:115]
v_exp_f32 v67, v67
v_mfma_f32_16x16x32_bf16 v[120:123], a[168:171], v[48:51], v[172:175]
v_mfma_f32_16x16x32_bf16 v[120:123], a[188:191], v[52:55], v[120:123]
v_exp_f32 v68, v68
v_mfma_f32_16x16x32_bf16 v[96:99], a[172:175], v[8:11], v[96:99]
s_waitcnt lgkmcnt(4)
v_cvt_pk_bf16_f32 v128, v64, v65
v_cvt_pk_bf16_f32 v129, v66, v67
v_mfma_f32_16x16x32_bf16 v[96:99], a[180:183], v[12:15], v[96:99]
v_exp_f32 v69, v69
v_mfma_f32_16x16x32_bf16 v[104:107], a[172:175], v[24:27], v[104:107]
v_exp_f32 v70, v70
v_mfma_f32_16x16x32_bf16 v[104:107], a[180:183], v[28:31], v[104:107]
v_mfma_f32_16x16x32_bf16 v[112:115], a[172:175], v[40:43], v[112:115]
s_waitcnt lgkmcnt(3)
v_exp_f32 v71, v71
v_mfma_f32_16x16x32_bf16 v[112:115], a[180:183], v[44:47], v[112:115]
v_exp_f32 v72, v72
v_mfma_f32_16x16x32_bf16 v[120:123], a[172:175], v[56:59], v[120:123]
v_cvt_pk_bf16_f32 v130, v68, v69
v_cvt_pk_bf16_f32 v131, v70, v71
v_mfma_f32_16x16x32_bf16 v[120:123], a[180:183], v[60:63], v[120:123]
s_add_u32 m0, s40, 16384
v_exp_f32 v73, v73
v_mfma_f32_16x16x32_bf16 v[100:103], a[144:147], v[0:3], v[160:163]
s_waitcnt lgkmcnt(2)
ds_read_b64_tr_b16 a[168:169], v199 offset:8192
ds_read_b64_tr_b16 a[170:171], v199 offset:12288
v_mfma_f32_16x16x32_bf16 v[100:103], a[184:187], v[4:7], v[100:103]
s_xor_b32 s78, s41, 98304
buffer_load_dwordx4 v210, s[16:19], s78 offen nt lds
v_mfma_f32_16x16x32_bf16 v[108:111], a[144:147], v[16:19], v[164:167]
v_mfma_f32_16x16x32_bf16 v[108:111], a[184:187], v[20:23], v[108:111]
buffer_load_dwordx4 v211, s[16:19], s78 offen offset:2048 nt lds
v_mfma_f32_16x16x32_bf16 v[116:119], a[144:147], v[32:35], v[168:171]
s_waitcnt lgkmcnt(3)
ds_read_b64_tr_b16 a[172:173], v202 offset:8192
ds_read_b64_tr_b16 a[174:175], v202 offset:12288
v_mfma_f32_16x16x32_bf16 v[116:119], a[184:187], v[36:39], v[116:119]
v_exp_f32 v74, v74
v_mfma_f32_16x16x32_bf16 v[124:127], a[144:147], v[48:51], v[172:175]
v_exp_f32 v75, v75
v_mfma_f32_16x16x32_bf16 v[124:127], a[184:187], v[52:55], v[124:127]
v_exp_f32 v76, v76
v_mfma_f32_16x16x32_bf16 v[100:103], a[156:159], v[8:11], v[100:103]
s_waitcnt lgkmcnt(4)
ds_read_b64_tr_b16 a[144:145], v199 offset:8704
ds_read_b64_tr_b16 a[146:147], v199 offset:12800
v_mfma_f32_16x16x32_bf16 v[100:103], a[192:195], v[12:15], v[100:103]
v_cvt_pk_bf16_f32 v132, v72, v73
v_mfma_f32_16x16x32_bf16 v[108:111], a[156:159], v[24:27], v[108:111]
v_cvt_pk_bf16_f32 v133, v74, v75
v_mfma_f32_16x16x32_bf16 v[108:111], a[192:195], v[28:31], v[108:111]
v_exp_f32 v77, v77
v_mfma_f32_16x16x32_bf16 v[116:119], a[156:159], v[40:43], v[116:119]
ds_read_b64_tr_b16 a[180:181], v202 offset:8704
ds_read_b64_tr_b16 a[182:183], v202 offset:12800
v_mfma_f32_16x16x32_bf16 v[116:119], a[192:195], v[44:47], v[116:119]
v_exp_f32 v78, v78
v_mfma_f32_16x16x32_bf16 v[124:127], a[156:159], v[56:59], v[124:127]
v_exp_f32 v79, v79
v_mfma_f32_16x16x32_bf16 v[124:127], a[192:195], v[60:63], v[124:127]
v_mfma_f32_16x16x32_bf16 v[176:179], v[232:235], v[144:147], v[176:179]
s_waitcnt lgkmcnt(6)
ds_read_b64_tr_b16 a[156:157], v199 offset:9216
ds_read_b64_tr_b16 a[158:159], v199 offset:13312
v_mfma_f32_16x16x32_bf16 v[180:183], v[232:235], v[148:151], v[180:183]
ds_read_b64_tr_b16 a[192:193], v202 offset:9728
ds_read_b64_tr_b16 a[194:195], v202 offset:13824
v_mfma_f32_16x16x32_bf16 v[184:187], v[232:235], v[152:155], v[184:187]
ds_read_b64_tr_b16 a[188:189], v199 offset:9728
ds_read_b64_tr_b16 a[190:191], v199 offset:13824
v_mfma_f32_16x16x32_bf16 v[188:191], v[232:235], v[156:159], v[188:191]
ds_read_b64_tr_b16 a[184:185], v202 offset:9216
ds_read_b64_tr_b16 a[186:187], v202 offset:13312
v_mfma_f32_16x16x32_bf16 a[0:3], a[168:171], v[144:147], a[0:3]
s_waitcnt lgkmcnt(12)
v_exp_f32 v80, v80
v_mfma_f32_16x16x32_bf16 a[4:7], a[168:171], v[148:151], a[4:7]
v_cvt_pk_bf16_f32 v134, v76, v77
v_cvt_pk_bf16_f32 v135, v78, v79
v_mfma_f32_16x16x32_bf16 a[8:11], a[168:171], v[152:155], a[8:11]
v_exp_f32 v81, v81
v_mfma_f32_16x16x32_bf16 a[12:15], a[168:171], v[156:159], a[12:15]
v_mfma_f32_16x16x32_bf16 a[16:19], a[172:175], v[144:147], a[16:19]
s_waitcnt lgkmcnt(10)
v_exp_f32 v82, v82
v_mfma_f32_16x16x32_bf16 a[20:23], a[172:175], v[148:151], a[20:23]
v_exp_f32 v83, v83
v_mfma_f32_16x16x32_bf16 a[24:27], a[172:175], v[152:155], a[24:27]
v_exp_f32 v84, v84
v_mfma_f32_16x16x32_bf16 a[28:31], a[172:175], v[156:159], a[28:31]
v_cvt_pk_bf16_f32 v136, v80, v81
v_cvt_pk_bf16_f32 v137, v82, v83
v_mfma_f32_16x16x32_bf16 a[32:35], a[144:147], v[144:147], a[32:35]
s_waitcnt vmcnt(10) lgkmcnt(0)
s_barrier
v_mfma_f32_16x16x32_bf16 a[36:39], a[144:147], v[148:151], a[36:39]
v_mfma_f32_16x16x32_bf16 a[40:43], a[144:147], v[152:155], a[40:43]
v_exp_f32 v85, v85
v_mfma_f32_16x16x32_bf16 a[44:47], a[144:147], v[156:159], a[44:47]
v_exp_f32 v86, v86
v_mfma_f32_16x16x32_bf16 a[48:51], a[180:183], v[144:147], a[48:51]
ds_read_b128 a[144:147], v192 offset:49152
ds_read_b128 a[168:171], v192 offset:49664
v_mfma_f32_16x16x32_bf16 a[52:55], a[180:183], v[148:151], a[52:55]
v_exp_f32 v87, v87
v_mfma_f32_16x16x32_bf16 a[56:59], a[180:183], v[152:155], a[56:59]
s_add_u32 m0, s40, 17408
v_exp_f32 v88, v88
v_mfma_f32_16x16x32_bf16 a[60:63], a[180:183], v[156:159], a[60:63]
s_xor_b32 s79, s41, 98432
buffer_load_dwordx4 v210, s[16:19], s79 offen nt lds
v_mfma_f32_16x16x32_bf16 a[64:67], a[156:159], v[144:147], a[64:67]
v_cvt_pk_bf16_f32 v138, v84, v85
v_mfma_f32_16x16x32_bf16 a[68:71], a[156:159], v[148:151], a[68:71]
buffer_load_dwordx4 v211, s[16:19], s79 offen offset:2048 nt lds
v_mfma_f32_16x16x32_bf16 a[72:75], a[156:159], v[152:155], a[72:75]
v_cvt_pk_bf16_f32 v139, v86, v87
v_mfma_f32_16x16x32_bf16 a[76:79], a[156:159], v[156:159], a[76:79]
v_exp_f32 v89, v89
v_mfma_f32_16x16x32_bf16 a[80:83], a[184:187], v[144:147], a[80:83]
ds_read_b128 a[156:159], v192 offset:50176
ds_read_b128 a[172:175], v192 offset:50688
v_mfma_f32_16x16x32_bf16 a[84:87], a[184:187], v[148:151], a[84:87]
v_exp_f32 v90, v90
v_mfma_f32_16x16x32_bf16 a[88:91], a[184:187], v[152:155], a[88:91]
v_mfma_f32_16x16x32_bf16 a[92:95], a[184:187], v[156:159], a[92:95]
v_exp_f32 v91, v91
v_mfma_f32_16x16x32_bf16 a[96:99], a[188:191], v[144:147], a[96:99]
v_exp_f32 v92, v92
v_mfma_f32_16x16x32_bf16 a[100:103], a[188:191], v[148:151], a[100:103]
v_cvt_pk_bf16_f32 v140, v88, v89
v_cvt_pk_bf16_f32 v141, v90, v91
v_mfma_f32_16x16x32_bf16 a[104:107], a[188:191], v[152:155], a[104:107]
v_exp_f32 v93, v93
v_mfma_f32_16x16x32_bf16 a[108:111], a[188:191], v[156:159], a[108:111]
v_mfma_f32_16x16x32_bf16 a[112:115], a[192:195], v[144:147], a[112:115]
s_waitcnt lgkmcnt(3)
ds_read_b128 a[180:183], v192 offset:53248
ds_read_b128 a[184:187], v192 offset:53760
v_mfma_f32_16x16x32_bf16 a[116:119], a[192:195], v[148:151], a[116:119]
v_exp_f32 v94, v94
v_mfma_f32_16x16x32_bf16 a[120:123], a[192:195], v[152:155], a[120:123]
v_exp_f32 v95, v95
v_mfma_f32_16x16x32_bf16 a[124:127], a[192:195], v[156:159], a[124:127]
v_cvt_pk_bf16_f32 v142, v92, v93
v_cvt_pk_bf16_f32 v143, v94, v95
v_mfma_f32_16x16x32_bf16 v[64:67], a[144:147], v[0:3], v[160:163]
s_waitcnt lgkmcnt(4)
v_exp_f32 v96, v96
v_mfma_f32_16x16x32_bf16 v[64:67], a[168:171], v[4:7], v[64:67]
v_mfma_f32_16x16x32_bf16 v[72:75], a[144:147], v[16:19], v[164:167]
v_exp_f32 v97, v97
v_mfma_f32_16x16x32_bf16 v[72:75], a[168:171], v[20:23], v[72:75]
v_exp_f32 v98, v98
v_mfma_f32_16x16x32_bf16 v[80:83], a[144:147], v[32:35], v[168:171]
s_waitcnt lgkmcnt(3)
ds_read_b128 a[188:191], v192 offset:54272
ds_read_b128 a[192:195], v192 offset:54784
v_mfma_f32_16x16x32_bf16 v[80:83], a[168:171], v[36:39], v[80:83]
v_exp_f32 v99, v99
v_mfma_f32_16x16x32_bf16 v[88:91], a[144:147], v[48:51], v[172:175]
v_mfma_f32_16x16x32_bf16 v[88:91], a[168:171], v[52:55], v[88:91]
v_exp_f32 v100, v100
v_mfma_f32_16x16x32_bf16 v[64:67], a[156:159], v[8:11], v[64:67]
s_waitcnt lgkmcnt(4)
v_cvt_pk_bf16_f32 v144, v96, v97
v_cvt_pk_bf16_f32 v145, v98, v99
v_mfma_f32_16x16x32_bf16 v[64:67], a[172:175], v[12:15], v[64:67]
v_exp_f32 v101, v101
v_mfma_f32_16x16x32_bf16 v[72:75], a[156:159], v[24:27], v[72:75]
v_mfma_f32_16x16x32_bf16 v[72:75], a[172:175], v[28:31], v[72:75]
v_exp_f32 v102, v102
v_mfma_f32_16x16x32_bf16 v[80:83], a[156:159], v[40:43], v[80:83]
s_waitcnt lgkmcnt(3)
v_exp_f32 v103, v103
v_mfma_f32_16x16x32_bf16 v[80:83], a[172:175], v[44:47], v[80:83]
v_exp_f32 v104, v104
v_mfma_f32_16x16x32_bf16 v[88:91], a[156:159], v[56:59], v[88:91]
v_cvt_pk_bf16_f32 v146, v100, v101
v_cvt_pk_bf16_f32 v147, v102, v103
v_mfma_f32_16x16x32_bf16 v[88:91], a[172:175], v[60:63], v[88:91]
s_add_u32 m0, s40, 147456
v_mfma_f32_16x16x32_bf16 v[68:71], a[180:183], v[0:3], v[160:163]
s_waitcnt lgkmcnt(2)
ds_read_b64_tr_b16 a[144:145], v199 offset:16384
ds_read_b64_tr_b16 a[146:147], v199 offset:20480
v_mfma_f32_16x16x32_bf16 v[68:71], a[184:187], v[4:7], v[68:71]
s_xor_b32 s78, s41, 81920
buffer_load_dwordx4 v212, s[20:23], s78 offen nt lds
v_mfma_f32_16x16x32_bf16 v[76:79], a[180:183], v[16:19], v[164:167]
v_exp_f32 v105, v105
v_mfma_f32_16x16x32_bf16 v[76:79], a[184:187], v[20:23], v[76:79]
buffer_load_dwordx4 v212, s[20:23], s78 offen offset:2048 nt lds
v_mfma_f32_16x16x32_bf16 v[84:87], a[180:183], v[32:35], v[168:171]
s_waitcnt lgkmcnt(3)
ds_read_b64_tr_b16 a[156:157], v202 offset:16384
ds_read_b64_tr_b16 a[158:159], v202 offset:20480
v_mfma_f32_16x16x32_bf16 v[84:87], a[184:187], v[36:39], v[84:87]
v_exp_f32 v106, v106
v_mfma_f32_16x16x32_bf16 v[92:95], a[180:183], v[48:51], v[172:175]
v_exp_f32 v107, v107
v_mfma_f32_16x16x32_bf16 v[92:95], a[184:187], v[52:55], v[92:95]
v_mfma_f32_16x16x32_bf16 v[68:71], a[188:191], v[8:11], v[68:71]
s_waitcnt lgkmcnt(4)
ds_read_b64_tr_b16 a[168:169], v199 offset:16896
ds_read_b64_tr_b16 a[170:171], v199 offset:20992
v_mfma_f32_16x16x32_bf16 v[68:71], a[192:195], v[12:15], v[68:71]
v_exp_f32 v108, v108
v_mfma_f32_16x16x32_bf16 v[76:79], a[188:191], v[24:27], v[76:79]
v_cvt_pk_bf16_f32 v148, v104, v105
v_cvt_pk_bf16_f32 v149, v106, v107
v_mfma_f32_16x16x32_bf16 v[76:79], a[192:195], v[28:31], v[76:79]
v_exp_f32 v109, v109
v_mfma_f32_16x16x32_bf16 v[84:87], a[188:191], v[40:43], v[84:87]
ds_read_b64_tr_b16 a[172:173], v202 offset:16896
ds_read_b64_tr_b16 a[174:175], v202 offset:20992
v_mfma_f32_16x16x32_bf16 v[84:87], a[192:195], v[44:47], v[84:87]
v_mfma_f32_16x16x32_bf16 v[92:95], a[188:191], v[56:59], v[92:95]
v_exp_f32 v110, v110
v_mfma_f32_16x16x32_bf16 v[92:95], a[192:195], v[60:63], v[92:95]
v_exp_f32 v111, v111
v_mfma_f32_16x16x32_bf16 v[176:179], v[232:235], v[128:131], v[176:179]
s_waitcnt lgkmcnt(6)
ds_read_b64_tr_b16 a[180:181], v199 offset:17408
ds_read_b64_tr_b16 a[182:183], v199 offset:21504
v_mfma_f32_16x16x32_bf16 v[180:183], v[232:235], v[132:135], v[180:183]
v_exp_f32 v112, v112
v_mfma_f32_16x16x32_bf16 v[184:187], v[232:235], v[136:139], v[184:187]
v_cvt_pk_bf16_f32 v150, v108, v109
v_cvt_pk_bf16_f32 v151, v110, v111
v_mfma_f32_16x16x32_bf16 v[188:191], v[232:235], v[140:143], v[188:191]
v_mfma_f32_16x16x32_bf16 a[0:3], a[144:147], v[128:131], a[0:3]
s_waitcnt lgkmcnt(6)
ds_read_b64_tr_b16 a[184:185], v202 offset:17408
ds_read_b64_tr_b16 a[186:187], v202 offset:21504
v_mfma_f32_16x16x32_bf16 a[4:7], a[144:147], v[132:135], a[4:7]
L_tch5_%=:
s_cbranch_execz L_tb0_%=
v_exp_f32 v113, v113
v_mfma_f32_16x16x32_bf16 a[8:11], a[144:147], v[136:139], a[8:11]
v_exp_f32 v114, v114
v_mfma_f32_16x16x32_bf16 a[12:15], a[144:147], v[140:143], a[12:15]
v_exp_f32 v115, v115
v_mfma_f32_16x16x32_bf16 a[16:19], a[156:159], v[128:131], a[16:19]
s_waitcnt lgkmcnt(6)
ds_read_b64_tr_b16 a[144:145], v199 offset:17920
ds_read_b64_tr_b16 a[146:147], v199 offset:22016
v_mfma_f32_16x16x32_bf16 a[20:23], a[156:159], v[132:135], a[20:23]
v_mfma_f32_16x16x32_bf16 a[24:27], a[156:159], v[136:139], a[24:27]
v_exp_f32 v116, v116
v_mfma_f32_16x16x32_bf16 a[28:31], a[156:159], v[140:143], a[28:31]
v_cvt_pk_bf16_f32 v152, v112, v113
v_cvt_pk_bf16_f32 v153, v114, v115
v_mfma_f32_16x16x32_bf16 a[32:35], a[168:171], v[128:131], a[32:35]
s_waitcnt lgkmcnt(6)
ds_read_b64_tr_b16 a[156:157], v202 offset:17920
ds_read_b64_tr_b16 a[158:159], v202 offset:22016
v_mfma_f32_16x16x32_bf16 a[36:39], a[168:171], v[132:135], a[36:39]
v_exp_f32 v117, v117
v_mfma_f32_16x16x32_bf16 a[40:43], a[168:171], v[136:139], a[40:43]
v_mfma_f32_16x16x32_bf16 a[44:47], a[168:171], v[140:143], a[44:47]
v_exp_f32 v118, v118
v_mfma_f32_16x16x32_bf16 a[48:51], a[172:175], v[128:131], a[48:51]
s_waitcnt lgkmcnt(6)
ds_read_b128 a[168:171], v192 offset:57344
ds_read_b128 a[188:191], v192 offset:57856
v_mfma_f32_16x16x32_bf16 a[52:55], a[172:175], v[132:135], a[52:55]
v_exp_f32 v119, v119
v_mfma_f32_16x16x32_bf16 a[56:59], a[172:175], v[136:139], a[56:59]
s_add_u32 m0, s40, 148480
v_exp_f32 v120, v120
v_mfma_f32_16x16x32_bf16 a[60:63], a[172:175], v[140:143], a[60:63]
s_xor_b32 s79, s41, 82048
buffer_load_dwordx4 v212, s[20:23], s79 offen nt lds
v_mfma_f32_16x16x32_bf16 a[64:67], a[180:183], v[128:131], a[64:67]
s_waitcnt lgkmcnt(6)
v_cvt_pk_bf16_f32 v154, v116, v117
v_cvt_pk_bf16_f32 v155, v118, v119
v_mfma_f32_16x16x32_bf16 a[68:71], a[180:183], v[132:135], a[68:71]
buffer_load_dwordx4 v212, s[20:23], s79 offen offset:2048 nt lds
v_mfma_f32_16x16x32_bf16 a[72:75], a[180:183], v[136:139], a[72:75]
v_mfma_f32_16x16x32_bf16 a[76:79], a[180:183], v[140:143], a[76:79]
v_exp_f32 v121, v121
v_mfma_f32_16x16x32_bf16 a[80:83], a[184:187], v[128:131], a[80:83]
s_waitcnt lgkmcnt(4)
ds_read_b128 a[172:175], v192 offset:58368
ds_read_b128 a[180:183], v192 offset:58880
v_mfma_f32_16x16x32_bf16 a[84:87], a[184:187], v[132:135], a[84:87]
v_exp_f32 v122, v122
v_mfma_f32_16x16x32_bf16 a[88:91], a[184:187], v[136:139], a[88:91]
v_exp_f32 v123, v123
v_mfma_f32_16x16x32_bf16 a[92:95], a[184:187], v[140:143], a[92:95]
v_mfma_f32_16x16x32_bf16 a[96:99], a[144:147], v[128:131], a[96:99]
s_waitcnt lgkmcnt(4)
v_exp_f32 v124, v124
v_mfma_f32_16x16x32_bf16 a[100:103], a[144:147], v[132:135], a[100:103]
v_cvt_pk_bf16_f32 v156, v120, v121
v_cvt_pk_bf16_f32 v157, v122, v123
v_mfma_f32_16x16x32_bf16 a[104:107], a[144:147], v[136:139], a[104:107]
v_exp_f32 v125, v125
v_mfma_f32_16x16x32_bf16 a[108:111], a[144:147], v[140:143], a[108:111]
v_mfma_f32_16x16x32_bf16 a[112:115], a[156:159], v[128:131], a[112:115]
s_waitcnt lgkmcnt(3)
ds_read_b128 a[144:147], v192 offset:61440
ds_read_b128 a[184:187], v192 offset:61952
v_mfma_f32_16x16x32_bf16 a[116:119], a[156:159], v[132:135], a[116:119]
v_exp_f32 v126, v126
v_mfma_f32_16x16x32_bf16 a[120:123], a[156:159], v[136:139], a[120:123]
v_exp_f32 v127, v127
v_mfma_f32_16x16x32_bf16 a[124:127], a[156:159], v[140:143], a[124:127]
v_cvt_pk_bf16_f32 v158, v124, v125
v_cvt_pk_bf16_f32 v159, v126, v127
v_mfma_f32_16x16x32_bf16 v[96:99], a[168:171], v[0:3], v[160:163]
s_waitcnt lgkmcnt(4)
v_exp_f32 v64, v64
v_mfma_f32_16x16x32_bf16 v[96:99], a[188:191], v[4:7], v[96:99]
v_mfma_f32_16x16x32_bf16 v[104:107], a[168:171], v[16:19], v[164:167]
v_exp_f32 v65, v65
v_mfma_f32_16x16x32_bf16 v[104:107], a[188:191], v[20:23], v[104:107]
v_exp_f32 v66, v66
v_mfma_f32_16x16x32_bf16 v[112:115], a[168:171], v[32:35], v[168:171]
s_waitcnt lgkmcnt(3)
ds_read_b128 a[156:159], v192 offset:62464
ds_read_b128 a[192:195], v192 offset:62976
v_mfma_f32_16x16x32_bf16 v[112:115], a[188:191], v[36:39], v[112:115]
v_exp_f32 v67, v67
v_mfma_f32_16x16x32_bf16 v[120:123], a[168:171], v[48:51], v[172:175]
v_mfma_f32_16x16x32_bf16 v[120:123], a[188:191], v[52:55], v[120:123]
v_exp_f32 v68, v68
v_mfma_f32_16x16x32_bf16 v[96:99], a[172:175], v[8:11], v[96:99]
s_waitcnt lgkmcnt(4)
v_cvt_pk_bf16_f32 v128, v64, v65
v_cvt_pk_bf16_f32 v129, v66, v67
v_mfma_f32_16x16x32_bf16 v[96:99], a[180:183], v[12:15], v[96:99]
v_exp_f32 v69, v69
v_mfma_f32_16x16x32_bf16 v[104:107], a[172:175], v[24:27], v[104:107]
v_exp_f32 v70, v70
v_mfma_f32_16x16x32_bf16 v[104:107], a[180:183], v[28:31], v[104:107]
v_mfma_f32_16x16x32_bf16 v[112:115], a[172:175], v[40:43], v[112:115]
s_waitcnt lgkmcnt(3)
v_exp_f32 v71, v71
v_mfma_f32_16x16x32_bf16 v[112:115], a[180:183], v[44:47], v[112:115]
v_exp_f32 v72, v72
v_mfma_f32_16x16x32_bf16 v[120:123], a[172:175], v[56:59], v[120:123]
v_cvt_pk_bf16_f32 v130, v68, v69
v_cvt_pk_bf16_f32 v131, v70, v71
v_mfma_f32_16x16x32_bf16 v[120:123], a[180:183], v[60:63], v[120:123]
s_add_u32 m0, s40, 32768
v_exp_f32 v73, v73
v_mfma_f32_16x16x32_bf16 v[100:103], a[144:147], v[0:3], v[160:163]
s_waitcnt lgkmcnt(2)
ds_read_b64_tr_b16 a[168:169], v199 offset:24576
ds_read_b64_tr_b16 a[170:171], v199 offset:28672
v_mfma_f32_16x16x32_bf16 v[100:103], a[184:187], v[4:7], v[100:103]
s_xor_b32 s78, s41, 114688
buffer_load_dwordx4 v210, s[16:19], s78 offen nt lds
v_mfma_f32_16x16x32_bf16 v[108:111], a[144:147], v[16:19], v[164:167]
v_mfma_f32_16x16x32_bf16 v[108:111], a[184:187], v[20:23], v[108:111]
buffer_load_dwordx4 v211, s[16:19], s78 offen offset:2048 nt lds
v_mfma_f32_16x16x32_bf16 v[116:119], a[144:147], v[32:35], v[168:171]
s_waitcnt lgkmcnt(3)
ds_read_b64_tr_b16 a[172:173], v202 offset:24576
ds_read_b64_tr_b16 a[174:175], v202 offset:28672
v_mfma_f32_16x16x32_bf16 v[116:119], a[184:187], v[36:39], v[116:119]
v_exp_f32 v74, v74
v_mfma_f32_16x16x32_bf16 v[124:127], a[144:147], v[48:51], v[172:175]
v_exp_f32 v75, v75
v_mfma_f32_16x16x32_bf16 v[124:127], a[184:187], v[52:55], v[124:127]
v_exp_f32 v76, v76
v_mfma_f32_16x16x32_bf16 v[100:103], a[156:159], v[8:11], v[100:103]
s_waitcnt lgkmcnt(4)
ds_read_b64_tr_b16 a[144:145], v199 offset:25088
ds_read_b64_tr_b16 a[146:147], v199 offset:29184
v_mfma_f32_16x16x32_bf16 v[100:103], a[192:195], v[12:15], v[100:103]
v_cvt_pk_bf16_f32 v132, v72, v73
v_mfma_f32_16x16x32_bf16 v[108:111], a[156:159], v[24:27], v[108:111]
v_cvt_pk_bf16_f32 v133, v74, v75
v_mfma_f32_16x16x32_bf16 v[108:111], a[192:195], v[28:31], v[108:111]
v_exp_f32 v77, v77
v_mfma_f32_16x16x32_bf16 v[116:119], a[156:159], v[40:43], v[116:119]
ds_read_b64_tr_b16 a[180:181], v202 offset:25088
ds_read_b64_tr_b16 a[182:183], v202 offset:29184
v_mfma_f32_16x16x32_bf16 v[116:119], a[192:195], v[44:47], v[116:119]
v_exp_f32 v78, v78
v_mfma_f32_16x16x32_bf16 v[124:127], a[156:159], v[56:59], v[124:127]
v_exp_f32 v79, v79
v_mfma_f32_16x16x32_bf16 v[124:127], a[192:195], v[60:63], v[124:127]
v_mfma_f32_16x16x32_bf16 v[176:179], v[232:235], v[144:147], v[176:179]
s_waitcnt lgkmcnt(6)
ds_read_b64_tr_b16 a[156:157], v199 offset:25600
ds_read_b64_tr_b16 a[158:159], v199 offset:29696
v_mfma_f32_16x16x32_bf16 v[180:183], v[232:235], v[148:151], v[180:183]
ds_read_b64_tr_b16 a[192:193], v202 offset:26112
ds_read_b64_tr_b16 a[194:195], v202 offset:30208
v_mfma_f32_16x16x32_bf16 v[184:187], v[232:235], v[152:155], v[184:187]
ds_read_b64_tr_b16 a[188:189], v199 offset:26112
ds_read_b64_tr_b16 a[190:191], v199 offset:30208
v_mfma_f32_16x16x32_bf16 v[188:191], v[232:235], v[156:159], v[188:191]
ds_read_b64_tr_b16 a[184:185], v202 offset:25600
ds_read_b64_tr_b16 a[186:187], v202 offset:29696
v_mfma_f32_16x16x32_bf16 a[0:3], a[168:171], v[144:147], a[0:3]
s_waitcnt lgkmcnt(12)
v_exp_f32 v80, v80
v_mfma_f32_16x16x32_bf16 a[4:7], a[168:171], v[148:151], a[4:7]
v_cvt_pk_bf16_f32 v134, v76, v77
v_cvt_pk_bf16_f32 v135, v78, v79
v_mfma_f32_16x16x32_bf16 a[8:11], a[168:171], v[152:155], a[8:11]
v_exp_f32 v81, v81
v_mfma_f32_16x16x32_bf16 a[12:15], a[168:171], v[156:159], a[12:15]
v_mfma_f32_16x16x32_bf16 a[16:19], a[172:175], v[144:147], a[16:19]
s_waitcnt lgkmcnt(10)
v_exp_f32 v82, v82
v_mfma_f32_16x16x32_bf16 a[20:23], a[172:175], v[148:151], a[20:23]
v_exp_f32 v83, v83
v_mfma_f32_16x16x32_bf16 a[24:27], a[172:175], v[152:155], a[24:27]
v_exp_f32 v84, v84
v_mfma_f32_16x16x32_bf16 a[28:31], a[172:175], v[156:159], a[28:31]
v_cvt_pk_bf16_f32 v136, v80, v81
v_cvt_pk_bf16_f32 v137, v82, v83
v_mfma_f32_16x16x32_bf16 a[32:35], a[144:147], v[144:147], a[32:35]
s_waitcnt vmcnt(10) lgkmcnt(0)
s_barrier
v_mfma_f32_16x16x32_bf16 a[36:39], a[144:147], v[148:151], a[36:39]
v_mfma_f32_16x16x32_bf16 a[40:43], a[144:147], v[152:155], a[40:43]
v_exp_f32 v85, v85
v_mfma_f32_16x16x32_bf16 a[44:47], a[144:147], v[156:159], a[44:47]
v_exp_f32 v86, v86
v_mfma_f32_16x16x32_bf16 a[48:51], a[180:183], v[144:147], a[48:51]
ds_read_b128 a[144:147], v193 offset:49152
ds_read_b128 a[168:171], v193 offset:49664
v_mfma_f32_16x16x32_bf16 a[52:55], a[180:183], v[148:151], a[52:55]
v_exp_f32 v87, v87
v_mfma_f32_16x16x32_bf16 a[56:59], a[180:183], v[152:155], a[56:59]
s_add_u32 m0, s40, 33792
v_exp_f32 v88, v88
v_mfma_f32_16x16x32_bf16 a[60:63], a[180:183], v[156:159], a[60:63]
s_xor_b32 s79, s41, 114816
buffer_load_dwordx4 v210, s[16:19], s79 offen nt lds
v_mfma_f32_16x16x32_bf16 a[64:67], a[156:159], v[144:147], a[64:67]
v_cvt_pk_bf16_f32 v138, v84, v85
v_mfma_f32_16x16x32_bf16 a[68:71], a[156:159], v[148:151], a[68:71]
buffer_load_dwordx4 v211, s[16:19], s79 offen offset:2048 nt lds
v_mfma_f32_16x16x32_bf16 a[72:75], a[156:159], v[152:155], a[72:75]
v_cvt_pk_bf16_f32 v139, v86, v87
v_mfma_f32_16x16x32_bf16 a[76:79], a[156:159], v[156:159], a[76:79]
v_exp_f32 v89, v89
v_mfma_f32_16x16x32_bf16 a[80:83], a[184:187], v[144:147], a[80:83]
ds_read_b128 a[156:159], v193 offset:50176
ds_read_b128 a[172:175], v193 offset:50688
v_mfma_f32_16x16x32_bf16 a[84:87], a[184:187], v[148:151], a[84:87]
v_exp_f32 v90, v90
v_mfma_f32_16x16x32_bf16 a[88:91], a[184:187], v[152:155], a[88:91]
v_mfma_f32_16x16x32_bf16 a[92:95], a[184:187], v[156:159], a[92:95]
v_exp_f32 v91, v91
v_mfma_f32_16x16x32_bf16 a[96:99], a[188:191], v[144:147], a[96:99]
v_exp_f32 v92, v92
v_mfma_f32_16x16x32_bf16 a[100:103], a[188:191], v[148:151], a[100:103]
v_cvt_pk_bf16_f32 v140, v88, v89
v_cvt_pk_bf16_f32 v141, v90, v91
v_mfma_f32_16x16x32_bf16 a[104:107], a[188:191], v[152:155], a[104:107]
v_exp_f32 v93, v93
v_mfma_f32_16x16x32_bf16 a[108:111], a[188:191], v[156:159], a[108:111]
v_mfma_f32_16x16x32_bf16 a[112:115], a[192:195], v[144:147], a[112:115]
s_waitcnt lgkmcnt(3)
ds_read_b128 a[180:183], v193 offset:53248
ds_read_b128 a[184:187], v193 offset:53760
v_mfma_f32_16x16x32_bf16 a[116:119], a[192:195], v[148:151], a[116:119]
v_exp_f32 v94, v94
v_mfma_f32_16x16x32_bf16 a[120:123], a[192:195], v[152:155], a[120:123]
v_exp_f32 v95, v95
v_mfma_f32_16x16x32_bf16 a[124:127], a[192:195], v[156:159], a[124:127]
v_cvt_pk_bf16_f32 v142, v92, v93
v_cvt_pk_bf16_f32 v143, v94, v95
v_mfma_f32_16x16x32_bf16 v[64:67], a[144:147], v[0:3], v[160:163]
s_waitcnt lgkmcnt(4)
v_exp_f32 v96, v96
v_mfma_f32_16x16x32_bf16 v[64:67], a[168:171], v[4:7], v[64:67]
v_mfma_f32_16x16x32_bf16 v[72:75], a[144:147], v[16:19], v[164:167]
v_exp_f32 v97, v97
v_mfma_f32_16x16x32_bf16 v[72:75], a[168:171], v[20:23], v[72:75]
v_exp_f32 v98, v98
v_mfma_f32_16x16x32_bf16 v[80:83], a[144:147], v[32:35], v[168:171]
s_waitcnt lgkmcnt(3)
ds_read_b128 a[188:191], v193 offset:54272
ds_read_b128 a[192:195], v193 offset:54784
v_mfma_f32_16x16x32_bf16 v[80:83], a[168:171], v[36:39], v[80:83]
v_exp_f32 v99, v99
v_mfma_f32_16x16x32_bf16 v[88:91], a[144:147], v[48:51], v[172:175]
v_mfma_f32_16x16x32_bf16 v[88:91], a[168:171], v[52:55], v[88:91]
v_exp_f32 v100, v100
v_mfma_f32_16x16x32_bf16 v[64:67], a[156:159], v[8:11], v[64:67]
s_waitcnt lgkmcnt(4)
v_cvt_pk_bf16_f32 v144, v96, v97
v_cvt_pk_bf16_f32 v145, v98, v99
v_mfma_f32_16x16x32_bf16 v[64:67], a[172:175], v[12:15], v[64:67]
v_exp_f32 v101, v101
v_mfma_f32_16x16x32_bf16 v[72:75], a[156:159], v[24:27], v[72:75]
v_mfma_f32_16x16x32_bf16 v[72:75], a[172:175], v[28:31], v[72:75]
v_exp_f32 v102, v102
v_mfma_f32_16x16x32_bf16 v[80:83], a[156:159], v[40:43], v[80:83]
s_waitcnt lgkmcnt(3)
v_exp_f32 v103, v103
v_mfma_f32_16x16x32_bf16 v[80:83], a[172:175], v[44:47], v[80:83]
v_exp_f32 v104, v104
v_mfma_f32_16x16x32_bf16 v[88:91], a[156:159], v[56:59], v[88:91]
v_cvt_pk_bf16_f32 v146, v100, v101
v_cvt_pk_bf16_f32 v147, v102, v103
v_mfma_f32_16x16x32_bf16 v[88:91], a[172:175], v[60:63], v[88:91]
s_add_u32 m0, s40, 98304
v_mfma_f32_16x16x32_bf16 v[68:71], a[180:183], v[0:3], v[160:163]
s_waitcnt lgkmcnt(2)
ds_read_b64_tr_b16 a[144:145], v200 offset:0
ds_read_b64_tr_b16 a[146:147], v200 offset:4096
v_mfma_f32_16x16x32_bf16 v[68:71], a[184:187], v[4:7], v[68:71]
s_xor_b32 s78, s41, 98304
buffer_load_dwordx4 v212, s[20:23], s78 offen nt lds
v_mfma_f32_16x16x32_bf16 v[76:79], a[180:183], v[16:19], v[164:167]
v_exp_f32 v105, v105
v_mfma_f32_16x16x32_bf16 v[76:79], a[184:187], v[20:23], v[76:79]
buffer_load_dwordx4 v212, s[20:23], s78 offen offset:2048 nt lds
v_mfma_f32_16x16x32_bf16 v[84:87], a[180:183], v[32:35], v[168:171]
s_waitcnt lgkmcnt(3)
ds_read_b64_tr_b16 a[156:157], v203 offset:0
ds_read_b64_tr_b16 a[158:159], v203 offset:4096
v_mfma_f32_16x16x32_bf16 v[84:87], a[184:187], v[36:39], v[84:87]
v_exp_f32 v106, v106
v_mfma_f32_16x16x32_bf16 v[92:95], a[180:183], v[48:51], v[172:175]
v_exp_f32 v107, v107
v_mfma_f32_16x16x32_bf16 v[92:95], a[184:187], v[52:55], v[92:95]
v_mfma_f32_16x16x32_bf16 v[68:71], a[188:191], v[8:11], v[68:71]
s_waitcnt lgkmcnt(4)
ds_read_b64_tr_b16 a[168:169], v200 offset:512
ds_read_b64_tr_b16 a[170:171], v200 offset:4608
v_mfma_f32_16x16x32_bf16 v[68:71], a[192:195], v[12:15], v[68:71]
v_exp_f32 v108, v108
v_mfma_f32_16x16x32_bf16 v[76:79], a[188:191], v[24:27], v[76:79]
v_cvt_pk_bf16_f32 v148, v104, v105
v_cvt_pk_bf16_f32 v149, v106, v107
v_mfma_f32_16x16x32_bf16 v[76:79], a[192:195], v[28:31], v[76:79]
v_exp_f32 v109, v109
v_mfma_f32_16x16x32_bf16 v[84:87], a[188:191], v[40:43], v[84:87]
ds_read_b64_tr_b16 a[172:173], v203 offset:512
ds_read_b64_tr_b16 a[174:175], v203 offset:4608
v_mfma_f32_16x16x32_bf16 v[84:87], a[192:195], v[44:47], v[84:87]
v_mfma_f32_16x16x32_bf16 v[92:95], a[188:191], v[56:59], v[92:95]
v_exp_f32 v110, v110
v_mfma_f32_16x16x32_bf16 v[92:95], a[192:195], v[60:63], v[92:95]
v_exp_f32 v111, v111
v_mfma_f32_16x16x32_bf16 v[176:179], v[232:235], v[128:131], v[176:179]
s_waitcnt lgkmcnt(6)
ds_read_b64_tr_b16 a[180:181], v200 offset:1024
ds_read_b64_tr_b16 a[182:183], v200 offset:5120
v_mfma_f32_16x16x32_bf16 v[180:183], v[232:235], v[132:135], v[180:183]
v_exp_f32 v112, v112
v_mfma_f32_16x16x32_bf16 v[184:187], v[232:235], v[136:139], v[184:187]
v_cvt_pk_bf16_f32 v150, v108, v109
v_cvt_pk_bf16_f32 v151, v110, v111
v_mfma_f32_16x16x32_bf16 v[188:191], v[232:235], v[140:143], v[188:191]
v_mfma_f32_16x16x32_bf16 a[0:3], a[144:147], v[128:131], a[0:3]
s_waitcnt lgkmcnt(6)
ds_read_b64_tr_b16 a[184:185], v203 offset:1024
ds_read_b64_tr_b16 a[186:187], v203 offset:5120
v_mfma_f32_16x16x32_bf16 a[4:7], a[144:147], v[132:135], a[4:7]
v_exp_f32 v113, v113
v_mfma_f32_16x16x32_bf16 a[8:11], a[144:147], v[136:139], a[8:11]
v_exp_f32 v114, v114
v_mfma_f32_16x16x32_bf16 a[12:15], a[144:147], v[140:143], a[12:15]
v_exp_f32 v115, v115
v_mfma_f32_16x16x32_bf16 a[16:19], a[156:159], v[128:131], a[16:19]
s_waitcnt lgkmcnt(6)
ds_read_b64_tr_b16 a[144:145], v200 offset:1536
ds_read_b64_tr_b16 a[146:147], v200 offset:5632
v_mfma_f32_16x16x32_bf16 a[20:23], a[156:159], v[132:135], a[20:23]
v_mfma_f32_16x16x32_bf16 a[24:27], a[156:159], v[136:139], a[24:27]
v_exp_f32 v116, v116
v_mfma_f32_16x16x32_bf16 a[28:31], a[156:159], v[140:143], a[28:31]
v_cvt_pk_bf16_f32 v152, v112, v113
v_cvt_pk_bf16_f32 v153, v114, v115
v_mfma_f32_16x16x32_bf16 a[32:35], a[168:171], v[128:131], a[32:35]
s_waitcnt lgkmcnt(6)
ds_read_b64_tr_b16 a[156:157], v203 offset:1536
ds_read_b64_tr_b16 a[158:159], v203 offset:5632
v_mfma_f32_16x16x32_bf16 a[36:39], a[168:171], v[132:135], a[36:39]
v_exp_f32 v117, v117
v_mfma_f32_16x16x32_bf16 a[40:43], a[168:171], v[136:139], a[40:43]
v_mfma_f32_16x16x32_bf16 a[44:47], a[168:171], v[140:143], a[44:47]
v_exp_f32 v118, v118
v_mfma_f32_16x16x32_bf16 a[48:51], a[172:175], v[128:131], a[48:51]
s_waitcnt lgkmcnt(6)
ds_read_b128 a[168:171], v193 offset:57344
ds_read_b128 a[188:191], v193 offset:57856
v_mfma_f32_16x16x32_bf16 a[52:55], a[172:175], v[132:135], a[52:55]
v_exp_f32 v119, v119
v_mfma_f32_16x16x32_bf16 a[56:59], a[172:175], v[136:139], a[56:59]
s_add_u32 m0, s40, 99328
v_exp_f32 v120, v120
v_mfma_f32_16x16x32_bf16 a[60:63], a[172:175], v[140:143], a[60:63]
s_xor_b32 s79, s41, 98432
buffer_load_dwordx4 v212, s[20:23], s79 offen nt lds
v_mfma_f32_16x16x32_bf16 a[64:67], a[180:183], v[128:131], a[64:67]
s_waitcnt lgkmcnt(6)
v_cvt_pk_bf16_f32 v154, v116, v117
v_cvt_pk_bf16_f32 v155, v118, v119
v_mfma_f32_16x16x32_bf16 a[68:71], a[180:183], v[132:135], a[68:71]
buffer_load_dwordx4 v212, s[20:23], s79 offen offset:2048 nt lds
v_mfma_f32_16x16x32_bf16 a[72:75], a[180:183], v[136:139], a[72:75]
v_mfma_f32_16x16x32_bf16 a[76:79], a[180:183], v[140:143], a[76:79]
v_exp_f32 v121, v121
v_mfma_f32_16x16x32_bf16 a[80:83], a[184:187], v[128:131], a[80:83]
s_waitcnt lgkmcnt(4)
ds_read_b128 a[172:175], v193 offset:58368
ds_read_b128 a[180:183], v193 offset:58880
v_mfma_f32_16x16x32_bf16 a[84:87], a[184:187], v[132:135], a[84:87]
v_exp_f32 v122, v122
v_mfma_f32_16x16x32_bf16 a[88:91], a[184:187], v[136:139], a[88:91]
v_exp_f32 v123, v123
v_mfma_f32_16x16x32_bf16 a[92:95], a[184:187], v[140:143], a[92:95]
v_mfma_f32_16x16x32_bf16 a[96:99], a[144:147], v[128:131], a[96:99]
s_waitcnt lgkmcnt(4)
v_exp_f32 v124, v124
v_mfma_f32_16x16x32_bf16 a[100:103], a[144:147], v[132:135], a[100:103]
v_cvt_pk_bf16_f32 v156, v120, v121
v_cvt_pk_bf16_f32 v157, v122, v123
v_mfma_f32_16x16x32_bf16 a[104:107], a[144:147], v[136:139], a[104:107]
v_exp_f32 v125, v125
v_mfma_f32_16x16x32_bf16 a[108:111], a[144:147], v[140:143], a[108:111]
v_mfma_f32_16x16x32_bf16 a[112:115], a[156:159], v[128:131], a[112:115]
s_waitcnt lgkmcnt(3)
ds_read_b128 a[144:147], v193 offset:61440
ds_read_b128 a[184:187], v193 offset:61952
v_mfma_f32_16x16x32_bf16 a[116:119], a[156:159], v[132:135], a[116:119]
s_lshl_b32 s56, s64, 12
v_exp_f32 v126, v126
v_mfma_f32_16x16x32_bf16 a[120:123], a[156:159], v[136:139], a[120:123]
s_add_u32 s56, s56, s44
v_exp_f32 v127, v127
v_mfma_f32_16x16x32_bf16 a[124:127], a[156:159], v[140:143], a[124:127]
s_add_u32 m0, s40, 65536
v_cvt_pk_bf16_f32 v158, v124, v125
v_cvt_pk_bf16_f32 v159, v126, v127
v_mfma_f32_16x16x32_bf16 v[96:99], a[168:171], v[0:3], v[160:163]
s_waitcnt lgkmcnt(4)
buffer_load_dwordx4 v213, s[12:15], s56 offen lds
v_mfma_f32_16x16x32_bf16 v[96:99], a[188:191], v[4:7], v[96:99]
v_exp_f32 v64, v64
v_mfma_f32_16x16x32_bf16 v[104:107], a[168:171], v[16:19], v[164:167]
buffer_load_dwordx4 v214, s[12:15], s56 offen offset:2048 lds
v_mfma_f32_16x16x32_bf16 v[104:107], a[188:191], v[20:23], v[104:107]
v_mfma_f32_16x16x32_bf16 v[112:115], a[168:171], v[32:35], v[168:171]
s_waitcnt lgkmcnt(3)
ds_read_b128 a[156:159], v193 offset:62464
ds_read_b128 a[192:195], v193 offset:62976
v_mfma_f32_16x16x32_bf16 v[112:115], a[188:191], v[36:39], v[112:115]
v_exp_f32 v65, v65
v_mfma_f32_16x16x32_bf16 v[120:123], a[168:171], v[48:51], v[172:175]
v_exp_f32 v66, v66
v_mfma_f32_16x16x32_bf16 v[120:123], a[188:191], v[52:55], v[120:123]
v_exp_f32 v67, v67
v_mfma_f32_16x16x32_bf16 v[96:99], a[172:175], v[8:11], v[96:99]
s_waitcnt lgkmcnt(4)
v_exp_f32 v68, v68
v_mfma_f32_16x16x32_bf16 v[96:99], a[180:183], v[12:15], v[96:99]
v_cvt_pk_bf16_f32 v128, v64, v65
v_cvt_pk_bf16_f32 v129, v66, v67
v_mfma_f32_16x16x32_bf16 v[104:107], a[172:175], v[24:27], v[104:107]
v_exp_f32 v69, v69
v_mfma_f32_16x16x32_bf16 v[104:107], a[180:183], v[28:31], v[104:107]
v_mfma_f32_16x16x32_bf16 v[112:115], a[172:175], v[40:43], v[112:115]
s_waitcnt lgkmcnt(3)
v_exp_f32 v70, v70
v_mfma_f32_16x16x32_bf16 v[112:115], a[180:183], v[44:47], v[112:115]
v_exp_f32 v71, v71
v_mfma_f32_16x16x32_bf16 v[120:123], a[172:175], v[56:59], v[120:123]
v_exp_f32 v72, v72
v_mfma_f32_16x16x32_bf16 v[120:123], a[180:183], v[60:63], v[120:123]
s_add_u32 m0, s40, 0
v_cvt_pk_bf16_f32 v130, v68, v69
v_cvt_pk_bf16_f32 v131, v70, v71
v_mfma_f32_16x16x32_bf16 v[100:103], a[144:147], v[0:3], v[160:163]
s_waitcnt lgkmcnt(2)
ds_read_b64_tr_b16 a[168:169], v200 offset:8192
ds_read_b64_tr_b16 a[170:171], v200 offset:12288
v_mfma_f32_16x16x32_bf16 v[100:103], a[184:187], v[4:7], v[100:103]
s_xor_b32 s78, s41, 114688
buffer_load_dwordx4 v212, s[20:23], s78 offen nt lds
v_mfma_f32_16x16x32_bf16 v[108:111], a[144:147], v[16:19], v[164:167]
v_exp_f32 v73, v73
v_mfma_f32_16x16x32_bf16 v[108:111], a[184:187], v[20:23], v[108:111]
buffer_load_dwordx4 v212, s[20:23], s78 offen offset:2048 nt lds
v_mfma_f32_16x16x32_bf16 v[116:119], a[144:147], v[32:35], v[168:171]
s_waitcnt lgkmcnt(3)
s_add_u32 m0, s40, 66560
ds_read_b64_tr_b16 a[172:173], v203 offset:8192
ds_read_b64_tr_b16 a[174:175], v203 offset:12288
v_mfma_f32_16x16x32_bf16 v[116:119], a[184:187], v[36:39], v[116:119]
s_add_u32 s57, s56, 128
buffer_load_dwordx4 v213, s[12:15], s57 offen lds
v_mfma_f32_16x16x32_bf16 v[124:127], a[144:147], v[48:51], v[172:175]
v_exp_f32 v74, v74
v_mfma_f32_16x16x32_bf16 v[124:127], a[184:187], v[52:55], v[124:127]
buffer_load_dwordx4 v214, s[12:15], s57 offen offset:2048 lds
v_mfma_f32_16x16x32_bf16 v[100:103], a[156:159], v[8:11], v[100:103]
s_waitcnt lgkmcnt(4)
ds_read_b64_tr_b16 a[144:145], v200 offset:8704
ds_read_b64_tr_b16 a[146:147], v200 offset:12800
v_mfma_f32_16x16x32_bf16 v[100:103], a[192:195], v[12:15], v[100:103]
v_mfma_f32_16x16x32_bf16 v[108:111], a[156:159], v[24:27], v[108:111]
v_exp_f32 v75, v75
v_mfma_f32_16x16x32_bf16 v[108:111], a[192:195], v[28:31], v[108:111]
v_exp_f32 v76, v76
v_mfma_f32_16x16x32_bf16 v[116:119], a[156:159], v[40:43], v[116:119]
ds_read_b64_tr_b16 a[180:181], v203 offset:8704
ds_read_b64_tr_b16 a[182:183], v203 offset:12800
v_mfma_f32_16x16x32_bf16 v[116:119], a[192:195], v[44:47], v[116:119]
v_cvt_pk_bf16_f32 v132, v72, v73
v_cvt_pk_bf16_f32 v133, v74, v75
v_mfma_f32_16x16x32_bf16 v[124:127], a[156:159], v[56:59], v[124:127]
v_exp_f32 v77, v77
v_mfma_f32_16x16x32_bf16 v[124:127], a[192:195], v[60:63], v[124:127]
v_exp_f32 v78, v78
v_mfma_f32_16x16x32_bf16 v[176:179], v[232:235], v[144:147], v[176:179]
s_waitcnt lgkmcnt(6)
ds_read_b64_tr_b16 a[156:157], v200 offset:9216
ds_read_b64_tr_b16 a[158:159], v200 offset:13312
v_mfma_f32_16x16x32_bf16 v[180:183], v[232:235], v[148:151], v[180:183]
ds_read_b64_tr_b16 a[192:193], v203 offset:9728
ds_read_b64_tr_b16 a[194:195], v203 offset:13824
v_mfma_f32_16x16x32_bf16 v[184:187], v[232:235], v[152:155], v[184:187]
ds_read_b64_tr_b16 a[188:189], v200 offset:9728
ds_read_b64_tr_b16 a[190:191], v200 offset:13824
v_mfma_f32_16x16x32_bf16 v[188:191], v[232:235], v[156:159], v[188:191]
ds_read_b64_tr_b16 a[184:185], v203 offset:9216
ds_read_b64_tr_b16 a[186:187], v203 offset:13312
v_mfma_f32_16x16x32_bf16 a[0:3], a[168:171], v[144:147], a[0:3]
s_waitcnt lgkmcnt(12)
v_mfma_f32_16x16x32_bf16 a[4:7], a[168:171], v[148:151], a[4:7]
v_exp_f32 v79, v79
v_mfma_f32_16x16x32_bf16 a[8:11], a[168:171], v[152:155], a[8:11]
v_exp_f32 v80, v80
v_mfma_f32_16x16x32_bf16 a[12:15], a[168:171], v[156:159], a[12:15]
v_cvt_pk_bf16_f32 v134, v76, v77
v_cvt_pk_bf16_f32 v135, v78, v79
v_mfma_f32_16x16x32_bf16 a[16:19], a[172:175], v[144:147], a[16:19]
s_waitcnt lgkmcnt(10)
v_exp_f32 v81, v81
v_mfma_f32_16x16x32_bf16 a[20:23], a[172:175], v[148:151], a[20:23]
v_exp_f32 v82, v82
v_mfma_f32_16x16x32_bf16 a[24:27], a[172:175], v[152:155], a[24:27]
v_exp_f32 v83, v83
v_mfma_f32_16x16x32_bf16 a[28:31], a[172:175], v[156:159], a[28:31]
v_mfma_f32_16x16x32_bf16 a[32:35], a[144:147], v[144:147], a[32:35]
s_waitcnt vmcnt(14) lgkmcnt(0)
s_barrier
v_mfma_f32_16x16x32_bf16 a[36:39], a[144:147], v[148:151], a[36:39]
v_exp_f32 v84, v84
v_mfma_f32_16x16x32_bf16 a[40:43], a[144:147], v[152:155], a[40:43]
v_cvt_pk_bf16_f32 v136, v80, v81
v_cvt_pk_bf16_f32 v137, v82, v83
v_mfma_f32_16x16x32_bf16 a[44:47], a[144:147], v[156:159], a[44:47]
v_exp_f32 v85, v85
v_mfma_f32_16x16x32_bf16 a[48:51], a[180:183], v[144:147], a[48:51]
ds_read_b128 a[144:147], v192 offset:16384
ds_read_b128 a[168:171], v192 offset:16896
v_mfma_f32_16x16x32_bf16 a[52:55], a[180:183], v[148:151], a[52:55]
v_exp_f32 v86, v86
v_mfma_f32_16x16x32_bf16 a[56:59], a[180:183], v[152:155], a[56:59]
s_add_u32 m0, s40, 1024
v_exp_f32 v87, v87
v_mfma_f32_16x16x32_bf16 a[60:63], a[180:183], v[156:159], a[60:63]
s_xor_b32 s79, s41, 114816
buffer_load_dwordx4 v212, s[20:23], s79 offen nt lds
v_mfma_f32_16x16x32_bf16 a[64:67], a[156:159], v[144:147], a[64:67]
v_exp_f32 v88, v88
v_mfma_f32_16x16x32_bf16 a[68:71], a[156:159], v[148:151], a[68:71]
buffer_load_dwordx4 v212, s[20:23], s79 offen offset:2048 nt lds
v_mfma_f32_16x16x32_bf16 a[72:75], a[156:159], v[152:155], a[72:75]
v_cvt_pk_bf16_f32 v138, v84, v85
v_mfma_f32_16x16x32_bf16 a[76:79], a[156:159], v[156:159], a[76:79]
v_cvt_pk_bf16_f32 v139, v86, v87
v_mfma_f32_16x16x32_bf16 a[80:83], a[184:187], v[144:147], a[80:83]
ds_read_b128 a[156:159], v192 offset:17408
ds_read_b128 a[172:175], v192 offset:17920
v_mfma_f32_16x16x32_bf16 a[84:87], a[184:187], v[148:151], a[84:87]
v_exp_f32 v89, v89
v_mfma_f32_16x16x32_bf16 a[88:91], a[184:187], v[152:155], a[88:91]
v_exp_f32 v90, v90
v_mfma_f32_16x16x32_bf16 a[92:95], a[184:187], v[156:159], a[92:95]
v_exp_f32 v91, v91
v_mfma_f32_16x16x32_bf16 a[96:99], a[188:191], v[144:147], a[96:99]
v_exp_f32 v92, v92
v_mfma_f32_16x16x32_bf16 a[100:103], a[188:191], v[148:151], a[100:103]
v_cvt_pk_bf16_f32 v140, v88, v89
v_mfma_f32_16x16x32_bf16 a[104:107], a[188:191], v[152:155], a[104:107]
v_cvt_pk_bf16_f32 v141, v90, v91
v_mfma_f32_16x16x32_bf16 a[108:111], a[188:191], v[156:159], a[108:111]
v_exp_f32 v93, v93
v_mfma_f32_16x16x32_bf16 a[112:115], a[192:195], v[144:147], a[112:115]
s_waitcnt lgkmcnt(3)
ds_read_b128 a[180:183], v192 offset:20480
ds_read_b128 a[184:187], v192 offset:20992
v_mfma_f32_16x16x32_bf16 a[116:119], a[192:195], v[148:151], a[116:119]
v_exp_f32 v94, v94
v_mfma_f32_16x16x32_bf16 a[120:123], a[192:195], v[152:155], a[120:123]
v_exp_f32 v95, v95
v_mfma_f32_16x16x32_bf16 a[124:127], a[192:195], v[156:159], a[124:127]
v_cvt_pk_bf16_f32 v142, v92, v93
v_cvt_pk_bf16_f32 v143, v94, v95
v_mfma_f32_16x16x32_bf16 v[64:67], a[144:147], v[0:3], v[160:163]
s_waitcnt lgkmcnt(4)
v_exp_f32 v96, v96
v_mfma_f32_16x16x32_bf16 v[64:67], a[168:171], v[4:7], v[64:67]
s_add_u32 m0, s40, 81920
v_exp_f32 v97, v97
v_mfma_f32_16x16x32_bf16 v[72:75], a[144:147], v[16:19], v[164:167]
s_add_u32 s58, s64, 0
buffer_load_dwordx4 v215, s[8:11], s58 offen lds
v_mfma_f32_16x16x32_bf16 v[72:75], a[168:171], v[20:23], v[72:75]
v_mfma_f32_16x16x32_bf16 v[80:83], a[144:147], v[32:35], v[168:171]
s_waitcnt lgkmcnt(3)
ds_read_b128 a[188:191], v192 offset:21504
ds_read_b128 a[192:195], v192 offset:22016
v_mfma_f32_16x16x32_bf16 v[80:83], a[168:171], v[36:39], v[80:83]
buffer_load_dwordx4 v216, s[8:11], s58 offen offset:1024 lds
v_mfma_f32_16x16x32_bf16 v[88:91], a[144:147], v[48:51], v[172:175]
v_exp_f32 v98, v98
v_mfma_f32_16x16x32_bf16 v[88:91], a[168:171], v[52:55], v[88:91]
v_exp_f32 v99, v99
v_mfma_f32_16x16x32_bf16 v[64:67], a[156:159], v[8:11], v[64:67]
s_waitcnt lgkmcnt(4)
v_exp_f32 v100, v100
v_mfma_f32_16x16x32_bf16 v[64:67], a[172:175], v[12:15], v[64:67]
v_cvt_pk_bf16_f32 v144, v96, v97
v_cvt_pk_bf16_f32 v145, v98, v99
v_mfma_f32_16x16x32_bf16 v[72:75], a[156:159], v[24:27], v[72:75]
v_exp_f32 v101, v101
v_mfma_f32_16x16x32_bf16 v[72:75], a[172:175], v[28:31], v[72:75]
L_tch6_%=:
s_cbranch_execz L_tb0_%=
v_exp_f32 v102, v102
v_mfma_f32_16x16x32_bf16 v[80:83], a[156:159], v[40:43], v[80:83]
s_waitcnt lgkmcnt(3)
v_exp_f32 v103, v103
v_mfma_f32_16x16x32_bf16 v[80:83], a[172:175], v[44:47], v[80:83]
v_exp_f32 v104, v104
v_mfma_f32_16x16x32_bf16 v[88:91], a[156:159], v[56:59], v[88:91]
v_cvt_pk_bf16_f32 v146, v100, v101
v_cvt_pk_bf16_f32 v147, v102, v103
v_mfma_f32_16x16x32_bf16 v[88:91], a[172:175], v[60:63], v[88:91]
s_add_u32 m0, s40, 49152
v_exp_f32 v105, v105
v_mfma_f32_16x16x32_bf16 v[68:71], a[180:183], v[0:3], v[160:163]
s_waitcnt lgkmcnt(2)
ds_read_b64_tr_b16 a[144:145], v200 offset:16384
ds_read_b64_tr_b16 a[146:147], v200 offset:20480
v_mfma_f32_16x16x32_bf16 v[68:71], a[184:187], v[4:7], v[68:71]
s_xor_b32 s78, s42, 0
buffer_load_dwordx4 v210, s[16:19], s78 offen lds
v_mfma_f32_16x16x32_bf16 v[76:79], a[180:183], v[16:19], v[164:167]
v_mfma_f32_16x16x32_bf16 v[76:79], a[184:187], v[20:23], v[76:79]
buffer_load_dwordx4 v211, s[16:19], s78 offen offset:2048 lds
v_mfma_f32_16x16x32_bf16 v[84:87], a[180:183], v[32:35], v[168:171]
s_waitcnt lgkmcnt(3)
s_add_u32 m0, s40, 83968
ds_read_b64_tr_b16 a[156:157], v203 offset:16384
ds_read_b64_tr_b16 a[158:159], v203 offset:20480
v_mfma_f32_16x16x32_bf16 v[84:87], a[184:187], v[36:39], v[84:87]
s_add_u32 s59, s64, 16384
buffer_load_dwordx4 v215, s[8:11], s59 offen lds
v_mfma_f32_16x16x32_bf16 v[92:95], a[180:183], v[48:51], v[172:175]
v_exp_f32 v106, v106
v_mfma_f32_16x16x32_bf16 v[92:95], a[184:187], v[52:55], v[92:95]
buffer_load_dwordx4 v216, s[8:11], s59 offen offset:1024 lds
v_mfma_f32_16x16x32_bf16 v[68:71], a[188:191], v[8:11], v[68:71]
s_waitcnt lgkmcnt(4)
ds_read_b64_tr_b16 a[168:169], v200 offset:16896
ds_read_b64_tr_b16 a[170:171], v200 offset:20992
v_mfma_f32_16x16x32_bf16 v[68:71], a[192:195], v[12:15], v[68:71]
v_exp_f32 v107, v107
v_mfma_f32_16x16x32_bf16 v[76:79], a[188:191], v[24:27], v[76:79]
v_exp_f32 v108, v108
v_mfma_f32_16x16x32_bf16 v[76:79], a[192:195], v[28:31], v[76:79]
v_cvt_pk_bf16_f32 v148, v104, v105
v_cvt_pk_bf16_f32 v149, v106, v107
v_mfma_f32_16x16x32_bf16 v[84:87], a[188:191], v[40:43], v[84:87]
ds_read_b64_tr_b16 a[172:173], v203 offset:16896
ds_read_b64_tr_b16 a[174:175], v203 offset:20992
v_mfma_f32_16x16x32_bf16 v[84:87], a[192:195], v[44:47], v[84:87]
v_exp_f32 v109, v109
v_mfma_f32_16x16x32_bf16 v[92:95], a[188:191], v[56:59], v[92:95]
v_exp_f32 v110, v110
v_mfma_f32_16x16x32_bf16 v[92:95], a[192:195], v[60:63], v[92:95]
v_exp_f32 v111, v111
v_mfma_f32_16x16x32_bf16 v[176:179], v[232:235], v[128:131], v[176:179]
s_waitcnt lgkmcnt(6)
ds_read_b64_tr_b16 a[180:181], v200 offset:17408
ds_read_b64_tr_b16 a[182:183], v200 offset:21504
v_mfma_f32_16x16x32_bf16 v[180:183], v[232:235], v[132:135], v[180:183]
v_exp_f32 v112, v112
v_mfma_f32_16x16x32_bf16 v[184:187], v[232:235], v[136:139], v[184:187]
v_cvt_pk_bf16_f32 v150, v108, v109
v_cvt_pk_bf16_f32 v151, v110, v111
v_mfma_f32_16x16x32_bf16 v[188:191], v[232:235], v[140:143], v[188:191]
s_add_u32 m0, s40, 114688
v_exp_f32 v113, v113
v_mfma_f32_16x16x32_bf16 a[0:3], a[144:147], v[128:131], a[0:3]
s_waitcnt lgkmcnt(6)
ds_read_b64_tr_b16 a[184:185], v203 offset:17408
ds_read_b64_tr_b16 a[186:187], v203 offset:21504
v_mfma_f32_16x16x32_bf16 a[4:7], a[144:147], v[132:135], a[4:7]
s_add_u32 s56, s64, 32768
buffer_load_dwordx4 v215, s[8:11], s56 offen lds
v_mfma_f32_16x16x32_bf16 a[8:11], a[144:147], v[136:139], a[8:11]
v_mfma_f32_16x16x32_bf16 a[12:15], a[144:147], v[140:143], a[12:15]
buffer_load_dwordx4 v216, s[8:11], s56 offen offset:1024 lds
v_mfma_f32_16x16x32_bf16 a[16:19], a[156:159], v[128:131], a[16:19]
s_waitcnt lgkmcnt(6)
ds_read_b64_tr_b16 a[144:145], v200 offset:17920
ds_read_b64_tr_b16 a[146:147], v200 offset:22016
v_mfma_f32_16x16x32_bf16 a[20:23], a[156:159], v[132:135], a[20:23]
v_exp_f32 v114, v114
v_mfma_f32_16x16x32_bf16 a[24:27], a[156:159], v[136:139], a[24:27]
v_exp_f32 v115, v115
v_mfma_f32_16x16x32_bf16 a[28:31], a[156:159], v[140:143], a[28:31]
v_exp_f32 v116, v116
v_mfma_f32_16x16x32_bf16 a[32:35], a[168:171], v[128:131], a[32:35]
s_waitcnt lgkmcnt(6)
ds_read_b64_tr_b16 a[156:157], v203 offset:17920
ds_read_b64_tr_b16 a[158:159], v203 offset:22016
v_mfma_f32_16x16x32_bf16 a[36:39], a[168:171], v[132:135], a[36:39]
v_cvt_pk_bf16_f32 v152, v112, v113
v_cvt_pk_bf16_f32 v153, v114, v115
v_mfma_f32_16x16x32_bf16 a[40:43], a[168:171], v[136:139], a[40:43]
v_exp_f32 v117, v117
v_mfma_f32_16x16x32_bf16 a[44:47], a[168:171], v[140:143], a[44:47]
v_exp_f32 v118, v118
v_mfma_f32_16x16x32_bf16 a[48:51], a[172:175], v[128:131], a[48:51]
s_waitcnt lgkmcnt(6)
ds_read_b128 a[168:171], v192 offset:24576
ds_read_b128 a[188:191], v192 offset:25088
v_mfma_f32_16x16x32_bf16 a[52:55], a[172:175], v[132:135], a[52:55]
v_exp_f32 v119, v119
v_mfma_f32_16x16x32_bf16 a[56:59], a[172:175], v[136:139], a[56:59]
s_add_u32 m0, s40, 50176
v_exp_f32 v120, v120
v_mfma_f32_16x16x32_bf16 a[60:63], a[172:175], v[140:143], a[60:63]
s_xor_b32 s79, s42, 128
buffer_load_dwordx4 v210, s[16:19], s79 offen lds
v_mfma_f32_16x16x32_bf16 a[64:67], a[180:183], v[128:131], a[64:67]
s_waitcnt lgkmcnt(6)
v_cvt_pk_bf16_f32 v154, v116, v117
v_cvt_pk_bf16_f32 v155, v118, v119
v_mfma_f32_16x16x32_bf16 a[68:71], a[180:183], v[132:135], a[68:71]
buffer_load_dwordx4 v211, s[16:19], s79 offen offset:2048 lds
v_mfma_f32_16x16x32_bf16 a[72:75], a[180:183], v[136:139], a[72:75]
s_add_u32 m0, s40, 116736
v_exp_f32 v121, v121
v_mfma_f32_16x16x32_bf16 a[76:79], a[180:183], v[140:143], a[76:79]
s_add_u32 s57, s64, 49152
buffer_load_dwordx4 v215, s[8:11], s57 offen lds
v_mfma_f32_16x16x32_bf16 a[80:83], a[184:187], v[128:131], a[80:83]
s_waitcnt lgkmcnt(4)
ds_read_b128 a[172:175], v192 offset:25600
ds_read_b128 a[180:183], v192 offset:26112
v_mfma_f32_16x16x32_bf16 a[84:87], a[184:187], v[132:135], a[84:87]
buffer_load_dwordx4 v216, s[8:11], s57 offen offset:1024 lds
v_mfma_f32_16x16x32_bf16 a[88:91], a[184:187], v[136:139], a[88:91]
v_mfma_f32_16x16x32_bf16 a[92:95], a[184:187], v[140:143], a[92:95]
v_exp_f32 v122, v122
v_mfma_f32_16x16x32_bf16 a[96:99], a[144:147], v[128:131], a[96:99]
s_waitcnt lgkmcnt(4)
v_exp_f32 v123, v123
v_mfma_f32_16x16x32_bf16 a[100:103], a[144:147], v[132:135], a[100:103]
v_exp_f32 v124, v124
v_mfma_f32_16x16x32_bf16 a[104:107], a[144:147], v[136:139], a[104:107]
v_cvt_pk_bf16_f32 v156, v120, v121
v_cvt_pk_bf16_f32 v157, v122, v123
v_mfma_f32_16x16x32_bf16 a[108:111], a[144:147], v[140:143], a[108:111]
v_exp_f32 v125, v125
v_mfma_f32_16x16x32_bf16 a[112:115], a[156:159], v[128:131], a[112:115]
s_waitcnt lgkmcnt(3)
ds_read_b128 a[144:147], v192 offset:28672
ds_read_b128 a[184:187], v192 offset:29184
v_mfma_f32_16x16x32_bf16 a[116:119], a[156:159], v[132:135], a[116:119]
v_exp_f32 v126, v126
v_mfma_f32_16x16x32_bf16 a[120:123], a[156:159], v[136:139], a[120:123]
v_exp_f32 v127, v127
v_mfma_f32_16x16x32_bf16 a[124:127], a[156:159], v[140:143], a[124:127]
v_cvt_pk_bf16_f32 v158, v124, v125
v_cvt_pk_bf16_f32 v159, v126, v127
v_mfma_f32_16x16x32_bf16 v[96:99], a[168:171], v[0:3], v[160:163]
s_waitcnt lgkmcnt(4)
v_exp_f32 v64, v64
v_mfma_f32_16x16x32_bf16 v[96:99], a[188:191], v[4:7], v[96:99]
v_mfma_f32_16x16x32_bf16 v[104:107], a[168:171], v[16:19], v[164:167]
v_exp_f32 v65, v65
v_mfma_f32_16x16x32_bf16 v[104:107], a[188:191], v[20:23], v[104:107]
v_exp_f32 v66, v66
v_mfma_f32_16x16x32_bf16 v[112:115], a[168:171], v[32:35], v[168:171]
s_waitcnt lgkmcnt(3)
ds_read_b128 a[156:159], v192 offset:29696
ds_read_b128 a[192:195], v192 offset:30208
v_mfma_f32_16x16x32_bf16 v[112:115], a[188:191], v[36:39], v[112:115]
v_exp_f32 v67, v67
v_mfma_f32_16x16x32_bf16 v[120:123], a[168:171], v[48:51], v[172:175]
v_exp_f32 v68, v68
v_mfma_f32_16x16x32_bf16 v[120:123], a[188:191], v[52:55], v[120:123]
v_cvt_pk_bf16_f32 v128, v64, v65
v_cvt_pk_bf16_f32 v129, v66, v67
v_mfma_f32_16x16x32_bf16 v[96:99], a[172:175], v[8:11], v[96:99]
s_waitcnt lgkmcnt(4)
v_exp_f32 v69, v69
v_mfma_f32_16x16x32_bf16 v[96:99], a[180:183], v[12:15], v[96:99]
v_mfma_f32_16x16x32_bf16 v[104:107], a[172:175], v[24:27], v[104:107]
v_exp_f32 v70, v70
v_mfma_f32_16x16x32_bf16 v[104:107], a[180:183], v[28:31], v[104:107]
v_exp_f32 v71, v71
v_mfma_f32_16x16x32_bf16 v[112:115], a[172:175], v[40:43], v[112:115]
s_waitcnt lgkmcnt(3)
v_exp_f32 v72, v72
v_mfma_f32_16x16x32_bf16 v[112:115], a[180:183], v[44:47], v[112:115]
v_cvt_pk_bf16_f32 v130, v68, v69
v_cvt_pk_bf16_f32 v131, v70, v71
v_mfma_f32_16x16x32_bf16 v[120:123], a[172:175], v[56:59], v[120:123]
v_exp_f32 v73, v73
v_mfma_f32_16x16x32_bf16 v[120:123], a[180:183], v[60:63], v[120:123]
s_add_u32 m0, s40, 131072
v_exp_f32 v74, v74
v_mfma_f32_16x16x32_bf16 v[100:103], a[144:147], v[0:3], v[160:163]
s_waitcnt lgkmcnt(2)
ds_read_b64_tr_b16 a[168:169], v200 offset:24576
ds_read_b64_tr_b16 a[170:171], v200 offset:28672
v_mfma_f32_16x16x32_bf16 v[100:103], a[184:187], v[4:7], v[100:103]
s_xor_b32 s78, s42, 16384
buffer_load_dwordx4 v210, s[16:19], s78 offen lds
v_mfma_f32_16x16x32_bf16 v[108:111], a[144:147], v[16:19], v[164:167]
v_mfma_f32_16x16x32_bf16 v[108:111], a[184:187], v[20:23], v[108:111]
buffer_load_dwordx4 v211, s[16:19], s78 offen offset:2048 lds
v_mfma_f32_16x16x32_bf16 v[116:119], a[144:147], v[32:35], v[168:171]
s_waitcnt lgkmcnt(3)
ds_read_b64_tr_b16 a[172:173], v203 offset:24576
ds_read_b64_tr_b16 a[174:175], v203 offset:28672
v_mfma_f32_16x16x32_bf16 v[116:119], a[184:187], v[36:39], v[116:119]
v_exp_f32 v75, v75
v_mfma_f32_16x16x32_bf16 v[124:127], a[144:147], v[48:51], v[172:175]
v_exp_f32 v76, v76
v_mfma_f32_16x16x32_bf16 v[124:127], a[184:187], v[52:55], v[124:127]
v_cvt_pk_bf16_f32 v132, v72, v73
v_cvt_pk_bf16_f32 v133, v74, v75
v_mfma_f32_16x16x32_bf16 v[100:103], a[156:159], v[8:11], v[100:103]
s_waitcnt lgkmcnt(4)
ds_read_b64_tr_b16 a[144:145], v200 offset:25088
ds_read_b64_tr_b16 a[146:147], v200 offset:29184
v_mfma_f32_16x16x32_bf16 v[100:103], a[192:195], v[12:15], v[100:103]
v_exp_f32 v77, v77
v_mfma_f32_16x16x32_bf16 v[108:111], a[156:159], v[24:27], v[108:111]
v_exp_f32 v78, v78
v_mfma_f32_16x16x32_bf16 v[108:111], a[192:195], v[28:31], v[108:111]
v_mfma_f32_16x16x32_bf16 v[116:119], a[156:159], v[40:43], v[116:119]
ds_read_b64_tr_b16 a[180:181], v203 offset:25088
ds_read_b64_tr_b16 a[182:183], v203 offset:29184
v_mfma_f32_16x16x32_bf16 v[116:119], a[192:195], v[44:47], v[116:119]
v_exp_f32 v79, v79
v_mfma_f32_16x16x32_bf16 v[124:127], a[156:159], v[56:59], v[124:127]
v_exp_f32 v80, v80
v_mfma_f32_16x16x32_bf16 v[124:127], a[192:195], v[60:63], v[124:127]
v_cvt_pk_bf16_f32 v134, v76, v77
v_cvt_pk_bf16_f32 v135, v78, v79
v_mfma_f32_16x16x32_bf16 v[176:179], v[232:235], v[144:147], v[176:179]
s_waitcnt lgkmcnt(6)
ds_read_b64_tr_b16 a[156:157], v200 offset:25600
ds_read_b64_tr_b16 a[158:159], v200 offset:29696
v_mfma_f32_16x16x32_bf16 v[180:183], v[232:235], v[148:151], v[180:183]
ds_read_b64_tr_b16 a[192:193], v203 offset:26112
ds_read_b64_tr_b16 a[194:195], v203 offset:30208
v_mfma_f32_16x16x32_bf16 v[184:187], v[232:235], v[152:155], v[184:187]
ds_read_b64_tr_b16 a[188:189], v200 offset:26112
ds_read_b64_tr_b16 a[190:191], v200 offset:30208
v_mfma_f32_16x16x32_bf16 v[188:191], v[232:235], v[156:159], v[188:191]
ds_read_b64_tr_b16 a[184:185], v203 offset:25600
ds_read_b64_tr_b16 a[186:187], v203 offset:29696
v_mfma_f32_16x16x32_bf16 a[0:3], a[168:171], v[144:147], a[0:3]
s_waitcnt lgkmcnt(12)
v_exp_f32 v81, v81
v_mfma_f32_16x16x32_bf16 a[4:7], a[168:171], v[148:151], a[4:7]
v_exp_f32 v82, v82
v_mfma_f32_16x16x32_bf16 a[8:11], a[168:171], v[152:155], a[8:11]
v_exp_f32 v83, v83
v_mfma_f32_16x16x32_bf16 a[12:15], a[168:171], v[156:159], a[12:15]
v_mfma_f32_16x16x32_bf16 a[16:19], a[172:175], v[144:147], a[16:19]
s_waitcnt lgkmcnt(10)
v_exp_f32 v84, v84
v_mfma_f32_16x16x32_bf16 a[20:23], a[172:175], v[148:151], a[20:23]
v_cvt_pk_bf16_f32 v136, v80, v81
v_cvt_pk_bf16_f32 v137, v82, v83
v_mfma_f32_16x16x32_bf16 a[24:27], a[172:175], v[152:155], a[24:27]
s_lshl_b32 s58, s65, 12
v_exp_f32 v85, v85
v_mfma_f32_16x16x32_bf16 a[28:31], a[172:175], v[156:159], a[28:31]
s_add_u32 m0, s40, 16384
v_exp_f32 v86, v86
v_mfma_f32_16x16x32_bf16 a[32:35], a[144:147], v[144:147], a[32:35]
s_waitcnt vmcnt(22) lgkmcnt(0)
s_barrier
v_mfma_f32_16x16x32_bf16 a[36:39], a[144:147], v[148:151], a[36:39]
s_add_u32 s58, s58, s44
buffer_load_dwordx4 v213, s[12:15], s58 offen lds
v_mfma_f32_16x16x32_bf16 a[40:43], a[144:147], v[152:155], a[40:43]
v_exp_f32 v87, v87
v_mfma_f32_16x16x32_bf16 a[44:47], a[144:147], v[156:159], a[44:47]
buffer_load_dwordx4 v214, s[12:15], s58 offen offset:2048 lds
v_mfma_f32_16x16x32_bf16 a[48:51], a[180:183], v[144:147], a[48:51]
ds_read_b128 a[144:147], v192 offset:32768
ds_read_b128 a[168:171], v192 offset:33280
v_mfma_f32_16x16x32_bf16 a[52:55], a[180:183], v[148:151], a[52:55]
v_exp_f32 v88, v88
v_mfma_f32_16x16x32_bf16 a[56:59], a[180:183], v[152:155], a[56:59]
s_add_u32 m0, s40, 132096
v_cvt_pk_bf16_f32 v138, v84, v85
v_mfma_f32_16x16x32_bf16 a[60:63], a[180:183], v[156:159], a[60:63]
s_xor_b32 s79, s42, 16512
buffer_load_dwordx4 v210, s[16:19], s79 offen lds
v_mfma_f32_16x16x32_bf16 a[64:67], a[156:159], v[144:147], a[64:67]
v_cvt_pk_bf16_f32 v139, v86, v87
v_mfma_f32_16x16x32_bf16 a[68:71], a[156:159], v[148:151], a[68:71]
buffer_load_dwordx4 v211, s[16:19], s79 offen offset:2048 lds
v_mfma_f32_16x16x32_bf16 a[72:75], a[156:159], v[152:155], a[72:75]
v_exp_f32 v89, v89
v_mfma_f32_16x16x32_bf16 a[76:79], a[156:159], v[156:159], a[76:79]
v_exp_f32 v90, v90
v_mfma_f32_16x16x32_bf16 a[80:83], a[184:187], v[144:147], a[80:83]
ds_read_b128 a[156:159], v192 offset:33792
ds_read_b128 a[172:175], v192 offset:34304
v_mfma_f32_16x16x32_bf16 a[84:87], a[184:187], v[148:151], a[84:87]
v_exp_f32 v91, v91
v_mfma_f32_16x16x32_bf16 a[88:91], a[184:187], v[152:155], a[88:91]
v_exp_f32 v92, v92
v_mfma_f32_16x16x32_bf16 a[92:95], a[184:187], v[156:159], a[92:95]
s_add_u32 m0, s40, 17408
v_cvt_pk_bf16_f32 v140, v88, v89
v_mfma_f32_16x16x32_bf16 a[96:99], a[188:191], v[144:147], a[96:99]
v_cvt_pk_bf16_f32 v141, v90, v91
v_mfma_f32_16x16x32_bf16 a[100:103], a[188:191], v[148:151], a[100:103]
s_add_u32 s59, s58, 128
buffer_load_dwordx4 v213, s[12:15], s59 offen lds
v_mfma_f32_16x16x32_bf16 a[104:107], a[188:191], v[152:155], a[104:107]
v_exp_f32 v93, v93
v_mfma_f32_16x16x32_bf16 a[108:111], a[188:191], v[156:159], a[108:111]
buffer_load_dwordx4 v214, s[12:15], s59 offen offset:2048 lds
v_mfma_f32_16x16x32_bf16 a[112:115], a[192:195], v[144:147], a[112:115]
s_waitcnt lgkmcnt(3)
ds_read_b128 a[180:183], v192 offset:36864
ds_read_b128 a[184:187], v192 offset:37376
v_mfma_f32_16x16x32_bf16 a[116:119], a[192:195], v[148:151], a[116:119]
v_exp_f32 v94, v94
v_mfma_f32_16x16x32_bf16 a[120:123], a[192:195], v[152:155], a[120:123]
v_exp_f32 v95, v95
v_mfma_f32_16x16x32_bf16 a[124:127], a[192:195], v[156:159], a[124:127]
v_cvt_pk_bf16_f32 v142, v92, v93
v_cvt_pk_bf16_f32 v143, v94, v95
v_mfma_f32_16x16x32_bf16 v[64:67], a[144:147], v[0:3], v[160:163]
s_waitcnt lgkmcnt(4)
v_exp_f32 v96, v96
v_mfma_f32_16x16x32_bf16 v[64:67], a[168:171], v[4:7], v[64:67]
v_mfma_f32_16x16x32_bf16 v[72:75], a[144:147], v[16:19], v[164:167]
v_exp_f32 v97, v97
v_mfma_f32_16x16x32_bf16 v[72:75], a[168:171], v[20:23], v[72:75]
v_exp_f32 v98, v98
v_mfma_f32_16x16x32_bf16 v[80:83], a[144:147], v[32:35], v[168:171]
s_waitcnt lgkmcnt(3)
ds_read_b128 a[188:191], v192 offset:37888
ds_read_b128 a[192:195], v192 offset:38400
v_mfma_f32_16x16x32_bf16 v[80:83], a[168:171], v[36:39], v[80:83]
v_exp_f32 v99, v99
v_mfma_f32_16x16x32_bf16 v[88:91], a[144:147], v[48:51], v[172:175]
v_mfma_f32_16x16x32_bf16 v[88:91], a[168:171], v[52:55], v[88:91]
s_add_u32 m0, s40, 147456
v_exp_f32 v100, v100
v_mfma_f32_16x16x32_bf16 v[64:67], a[156:159], v[8:11], v[64:67]
s_waitcnt lgkmcnt(4)
v_cvt_pk_bf16_f32 v144, v96, v97
v_cvt_pk_bf16_f32 v145, v98, v99
v_mfma_f32_16x16x32_bf16 v[64:67], a[172:175], v[12:15], v[64:67]
s_add_u32 s56, s65, 0
buffer_load_dwordx4 v215, s[8:11], s56 offen lds
v_mfma_f32_16x16x32_bf16 v[72:75], a[156:159], v[24:27], v[72:75]
v_exp_f32 v101, v101
v_mfma_f32_16x16x32_bf16 v[72:75], a[172:175], v[28:31], v[72:75]
buffer_load_dwordx4 v216, s[8:11], s56 offen offset:1024 lds
v_mfma_f32_16x16x32_bf16 v[80:83], a[156:159], v[40:43], v[80:83]
s_waitcnt lgkmcnt(3)
v_mfma_f32_16x16x32_bf16 v[80:83], a[172:175], v[44:47], v[80:83]
v_exp_f32 v102, v102
v_mfma_f32_16x16x32_bf16 v[88:91], a[156:159], v[56:59], v[88:91]
v_exp_f32 v103, v103
v_mfma_f32_16x16x32_bf16 v[88:91], a[172:175], v[60:63], v[88:91]
v_exp_f32 v104, v104
v_mfma_f32_16x16x32_bf16 v[68:71], a[180:183], v[0:3], v[160:163]
s_waitcnt lgkmcnt(2)
ds_read_b64_tr_b16 a[144:145], v199 offset:32768
ds_read_b64_tr_b16 a[146:147], v199 offset:36864
v_mfma_f32_16x16x32_bf16 v[68:71], a[184:187], v[4:7], v[68:71]
v_cvt_pk_bf16_f32 v146, v100, v101
v_cvt_pk_bf16_f32 v147, v102, v103
v_mfma_f32_16x16x32_bf16 v[76:79], a[180:183], v[16:19], v[164:167]
v_mfma_f32_16x16x32_bf16 v[76:79], a[184:187], v[20:23], v[76:79]
v_exp_f32 v105, v105
v_mfma_f32_16x16x32_bf16 v[84:87], a[180:183], v[32:35], v[168:171]
s_waitcnt lgkmcnt(3)
ds_read_b64_tr_b16 a[156:157], v202 offset:32768
ds_read_b64_tr_b16 a[158:159], v202 offset:36864
v_mfma_f32_16x16x32_bf16 v[84:87], a[184:187], v[36:39], v[84:87]
v_exp_f32 v106, v106
v_mfma_f32_16x16x32_bf16 v[92:95], a[180:183], v[48:51], v[172:175]
v_exp_f32 v107, v107
v_mfma_f32_16x16x32_bf16 v[92:95], a[184:187], v[52:55], v[92:95]
s_add_u32 m0, s40, 149504
v_mfma_f32_16x16x32_bf16 v[68:71], a[188:191], v[8:11], v[68:71]
s_waitcnt lgkmcnt(4)
ds_read_b64_tr_b16 a[168:169], v199 offset:33280
ds_read_b64_tr_b16 a[170:171], v199 offset:37376
v_mfma_f32_16x16x32_bf16 v[68:71], a[192:195], v[12:15], v[68:71]
s_add_u32 s57, s65, 16384
buffer_load_dwordx4 v215, s[8:11], s57 offen lds
v_mfma_f32_16x16x32_bf16 v[76:79], a[188:191], v[24:27], v[76:79]
v_exp_f32 v108, v108
v_mfma_f32_16x16x32_bf16 v[76:79], a[192:195], v[28:31], v[76:79]
buffer_load_dwordx4 v216, s[8:11], s57 offen offset:1024 lds
v_mfma_f32_16x16x32_bf16 v[84:87], a[188:191], v[40:43], v[84:87]
ds_read_b64_tr_b16 a[172:173], v202 offset:33280
ds_read_b64_tr_b16 a[174:175], v202 offset:37376
v_mfma_f32_16x16x32_bf16 v[84:87], a[192:195], v[44:47], v[84:87]
v_cvt_pk_bf16_f32 v148, v104, v105
v_cvt_pk_bf16_f32 v149, v106, v107
v_mfma_f32_16x16x32_bf16 v[92:95], a[188:191], v[56:59], v[92:95]
v_exp_f32 v109, v109
v_mfma_f32_16x16x32_bf16 v[92:95], a[192:195], v[60:63], v[92:95]
v_mfma_f32_16x16x32_bf16 v[176:179], v[232:235], v[128:131], v[176:179]
s_waitcnt lgkmcnt(6)
ds_read_b64_tr_b16 a[180:181], v199 offset:33792
ds_read_b64_tr_b16 a[182:183], v199 offset:37888
v_mfma_f32_16x16x32_bf16 v[180:183], v[232:235], v[132:135], v[180:183]
v_exp_f32 v110, v110
v_mfma_f32_16x16x32_bf16 v[184:187], v[232:235], v[136:139], v[184:187]
v_exp_f32 v111, v111
v_mfma_f32_16x16x32_bf16 v[188:191], v[232:235], v[140:143], v[188:191]
v_exp_f32 v112, v112
v_mfma_f32_16x16x32_bf16 a[0:3], a[144:147], v[128:131], a[0:3]
s_waitcnt lgkmcnt(6)
ds_read_b64_tr_b16 a[184:185], v202 offset:33792
ds_read_b64_tr_b16 a[186:187], v202 offset:37888
v_mfma_f32_16x16x32_bf16 a[4:7], a[144:147], v[132:135], a[4:7]
v_cvt_pk_bf16_f32 v150, v108, v109
v_cvt_pk_bf16_f32 v151, v110, v111
v_mfma_f32_16x16x32_bf16 a[8:11], a[144:147], v[136:139], a[8:11]
v_mfma_f32_16x16x32_bf16 a[12:15], a[144:147], v[140:143], a[12:15]
v_exp_f32 v113, v113
v_mfma_f32_16x16x32_bf16 a[16:19], a[156:159], v[128:131], a[16:19]
s_waitcnt lgkmcnt(6)
ds_read_b64_tr_b16 a[144:145], v199 offset:34304
ds_read_b64_tr_b16 a[146:147], v199 offset:38400
v_mfma_f32_16x16x32_bf16 a[20:23], a[156:159], v[132:135], a[20:23]
v_exp_f32 v114, v114
v_mfma_f32_16x16x32_bf16 a[24:27], a[156:159], v[136:139], a[24:27]
v_exp_f32 v115, v115
v_mfma_f32_16x16x32_bf16 a[28:31], a[156:159], v[140:143], a[28:31]
v_mfma_f32_16x16x32_bf16 a[32:35], a[168:171], v[128:131], a[32:35]
s_waitcnt lgkmcnt(6)
ds_read_b64_tr_b16 a[156:157], v202 offset:34304
ds_read_b64_tr_b16 a[158:159], v202 offset:38400
v_mfma_f32_16x16x32_bf16 a[36:39], a[168:171], v[132:135], a[36:39]
v_exp_f32 v116, v116
v_mfma_f32_16x16x32_bf16 a[40:43], a[168:171], v[136:139], a[40:43]
v_cvt_pk_bf16_f32 v152, v112, v113
v_cvt_pk_bf16_f32 v153, v114, v115
v_mfma_f32_16x16x32_bf16 a[44:47], a[168:171], v[140:143], a[44:47]
v_exp_f32 v117, v117
v_mfma_f32_16x16x32_bf16 a[48:51], a[172:175], v[128:131], a[48:51]
s_waitcnt lgkmcnt(6)
ds_read_b128 a[168:171], v192 offset:40960
ds_read_b128 a[188:191], v192 offset:41472
v_mfma_f32_16x16x32_bf16 a[52:55], a[172:175], v[132:135], a[52:55]
v_mfma_f32_16x16x32_bf16 a[56:59], a[172:175], v[136:139], a[56:59]
v_exp_f32 v118, v118
v_mfma_f32_16x16x32_bf16 a[60:63], a[172:175], v[140:143], a[60:63]
v_exp_f32 v119, v119
v_mfma_f32_16x16x32_bf16 a[64:67], a[180:183], v[128:131], a[64:67]
s_waitcnt lgkmcnt(6)
v_exp_f32 v120, v120
v_mfma_f32_16x16x32_bf16 a[68:71], a[180:183], v[132:135], a[68:71]
v_cvt_pk_bf16_f32 v154, v116, v117
v_cvt_pk_bf16_f32 v155, v118, v119
v_mfma_f32_16x16x32_bf16 a[72:75], a[180:183], v[136:139], a[72:75]
v_mfma_f32_16x16x32_bf16 a[76:79], a[180:183], v[140:143], a[76:79]
v_exp_f32 v121, v121
v_mfma_f32_16x16x32_bf16 a[80:83], a[184:187], v[128:131], a[80:83]
s_waitcnt lgkmcnt(4)
ds_read_b128 a[172:175], v192 offset:41984
ds_read_b128 a[180:183], v192 offset:42496
v_mfma_f32_16x16x32_bf16 a[84:87], a[184:187], v[132:135], a[84:87]
v_exp_f32 v122, v122
v_mfma_f32_16x16x32_bf16 a[88:91], a[184:187], v[136:139], a[88:91]
v_exp_f32 v123, v123
v_mfma_f32_16x16x32_bf16 a[92:95], a[184:187], v[140:143], a[92:95]
v_mfma_f32_16x16x32_bf16 a[96:99], a[144:147], v[128:131], a[96:99]
s_waitcnt lgkmcnt(4)
v_exp_f32 v124, v124
v_mfma_f32_16x16x32_bf16 a[100:103], a[144:147], v[132:135], a[100:103]
v_cvt_pk_bf16_f32 v156, v120, v121
v_cvt_pk_bf16_f32 v157, v122, v123
v_mfma_f32_16x16x32_bf16 a[104:107], a[144:147], v[136:139], a[104:107]
v_exp_f32 v125, v125
v_mfma_f32_16x16x32_bf16 a[108:111], a[144:147], v[140:143], a[108:111]
v_mfma_f32_16x16x32_bf16 a[112:115], a[156:159], v[128:131], a[112:115]
s_waitcnt lgkmcnt(3)
ds_read_b128 a[144:147], v192 offset:45056
ds_read_b128 a[184:187], v192 offset:45568
v_mfma_f32_16x16x32_bf16 a[116:119], a[156:159], v[132:135], a[116:119]
v_exp_f32 v126, v126
v_mfma_f32_16x16x32_bf16 a[120:123], a[156:159], v[136:139], a[120:123]
v_exp_f32 v127, v127
v_mfma_f32_16x16x32_bf16 a[124:127], a[156:159], v[140:143], a[124:127]
v_cvt_pk_bf16_f32 v158, v124, v125
v_cvt_pk_bf16_f32 v159, v126, v127
v_mfma_f32_16x16x32_bf16 v[96:99], a[168:171], v[0:3], v[160:163]
s_waitcnt lgkmcnt(4)
v_exp_f32 v64, v64
v_mfma_f32_16x16x32_bf16 v[96:99], a[188:191], v[4:7], v[96:99]
v_mfma_f32_16x16x32_bf16 v[104:107], a[168:171], v[16:19], v[164:167]
v_exp_f32 v65, v65
v_mfma_f32_16x16x32_bf16 v[104:107], a[188:191], v[20:23], v[104:107]
v_exp_f32 v66, v66
v_mfma_f32_16x16x32_bf16 v[112:115], a[168:171], v[32:35], v[168:171]
s_waitcnt lgkmcnt(3)
ds_read_b128 a[156:159], v192 offset:46080
ds_read_b128 a[192:195], v192 offset:46592
v_mfma_f32_16x16x32_bf16 v[112:115], a[188:191], v[36:39], v[112:115]
v_exp_f32 v67, v67
v_mfma_f32_16x16x32_bf16 v[120:123], a[168:171], v[48:51], v[172:175]
v_exp_f32 v68, v68
v_mfma_f32_16x16x32_bf16 v[120:123], a[188:191], v[52:55], v[120:123]
v_cvt_pk_bf16_f32 v128, v64, v65
v_cvt_pk_bf16_f32 v129, v66, v67
v_mfma_f32_16x16x32_bf16 v[96:99], a[172:175], v[8:11], v[96:99]
s_waitcnt lgkmcnt(4)
v_mfma_f32_16x16x32_bf16 v[96:99], a[180:183], v[12:15], v[96:99]
v_exp_f32 v69, v69
v_mfma_f32_16x16x32_bf16 v[104:107], a[172:175], v[24:27], v[104:107]
v_exp_f32 v70, v70
v_mfma_f32_16x16x32_bf16 v[104:107], a[180:183], v[28:31], v[104:107]
v_exp_f32 v71, v71
v_mfma_f32_16x16x32_bf16 v[112:115], a[172:175], v[40:43], v[112:115]
s_waitcnt lgkmcnt(3)
v_exp_f32 v72, v72
v_mfma_f32_16x16x32_bf16 v[112:115], a[180:183], v[44:47], v[112:115]
v_cvt_pk_bf16_f32 v130, v68, v69
v_cvt_pk_bf16_f32 v131, v70, v71
v_mfma_f32_16x16x32_bf16 v[120:123], a[172:175], v[56:59], v[120:123]
v_mfma_f32_16x16x32_bf16 v[120:123], a[180:183], v[60:63], v[120:123]
v_exp_f32 v73, v73
v_mfma_f32_16x16x32_bf16 v[100:103], a[144:147], v[0:3], v[160:163]
s_waitcnt lgkmcnt(2)
ds_read_b64_tr_b16 a[168:169], v199 offset:40960
ds_read_b64_tr_b16 a[170:171], v199 offset:45056
v_mfma_f32_16x16x32_bf16 v[100:103], a[184:187], v[4:7], v[100:103]
v_exp_f32 v74, v74
v_mfma_f32_16x16x32_bf16 v[108:111], a[144:147], v[16:19], v[164:167]
v_exp_f32 v75, v75
v_mfma_f32_16x16x32_bf16 v[108:111], a[184:187], v[20:23], v[108:111]
v_exp_f32 v76, v76
v_mfma_f32_16x16x32_bf16 v[116:119], a[144:147], v[32:35], v[168:171]
s_waitcnt lgkmcnt(3)
ds_read_b64_tr_b16 a[172:173], v202 offset:40960
ds_read_b64_tr_b16 a[174:175], v202 offset:45056
v_mfma_f32_16x16x32_bf16 v[116:119], a[184:187], v[36:39], v[116:119]
v_cvt_pk_bf16_f32 v132, v72, v73
v_cvt_pk_bf16_f32 v133, v74, v75
v_mfma_f32_16x16x32_bf16 v[124:127], a[144:147], v[48:51], v[172:175]
v_mfma_f32_16x16x32_bf16 v[124:127], a[184:187], v[52:55], v[124:127]
v_exp_f32 v77, v77
v_mfma_f32_16x16x32_bf16 v[100:103], a[156:159], v[8:11], v[100:103]
s_waitcnt lgkmcnt(4)
ds_read_b64_tr_b16 a[144:145], v199 offset:41472
ds_read_b64_tr_b16 a[146:147], v199 offset:45568
v_mfma_f32_16x16x32_bf16 v[100:103], a[192:195], v[12:15], v[100:103]
v_exp_f32 v78, v78
v_mfma_f32_16x16x32_bf16 v[108:111], a[156:159], v[24:27], v[108:111]
v_exp_f32 v79, v79
v_mfma_f32_16x16x32_bf16 v[108:111], a[192:195], v[28:31], v[108:111]
v_exp_f32 v80, v80
v_mfma_f32_16x16x32_bf16 v[116:119], a[156:159], v[40:43], v[116:119]
ds_read_b64_tr_b16 a[180:181], v202 offset:41472
ds_read_b64_tr_b16 a[182:183], v202 offset:45568
v_mfma_f32_16x16x32_bf16 v[116:119], a[192:195], v[44:47], v[116:119]
v_cvt_pk_bf16_f32 v134, v76, v77
v_cvt_pk_bf16_f32 v135, v78, v79
v_mfma_f32_16x16x32_bf16 v[124:127], a[156:159], v[56:59], v[124:127]
v_mfma_f32_16x16x32_bf16 v[124:127], a[192:195], v[60:63], v[124:127]
v_exp_f32 v81, v81
v_mfma_f32_16x16x32_bf16 v[176:179], v[232:235], v[144:147], v[176:179]
s_waitcnt lgkmcnt(6)
ds_read_b64_tr_b16 a[156:157], v199 offset:41984
ds_read_b64_tr_b16 a[158:159], v199 offset:46080
v_mfma_f32_16x16x32_bf16 v[180:183], v[232:235], v[148:151], v[180:183]
v_exp_f32 v82, v82
v_mfma_f32_16x16x32_bf16 v[184:187], v[232:235], v[152:155], v[184:187]
v_exp_f32 v83, v83
v_mfma_f32_16x16x32_bf16 v[188:191], v[232:235], v[156:159], v[188:191]
v_exp_f32 v84, v84
v_mfma_f32_16x16x32_bf16 a[0:3], a[168:171], v[144:147], a[0:3]
s_waitcnt lgkmcnt(6)
ds_read_b64_tr_b16 a[184:185], v202 offset:41984
ds_read_b64_tr_b16 a[186:187], v202 offset:46080
v_mfma_f32_16x16x32_bf16 a[4:7], a[168:171], v[148:151], a[4:7]
v_cvt_pk_bf16_f32 v136, v80, v81
v_cvt_pk_bf16_f32 v137, v82, v83
v_mfma_f32_16x16x32_bf16 a[8:11], a[168:171], v[152:155], a[8:11]
ds_read_b64_tr_b16 a[188:189], v202 offset:42496
ds_read_b64_tr_b16 a[190:191], v202 offset:46592
v_mfma_f32_16x16x32_bf16 a[12:15], a[168:171], v[156:159], a[12:15]
ds_read_b64_tr_b16 a[168:169], v199 offset:42496
ds_read_b64_tr_b16 a[170:171], v199 offset:46592
v_mfma_f32_16x16x32_bf16 a[16:19], a[172:175], v[144:147], a[16:19]
s_waitcnt lgkmcnt(10)
v_mfma_f32_16x16x32_bf16 a[20:23], a[172:175], v[148:151], a[20:23]
v_exp_f32 v85, v85
v_mfma_f32_16x16x32_bf16 a[24:27], a[172:175], v[152:155], a[24:27]
v_exp_f32 v86, v86
v_mfma_f32_16x16x32_bf16 a[28:31], a[172:175], v[156:159], a[28:31]
v_exp_f32 v87, v87
v_mfma_f32_16x16x32_bf16 a[32:35], a[144:147], v[144:147], a[32:35]
s_waitcnt lgkmcnt(8)
v_exp_f32 v88, v88
v_mfma_f32_16x16x32_bf16 a[36:39], a[144:147], v[148:151], a[36:39]
v_cvt_pk_bf16_f32 v138, v84, v85
v_cvt_pk_bf16_f32 v139, v86, v87
v_mfma_f32_16x16x32_bf16 a[40:43], a[144:147], v[152:155], a[40:43]
L_tch7_%=:
s_cbranch_execz L_tb0_%=
v_mfma_f32_16x16x32_bf16 a[44:47], a[144:147], v[156:159], a[44:47]
s_add_u32 m0, s40, 32768
v_exp_f32 v89, v89
v_mfma_f32_16x16x32_bf16 a[48:51], a[180:183], v[144:147], a[48:51]
s_waitcnt vmcnt(24) lgkmcnt(0)
s_barrier
v_mfma_f32_16x16x32_bf16 a[52:55], a[180:183], v[148:151], a[52:55]
s_add_u32 s58, s65, 32768
buffer_load_dwordx4 v215, s[8:11], s58 offen lds
v_mfma_f32_16x16x32_bf16 a[56:59], a[180:183], v[152:155], a[56:59]
v_exp_f32 v90, v90
v_mfma_f32_16x16x32_bf16 a[60:63], a[180:183], v[156:159], a[60:63]
buffer_load_dwordx4 v216, s[8:11], s58 offen offset:1024 lds
v_mfma_f32_16x16x32_bf16 a[64:67], a[156:159], v[144:147], a[64:67]
s_add_u32 m0, s40, 98304
ds_read_b64_tr_b16 a[144:145], v198 offset:0
ds_read_b64_tr_b16 a[146:147], v198 offset:4096
v_mfma_f32_16x16x32_bf16 a[68:71], a[156:159], v[148:151], a[68:71]
s_xor_b32 s78, s42, 0
buffer_load_dwordx4 v212, s[20:23], s78 offen lds
v_mfma_f32_16x16x32_bf16 a[72:75], a[156:159], v[152:155], a[72:75]
v_exp_f32 v91, v91
v_mfma_f32_16x16x32_bf16 a[76:79], a[156:159], v[156:159], a[76:79]
buffer_load_dwordx4 v212, s[20:23], s78 offen offset:2048 lds
v_mfma_f32_16x16x32_bf16 a[80:83], a[184:187], v[144:147], a[80:83]
ds_read_b64_tr_b16 a[156:157], v201 offset:0
ds_read_b64_tr_b16 a[158:159], v201 offset:4096
v_mfma_f32_16x16x32_bf16 a[84:87], a[184:187], v[148:151], a[84:87]
v_exp_f32 v92, v92
v_mfma_f32_16x16x32_bf16 a[88:91], a[184:187], v[152:155], a[88:91]
v_cvt_pk_bf16_f32 v140, v88, v89
v_cvt_pk_bf16_f32 v141, v90, v91
v_mfma_f32_16x16x32_bf16 a[92:95], a[184:187], v[156:159], a[92:95]
v_mfma_f32_16x16x32_bf16 a[96:99], a[168:171], v[144:147], a[96:99]
ds_read_b64_tr_b16 a[172:173], v198 offset:512
ds_read_b64_tr_b16 a[174:175], v198 offset:4608
v_mfma_f32_16x16x32_bf16 a[100:103], a[168:171], v[148:151], a[100:103]
v_exp_f32 v93, v93
v_mfma_f32_16x16x32_bf16 a[104:107], a[168:171], v[152:155], a[104:107]
v_exp_f32 v94, v94
v_mfma_f32_16x16x32_bf16 a[108:111], a[168:171], v[156:159], a[108:111]
s_add_u32 m0, s40, 34816
v_exp_f32 v95, v95
v_mfma_f32_16x16x32_bf16 a[112:115], a[188:191], v[144:147], a[112:115]
ds_read_b64_tr_b16 a[168:169], v201 offset:512
ds_read_b64_tr_b16 a[170:171], v201 offset:4608
v_mfma_f32_16x16x32_bf16 a[116:119], a[188:191], v[148:151], a[116:119]
s_add_u32 s59, s65, 49152
buffer_load_dwordx4 v215, s[8:11], s59 offen lds
v_mfma_f32_16x16x32_bf16 a[120:123], a[188:191], v[152:155], a[120:123]
v_cvt_pk_bf16_f32 v142, v92, v93
v_cvt_pk_bf16_f32 v143, v94, v95
v_mfma_f32_16x16x32_bf16 a[124:127], a[188:191], v[156:159], a[124:127]
buffer_load_dwordx4 v216, s[8:11], s59 offen offset:1024 lds
v_mfma_f32_16x16x32_bf16 v[176:179], v[232:235], v[128:131], v[176:179]
s_waitcnt lgkmcnt(6)
ds_read_b64_tr_b16 a[180:181], v198 offset:1024
ds_read_b64_tr_b16 a[182:183], v198 offset:5120
v_mfma_f32_16x16x32_bf16 v[180:183], v[232:235], v[132:135], v[180:183]
v_exp_f32 v96, v96
v_mfma_f32_16x16x32_bf16 v[184:187], v[232:235], v[136:139], v[184:187]
v_exp_f32 v97, v97
v_mfma_f32_16x16x32_bf16 v[188:191], v[232:235], v[140:143], v[188:191]
v_exp_f32 v98, v98
v_mfma_f32_16x16x32_bf16 a[0:3], a[144:147], v[128:131], a[0:3]
s_waitcnt lgkmcnt(6)
ds_read_b64_tr_b16 a[184:185], v201 offset:1024
ds_read_b64_tr_b16 a[186:187], v201 offset:5120
v_mfma_f32_16x16x32_bf16 a[4:7], a[144:147], v[132:135], a[4:7]
v_exp_f32 v99, v99
v_mfma_f32_16x16x32_bf16 a[8:11], a[144:147], v[136:139], a[8:11]
v_exp_f32 v100, v100
v_mfma_f32_16x16x32_bf16 a[12:15], a[144:147], v[140:143], a[12:15]
v_cvt_pk_bf16_f32 v144, v96, v97
v_cvt_pk_bf16_f32 v145, v98, v99
v_mfma_f32_16x16x32_bf16 a[16:19], a[156:159], v[128:131], a[16:19]
s_waitcnt lgkmcnt(6)
ds_read_b64_tr_b16 a[144:145], v198 offset:1536
ds_read_b64_tr_b16 a[146:147], v198 offset:5632
v_mfma_f32_16x16x32_bf16 a[20:23], a[156:159], v[132:135], a[20:23]
v_exp_f32 v101, v101
v_mfma_f32_16x16x32_bf16 a[24:27], a[156:159], v[136:139], a[24:27]
v_exp_f32 v102, v102
v_mfma_f32_16x16x32_bf16 a[28:31], a[156:159], v[140:143], a[28:31]
v_exp_f32 v103, v103
v_mfma_f32_16x16x32_bf16 a[32:35], a[172:175], v[128:131], a[32:35]
s_waitcnt lgkmcnt(6)
ds_read_b64_tr_b16 a[156:157], v201 offset:1536
ds_read_b64_tr_b16 a[158:159], v201 offset:5632
v_mfma_f32_16x16x32_bf16 a[36:39], a[172:175], v[132:135], a[36:39]
v_exp_f32 v104, v104
v_mfma_f32_16x16x32_bf16 a[40:43], a[172:175], v[136:139], a[40:43]
v_cvt_pk_bf16_f32 v146, v100, v101
v_cvt_pk_bf16_f32 v147, v102, v103
v_mfma_f32_16x16x32_bf16 a[44:47], a[172:175], v[140:143], a[44:47]
v_exp_f32 v105, v105
v_mfma_f32_16x16x32_bf16 a[48:51], a[168:171], v[128:131], a[48:51]
s_waitcnt lgkmcnt(6)
v_exp_f32 v106, v106
v_mfma_f32_16x16x32_bf16 a[52:55], a[168:171], v[132:135], a[52:55]
v_exp_f32 v107, v107
v_mfma_f32_16x16x32_bf16 a[56:59], a[168:171], v[136:139], a[56:59]
s_add_u32 m0, s40, 99328
v_exp_f32 v108, v108
v_mfma_f32_16x16x32_bf16 a[60:63], a[168:171], v[140:143], a[60:63]
s_xor_b32 s79, s42, 128
buffer_load_dwordx4 v212, s[20:23], s79 offen lds
v_mfma_f32_16x16x32_bf16 a[64:67], a[180:183], v[128:131], a[64:67]
s_waitcnt lgkmcnt(4)
ds_read_b64_tr_b16 a[168:169], v198 offset:8192
ds_read_b64_tr_b16 a[170:171], v198 offset:12288
v_mfma_f32_16x16x32_bf16 a[68:71], a[180:183], v[132:135], a[68:71]
buffer_load_dwordx4 v212, s[20:23], s79 offen offset:2048 lds
v_mfma_f32_16x16x32_bf16 a[72:75], a[180:183], v[136:139], a[72:75]
v_cvt_pk_bf16_f32 v148, v104, v105
v_cvt_pk_bf16_f32 v149, v106, v107
v_mfma_f32_16x16x32_bf16 a[76:79], a[180:183], v[140:143], a[76:79]
v_exp_f32 v109, v109
v_mfma_f32_16x16x32_bf16 a[80:83], a[184:187], v[128:131], a[80:83]
s_waitcnt lgkmcnt(4)
ds_read_b64_tr_b16 a[172:173], v201 offset:8192
ds_read_b64_tr_b16 a[174:175], v201 offset:12288
v_mfma_f32_16x16x32_bf16 a[84:87], a[184:187], v[132:135], a[84:87]
v_exp_f32 v110, v110
v_mfma_f32_16x16x32_bf16 a[88:91], a[184:187], v[136:139], a[88:91]
v_exp_f32 v111, v111
v_mfma_f32_16x16x32_bf16 a[92:95], a[184:187], v[140:143], a[92:95]
v_exp_f32 v112, v112
v_mfma_f32_16x16x32_bf16 a[96:99], a[144:147], v[128:131], a[96:99]
s_waitcnt lgkmcnt(4)
ds_read_b64_tr_b16 a[180:181], v198 offset:8704
ds_read_b64_tr_b16 a[182:183], v198 offset:12800
v_mfma_f32_16x16x32_bf16 a[100:103], a[144:147], v[132:135], a[100:103]
v_cvt_pk_bf16_f32 v150, v108, v109
v_cvt_pk_bf16_f32 v151, v110, v111
v_mfma_f32_16x16x32_bf16 a[104:107], a[144:147], v[136:139], a[104:107]
v_exp_f32 v113, v113
v_mfma_f32_16x16x32_bf16 a[108:111], a[144:147], v[140:143], a[108:111]
v_exp_f32 v114, v114
v_mfma_f32_16x16x32_bf16 a[112:115], a[156:159], v[128:131], a[112:115]
ds_read_b64_tr_b16 a[144:145], v201 offset:8704
ds_read_b64_tr_b16 a[146:147], v201 offset:12800
v_mfma_f32_16x16x32_bf16 a[116:119], a[156:159], v[132:135], a[116:119]
v_exp_f32 v115, v115
v_mfma_f32_16x16x32_bf16 a[120:123], a[156:159], v[136:139], a[120:123]
v_exp_f32 v116, v116
v_mfma_f32_16x16x32_bf16 a[124:127], a[156:159], v[140:143], a[124:127]
v_cvt_pk_bf16_f32 v152, v112, v113
v_cvt_pk_bf16_f32 v153, v114, v115
v_exp_f32 v117, v117
v_exp_f32 v118, v118
v_exp_f32 v119, v119
v_exp_f32 v120, v120
v_cvt_pk_bf16_f32 v154, v116, v117
v_cvt_pk_bf16_f32 v155, v118, v119
v_exp_f32 v121, v121
v_exp_f32 v122, v122
v_exp_f32 v123, v123
v_exp_f32 v124, v124
v_cvt_pk_bf16_f32 v156, v120, v121
v_cvt_pk_bf16_f32 v157, v122, v123
v_exp_f32 v125, v125
v_exp_f32 v126, v126
v_exp_f32 v127, v127
v_cvt_pk_bf16_f32 v158, v124, v125
v_cvt_pk_bf16_f32 v159, v126, v127
v_mfma_f32_16x16x32_bf16 v[176:179], v[232:235], v[144:147], v[176:179]
s_waitcnt lgkmcnt(6)
ds_read_b64_tr_b16 a[156:157], v198 offset:9216
ds_read_b64_tr_b16 a[158:159], v198 offset:13312
v_mfma_f32_16x16x32_bf16 v[180:183], v[232:235], v[148:151], v[180:183]
v_mfma_f32_16x16x32_bf16 v[184:187], v[232:235], v[152:155], v[184:187]
v_mfma_f32_16x16x32_bf16 v[188:191], v[232:235], v[156:159], v[188:191]
v_mfma_f32_16x16x32_bf16 a[0:3], a[168:171], v[144:147], a[0:3]
s_waitcnt lgkmcnt(6)
ds_read_b64_tr_b16 a[184:185], v201 offset:9216
ds_read_b64_tr_b16 a[186:187], v201 offset:13312
v_mfma_f32_16x16x32_bf16 a[4:7], a[168:171], v[148:151], a[4:7]
v_mfma_f32_16x16x32_bf16 a[8:11], a[168:171], v[152:155], a[8:11]
v_mfma_f32_16x16x32_bf16 a[12:15], a[168:171], v[156:159], a[12:15]
v_mfma_f32_16x16x32_bf16 a[16:19], a[172:175], v[144:147], a[16:19]
s_waitcnt lgkmcnt(6)
ds_read_b64_tr_b16 a[168:169], v198 offset:9728
ds_read_b64_tr_b16 a[170:171], v198 offset:13824
v_mfma_f32_16x16x32_bf16 a[20:23], a[172:175], v[148:151], a[20:23]
v_mfma_f32_16x16x32_bf16 a[24:27], a[172:175], v[152:155], a[24:27]
v_rcp_f32 v222, v176
v_mfma_f32_16x16x32_bf16 a[28:31], a[172:175], v[156:159], a[28:31]
v_rcp_f32 v223, v180
v_mfma_f32_16x16x32_bf16 a[32:35], a[180:183], v[144:147], a[32:35]
s_waitcnt vmcnt(26) lgkmcnt(6)
ds_read_b64_tr_b16 a[172:173], v201 offset:9728
ds_read_b64_tr_b16 a[174:175], v201 offset:13824
v_mfma_f32_16x16x32_bf16 a[36:39], a[180:183], v[148:151], a[36:39]
v_mfma_f32_16x16x32_bf16 a[40:43], a[180:183], v[152:155], a[40:43]
ds_read_b128 a[196:199], v196 offset:18432
ds_read_b128 a[200:203], v196 offset:18944
v_mfma_f32_16x16x32_bf16 a[44:47], a[180:183], v[156:159], a[44:47]
ds_read_b64_tr_b16 a[192:193], v205 offset:0
ds_read_b64_tr_b16 a[194:195], v205 offset:256
v_mfma_f32_16x16x32_bf16 a[48:51], a[144:147], v[144:147], a[48:51]
s_waitcnt vmcnt(20) lgkmcnt(10)
ds_read_b128 a[180:183], v196 offset:16384
ds_read_b128 a[188:191], v196 offset:16896
v_mfma_f32_16x16x32_bf16 a[52:55], a[144:147], v[148:151], a[52:55]
ds_read_b64_tr_b16 a[208:209], v205 offset:8192
ds_read_b64_tr_b16 a[210:211], v205 offset:8448
v_mfma_f32_16x16x32_bf16 a[56:59], a[144:147], v[152:155], a[56:59]
v_rcp_f32 v224, v184
v_mfma_f32_16x16x32_bf16 a[60:63], a[144:147], v[156:159], a[60:63]
ds_read_b128 a[236:239], v196 offset:51200
ds_read_b128 a[240:243], v196 offset:51712
v_mfma_f32_16x16x32_bf16 a[64:67], a[156:159], v[144:147], a[64:67]
s_waitcnt lgkmcnt(14)
ds_read_b128 a[144:147], v196 offset:49152
ds_read_b128 a[212:215], v196 offset:49664
v_mfma_f32_16x16x32_bf16 a[68:71], a[156:159], v[148:151], a[68:71]
v_rcp_f32 v225, v188
v_mfma_f32_16x16x32_bf16 a[72:75], a[156:159], v[152:155], a[72:75]
v_add_f32 v227, v176, v180
v_mfma_f32_16x16x32_bf16 a[76:79], a[156:159], v[156:159], a[76:79]
s_lshl_b32 s56, s66, 12
v_min_f32 v225, s85, v225
v_mfma_f32_16x16x32_bf16 a[80:83], a[184:187], v[144:147], a[80:83]
s_waitcnt lgkmcnt(14)
ds_read_b64_tr_b16 a[156:157], v208 offset:0
ds_read_b64_tr_b16 a[158:159], v208 offset:256
v_mfma_f32_16x16x32_bf16 a[84:87], a[184:187], v[148:151], a[84:87]
s_add_u32 m0, s40, 81920
ds_read_b64_tr_b16 a[244:245], v208 offset:8192
ds_read_b64_tr_b16 a[246:247], v208 offset:8448
v_mfma_f32_16x16x32_bf16 a[88:91], a[184:187], v[152:155], a[88:91]
s_waitcnt lgkmcnt(10)
s_add_u32 s56, s56, s44
buffer_load_dwordx4 v213, s[12:15], s56 offen lds
v_mfma_f32_16x16x32_bf16 a[92:95], a[184:187], v[156:159], a[92:95]
v_add_f32 v228, v184, v188
v_add_f32 v227, v227, v228
v_mfma_f32_16x16x32_bf16 a[96:99], a[168:171], v[144:147], a[96:99]
buffer_load_dwordx4 v214, s[12:15], s56 offen offset:2048 lds
v_mfma_f32_16x16x32_bf16 a[100:103], a[168:171], v[148:151], a[100:103]
v_cmp_ngt_f32 vcc, s48, v227
v_mfma_f32_16x16x32_bf16 a[104:107], a[168:171], v[152:155], a[104:107]
v_mfma_f32_16x16x32_bf16 a[108:111], a[168:171], v[156:159], a[108:111]
v_mfma_f32_16x16x32_bf16 a[112:115], a[172:175], v[144:147], a[112:115]
ds_read_b64_tr_b16 a[168:169], v205 offset:512
ds_read_b64_tr_b16 a[170:171], v205 offset:768
v_mfma_f32_16x16x32_bf16 a[116:119], a[172:175], v[148:151], a[116:119]
s_add_u32 m0, s40, 82944
ds_read_b64_tr_b16 a[184:185], v205 offset:8704
ds_read_b64_tr_b16 a[186:187], v205 offset:8960
v_mfma_f32_16x16x32_bf16 a[120:123], a[172:175], v[152:155], a[120:123]
s_add_u32 s57, s56, 128
buffer_load_dwordx4 v213, s[12:15], s57 offen lds
v_mfma_f32_16x16x32_bf16 a[124:127], a[172:175], v[156:159], a[124:127]
s_cbranch_vccnz L_slow0_%=
L_ret0_%=:
v_mfma_f32_16x16x32_bf16 v[64:67], a[192:195], a[180:183], 0
s_waitcnt lgkmcnt(9)
buffer_load_dwordx4 v214, s[12:15], s57 offen offset:2048 lds
v_mfma_f32_16x16x32_bf16 v[64:67], a[208:211], a[188:191], v[64:67]
v_mfma_f32_16x16x32_bf16 v[72:75], a[192:195], a[196:199], 0
v_accvgpr_read_b32 v236, a0
v_accvgpr_read_b32 v237, a1
v_mfma_f32_16x16x32_bf16 v[72:75], a[208:211], a[200:203], v[72:75]
v_accvgpr_read_b32 v238, a2
v_mfma_f32_16x16x32_bf16 v[80:83], a[192:195], a[144:147], 0
s_waitcnt lgkmcnt(6)
ds_read_b64_tr_b16 a[172:173], v208 offset:512
ds_read_b64_tr_b16 a[174:175], v208 offset:768
v_mfma_f32_16x16x32_bf16 v[80:83], a[208:211], a[212:215], v[80:83]
s_add_u32 m0, s40, 114688
ds_read_b64_tr_b16 a[248:249], v208 offset:8704
ds_read_b64_tr_b16 a[250:251], v208 offset:8960
v_mfma_f32_16x16x32_bf16 v[88:91], a[192:195], a[236:239], 0
s_add_u32 s58, s66, 0
buffer_load_dwordx4 v215, s[8:11], s58 offen lds
v_mfma_f32_16x16x32_bf16 v[88:91], a[208:211], a[240:243], v[88:91]
v_accvgpr_read_b32 v239, a3
v_accvgpr_read_b32 v240, a16
v_mfma_f32_16x16x32_bf16 v[68:71], a[156:159], a[180:183], 0
s_waitcnt lgkmcnt(8)
buffer_load_dwordx4 v216, s[8:11], s58 offen offset:1024 lds
v_mfma_f32_16x16x32_bf16 v[68:71], a[244:247], a[188:191], v[68:71]
v_accvgpr_read_b32 v241, a17
v_mfma_f32_16x16x32_bf16 v[76:79], a[156:159], a[196:199], 0
v_accvgpr_read_b32 v242, a18
v_accvgpr_read_b32 v243, a19
v_mfma_f32_16x16x32_bf16 v[76:79], a[244:247], a[200:203], v[76:79]
v_mul_f32 v236, v222, v236
v_mfma_f32_16x16x32_bf16 v[84:87], a[156:159], a[144:147], 0
s_waitcnt lgkmcnt(6)
ds_read_b64_tr_b16 a[192:193], v205 offset:1024
ds_read_b64_tr_b16 a[194:195], v205 offset:1280
v_mfma_f32_16x16x32_bf16 v[84:87], a[244:247], a[212:215], v[84:87]
s_add_u32 m0, s40, 116736
ds_read_b64_tr_b16 a[208:209], v205 offset:9216
ds_read_b64_tr_b16 a[210:211], v205 offset:9472
v_mfma_f32_16x16x32_bf16 v[92:95], a[156:159], a[236:239], 0
s_add_u32 s59, s66, 16384
buffer_load_dwordx4 v215, s[8:11], s59 offen lds
v_mfma_f32_16x16x32_bf16 v[92:95], a[244:247], a[240:243], v[92:95]
v_mul_f32 v237, v222, v237
v_mul_f32 v238, v222, v238
v_mfma_f32_16x16x32_bf16 v[96:99], a[168:171], a[180:183], 0
s_waitcnt lgkmcnt(8)
buffer_load_dwordx4 v216, s[8:11], s59 offen offset:1024 lds
v_mfma_f32_16x16x32_bf16 v[96:99], a[184:187], a[188:191], v[96:99]
v_mul_f32 v239, v222, v239
v_mfma_f32_16x16x32_bf16 v[104:107], a[168:171], a[196:199], 0
v_mul_f32 v240, v222, v240
v_mul_f32 v241, v222, v241
v_mfma_f32_16x16x32_bf16 v[104:107], a[184:187], a[200:203], v[104:107]
v_mul_f32 v242, v222, v242
v_mfma_f32_16x16x32_bf16 v[112:115], a[168:171], a[144:147], 0
s_waitcnt lgkmcnt(6)
ds_read_b64_tr_b16 a[156:157], v208 offset:1024
ds_read_b64_tr_b16 a[158:159], v208 offset:1280
v_mfma_f32_16x16x32_bf16 v[112:115], a[184:187], a[212:215], v[112:115]
ds_read_b64_tr_b16 a[244:245], v208 offset:9216
ds_read_b64_tr_b16 a[246:247], v208 offset:9472
v_mfma_f32_16x16x32_bf16 v[120:123], a[168:171], a[236:239], 0
v_mul_f32 v243, v222, v243
v_cvt_pk_bf16_f32 v0, v236, v237
v_mfma_f32_16x16x32_bf16 v[120:123], a[184:187], a[240:243], v[120:123]
v_cvt_pk_bf16_f32 v1, v238, v239
v_mfma_f32_16x16x32_bf16 v[100:103], a[172:175], a[180:183], 0
s_waitcnt lgkmcnt(8)
v_cvt_pk_bf16_f32 v2, v240, v241
v_cvt_pk_bf16_f32 v3, v242, v243
v_mfma_f32_16x16x32_bf16 v[100:103], a[248:251], a[188:191], v[100:103]
s_xor_b32 s60, s45, 0
buffer_store_dwordx4 v[0:3], v217, s[24:27], s60 offen sc1
v_mfma_f32_16x16x32_bf16 v[108:111], a[172:175], a[196:199], 0
v_accvgpr_read_b32 v244, a4
v_accvgpr_read_b32 v245, a5
v_mfma_f32_16x16x32_bf16 v[108:111], a[248:251], a[200:203], v[108:111]
v_accvgpr_read_b32 v246, a6
v_accvgpr_read_b32 v247, a7
v_mfma_f32_16x16x32_bf16 v[116:119], a[172:175], a[144:147], 0
s_waitcnt lgkmcnt(6)
ds_read_b64_tr_b16 a[168:169], v205 offset:1536
ds_read_b64_tr_b16 a[170:171], v205 offset:1792
v_mfma_f32_16x16x32_bf16 v[116:119], a[248:251], a[212:215], v[116:119]
ds_read_b64_tr_b16 a[184:185], v205 offset:9728
ds_read_b64_tr_b16 a[186:187], v205 offset:9984
v_mfma_f32_16x16x32_bf16 v[124:127], a[172:175], a[236:239], 0
ds_read_b64_tr_b16 a[0:1], v208 offset:9728
ds_read_b64_tr_b16 a[2:3], v208 offset:9984
v_mfma_f32_16x16x32_bf16 v[124:127], a[248:251], a[240:243], v[124:127]
ds_read_b64_tr_b16 a[172:173], v208 offset:1536
ds_read_b64_tr_b16 a[174:175], v208 offset:1792
v_mfma_f32_16x16x32_bf16 v[128:131], a[192:195], a[180:183], 0
s_waitcnt lgkmcnt(12)
v_accvgpr_read_b32 v248, a20
v_mfma_f32_16x16x32_bf16 v[128:131], a[208:211], a[188:191], v[128:131]
v_accvgpr_read_b32 v249, a21
v_accvgpr_read_b32 v250, a22
v_mfma_f32_16x16x32_bf16 v[136:139], a[192:195], a[196:199], 0
v_accvgpr_read_b32 v251, a23
v_mul_f32 v244, v223, v244
v_mfma_f32_16x16x32_bf16 v[136:139], a[208:211], a[200:203], v[136:139]
v_mul_f32 v245, v223, v245
v_mul_f32 v246, v223, v246
v_mfma_f32_16x16x32_bf16 v[144:147], a[192:195], a[144:147], 0
s_waitcnt lgkmcnt(10)
v_mul_f32 v247, v223, v247
v_mul_f32 v248, v223, v248
v_mfma_f32_16x16x32_bf16 v[144:147], a[208:211], a[212:215], v[144:147]
v_mul_f32 v249, v223, v249
v_mul_f32 v250, v223, v250
v_mfma_f32_16x16x32_bf16 v[152:155], a[192:195], a[236:239], 0
v_mul_f32 v251, v223, v251
v_mfma_f32_16x16x32_bf16 v[152:155], a[208:211], a[240:243], v[152:155]
s_add_u32 m0, s40, 0
v_cvt_pk_bf16_f32 v16, v244, v245
v_cvt_pk_bf16_f32 v17, v246, v247
v_mfma_f32_16x16x32_bf16 v[132:135], a[156:159], a[180:183], 0
s_waitcnt vmcnt(21) lgkmcnt(0)
s_barrier
v_mfma_f32_16x16x32_bf16 v[132:135], a[244:247], a[188:191], v[132:135]
s_add_u32 s56, s66, 32768
buffer_load_dwordx4 v215, s[8:11], s56 offen lds
v_mfma_f32_16x16x32_bf16 v[140:143], a[156:159], a[196:199], 0
v_cvt_pk_bf16_f32 v18, v248, v249
v_cvt_pk_bf16_f32 v19, v250, v251
v_mfma_f32_16x16x32_bf16 v[140:143], a[244:247], a[200:203], v[140:143]
ds_read_b64_tr_b16 a[248:249], v204 offset:16384
ds_read_b64_tr_b16 a[250:251], v204 offset:16640
v_mfma_f32_16x16x32_bf16 v[148:151], a[156:159], a[144:147], 0
s_waitcnt vmcnt(18)
ds_read_b128 a[192:195], v197 offset:16384
ds_read_b128 a[208:211], v197 offset:16896
v_mfma_f32_16x16x32_bf16 v[148:151], a[244:247], a[212:215], v[148:151]
buffer_load_dwordx4 v216, s[8:11], s56 offen offset:1024 lds
v_mfma_f32_16x16x32_bf16 v[156:159], a[156:159], a[236:239], 0
ds_read_b128 a[156:159], v197 offset:18432
ds_read_b128 a[16:19], v197 offset:18944
v_mfma_f32_16x16x32_bf16 v[156:159], a[244:247], a[240:243], v[156:159]
s_xor_b32 s60, s45, 4096
ds_read_b64_tr_b16 a[244:245], v204 offset:24576
ds_read_b64_tr_b16 a[246:247], v204 offset:24832
v_mfma_f32_16x16x32_bf16 v[160:163], a[168:171], a[180:183], 0
s_waitcnt vmcnt(13)
buffer_store_dwordx4 v[16:19], v217, s[24:27], s60 offen sc1
v_mfma_f32_16x16x32_bf16 v[160:163], a[184:187], a[188:191], v[160:163]
s_add_u32 m0, s40, 2048
ds_read_b128 a[4:7], v195 offset:32768
ds_read_b128 a[20:23], v195 offset:33280
v_mfma_f32_16x16x32_bf16 v[168:171], a[168:171], a[196:199], 0
s_add_u32 s57, s66, 49152
buffer_load_dwordx4 v215, s[8:11], s57 offen lds
v_mfma_f32_16x16x32_bf16 v[168:171], a[184:187], a[200:203], v[168:171]
v_accvgpr_read_b32 v236, a8
v_accvgpr_read_b32 v237, a9
v_mfma_f32_16x16x32_bf16 v[176:179], a[168:171], a[144:147], 0
buffer_load_dwordx4 v216, s[8:11], s57 offen offset:1024 lds
v_mfma_f32_16x16x32_bf16 v[176:179], a[184:187], a[212:215], v[176:179]
v_accvgpr_read_b32 v238, a10
v_accvgpr_read_b32 v239, a11
v_mfma_f32_16x16x32_bf16 v[184:187], a[168:171], a[236:239], 0
v_accvgpr_read_b32 v240, a24
v_accvgpr_read_b32 v241, a25
v_mfma_f32_16x16x32_bf16 v[184:187], a[184:187], a[240:243], v[184:187]
s_lshl_b32 s58, s67, 12
ds_read_b128 a[168:171], v195 offset:34816
ds_read_b128 a[184:187], v195 offset:35328
v_mfma_f32_16x16x32_bf16 v[164:167], a[172:175], a[180:183], 0
ds_read_b64_tr_b16 a[180:181], v207 offset:16384
ds_read_b64_tr_b16 a[182:183], v207 offset:16640
v_mfma_f32_16x16x32_bf16 v[164:167], a[0:3], a[188:191], v[164:167]
s_add_u32 m0, s40, 65536
ds_read_b64_tr_b16 a[188:189], v207 offset:24576
ds_read_b64_tr_b16 a[190:191], v207 offset:24832
v_mfma_f32_16x16x32_bf16 v[172:175], a[172:175], a[196:199], 0
s_add_u32 s58, s58, s44
buffer_load_dwordx4 v213, s[12:15], s58 offen lds
v_mfma_f32_16x16x32_bf16 v[172:175], a[0:3], a[200:203], v[172:175]
v_accvgpr_read_b32 v242, a26
v_accvgpr_read_b32 v243, a27
v_mfma_f32_16x16x32_bf16 v[180:183], a[172:175], a[144:147], 0
s_waitcnt lgkmcnt(13)
ds_read_b64_tr_b16 a[144:145], v204 offset:16896
ds_read_b64_tr_b16 a[146:147], v204 offset:17152
v_mfma_f32_16x16x32_bf16 v[180:183], a[0:3], a[212:215], v[180:183]
ds_read_b64_tr_b16 a[196:197], v204 offset:25088
ds_read_b64_tr_b16 a[198:199], v204 offset:25344
v_mfma_f32_16x16x32_bf16 v[188:191], a[172:175], a[236:239], 0
buffer_load_dwordx4 v214, s[12:15], s58 offen offset:2048 lds
v_mfma_f32_16x16x32_bf16 v[188:191], a[0:3], a[240:243], v[188:191]
v_mul_f32 v236, v224, v236
v_mul_f32 v237, v224, v237
v_mfma_f32_16x16x32_bf16 v[64:67], a[248:251], a[192:195], v[64:67]
s_waitcnt lgkmcnt(11)
v_mul_f32 v238, v224, v238
v_mul_f32 v239, v224, v239
v_mfma_f32_16x16x32_bf16 v[64:67], a[244:247], a[208:211], v[64:67]
s_add_u32 m0, s40, 66560
v_mul_f32 v240, v224, v240
v_mul_f32 v241, v224, v241
v_mfma_f32_16x16x32_bf16 v[72:75], a[248:251], a[156:159], v[72:75]
s_add_u32 s59, s58, 128
buffer_load_dwordx4 v213, s[12:15], s59 offen lds
v_mfma_f32_16x16x32_bf16 v[72:75], a[244:247], a[16:19], v[72:75]
v_mul_f32 v242, v224, v242
v_mul_f32 v243, v224, v243
v_mfma_f32_16x16x32_bf16 v[80:83], a[248:251], a[4:7], v[80:83]
s_waitcnt lgkmcnt(6)
ds_read_b64_tr_b16 a[172:173], v207 offset:16896
ds_read_b64_tr_b16 a[174:175], v207 offset:17152
v_mfma_f32_16x16x32_bf16 v[80:83], a[244:247], a[20:23], v[80:83]
ds_read_b64_tr_b16 a[200:201], v207 offset:25088
ds_read_b64_tr_b16 a[202:203], v207 offset:25344
v_mfma_f32_16x16x32_bf16 v[88:91], a[248:251], a[168:171], v[88:91]
buffer_load_dwordx4 v214, s[12:15], s59 offen offset:2048 lds
v_cvt_pk_bf16_f32 v32, v236, v237
v_cvt_pk_bf16_f32 v33, v238, v239
v_cvt_pk_bf16_f32 v34, v240, v241
v_cvt_pk_bf16_f32 v35, v242, v243
v_mfma_f32_16x16x32_bf16 v[88:91], a[244:247], a[184:187], v[88:91]
s_xor_b32 s60, s45, 8192
buffer_store_dwordx4 v[32:35], v217, s[24:27], s60 offen sc1
v_mfma_f32_16x16x32_bf16 v[68:71], a[180:183], a[192:195], v[68:71]
s_waitcnt lgkmcnt(8)
v_accvgpr_read_b32 v244, a12
v_accvgpr_read_b32 v245, a13
v_mfma_f32_16x16x32_bf16 v[68:71], a[188:191], a[208:211], v[68:71]
s_add_u32 m0, s40, 147456
v_accvgpr_read_b32 v246, a14
v_accvgpr_read_b32 v247, a15
v_mfma_f32_16x16x32_bf16 v[76:79], a[180:183], a[156:159], v[76:79]
s_add_u32 s56, s67, 0
buffer_load_dwordx4 v215, s[8:11], s56 offen lds
v_mfma_f32_16x16x32_bf16 v[76:79], a[188:191], a[16:19], v[76:79]
v_accvgpr_read_b32 v248, a28
v_mfma_f32_16x16x32_bf16 v[84:87], a[180:183], a[4:7], v[84:87]
s_waitcnt lgkmcnt(6)
ds_read_b64_tr_b16 a[212:213], v204 offset:17408
ds_read_b64_tr_b16 a[214:215], v204 offset:17664
v_mfma_f32_16x16x32_bf16 v[84:87], a[188:191], a[20:23], v[84:87]
ds_read_b64_tr_b16 a[236:237], v204 offset:25600
ds_read_b64_tr_b16 a[238:239], v204 offset:25856
v_mfma_f32_16x16x32_bf16 v[92:95], a[180:183], a[168:171], v[92:95]
buffer_load_dwordx4 v216, s[8:11], s56 offen offset:1024 lds
v_mfma_f32_16x16x32_bf16 v[92:95], a[188:191], a[184:187], v[92:95]
v_accvgpr_read_b32 v249, a29
v_accvgpr_read_b32 v250, a30
v_mfma_f32_16x16x32_bf16 v[96:99], a[144:147], a[192:195], v[96:99]
s_waitcnt lgkmcnt(8)
v_accvgpr_read_b32 v251, a31
v_mul_f32 v244, v225, v244
v_mfma_f32_16x16x32_bf16 v[96:99], a[196:199], a[208:211], v[96:99]
s_add_u32 m0, s40, 149504
v_mul_f32 v245, v225, v245
v_mul_f32 v246, v225, v246
v_mfma_f32_16x16x32_bf16 v[104:107], a[144:147], a[156:159], v[104:107]
s_add_u32 s57, s67, 16384
buffer_load_dwordx4 v215, s[8:11], s57 offen lds
v_mfma_f32_16x16x32_bf16 v[104:107], a[196:199], a[16:19], v[104:107]
v_mul_f32 v247, v225, v247
v_mul_f32 v248, v225, v248
v_mfma_f32_16x16x32_bf16 v[112:115], a[144:147], a[4:7], v[112:115]
s_waitcnt lgkmcnt(6)
ds_read_b64_tr_b16 a[180:181], v207 offset:17408
ds_read_b64_tr_b16 a[182:183], v207 offset:17664
v_mfma_f32_16x16x32_bf16 v[112:115], a[196:199], a[20:23], v[112:115]
ds_read_b64_tr_b16 a[188:189], v207 offset:25600
ds_read_b64_tr_b16 a[190:191], v207 offset:25856
v_mfma_f32_16x16x32_bf16 v[120:123], a[144:147], a[168:171], v[120:123]
buffer_load_dwordx4 v216, s[8:11], s57 offen offset:1024 lds
v_mfma_f32_16x16x32_bf16 v[120:123], a[196:199], a[184:187], v[120:123]
v_mul_f32 v249, v225, v249
v_mul_f32 v250, v225, v250
v_mfma_f32_16x16x32_bf16 v[100:103], a[172:175], a[192:195], v[100:103]
s_waitcnt lgkmcnt(8)
v_mul_f32 v251, v225, v251
v_mfma_f32_16x16x32_bf16 v[100:103], a[200:203], a[208:211], v[100:103]
s_add_u32 m0, s40, 32768
v_cvt_pk_bf16_f32 v48, v244, v245
v_cvt_pk_bf16_f32 v49, v246, v247
v_mfma_f32_16x16x32_bf16 v[108:111], a[172:175], a[156:159], v[108:111]
s_add_u32 s58, s67, 32768
buffer_load_dwordx4 v215, s[8:11], s58 offen lds
v_mfma_f32_16x16x32_bf16 v[108:111], a[200:203], a[16:19], v[108:111]
v_cvt_pk_bf16_f32 v50, v248, v249
v_cvt_pk_bf16_f32 v51, v250, v251
v_mfma_f32_16x16x32_bf16 v[116:119], a[172:175], a[4:7], v[116:119]
s_waitcnt lgkmcnt(6)
ds_read_b64_tr_b16 a[144:145], v204 offset:17920
ds_read_b64_tr_b16 a[146:147], v204 offset:18176
v_mfma_f32_16x16x32_bf16 v[116:119], a[200:203], a[20:23], v[116:119]
ds_read_b64_tr_b16 a[196:197], v204 offset:26112
ds_read_b64_tr_b16 a[198:199], v204 offset:26368
v_mfma_f32_16x16x32_bf16 v[124:127], a[172:175], a[168:171], v[124:127]
ds_read_b64_tr_b16 a[240:241], v207 offset:26112
ds_read_b64_tr_b16 a[242:243], v207 offset:26368
v_mfma_f32_16x16x32_bf16 v[124:127], a[200:203], a[184:187], v[124:127]
s_xor_b32 s60, s45, 12288
ds_read_b64_tr_b16 a[172:173], v207 offset:17920
ds_read_b64_tr_b16 a[174:175], v207 offset:18176
v_mfma_f32_16x16x32_bf16 v[128:131], a[212:215], a[192:195], v[128:131]
s_waitcnt lgkmcnt(12)
buffer_load_dwordx4 v216, s[8:11], s58 offen offset:1024 lds
v_mfma_f32_16x16x32_bf16 v[128:131], a[236:239], a[208:211], v[128:131]
s_add_u32 m0, s40, 34816
buffer_store_dwordx4 v[48:51], v217, s[24:27], s60 offen sc1
v_mfma_f32_16x16x32_bf16 v[136:139], a[212:215], a[156:159], v[136:139]
s_add_u32 s59, s67, 49152
buffer_load_dwordx4 v215, s[8:11], s59 offen lds
v_mfma_f32_16x16x32_bf16 v[136:139], a[236:239], a[16:19], v[136:139]
v_accvgpr_read_b32 v236, a32
v_accvgpr_read_b32 v237, a33
v_mfma_f32_16x16x32_bf16 v[144:147], a[212:215], a[4:7], v[144:147]
s_waitcnt lgkmcnt(10)
buffer_load_dwordx4 v216, s[8:11], s59 offen offset:1024 lds
v_mfma_f32_16x16x32_bf16 v[144:147], a[236:239], a[20:23], v[144:147]
v_accvgpr_read_b32 v238, a34
v_accvgpr_read_b32 v239, a35
v_mfma_f32_16x16x32_bf16 v[152:155], a[212:215], a[168:171], v[152:155]
L_tch8_%=:
s_cbranch_execz L_tb0_%=
v_accvgpr_read_b32 v240, a48
v_accvgpr_read_b32 v241, a49
v_mfma_f32_16x16x32_bf16 v[152:155], a[236:239], a[184:187], v[152:155]
v_accvgpr_read_b32 v242, a50
v_accvgpr_read_b32 v243, a51
v_mfma_f32_16x16x32_bf16 v[132:135], a[180:183], a[192:195], v[132:135]
s_mov_b64 exec, 0
s_cmp_eq_u32 %2, 0
s_cbranch_scc1 L_tch9_%=
s_cmp_eq_u32 %2, 1
s_cbranch_scc1 L_tch10_%=
s_cmp_eq_u32 %2, 2
s_cbranch_scc1 L_tch11_%=
s_cmp_eq_u32 %2, 3
s_cbranch_scc1 L_tch12_%=
L_tb1_%=:
s_mov_b64 exec, -1
s_nop 1
s_waitcnt vmcnt(20) lgkmcnt(0)
s_barrier
v_mfma_f32_16x16x32_bf16 v[132:135], a[188:191], a[208:211], v[132:135]
v_mul_f32 v236, v222, v236
v_mul_f32 v237, v222, v237
v_mfma_f32_16x16x32_bf16 v[140:143], a[180:183], a[156:159], v[140:143]
ds_read_b128 a[244:247], v196 offset:51200
ds_read_b128 a[248:251], v196 offset:51712
v_mfma_f32_16x16x32_bf16 v[140:143], a[188:191], a[16:19], v[140:143]
s_lshl_b32 s56, s68, 12
ds_read_b64_tr_b16 a[236:237], v205 offset:16384
ds_read_b64_tr_b16 a[238:239], v205 offset:16640
v_mfma_f32_16x16x32_bf16 v[148:151], a[180:183], a[4:7], v[148:151]
s_waitcnt vmcnt(14)
ds_read_b128 a[200:203], v196 offset:49152
ds_read_b128 a[212:215], v196 offset:49664
v_mfma_f32_16x16x32_bf16 v[148:151], a[188:191], a[20:23], v[148:151]
s_add_u32 m0, s40, 16384
ds_read_b64_tr_b16 a[0:1], v205 offset:24576
ds_read_b64_tr_b16 a[2:3], v205 offset:24832
v_mfma_f32_16x16x32_bf16 v[156:159], a[180:183], a[168:171], v[156:159]
s_add_u32 s56, s56, s44
buffer_load_dwordx4 v213, s[12:15], s56 offen lds
v_mfma_f32_16x16x32_bf16 v[156:159], a[188:191], a[184:187], v[156:159]
ds_read_b128 a[8:11], v195 offset:2048
ds_read_b128 a[12:15], v195 offset:2560
v_mfma_f32_16x16x32_bf16 v[160:163], a[144:147], a[192:195], v[160:163]
ds_read_b128 a[180:183], v195 offset:0
ds_read_b128 a[188:191], v195 offset:512
v_mfma_f32_16x16x32_bf16 v[160:163], a[196:199], a[208:211], v[160:163]
buffer_load_dwordx4 v214, s[12:15], s56 offen offset:2048 lds
v_mfma_f32_16x16x32_bf16 v[168:171], a[144:147], a[156:159], v[168:171]
v_mul_f32 v238, v222, v238
v_mul_f32 v239, v222, v239
v_mfma_f32_16x16x32_bf16 v[168:171], a[196:199], a[16:19], v[168:171]
v_mul_f32 v240, v222, v240
v_mul_f32 v241, v222, v241
v_mfma_f32_16x16x32_bf16 v[176:179], a[144:147], a[4:7], v[176:179]
ds_read_b64_tr_b16 a[24:25], v208 offset:16384
ds_read_b64_tr_b16 a[26:27], v208 offset:16640
v_mul_f32 v242, v222, v242
v_mul_f32 v243, v222, v243
v_cvt_pk_bf16_f32 v4, v236, v237
v_cvt_pk_bf16_f32 v5, v238, v239
v_cvt_pk_bf16_f32 v6, v240, v241
v_cvt_pk_bf16_f32 v7, v242, v243
v_mfma_f32_16x16x32_bf16 v[176:179], a[196:199], a[20:23], v[176:179]
s_add_u32 m0, s40, 17408
ds_read_b64_tr_b16 a[28:29], v208 offset:24576
ds_read_b64_tr_b16 a[30:31], v208 offset:24832
v_mfma_f32_16x16x32_bf16 v[184:187], a[144:147], a[168:171], v[184:187]
s_add_u32 s57, s56, 128
buffer_load_dwordx4 v213, s[12:15], s57 offen lds
v_mfma_f32_16x16x32_bf16 v[184:187], a[196:199], a[184:187], v[184:187]
s_xor_b32 s60, s45, 1024
buffer_store_dwordx4 v[4:7], v217, s[24:27], s60 offen sc1
v_mfma_f32_16x16x32_bf16 v[164:167], a[172:175], a[192:195], v[164:167]
buffer_load_dwordx4 v214, s[12:15], s57 offen offset:2048 lds
v_mfma_f32_16x16x32_bf16 v[164:167], a[240:243], a[208:211], v[164:167]
v_accvgpr_read_b32 v244, a36
v_accvgpr_read_b32 v245, a37
v_mfma_f32_16x16x32_bf16 v[172:175], a[172:175], a[156:159], v[172:175]
v_accvgpr_read_b32 v246, a38
v_accvgpr_read_b32 v247, a39
v_mfma_f32_16x16x32_bf16 v[172:175], a[240:243], a[16:19], v[172:175]
v_accvgpr_read_b32 v248, a52
v_mfma_f32_16x16x32_bf16 v[180:183], a[172:175], a[4:7], v[180:183]
s_waitcnt lgkmcnt(11)
ds_read_b64_tr_b16 a[144:145], v205 offset:16896
ds_read_b64_tr_b16 a[146:147], v205 offset:17152
v_mfma_f32_16x16x32_bf16 v[180:183], a[240:243], a[20:23], v[180:183]
s_add_u32 m0, s40, 114688
ds_read_b64_tr_b16 a[156:157], v205 offset:25088
ds_read_b64_tr_b16 a[158:159], v205 offset:25344
v_mfma_f32_16x16x32_bf16 v[188:191], a[172:175], a[168:171], v[188:191]
s_waitcnt lgkmcnt(14)
s_add_u32 s58, s68, 0
buffer_load_dwordx4 v215, s[8:11], s58 offen lds
v_mfma_f32_16x16x32_bf16 v[188:191], a[240:243], a[184:187], v[188:191]
v_accvgpr_read_b32 v249, a53
v_accvgpr_read_b32 v250, a54
v_mfma_f32_16x16x32_bf16 v[64:67], a[236:239], a[200:203], v[64:67]
s_waitcnt lgkmcnt(9)
buffer_load_dwordx4 v216, s[8:11], s58 offen offset:1024 lds
v_mfma_f32_16x16x32_bf16 v[64:67], a[0:3], a[212:215], v[64:67]
v_accvgpr_read_b32 v251, a55
v_mul_f32 v244, v223, v244
v_mfma_f32_16x16x32_bf16 v[72:75], a[236:239], a[244:247], v[72:75]
v_mul_f32 v245, v223, v245
v_mul_f32 v246, v223, v246
v_mfma_f32_16x16x32_bf16 v[72:75], a[0:3], a[248:251], v[72:75]
v_mul_f32 v247, v223, v247
v_mul_f32 v248, v223, v248
v_mfma_f32_16x16x32_bf16 v[80:83], a[236:239], a[180:183], v[80:83]
s_waitcnt lgkmcnt(6)
ds_read_b64_tr_b16 a[168:169], v208 offset:16896
ds_read_b64_tr_b16 a[170:171], v208 offset:17152
v_mfma_f32_16x16x32_bf16 v[80:83], a[0:3], a[188:191], v[80:83]
s_add_u32 m0, s40, 116736
ds_read_b64_tr_b16 a[172:173], v208 offset:25088
ds_read_b64_tr_b16 a[174:175], v208 offset:25344
v_mfma_f32_16x16x32_bf16 v[88:91], a[236:239], a[8:11], v[88:91]
s_add_u32 s59, s68, 16384
buffer_load_dwordx4 v215, s[8:11], s59 offen lds
v_mfma_f32_16x16x32_bf16 v[88:91], a[0:3], a[12:15], v[88:91]
v_mul_f32 v249, v223, v249
v_mul_f32 v250, v223, v250
v_mfma_f32_16x16x32_bf16 v[68:71], a[24:27], a[200:203], v[68:71]
s_waitcnt lgkmcnt(8)
buffer_load_dwordx4 v216, s[8:11], s59 offen offset:1024 lds
v_mfma_f32_16x16x32_bf16 v[68:71], a[28:31], a[212:215], v[68:71]
v_mul_f32 v251, v223, v251
v_mfma_f32_16x16x32_bf16 v[76:79], a[24:27], a[244:247], v[76:79]
v_cvt_pk_bf16_f32 v20, v244, v245
v_cvt_pk_bf16_f32 v21, v246, v247
v_mfma_f32_16x16x32_bf16 v[76:79], a[28:31], a[248:251], v[76:79]
v_cvt_pk_bf16_f32 v22, v248, v249
v_cvt_pk_bf16_f32 v23, v250, v251
v_mfma_f32_16x16x32_bf16 v[84:87], a[24:27], a[180:183], v[84:87]
s_waitcnt lgkmcnt(6)
ds_read_b64_tr_b16 a[184:185], v205 offset:17408
ds_read_b64_tr_b16 a[186:187], v205 offset:17664
v_mfma_f32_16x16x32_bf16 v[84:87], a[28:31], a[188:191], v[84:87]
s_add_u32 m0, s40, 0
ds_read_b64_tr_b16 a[192:193], v205 offset:25600
ds_read_b64_tr_b16 a[194:195], v205 offset:25856
v_mfma_f32_16x16x32_bf16 v[92:95], a[24:27], a[8:11], v[92:95]
s_add_u32 s56, s68, 32768
buffer_load_dwordx4 v215, s[8:11], s56 offen lds
v_mfma_f32_16x16x32_bf16 v[92:95], a[28:31], a[12:15], v[92:95]
s_xor_b32 s60, s45, 5120
buffer_store_dwordx4 v[20:23], v217, s[24:27], s60 offen sc1
v_mfma_f32_16x16x32_bf16 v[96:99], a[144:147], a[200:203], v[96:99]
s_waitcnt lgkmcnt(8)
buffer_load_dwordx4 v216, s[8:11], s56 offen offset:1024 lds
v_mfma_f32_16x16x32_bf16 v[96:99], a[156:159], a[212:215], v[96:99]
v_accvgpr_read_b32 v236, a40
v_accvgpr_read_b32 v237, a41
v_mfma_f32_16x16x32_bf16 v[104:107], a[144:147], a[244:247], v[104:107]
v_accvgpr_read_b32 v238, a42
v_accvgpr_read_b32 v239, a43
v_mfma_f32_16x16x32_bf16 v[104:107], a[156:159], a[248:251], v[104:107]
v_accvgpr_read_b32 v240, a56
v_accvgpr_read_b32 v241, a57
v_mfma_f32_16x16x32_bf16 v[112:115], a[144:147], a[180:183], v[112:115]
s_waitcnt lgkmcnt(6)
ds_read_b64_tr_b16 a[196:197], v208 offset:17408
ds_read_b64_tr_b16 a[198:199], v208 offset:17664
v_mfma_f32_16x16x32_bf16 v[112:115], a[156:159], a[188:191], v[112:115]
s_add_u32 m0, s40, 2048
ds_read_b64_tr_b16 a[208:209], v208 offset:25600
ds_read_b64_tr_b16 a[210:211], v208 offset:25856
v_mfma_f32_16x16x32_bf16 v[120:123], a[144:147], a[8:11], v[120:123]
s_add_u32 s57, s68, 49152
buffer_load_dwordx4 v215, s[8:11], s57 offen lds
v_mfma_f32_16x16x32_bf16 v[120:123], a[156:159], a[12:15], v[120:123]
v_accvgpr_read_b32 v242, a58
v_accvgpr_read_b32 v243, a59
v_mfma_f32_16x16x32_bf16 v[100:103], a[168:171], a[200:203], v[100:103]
s_waitcnt lgkmcnt(8)
buffer_load_dwordx4 v216, s[8:11], s57 offen offset:1024 lds
v_mfma_f32_16x16x32_bf16 v[100:103], a[172:175], a[212:215], v[100:103]
v_mul_f32 v236, v224, v236
v_mul_f32 v237, v224, v237
v_mfma_f32_16x16x32_bf16 v[108:111], a[168:171], a[244:247], v[108:111]
v_mul_f32 v238, v224, v238
v_mul_f32 v239, v224, v239
v_mfma_f32_16x16x32_bf16 v[108:111], a[172:175], a[248:251], v[108:111]
v_mul_f32 v240, v224, v240
v_mul_f32 v241, v224, v241
v_mfma_f32_16x16x32_bf16 v[116:119], a[168:171], a[180:183], v[116:119]
s_waitcnt lgkmcnt(6)
ds_read_b64_tr_b16 a[144:145], v205 offset:17920
ds_read_b64_tr_b16 a[146:147], v205 offset:18176
v_mfma_f32_16x16x32_bf16 v[116:119], a[172:175], a[188:191], v[116:119]
ds_read_b64_tr_b16 a[156:157], v205 offset:26112
ds_read_b64_tr_b16 a[158:159], v205 offset:26368
v_mfma_f32_16x16x32_bf16 v[124:127], a[168:171], a[8:11], v[124:127]
ds_read_b64_tr_b16 a[236:237], v208 offset:26112
ds_read_b64_tr_b16 a[238:239], v208 offset:26368
v_mfma_f32_16x16x32_bf16 v[124:127], a[172:175], a[12:15], v[124:127]
ds_read_b64_tr_b16 a[168:169], v208 offset:17920
ds_read_b64_tr_b16 a[170:171], v208 offset:18176
v_mfma_f32_16x16x32_bf16 v[128:131], a[184:187], a[200:203], v[128:131]
s_waitcnt lgkmcnt(12)
v_mul_f32 v242, v224, v242
v_mul_f32 v243, v224, v243
v_mfma_f32_16x16x32_bf16 v[128:131], a[192:195], a[212:215], v[128:131]
v_cvt_pk_bf16_f32 v36, v236, v237
v_cvt_pk_bf16_f32 v37, v238, v239
v_mfma_f32_16x16x32_bf16 v[136:139], a[184:187], a[244:247], v[136:139]
v_cvt_pk_bf16_f32 v38, v240, v241
v_cvt_pk_bf16_f32 v39, v242, v243
v_mfma_f32_16x16x32_bf16 v[136:139], a[192:195], a[248:251], v[136:139]
s_xor_b32 s60, s45, 9216
buffer_store_dwordx4 v[36:39], v217, s[24:27], s60 offen sc1
v_mfma_f32_16x16x32_bf16 v[144:147], a[184:187], a[180:183], v[144:147]
s_waitcnt lgkmcnt(10)
v_accvgpr_read_b32 v244, a44
v_accvgpr_read_b32 v245, a45
v_mfma_f32_16x16x32_bf16 v[144:147], a[192:195], a[188:191], v[144:147]
v_accvgpr_read_b32 v246, a46
v_accvgpr_read_b32 v247, a47
v_mfma_f32_16x16x32_bf16 v[152:155], a[184:187], a[8:11], v[152:155]
s_lshl_b32 s58, s69, 12
v_accvgpr_read_b32 v248, a60
v_accvgpr_read_b32 v249, a61
v_mfma_f32_16x16x32_bf16 v[152:155], a[192:195], a[12:15], v[152:155]
s_add_u32 m0, s40, 81920
v_accvgpr_read_b32 v250, a62
v_accvgpr_read_b32 v251, a63
v_mfma_f32_16x16x32_bf16 v[132:135], a[196:199], a[200:203], v[132:135]
s_waitcnt vmcnt(20) lgkmcnt(0)
s_barrier
v_mfma_f32_16x16x32_bf16 v[132:135], a[208:211], a[212:215], v[132:135]
s_add_u32 s58, s58, s44
buffer_load_dwordx4 v213, s[12:15], s58 offen lds
v_mfma_f32_16x16x32_bf16 v[140:143], a[196:199], a[244:247], v[140:143]
ds_read_b128 a[240:243], v197 offset:18432
ds_read_b128 a[0:3], v197 offset:18944
v_mfma_f32_16x16x32_bf16 v[140:143], a[208:211], a[248:251], v[140:143]
ds_read_b64_tr_b16 a[192:193], v205 offset:0
ds_read_b64_tr_b16 a[194:195], v205 offset:256
v_mfma_f32_16x16x32_bf16 v[148:151], a[196:199], a[180:183], v[148:151]
s_waitcnt vmcnt(16)
ds_read_b128 a[172:175], v197 offset:16384
ds_read_b128 a[184:187], v197 offset:16896
v_mfma_f32_16x16x32_bf16 v[148:151], a[208:211], a[188:191], v[148:151]
ds_read_b64_tr_b16 a[4:5], v205 offset:8192
ds_read_b64_tr_b16 a[6:7], v205 offset:8448
v_mfma_f32_16x16x32_bf16 v[156:159], a[196:199], a[8:11], v[156:159]
buffer_load_dwordx4 v214, s[12:15], s58 offen offset:2048 lds
v_mfma_f32_16x16x32_bf16 v[156:159], a[208:211], a[12:15], v[156:159]
s_add_u32 m0, s40, 82944
ds_read_b128 a[16:19], v195 offset:34816
ds_read_b128 a[20:23], v195 offset:35328
v_mfma_f32_16x16x32_bf16 v[160:163], a[144:147], a[200:203], v[160:163]
ds_read_b128 a[196:199], v195 offset:32768
ds_read_b128 a[208:211], v195 offset:33280
v_mfma_f32_16x16x32_bf16 v[160:163], a[156:159], a[212:215], v[160:163]
s_add_u32 s59, s58, 128
buffer_load_dwordx4 v213, s[12:15], s59 offen lds
v_mfma_f32_16x16x32_bf16 v[168:171], a[144:147], a[244:247], v[168:171]
v_mul_f32 v244, v225, v244
v_mul_f32 v245, v225, v245
v_mfma_f32_16x16x32_bf16 v[168:171], a[156:159], a[248:251], v[168:171]
buffer_load_dwordx4 v214, s[12:15], s59 offen offset:2048 lds
v_mfma_f32_16x16x32_bf16 v[176:179], a[144:147], a[180:183], v[176:179]
ds_read_b64_tr_b16 a[24:25], v208 offset:0
ds_read_b64_tr_b16 a[26:27], v208 offset:256
v_mfma_f32_16x16x32_bf16 v[176:179], a[156:159], a[188:191], v[176:179]
ds_read_b64_tr_b16 a[28:29], v208 offset:8192
ds_read_b64_tr_b16 a[30:31], v208 offset:8448
v_mfma_f32_16x16x32_bf16 v[184:187], a[144:147], a[8:11], v[184:187]
v_mul_f32 v246, v225, v246
v_mul_f32 v247, v225, v247
v_mfma_f32_16x16x32_bf16 v[184:187], a[156:159], a[12:15], v[184:187]
s_add_u32 m0, s40, 147456
v_mul_f32 v248, v225, v248
v_mul_f32 v249, v225, v249
v_mfma_f32_16x16x32_bf16 v[164:167], a[168:171], a[200:203], v[164:167]
v_mul_f32 v250, v225, v250
v_mul_f32 v251, v225, v251
v_mfma_f32_16x16x32_bf16 v[164:167], a[236:239], a[212:215], v[164:167]
s_waitcnt lgkmcnt(10)
s_add_u32 s56, s69, 0
buffer_load_dwordx4 v215, s[8:11], s56 offen lds
v_cvt_pk_bf16_f32 v52, v244, v245
v_cvt_pk_bf16_f32 v53, v246, v247
v_cvt_pk_bf16_f32 v54, v248, v249
v_cvt_pk_bf16_f32 v55, v250, v251
v_mfma_f32_16x16x32_bf16 v[172:175], a[168:171], a[244:247], v[172:175]
s_xor_b32 s60, s45, 13312
buffer_store_dwordx4 v[52:55], v217, s[24:27], s60 offen sc1
v_mfma_f32_16x16x32_bf16 v[172:175], a[236:239], a[248:251], v[172:175]
buffer_load_dwordx4 v216, s[8:11], s56 offen offset:1024 lds
v_mfma_f32_16x16x32_bf16 v[180:183], a[168:171], a[180:183], v[180:183]
ds_read_b64_tr_b16 a[144:145], v205 offset:512
ds_read_b64_tr_b16 a[146:147], v205 offset:768
v_mfma_f32_16x16x32_bf16 v[180:183], a[236:239], a[188:191], v[180:183]
ds_read_b64_tr_b16 a[156:157], v205 offset:8704
ds_read_b64_tr_b16 a[158:159], v205 offset:8960
v_mfma_f32_16x16x32_bf16 v[188:191], a[168:171], a[8:11], v[188:191]
v_accvgpr_read_b32 v236, a64
v_accvgpr_read_b32 v237, a65
v_mfma_f32_16x16x32_bf16 v[188:191], a[236:239], a[12:15], v[188:191]
s_add_u32 m0, s40, 149504
v_accvgpr_read_b32 v238, a66
v_accvgpr_read_b32 v239, a67
v_mfma_f32_16x16x32_bf16 v[64:67], a[192:195], a[172:175], v[64:67]
s_waitcnt lgkmcnt(9)
v_accvgpr_read_b32 v240, a80
v_mfma_f32_16x16x32_bf16 v[64:67], a[4:7], a[184:187], v[64:67]
s_add_u32 s57, s69, 16384
buffer_load_dwordx4 v215, s[8:11], s57 offen lds
v_mfma_f32_16x16x32_bf16 v[72:75], a[192:195], a[240:243], v[72:75]
v_accvgpr_read_b32 v241, a81
v_accvgpr_read_b32 v242, a82
v_mfma_f32_16x16x32_bf16 v[72:75], a[4:7], a[0:3], v[72:75]
buffer_load_dwordx4 v216, s[8:11], s57 offen offset:1024 lds
v_mfma_f32_16x16x32_bf16 v[80:83], a[192:195], a[196:199], v[80:83]
s_waitcnt lgkmcnt(6)
ds_read_b64_tr_b16 a[168:169], v208 offset:512
ds_read_b64_tr_b16 a[170:171], v208 offset:768
v_mfma_f32_16x16x32_bf16 v[80:83], a[4:7], a[208:211], v[80:83]
ds_read_b64_tr_b16 a[180:181], v208 offset:8704
ds_read_b64_tr_b16 a[182:183], v208 offset:8960
v_mfma_f32_16x16x32_bf16 v[88:91], a[192:195], a[16:19], v[88:91]
v_accvgpr_read_b32 v243, a83
v_mul_f32 v236, v222, v236
v_mfma_f32_16x16x32_bf16 v[88:91], a[4:7], a[20:23], v[88:91]
s_add_u32 m0, s40, 32768
v_mul_f32 v237, v222, v237
v_mul_f32 v238, v222, v238
v_mfma_f32_16x16x32_bf16 v[68:71], a[24:27], a[172:175], v[68:71]
s_waitcnt lgkmcnt(8)
v_mul_f32 v239, v222, v239
v_mul_f32 v240, v222, v240
v_mfma_f32_16x16x32_bf16 v[68:71], a[28:31], a[184:187], v[68:71]
s_add_u32 s58, s69, 32768
buffer_load_dwordx4 v215, s[8:11], s58 offen lds
v_mfma_f32_16x16x32_bf16 v[76:79], a[24:27], a[240:243], v[76:79]
v_mul_f32 v241, v222, v241
v_mul_f32 v242, v222, v242
v_mfma_f32_16x16x32_bf16 v[76:79], a[28:31], a[0:3], v[76:79]
buffer_load_dwordx4 v216, s[8:11], s58 offen offset:1024 lds
v_mfma_f32_16x16x32_bf16 v[84:87], a[24:27], a[196:199], v[84:87]
s_waitcnt lgkmcnt(6)
ds_read_b64_tr_b16 a[188:189], v205 offset:1024
ds_read_b64_tr_b16 a[190:191], v205 offset:1280
v_mfma_f32_16x16x32_bf16 v[84:87], a[28:31], a[208:211], v[84:87]
ds_read_b64_tr_b16 a[192:193], v205 offset:9216
ds_read_b64_tr_b16 a[194:195], v205 offset:9472
v_mfma_f32_16x16x32_bf16 v[92:95], a[24:27], a[16:19], v[92:95]
v_mul_f32 v243, v222, v243
v_mfma_f32_16x16x32_bf16 v[92:95], a[28:31], a[20:23], v[92:95]
s_add_u32 m0, s40, 34816
v_cvt_pk_bf16_f32 v8, v236, v237
v_cvt_pk_bf16_f32 v9, v238, v239
v_mfma_f32_16x16x32_bf16 v[96:99], a[144:147], a[172:175], v[96:99]
s_waitcnt lgkmcnt(8)
v_cvt_pk_bf16_f32 v10, v240, v241
v_cvt_pk_bf16_f32 v11, v242, v243
v_mfma_f32_16x16x32_bf16 v[96:99], a[156:159], a[184:187], v[96:99]
s_add_u32 s59, s69, 49152
buffer_load_dwordx4 v215, s[8:11], s59 offen lds
v_mfma_f32_16x16x32_bf16 v[104:107], a[144:147], a[240:243], v[104:107]
s_xor_b32 s60, s45, 2048
buffer_store_dwordx4 v[8:11], v217, s[24:27], s60 offen sc1
v_mfma_f32_16x16x32_bf16 v[104:107], a[156:159], a[0:3], v[104:107]
buffer_load_dwordx4 v216, s[8:11], s59 offen offset:1024 lds
v_mfma_f32_16x16x32_bf16 v[112:115], a[144:147], a[196:199], v[112:115]
s_waitcnt lgkmcnt(6)
ds_read_b64_tr_b16 a[200:201], v208 offset:1024
ds_read_b64_tr_b16 a[202:203], v208 offset:1280
v_mfma_f32_16x16x32_bf16 v[112:115], a[156:159], a[208:211], v[112:115]
ds_read_b64_tr_b16 a[212:213], v208 offset:9216
ds_read_b64_tr_b16 a[214:215], v208 offset:9472
v_mfma_f32_16x16x32_bf16 v[120:123], a[144:147], a[16:19], v[120:123]
v_accvgpr_read_b32 v244, a68
v_mfma_f32_16x16x32_bf16 v[120:123], a[156:159], a[20:23], v[120:123]
v_accvgpr_read_b32 v245, a69
v_accvgpr_read_b32 v246, a70
v_mfma_f32_16x16x32_bf16 v[100:103], a[168:171], a[172:175], v[100:103]
s_waitcnt lgkmcnt(8)
v_accvgpr_read_b32 v247, a71
v_mfma_f32_16x16x32_bf16 v[100:103], a[180:183], a[184:187], v[100:103]
v_accvgpr_read_b32 v248, a84
v_accvgpr_read_b32 v249, a85
v_mfma_f32_16x16x32_bf16 v[108:111], a[168:171], a[240:243], v[108:111]
v_accvgpr_read_b32 v250, a86
v_mfma_f32_16x16x32_bf16 v[108:111], a[180:183], a[0:3], v[108:111]
v_accvgpr_read_b32 v251, a87
v_mul_f32 v244, v223, v244
v_mfma_f32_16x16x32_bf16 v[116:119], a[168:171], a[196:199], v[116:119]
s_waitcnt lgkmcnt(6)
ds_read_b64_tr_b16 a[144:145], v205 offset:1536
ds_read_b64_tr_b16 a[146:147], v205 offset:1792
v_mfma_f32_16x16x32_bf16 v[116:119], a[180:183], a[208:211], v[116:119]
ds_read_b64_tr_b16 a[156:157], v205 offset:9728
ds_read_b64_tr_b16 a[158:159], v205 offset:9984
v_mfma_f32_16x16x32_bf16 v[124:127], a[168:171], a[16:19], v[124:127]
ds_read_b64_tr_b16 a[236:237], v208 offset:9728
ds_read_b64_tr_b16 a[238:239], v208 offset:9984
v_mfma_f32_16x16x32_bf16 v[124:127], a[180:183], a[20:23], v[124:127]
ds_read_b64_tr_b16 a[168:169], v208 offset:1536
ds_read_b64_tr_b16 a[170:171], v208 offset:1792
v_mfma_f32_16x16x32_bf16 v[128:131], a[188:191], a[172:175], v[128:131]
s_waitcnt lgkmcnt(12)
v_mul_f32 v245, v223, v245
v_mfma_f32_16x16x32_bf16 v[128:131], a[192:195], a[184:187], v[128:131]
v_mul_f32 v246, v223, v246
v_mfma_f32_16x16x32_bf16 v[136:139], a[188:191], a[240:243], v[136:139]
v_mul_f32 v247, v223, v247
v_mul_f32 v248, v223, v248
v_mfma_f32_16x16x32_bf16 v[136:139], a[192:195], a[0:3], v[136:139]
v_mul_f32 v249, v223, v249
v_mfma_f32_16x16x32_bf16 v[144:147], a[188:191], a[196:199], v[144:147]
s_waitcnt lgkmcnt(10)
v_mul_f32 v250, v223, v250
v_mul_f32 v251, v223, v251
v_mfma_f32_16x16x32_bf16 v[144:147], a[192:195], a[208:211], v[144:147]
v_cvt_pk_bf16_f32 v24, v244, v245
v_mfma_f32_16x16x32_bf16 v[152:155], a[188:191], a[16:19], v[152:155]
s_lshl_b32 s56, s70, 12
v_cvt_pk_bf16_f32 v25, v246, v247
v_cvt_pk_bf16_f32 v26, v248, v249
v_mfma_f32_16x16x32_bf16 v[152:155], a[192:195], a[20:23], v[152:155]
s_add_u32 m0, s40, 65536
v_cvt_pk_bf16_f32 v27, v250, v251
v_mfma_f32_16x16x32_bf16 v[132:135], a[200:203], a[172:175], v[132:135]
s_waitcnt vmcnt(20) lgkmcnt(0)
s_barrier
v_mfma_f32_16x16x32_bf16 v[132:135], a[212:215], a[184:187], v[132:135]
s_add_u32 s56, s56, s44
buffer_load_dwordx4 v213, s[12:15], s56 offen lds
v_mfma_f32_16x16x32_bf16 v[140:143], a[200:203], a[240:243], v[140:143]
ds_read_b128 a[244:247], v196 offset:51200
ds_read_b128 a[248:251], v196 offset:51712
v_mfma_f32_16x16x32_bf16 v[140:143], a[212:215], a[0:3], v[140:143]
ds_read_b64_tr_b16 a[192:193], v204 offset:16384
ds_read_b64_tr_b16 a[194:195], v204 offset:16640
v_mfma_f32_16x16x32_bf16 v[148:151], a[200:203], a[196:199], v[148:151]
s_waitcnt vmcnt(16)
ds_read_b128 a[180:183], v196 offset:49152
ds_read_b128 a[188:191], v196 offset:49664
v_mfma_f32_16x16x32_bf16 v[148:151], a[212:215], a[208:211], v[148:151]
ds_read_b64_tr_b16 a[4:5], v204 offset:24576
ds_read_b64_tr_b16 a[6:7], v204 offset:24832
v_mfma_f32_16x16x32_bf16 v[156:159], a[200:203], a[16:19], v[156:159]
buffer_load_dwordx4 v214, s[12:15], s56 offen offset:2048 lds
v_mfma_f32_16x16x32_bf16 v[156:159], a[212:215], a[20:23], v[156:159]
s_add_u32 m0, s40, 66560
ds_read_b128 a[8:11], v195 offset:2048
ds_read_b128 a[12:15], v195 offset:2560
v_mfma_f32_16x16x32_bf16 v[160:163], a[144:147], a[172:175], v[160:163]
ds_read_b128 a[200:203], v195 offset:0
ds_read_b128 a[212:215], v195 offset:512
v_mfma_f32_16x16x32_bf16 v[160:163], a[156:159], a[184:187], v[160:163]
s_add_u32 s57, s56, 128
buffer_load_dwordx4 v213, s[12:15], s57 offen lds
v_mfma_f32_16x16x32_bf16 v[168:171], a[144:147], a[240:243], v[168:171]
s_xor_b32 s60, s45, 6144
buffer_store_dwordx4 v[24:27], v217, s[24:27], s60 offen sc1
v_mfma_f32_16x16x32_bf16 v[168:171], a[156:159], a[0:3], v[168:171]
buffer_load_dwordx4 v214, s[12:15], s57 offen offset:2048 lds
v_mfma_f32_16x16x32_bf16 v[176:179], a[144:147], a[196:199], v[176:179]
ds_read_b64_tr_b16 a[24:25], v207 offset:16384
ds_read_b64_tr_b16 a[26:27], v207 offset:16640
v_mfma_f32_16x16x32_bf16 v[176:179], a[156:159], a[208:211], v[176:179]
ds_read_b64_tr_b16 a[28:29], v207 offset:24576
ds_read_b64_tr_b16 a[30:31], v207 offset:24832
v_mfma_f32_16x16x32_bf16 v[184:187], a[144:147], a[16:19], v[184:187]
s_lshl_b32 s58, s71, 12
v_accvgpr_read_b32 v236, a72
v_accvgpr_read_b32 v237, a73
v_mfma_f32_16x16x32_bf16 v[184:187], a[156:159], a[20:23], v[184:187]
s_add_u32 m0, s40, 114688
v_accvgpr_read_b32 v238, a74
v_accvgpr_read_b32 v239, a75
v_mfma_f32_16x16x32_bf16 v[164:167], a[168:171], a[172:175], v[164:167]
v_accvgpr_read_b32 v240, a88
v_accvgpr_read_b32 v241, a89
v_mfma_f32_16x16x32_bf16 v[164:167], a[236:239], a[184:187], v[164:167]
s_waitcnt lgkmcnt(10)
s_add_u32 s58, s58, s44
buffer_load_dwordx4 v213, s[12:15], s58 offen lds
v_mfma_f32_16x16x32_bf16 v[172:175], a[168:171], a[240:243], v[172:175]
v_accvgpr_read_b32 v242, a90
v_accvgpr_read_b32 v243, a91
v_mfma_f32_16x16x32_bf16 v[172:175], a[236:239], a[0:3], v[172:175]
buffer_load_dwordx4 v214, s[12:15], s58 offen offset:2048 lds
v_mfma_f32_16x16x32_bf16 v[180:183], a[168:171], a[196:199], v[180:183]
ds_read_b64_tr_b16 a[144:145], v204 offset:16896
ds_read_b64_tr_b16 a[146:147], v204 offset:17152
v_mfma_f32_16x16x32_bf16 v[180:183], a[236:239], a[208:211], v[180:183]
ds_read_b64_tr_b16 a[156:157], v204 offset:25088
ds_read_b64_tr_b16 a[158:159], v204 offset:25344
v_mfma_f32_16x16x32_bf16 v[188:191], a[168:171], a[16:19], v[188:191]
s_xor_b32 s60, s45, 10240
v_mul_f32 v236, v224, v236
v_mul_f32 v237, v224, v237
v_mul_f32 v238, v224, v238
v_mul_f32 v239, v224, v239
v_mul_f32 v240, v224, v240
v_mul_f32 v241, v224, v241
v_mul_f32 v242, v224, v242
v_mul_f32 v243, v224, v243
v_cvt_pk_bf16_f32 v40, v236, v237
v_cvt_pk_bf16_f32 v41, v238, v239
v_cvt_pk_bf16_f32 v42, v240, v241
v_cvt_pk_bf16_f32 v43, v242, v243
v_mfma_f32_16x16x32_bf16 v[188:191], a[236:239], a[20:23], v[188:191]
s_add_u32 m0, s40, 115712
buffer_store_dwordx4 v[40:43], v217, s[24:27], s60 offen sc1
v_mfma_f32_16x16x32_bf16 v[64:67], a[192:195], a[180:183], v[64:67]
s_waitcnt lgkmcnt(9)
v_accvgpr_read_b32 v244, a76
v_accvgpr_read_b32 v245, a77
v_mfma_f32_16x16x32_bf16 v[64:67], a[4:7], a[188:191], v[64:67]
s_add_u32 s59, s58, 128
buffer_load_dwordx4 v213, s[12:15], s59 offen lds
v_mfma_f32_16x16x32_bf16 v[72:75], a[192:195], a[244:247], v[72:75]
v_accvgpr_read_b32 v246, a78
v_mfma_f32_16x16x32_bf16 v[72:75], a[4:7], a[248:251], v[72:75]
buffer_load_dwordx4 v214, s[12:15], s59 offen offset:2048 lds
v_mfma_f32_16x16x32_bf16 v[80:83], a[192:195], a[200:203], v[80:83]
s_waitcnt lgkmcnt(6)
ds_read_b64_tr_b16 a[168:169], v207 offset:16896
ds_read_b64_tr_b16 a[170:171], v207 offset:17152
v_mfma_f32_16x16x32_bf16 v[80:83], a[4:7], a[212:215], v[80:83]
s_add_u32 m0, s40, 0
ds_read_b64_tr_b16 a[172:173], v207 offset:25088
ds_read_b64_tr_b16 a[174:175], v207 offset:25344
v_mfma_f32_16x16x32_bf16 v[88:91], a[192:195], a[8:11], v[88:91]
s_xor_b32 s78, s42, 32768
buffer_load_dwordx4 v210, s[16:19], s78 offen lds
v_mfma_f32_16x16x32_bf16 v[88:91], a[4:7], a[12:15], v[88:91]
v_accvgpr_read_b32 v247, a79
v_accvgpr_read_b32 v248, a92
v_mfma_f32_16x16x32_bf16 v[68:71], a[24:27], a[180:183], v[68:71]
s_waitcnt lgkmcnt(8)
buffer_load_dwordx4 v211, s[16:19], s78 offen offset:2048 lds
v_mfma_f32_16x16x32_bf16 v[68:71], a[28:31], a[188:191], v[68:71]
v_accvgpr_read_b32 v249, a93
v_accvgpr_read_b32 v250, a94
v_mfma_f32_16x16x32_bf16 v[76:79], a[24:27], a[244:247], v[76:79]
v_accvgpr_read_b32 v251, a95
v_mfma_f32_16x16x32_bf16 v[76:79], a[28:31], a[248:251], v[76:79]
v_mul_f32 v244, v225, v244
v_mul_f32 v245, v225, v245
v_mfma_f32_16x16x32_bf16 v[84:87], a[24:27], a[200:203], v[84:87]
s_waitcnt lgkmcnt(6)
ds_read_b64_tr_b16 a[184:185], v204 offset:17408
ds_read_b64_tr_b16 a[186:187], v204 offset:17664
v_mfma_f32_16x16x32_bf16 v[84:87], a[28:31], a[212:215], v[84:87]
ds_read_b64_tr_b16 a[192:193], v204 offset:25600
ds_read_b64_tr_b16 a[194:195], v204 offset:25856
v_mfma_f32_16x16x32_bf16 v[92:95], a[24:27], a[8:11], v[92:95]
v_mul_f32 v246, v225, v246
v_mul_f32 v247, v225, v247
v_mfma_f32_16x16x32_bf16 v[92:95], a[28:31], a[12:15], v[92:95]
v_mul_f32 v248, v225, v248
v_mfma_f32_16x16x32_bf16 v[96:99], a[144:147], a[180:183], v[96:99]
s_waitcnt lgkmcnt(8)
v_mul_f32 v249, v225, v249
v_mul_f32 v250, v225, v250
v_mfma_f32_16x16x32_bf16 v[96:99], a[156:159], a[188:191], v[96:99]
v_mul_f32 v251, v225, v251
v_cvt_pk_bf16_f32 v56, v244, v245
v_mfma_f32_16x16x32_bf16 v[104:107], a[144:147], a[244:247], v[104:107]
v_cvt_pk_bf16_f32 v57, v246, v247
v_mfma_f32_16x16x32_bf16 v[104:107], a[156:159], a[248:251], v[104:107]
v_cvt_pk_bf16_f32 v58, v248, v249
v_cvt_pk_bf16_f32 v59, v250, v251
v_mfma_f32_16x16x32_bf16 v[112:115], a[144:147], a[200:203], v[112:115]
s_waitcnt lgkmcnt(6)
ds_read_b64_tr_b16 a[196:197], v207 offset:17408
ds_read_b64_tr_b16 a[198:199], v207 offset:17664
v_mfma_f32_16x16x32_bf16 v[112:115], a[156:159], a[212:215], v[112:115]
L_tch9_%=:
s_cbranch_execz L_tch13_%=
s_add_u32 m0, s40, 1024
ds_read_b64_tr_b16 a[208:209], v207 offset:25600
ds_read_b64_tr_b16 a[210:211], v207 offset:25856
v_mfma_f32_16x16x32_bf16 v[120:123], a[144:147], a[8:11], v[120:123]
s_xor_b32 s79, s42, 32896
buffer_load_dwordx4 v210, s[16:19], s79 offen lds
v_mfma_f32_16x16x32_bf16 v[120:123], a[156:159], a[12:15], v[120:123]
s_xor_b32 s60, s45, 14336
buffer_store_dwordx4 v[56:59], v217, s[24:27], s60 offen sc1
v_mfma_f32_16x16x32_bf16 v[100:103], a[168:171], a[180:183], v[100:103]
s_waitcnt lgkmcnt(8)
buffer_load_dwordx4 v211, s[16:19], s79 offen offset:2048 lds
v_mfma_f32_16x16x32_bf16 v[100:103], a[172:175], a[188:191], v[100:103]
v_accvgpr_read_b32 v236, a96
v_accvgpr_read_b32 v237, a97
v_mfma_f32_16x16x32_bf16 v[108:111], a[168:171], a[244:247], v[108:111]
v_accvgpr_read_b32 v238, a98
v_mfma_f32_16x16x32_bf16 v[108:111], a[172:175], a[248:251], v[108:111]
v_accvgpr_read_b32 v239, a99
v_accvgpr_read_b32 v240, a112
v_mfma_f32_16x16x32_bf16 v[116:119], a[168:171], a[200:203], v[116:119]
s_waitcnt lgkmcnt(6)
ds_read_b64_tr_b16 a[144:145], v204 offset:17920
ds_read_b64_tr_b16 a[146:147], v204 offset:18176
v_mfma_f32_16x16x32_bf16 v[116:119], a[172:175], a[212:215], v[116:119]
ds_read_b64_tr_b16 a[156:157], v204 offset:26112
ds_read_b64_tr_b16 a[158:159], v204 offset:26368
v_mfma_f32_16x16x32_bf16 v[124:127], a[168:171], a[8:11], v[124:127]
ds_read_b64_tr_b16 a[236:237], v207 offset:26112
ds_read_b64_tr_b16 a[238:239], v207 offset:26368
v_mfma_f32_16x16x32_bf16 v[124:127], a[172:175], a[12:15], v[124:127]
ds_read_b64_tr_b16 a[168:169], v207 offset:17920
ds_read_b64_tr_b16 a[170:171], v207 offset:18176
v_mfma_f32_16x16x32_bf16 v[128:131], a[184:187], a[180:183], v[128:131]
s_waitcnt lgkmcnt(12)
v_accvgpr_read_b32 v241, a113
v_accvgpr_read_b32 v242, a114
v_mfma_f32_16x16x32_bf16 v[128:131], a[192:195], a[188:191], v[128:131]
v_accvgpr_read_b32 v243, a115
v_mfma_f32_16x16x32_bf16 v[136:139], a[184:187], a[244:247], v[136:139]
v_mul_f32 v236, v222, v236
v_mul_f32 v237, v222, v237
v_mfma_f32_16x16x32_bf16 v[136:139], a[192:195], a[248:251], v[136:139]
v_mul_f32 v238, v222, v238
v_mul_f32 v239, v222, v239
v_mfma_f32_16x16x32_bf16 v[144:147], a[184:187], a[200:203], v[144:147]
s_waitcnt lgkmcnt(10)
v_mul_f32 v240, v222, v240
v_mfma_f32_16x16x32_bf16 v[144:147], a[192:195], a[212:215], v[144:147]
v_mul_f32 v241, v222, v241
v_mul_f32 v242, v222, v242
v_mfma_f32_16x16x32_bf16 v[152:155], a[184:187], a[8:11], v[152:155]
v_mul_f32 v243, v222, v243
v_cvt_pk_bf16_f32 v12, v236, v237
v_mfma_f32_16x16x32_bf16 v[152:155], a[192:195], a[12:15], v[152:155]
v_cvt_pk_bf16_f32 v13, v238, v239
v_mfma_f32_16x16x32_bf16 v[132:135], a[196:199], a[180:183], v[132:135]
s_waitcnt vmcnt(20) lgkmcnt(0)
s_barrier
v_mfma_f32_16x16x32_bf16 v[132:135], a[208:211], a[188:191], v[132:135]
v_cvt_pk_bf16_f32 v14, v240, v241
v_cvt_pk_bf16_f32 v15, v242, v243
v_mfma_f32_16x16x32_bf16 v[140:143], a[196:199], a[244:247], v[140:143]
ds_read_b128 a[240:243], v197 offset:18432
ds_read_b128 a[0:3], v197 offset:18944
v_mfma_f32_16x16x32_bf16 v[140:143], a[208:211], a[248:251], v[140:143]
ds_read_b64_tr_b16 a[192:193], v205 offset:16384
ds_read_b64_tr_b16 a[194:195], v205 offset:16640
v_mfma_f32_16x16x32_bf16 v[148:151], a[196:199], a[200:203], v[148:151]
s_waitcnt vmcnt(15)
ds_read_b128 a[172:175], v197 offset:16384
ds_read_b128 a[184:187], v197 offset:16896
v_mfma_f32_16x16x32_bf16 v[148:151], a[208:211], a[212:215], v[148:151]
ds_read_b64_tr_b16 a[4:5], v205 offset:24576
ds_read_b64_tr_b16 a[6:7], v205 offset:24832
v_mfma_f32_16x16x32_bf16 v[156:159], a[196:199], a[8:11], v[156:159]
s_xor_b32 s60, s45, 3072
buffer_store_dwordx4 v[12:15], v217, s[24:27], s60 offen sc1
v_mfma_f32_16x16x32_bf16 v[156:159], a[208:211], a[12:15], v[156:159]
ds_read_b128 a[16:19], v195 offset:34816
ds_read_b128 a[20:23], v195 offset:35328
v_mfma_f32_16x16x32_bf16 v[160:163], a[144:147], a[180:183], v[160:163]
ds_read_b128 a[196:199], v195 offset:32768
ds_read_b128 a[208:211], v195 offset:33280
v_mfma_f32_16x16x32_bf16 v[160:163], a[156:159], a[188:191], v[160:163]
v_accvgpr_read_b32 v244, a100
v_accvgpr_read_b32 v245, a101
v_mfma_f32_16x16x32_bf16 v[168:171], a[144:147], a[244:247], v[168:171]
v_accvgpr_read_b32 v246, a102
v_mfma_f32_16x16x32_bf16 v[168:171], a[156:159], a[248:251], v[168:171]
v_accvgpr_read_b32 v247, a103
v_accvgpr_read_b32 v248, a116
v_mfma_f32_16x16x32_bf16 v[176:179], a[144:147], a[200:203], v[176:179]
ds_read_b64_tr_b16 a[24:25], v208 offset:16384
ds_read_b64_tr_b16 a[26:27], v208 offset:16640
v_mfma_f32_16x16x32_bf16 v[176:179], a[156:159], a[212:215], v[176:179]
ds_read_b64_tr_b16 a[28:29], v208 offset:24576
ds_read_b64_tr_b16 a[30:31], v208 offset:24832
v_mfma_f32_16x16x32_bf16 v[184:187], a[144:147], a[8:11], v[184:187]
v_accvgpr_read_b32 v249, a117
v_mfma_f32_16x16x32_bf16 v[184:187], a[156:159], a[12:15], v[184:187]
v_accvgpr_read_b32 v250, a118
v_accvgpr_read_b32 v251, a119
v_mfma_f32_16x16x32_bf16 v[164:167], a[168:171], a[180:183], v[164:167]
v_mul_f32 v244, v223, v244
v_mfma_f32_16x16x32_bf16 v[164:167], a[236:239], a[188:191], v[164:167]
v_mul_f32 v245, v223, v245
v_mul_f32 v246, v223, v246
v_mfma_f32_16x16x32_bf16 v[172:175], a[168:171], a[244:247], v[172:175]
v_mul_f32 v247, v223, v247
v_mfma_f32_16x16x32_bf16 v[172:175], a[236:239], a[248:251], v[172:175]
v_mul_f32 v248, v223, v248
v_mul_f32 v249, v223, v249
v_mfma_f32_16x16x32_bf16 v[180:183], a[168:171], a[200:203], v[180:183]
s_waitcnt lgkmcnt(11)
ds_read_b64_tr_b16 a[144:145], v205 offset:16896
ds_read_b64_tr_b16 a[146:147], v205 offset:17152
v_mfma_f32_16x16x32_bf16 v[180:183], a[236:239], a[212:215], v[180:183]
s_add_u32 m0, s40, 16384
ds_read_b64_tr_b16 a[156:157], v205 offset:25088
ds_read_b64_tr_b16 a[158:159], v205 offset:25344
v_mfma_f32_16x16x32_bf16 v[188:191], a[168:171], a[8:11], v[188:191]
s_xor_b32 s78, s42, 16384
buffer_load_dwordx4 v212, s[20:23], s78 offen lds
v_mfma_f32_16x16x32_bf16 v[188:191], a[236:239], a[12:15], v[188:191]
v_mul_f32 v250, v223, v250
v_mfma_f32_16x16x32_bf16 v[64:67], a[192:195], a[172:175], v[64:67]
s_waitcnt lgkmcnt(9)
buffer_load_dwordx4 v212, s[20:23], s78 offen offset:2048 lds
v_mfma_f32_16x16x32_bf16 v[64:67], a[4:7], a[184:187], v[64:67]
v_mul_f32 v251, v223, v251
v_cvt_pk_bf16_f32 v28, v244, v245
v_mfma_f32_16x16x32_bf16 v[72:75], a[192:195], a[240:243], v[72:75]
v_cvt_pk_bf16_f32 v29, v246, v247
v_mfma_f32_16x16x32_bf16 v[72:75], a[4:7], a[0:3], v[72:75]
v_cvt_pk_bf16_f32 v30, v248, v249
v_cvt_pk_bf16_f32 v31, v250, v251
v_mfma_f32_16x16x32_bf16 v[80:83], a[192:195], a[196:199], v[80:83]
s_waitcnt lgkmcnt(6)
ds_read_b64_tr_b16 a[168:169], v208 offset:16896
ds_read_b64_tr_b16 a[170:171], v208 offset:17152
v_mfma_f32_16x16x32_bf16 v[80:83], a[4:7], a[208:211], v[80:83]
ds_read_b64_tr_b16 a[180:181], v208 offset:25088
ds_read_b64_tr_b16 a[182:183], v208 offset:25344
v_mfma_f32_16x16x32_bf16 v[88:91], a[192:195], a[16:19], v[88:91]
s_xor_b32 s60, s45, 7168
buffer_store_dwordx4 v[28:31], v217, s[24:27], s60 offen sc1
v_mfma_f32_16x16x32_bf16 v[88:91], a[4:7], a[20:23], v[88:91]
v_accvgpr_read_b32 v236, a104
v_mfma_f32_16x16x32_bf16 v[68:71], a[24:27], a[172:175], v[68:71]
s_waitcnt lgkmcnt(8)
v_accvgpr_read_b32 v237, a105
v_accvgpr_read_b32 v238, a106
v_mfma_f32_16x16x32_bf16 v[68:71], a[28:31], a[184:187], v[68:71]
v_accvgpr_read_b32 v239, a107
v_mfma_f32_16x16x32_bf16 v[76:79], a[24:27], a[240:243], v[76:79]
v_accvgpr_read_b32 v240, a120
v_mfma_f32_16x16x32_bf16 v[76:79], a[28:31], a[0:3], v[76:79]
v_accvgpr_read_b32 v241, a121
v_mfma_f32_16x16x32_bf16 v[84:87], a[24:27], a[196:199], v[84:87]
s_waitcnt lgkmcnt(6)
ds_read_b64_tr_b16 a[188:189], v205 offset:17408
ds_read_b64_tr_b16 a[190:191], v205 offset:17664
v_mfma_f32_16x16x32_bf16 v[84:87], a[28:31], a[208:211], v[84:87]
ds_read_b64_tr_b16 a[192:193], v205 offset:25600
ds_read_b64_tr_b16 a[194:195], v205 offset:25856
v_mfma_f32_16x16x32_bf16 v[92:95], a[24:27], a[16:19], v[92:95]
v_accvgpr_read_b32 v242, a122
v_accvgpr_read_b32 v243, a123
v_mfma_f32_16x16x32_bf16 v[92:95], a[28:31], a[20:23], v[92:95]
v_mul_f32 v236, v224, v236
v_mfma_f32_16x16x32_bf16 v[96:99], a[144:147], a[172:175], v[96:99]
s_waitcnt lgkmcnt(8)
v_mul_f32 v237, v224, v237
v_mfma_f32_16x16x32_bf16 v[96:99], a[156:159], a[184:187], v[96:99]
v_mul_f32 v238, v224, v238
v_mfma_f32_16x16x32_bf16 v[104:107], a[144:147], a[240:243], v[104:107]
v_mul_f32 v239, v224, v239
v_mul_f32 v240, v224, v240
v_mfma_f32_16x16x32_bf16 v[104:107], a[156:159], a[0:3], v[104:107]
v_mul_f32 v241, v224, v241
v_mfma_f32_16x16x32_bf16 v[112:115], a[144:147], a[196:199], v[112:115]
s_waitcnt lgkmcnt(6)
ds_read_b64_tr_b16 a[200:201], v208 offset:17408
ds_read_b64_tr_b16 a[202:203], v208 offset:17664
v_mfma_f32_16x16x32_bf16 v[112:115], a[156:159], a[208:211], v[112:115]
ds_read_b64_tr_b16 a[212:213], v208 offset:25600
ds_read_b64_tr_b16 a[214:215], v208 offset:25856
v_mfma_f32_16x16x32_bf16 v[120:123], a[144:147], a[16:19], v[120:123]
v_mul_f32 v242, v224, v242
v_mfma_f32_16x16x32_bf16 v[120:123], a[156:159], a[20:23], v[120:123]
v_mul_f32 v243, v224, v243
v_mfma_f32_16x16x32_bf16 v[100:103], a[168:171], a[172:175], v[100:103]
s_waitcnt lgkmcnt(8)
v_cvt_pk_bf16_f32 v44, v236, v237
v_cvt_pk_bf16_f32 v45, v238, v239
v_mfma_f32_16x16x32_bf16 v[100:103], a[180:183], a[184:187], v[100:103]
v_cvt_pk_bf16_f32 v46, v240, v241
v_mfma_f32_16x16x32_bf16 v[108:111], a[168:171], a[240:243], v[108:111]
v_cvt_pk_bf16_f32 v47, v242, v243
v_mfma_f32_16x16x32_bf16 v[108:111], a[180:183], a[0:3], v[108:111]
s_xor_b32 s60, s45, 11264
buffer_store_dwordx4 v[44:47], v217, s[24:27], s60 offen sc1
v_mfma_f32_16x16x32_bf16 v[116:119], a[168:171], a[196:199], v[116:119]
s_waitcnt lgkmcnt(6)
ds_read_b64_tr_b16 a[144:145], v205 offset:17920
ds_read_b64_tr_b16 a[146:147], v205 offset:18176
v_mfma_f32_16x16x32_bf16 v[116:119], a[180:183], a[208:211], v[116:119]
ds_read_b64_tr_b16 a[156:157], v205 offset:26112
ds_read_b64_tr_b16 a[158:159], v205 offset:26368
v_mfma_f32_16x16x32_bf16 v[124:127], a[168:171], a[16:19], v[124:127]
ds_read_b64_tr_b16 a[236:237], v208 offset:26112
ds_read_b64_tr_b16 a[238:239], v208 offset:26368
v_mfma_f32_16x16x32_bf16 v[124:127], a[180:183], a[20:23], v[124:127]
ds_read_b64_tr_b16 a[168:169], v208 offset:17920
ds_read_b64_tr_b16 a[170:171], v208 offset:18176
v_mfma_f32_16x16x32_bf16 v[128:131], a[188:191], a[172:175], v[128:131]
s_waitcnt lgkmcnt(12)
v_accvgpr_read_b32 v244, a108
v_accvgpr_read_b32 v245, a109
v_mfma_f32_16x16x32_bf16 v[128:131], a[192:195], a[184:187], v[128:131]
v_accvgpr_read_b32 v246, a110
v_accvgpr_read_b32 v247, a111
v_mfma_f32_16x16x32_bf16 v[136:139], a[188:191], a[240:243], v[136:139]
v_accvgpr_read_b32 v248, a124
v_accvgpr_read_b32 v249, a125
v_mfma_f32_16x16x32_bf16 v[136:139], a[192:195], a[0:3], v[136:139]
v_accvgpr_read_b32 v250, a126
v_accvgpr_read_b32 v251, a127
v_mfma_f32_16x16x32_bf16 v[144:147], a[188:191], a[196:199], v[144:147]
s_waitcnt lgkmcnt(10)
v_mul_f32 v244, v225, v244
v_mul_f32 v245, v225, v245
v_mfma_f32_16x16x32_bf16 v[144:147], a[192:195], a[208:211], v[144:147]
s_add_u32 m0, s40, 17408
v_mul_f32 v246, v225, v246
v_mul_f32 v247, v225, v247
v_mfma_f32_16x16x32_bf16 v[152:155], a[188:191], a[16:19], v[152:155]
s_xor_b32 s79, s42, 16512
buffer_load_dwordx4 v212, s[20:23], s79 offen lds
v_mfma_f32_16x16x32_bf16 v[152:155], a[192:195], a[20:23], v[152:155]
v_mul_f32 v248, v225, v248
v_mul_f32 v249, v225, v249
v_mfma_f32_16x16x32_bf16 v[132:135], a[200:203], a[172:175], v[132:135]
s_waitcnt vmcnt(11) lgkmcnt(0)
s_barrier
v_mfma_f32_16x16x32_bf16 v[132:135], a[212:215], a[184:187], v[132:135]
buffer_load_dwordx4 v212, s[20:23], s79 offen offset:2048 lds
v_mfma_f32_16x16x32_bf16 v[140:143], a[200:203], a[240:243], v[140:143]
v_mul_f32 v250, v225, v250
v_mul_f32 v251, v225, v251
v_mfma_f32_16x16x32_bf16 v[140:143], a[212:215], a[0:3], v[140:143]
v_cvt_pk_bf16_f32 v60, v244, v245
v_cvt_pk_bf16_f32 v61, v246, v247
v_mfma_f32_16x16x32_bf16 v[148:151], a[200:203], a[196:199], v[148:151]
ds_read_b64_tr_b16 a[180:181], v205 offset:0
ds_read_b64_tr_b16 a[182:183], v205 offset:256
v_mfma_f32_16x16x32_bf16 v[148:151], a[212:215], a[208:211], v[148:151]
ds_read_b64_tr_b16 a[188:189], v205 offset:8192
ds_read_b64_tr_b16 a[190:191], v205 offset:8448
v_mfma_f32_16x16x32_bf16 v[156:159], a[200:203], a[16:19], v[156:159]
ds_read_b64_tr_b16 a[192:193], v205 offset:49152
ds_read_b64_tr_b16 a[194:195], v205 offset:49408
v_mfma_f32_16x16x32_bf16 v[156:159], a[212:215], a[20:23], v[156:159]
ds_read_b64_tr_b16 a[200:201], v205 offset:57344
ds_read_b64_tr_b16 a[202:203], v205 offset:57600
v_mfma_f32_16x16x32_bf16 v[160:163], a[144:147], a[172:175], v[160:163]
ds_read_b64_tr_b16 a[212:213], v208 offset:0
ds_read_b64_tr_b16 a[214:215], v208 offset:256
v_mfma_f32_16x16x32_bf16 v[160:163], a[156:159], a[184:187], v[160:163]
ds_read_b64_tr_b16 a[244:245], v208 offset:8192
ds_read_b64_tr_b16 a[246:247], v208 offset:8448
v_cvt_pk_bf16_f32 v62, v248, v249
v_cvt_pk_bf16_f32 v63, v250, v251
v_mfma_f32_16x16x32_bf16 v[168:171], a[144:147], a[240:243], v[168:171]
ds_read_b64_tr_b16 a[248:249], v208 offset:49152
ds_read_b64_tr_b16 a[250:251], v208 offset:49408
v_mfma_f32_16x16x32_bf16 v[168:171], a[156:159], a[0:3], v[168:171]
s_xor_b32 s60, s45, 15360
ds_read_b64_tr_b16 a[4:5], v208 offset:57344
ds_read_b64_tr_b16 a[6:7], v208 offset:57600
v_mfma_f32_16x16x32_bf16 v[176:179], a[144:147], a[196:199], v[176:179]
buffer_store_dwordx4 v[60:63], v217, s[24:27], s60 offen sc1
v_mfma_f32_16x16x32_bf16 v[176:179], a[156:159], a[208:211], v[176:179]
v_mfma_f32_16x16x32_bf16 v[184:187], a[144:147], a[16:19], v[184:187]
v_mfma_f32_16x16x32_bf16 v[184:187], a[156:159], a[20:23], v[184:187]
v_mfma_f32_16x16x32_bf16 v[164:167], a[168:171], a[172:175], v[164:167]
v_mfma_f32_16x16x32_bf16 v[164:167], a[236:239], a[184:187], v[164:167]
v_mfma_f32_16x16x32_bf16 v[172:175], a[168:171], a[240:243], v[172:175]
v_mfma_f32_16x16x32_bf16 v[172:175], a[236:239], a[0:3], v[172:175]
v_mfma_f32_16x16x32_bf16 v[180:183], a[168:171], a[196:199], v[180:183]
s_waitcnt lgkmcnt(14)
v_mfma_f32_16x16x32_bf16 v[180:183], a[236:239], a[208:211], v[180:183]
v_mfma_f32_16x16x32_bf16 v[188:191], a[168:171], a[16:19], v[188:191]
v_mfma_f32_16x16x32_bf16 v[188:191], a[236:239], a[20:23], v[188:191]
v_mfma_f32_16x16x32_bf16 v[64:67], a[180:183], a[128:131], v[64:67]
s_waitcnt lgkmcnt(6)
v_mfma_f32_16x16x32_bf16 v[64:67], a[188:191], a[224:227], v[64:67]
v_mfma_f32_16x16x32_bf16 v[64:67], a[192:195], a[136:139], v[64:67]
v_mfma_f32_16x16x32_bf16 v[64:67], a[200:203], a[204:207], v[64:67]
v_mfma_f32_16x16x32_bf16 v[68:71], a[212:215], a[128:131], v[68:71]
s_waitcnt lgkmcnt(0)
v_mfma_f32_16x16x32_bf16 v[68:71], a[244:247], a[224:227], v[68:71]
v_mfma_f32_16x16x32_bf16 v[68:71], a[248:251], a[136:139], v[68:71]
v_mfma_f32_16x16x32_bf16 v[68:71], a[4:7], a[204:207], v[68:71]
v_mfma_f32_16x16x32_bf16 v[72:75], a[180:183], a[228:231], v[72:75]
v_mfma_f32_16x16x32_bf16 v[72:75], a[188:191], a[232:235], v[72:75]
v_mfma_f32_16x16x32_bf16 v[72:75], a[192:195], a[216:219], v[72:75]
v_mul_f32 v64, s47, v64
v_mul_f32 v65, s47, v65
v_mfma_f32_16x16x32_bf16 v[72:75], a[200:203], a[220:223], v[72:75]
v_mul_f32 v66, s47, v66
v_mul_f32 v67, s47, v67
v_mfma_f32_16x16x32_bf16 v[76:79], a[212:215], a[228:231], v[76:79]
ds_read_b64_tr_b16 a[144:145], v205 offset:512
ds_read_b64_tr_b16 a[146:147], v205 offset:768
v_mfma_f32_16x16x32_bf16 v[76:79], a[244:247], a[232:235], v[76:79]
ds_read_b64_tr_b16 a[156:157], v205 offset:8704
ds_read_b64_tr_b16 a[158:159], v205 offset:8960
v_mfma_f32_16x16x32_bf16 v[76:79], a[248:251], a[216:219], v[76:79]
ds_read_b64_tr_b16 a[168:169], v205 offset:49664
ds_read_b64_tr_b16 a[170:171], v205 offset:49920
v_mfma_f32_16x16x32_bf16 v[76:79], a[4:7], a[220:223], v[76:79]
ds_read_b64_tr_b16 a[172:173], v205 offset:57856
ds_read_b64_tr_b16 a[174:175], v205 offset:58112
v_mfma_f32_16x16x32_bf16 v[80:83], a[180:183], a[148:151], v[80:83]
ds_read_b64_tr_b16 a[184:185], v208 offset:512
ds_read_b64_tr_b16 a[186:187], v208 offset:768
v_mfma_f32_16x16x32_bf16 v[80:83], a[188:191], a[160:163], v[80:83]
ds_read_b64_tr_b16 a[196:197], v208 offset:8704
ds_read_b64_tr_b16 a[198:199], v208 offset:8960
v_mfma_f32_16x16x32_bf16 v[80:83], a[192:195], a[132:135], v[80:83]
s_xor_b32 s78, s42, 49152
ds_read_b64_tr_b16 a[208:209], v208 offset:49664
ds_read_b64_tr_b16 a[210:211], v208 offset:49920
v_mfma_f32_16x16x32_bf16 v[80:83], a[200:203], a[252:255], v[80:83]
s_add_u32 m0, s40, 147456
ds_read_b64_tr_b16 a[236:237], v208 offset:57856
ds_read_b64_tr_b16 a[238:239], v208 offset:58112
v_mfma_f32_16x16x32_bf16 v[84:87], a[212:215], a[148:151], v[84:87]
buffer_load_dwordx4 v210, s[16:19], s78 offen lds
v_mfma_f32_16x16x32_bf16 v[84:87], a[244:247], a[160:163], v[84:87]
v_mul_f32 v68, s47, v68
v_mul_f32 v69, s47, v69
v_mfma_f32_16x16x32_bf16 v[84:87], a[248:251], a[132:135], v[84:87]
buffer_load_dwordx4 v211, s[16:19], s78 offen offset:2048 lds
v_mfma_f32_16x16x32_bf16 v[84:87], a[4:7], a[252:255], v[84:87]
v_mul_f32 v70, s47, v70
v_mul_f32 v71, s47, v71
v_mfma_f32_16x16x32_bf16 v[88:91], a[180:183], a[164:167], v[88:91]
v_cvt_pk_bf16_f32 v0, v64, v65
v_cvt_pk_bf16_f32 v1, v66, v67
v_mfma_f32_16x16x32_bf16 v[88:91], a[188:191], a[176:179], v[88:91]
v_cvt_pk_bf16_f32 v2, v68, v69
v_cvt_pk_bf16_f32 v3, v70, v71
v_mfma_f32_16x16x32_bf16 v[88:91], a[192:195], a[140:143], v[88:91]
v_permlane16_swap_b32 v0, v2
v_permlane16_swap_b32 v1, v3
v_mfma_f32_16x16x32_bf16 v[88:91], a[200:203], a[152:155], v[88:91]
v_mul_f32 v72, s47, v72
v_mul_f32 v73, s47, v73
v_mfma_f32_16x16x32_bf16 v[92:95], a[212:215], a[164:167], v[92:95]
s_waitcnt lgkmcnt(14)
v_mul_f32 v74, s47, v74
v_mul_f32 v75, s47, v75
v_mfma_f32_16x16x32_bf16 v[92:95], a[244:247], a[176:179], v[92:95]
v_mul_f32 v76, s47, v76
v_mul_f32 v77, s47, v77
v_mfma_f32_16x16x32_bf16 v[92:95], a[248:251], a[140:143], v[92:95]
v_mul_f32 v78, s47, v78
v_mul_f32 v79, s47, v79
v_mfma_f32_16x16x32_bf16 v[92:95], a[4:7], a[152:155], v[92:95]
v_cvt_pk_bf16_f32 v16, v72, v73
v_cvt_pk_bf16_f32 v17, v74, v75
v_mfma_f32_16x16x32_bf16 v[96:99], a[144:147], a[128:131], v[96:99]
s_waitcnt lgkmcnt(6)
v_cvt_pk_bf16_f32 v18, v76, v77
v_cvt_pk_bf16_f32 v19, v78, v79
v_mfma_f32_16x16x32_bf16 v[96:99], a[156:159], a[224:227], v[96:99]
v_permlane16_swap_b32 v16, v18
v_permlane16_swap_b32 v17, v19
v_mfma_f32_16x16x32_bf16 v[96:99], a[168:171], a[136:139], v[96:99]
v_mul_f32 v80, s47, v80
v_mul_f32 v81, s47, v81
v_mfma_f32_16x16x32_bf16 v[96:99], a[172:175], a[204:207], v[96:99]
v_mul_f32 v82, s47, v82
v_mul_f32 v83, s47, v83
v_mfma_f32_16x16x32_bf16 v[100:103], a[184:187], a[128:131], v[100:103]
s_waitcnt lgkmcnt(0)
v_mul_f32 v84, s47, v84
v_mul_f32 v85, s47, v85
v_mfma_f32_16x16x32_bf16 v[100:103], a[196:199], a[224:227], v[100:103]
v_mul_f32 v86, s47, v86
v_mul_f32 v87, s47, v87
v_mfma_f32_16x16x32_bf16 v[100:103], a[208:211], a[136:139], v[100:103]
v_cvt_pk_bf16_f32 v32, v80, v81
v_cvt_pk_bf16_f32 v33, v82, v83
v_mfma_f32_16x16x32_bf16 v[100:103], a[236:239], a[204:207], v[100:103]
v_cvt_pk_bf16_f32 v34, v84, v85
v_cvt_pk_bf16_f32 v35, v86, v87
v_mfma_f32_16x16x32_bf16 v[104:107], a[144:147], a[228:231], v[104:107]
v_permlane16_swap_b32 v32, v34
v_permlane16_swap_b32 v33, v35
v_mfma_f32_16x16x32_bf16 v[104:107], a[156:159], a[232:235], v[104:107]
v_mul_f32 v88, s47, v88
v_mul_f32 v89, s47, v89
v_mfma_f32_16x16x32_bf16 v[104:107], a[168:171], a[216:219], v[104:107]
v_mul_f32 v90, s47, v90
v_mul_f32 v91, s47, v91
v_mfma_f32_16x16x32_bf16 v[104:107], a[172:175], a[220:223], v[104:107]
v_mul_f32 v92, s47, v92
v_mul_f32 v93, s47, v93
v_mfma_f32_16x16x32_bf16 v[108:111], a[184:187], a[228:231], v[108:111]
ds_read_b64_tr_b16 a[180:181], v205 offset:1024
ds_read_b64_tr_b16 a[182:183], v205 offset:1280
v_mfma_f32_16x16x32_bf16 v[108:111], a[196:199], a[232:235], v[108:111]
ds_read_b64_tr_b16 a[188:189], v205 offset:9216
ds_read_b64_tr_b16 a[190:191], v205 offset:9472
v_mfma_f32_16x16x32_bf16 v[108:111], a[208:211], a[216:219], v[108:111]
ds_read_b64_tr_b16 a[192:193], v205 offset:50176
ds_read_b64_tr_b16 a[194:195], v205 offset:50432
v_mfma_f32_16x16x32_bf16 v[108:111], a[236:239], a[220:223], v[108:111]
ds_read_b64_tr_b16 a[200:201], v205 offset:58368
ds_read_b64_tr_b16 a[202:203], v205 offset:58624
v_mfma_f32_16x16x32_bf16 v[112:115], a[144:147], a[148:151], v[112:115]
ds_read_b64_tr_b16 a[212:213], v208 offset:1024
ds_read_b64_tr_b16 a[214:215], v208 offset:1280
v_mfma_f32_16x16x32_bf16 v[112:115], a[156:159], a[160:163], v[112:115]
ds_read_b64_tr_b16 a[240:241], v208 offset:9216
ds_read_b64_tr_b16 a[242:243], v208 offset:9472
v_mfma_f32_16x16x32_bf16 v[112:115], a[168:171], a[132:135], v[112:115]
ds_read_b64_tr_b16 a[244:245], v208 offset:50176
ds_read_b64_tr_b16 a[246:247], v208 offset:50432
v_mfma_f32_16x16x32_bf16 v[112:115], a[172:175], a[252:255], v[112:115]
ds_read_b64_tr_b16 a[248:249], v208 offset:58368
ds_read_b64_tr_b16 a[250:251], v208 offset:58624
v_mfma_f32_16x16x32_bf16 v[116:119], a[184:187], a[148:151], v[116:119]
v_mul_f32 v94, s47, v94
v_mul_f32 v95, s47, v95
v_mfma_f32_16x16x32_bf16 v[116:119], a[196:199], a[160:163], v[116:119]
v_cvt_pk_bf16_f32 v48, v88, v89
v_cvt_pk_bf16_f32 v49, v90, v91
v_mfma_f32_16x16x32_bf16 v[116:119], a[208:211], a[132:135], v[116:119]
v_cvt_pk_bf16_f32 v50, v92, v93
v_cvt_pk_bf16_f32 v51, v94, v95
v_mfma_f32_16x16x32_bf16 v[116:119], a[236:239], a[252:255], v[116:119]
v_permlane16_swap_b32 v48, v50
v_permlane16_swap_b32 v49, v51
v_mfma_f32_16x16x32_bf16 v[120:123], a[144:147], a[164:167], v[120:123]
v_mul_f32 v96, s47, v96
v_mul_f32 v97, s47, v97
v_mfma_f32_16x16x32_bf16 v[120:123], a[156:159], a[176:179], v[120:123]
v_mul_f32 v98, s47, v98
v_mul_f32 v99, s47, v99
v_mfma_f32_16x16x32_bf16 v[120:123], a[168:171], a[140:143], v[120:123]
s_xor_b32 s79, s42, 49280
v_mul_f32 v100, s47, v100
v_mul_f32 v101, s47, v101
v_mfma_f32_16x16x32_bf16 v[120:123], a[172:175], a[152:155], v[120:123]
s_add_u32 m0, s40, 148480
v_mul_f32 v102, s47, v102
v_mul_f32 v103, s47, v103
v_mfma_f32_16x16x32_bf16 v[124:127], a[184:187], a[164:167], v[124:127]
s_waitcnt lgkmcnt(14)
buffer_load_dwordx4 v210, s[16:19], s79 offen lds
v_mfma_f32_16x16x32_bf16 v[124:127], a[196:199], a[176:179], v[124:127]
v_cvt_pk_bf16_f32 v4, v96, v97
v_cvt_pk_bf16_f32 v5, v98, v99
v_mfma_f32_16x16x32_bf16 v[124:127], a[208:211], a[140:143], v[124:127]
buffer_load_dwordx4 v211, s[16:19], s79 offen offset:2048 lds
v_mfma_f32_16x16x32_bf16 v[124:127], a[236:239], a[152:155], v[124:127]
v_cvt_pk_bf16_f32 v6, v100, v101
v_cvt_pk_bf16_f32 v7, v102, v103
v_mfma_f32_16x16x32_bf16 v[128:131], a[180:183], a[128:131], v[128:131]
s_waitcnt lgkmcnt(6)
v_permlane16_swap_b32 v4, v6
v_permlane16_swap_b32 v5, v7
v_mfma_f32_16x16x32_bf16 v[128:131], a[188:191], a[224:227], v[128:131]
v_mul_f32 v104, s47, v104
v_mul_f32 v105, s47, v105
v_mfma_f32_16x16x32_bf16 v[128:131], a[192:195], a[136:139], v[128:131]
v_mul_f32 v106, s47, v106
v_mul_f32 v107, s47, v107
v_mfma_f32_16x16x32_bf16 v[128:131], a[200:203], a[204:207], v[128:131]
v_mul_f32 v108, s47, v108
v_mul_f32 v109, s47, v109
v_mfma_f32_16x16x32_bf16 v[132:135], a[212:215], a[128:131], v[132:135]
s_waitcnt lgkmcnt(0)
v_mul_f32 v110, s47, v110
v_mul_f32 v111, s47, v111
v_mfma_f32_16x16x32_bf16 v[132:135], a[240:243], a[224:227], v[132:135]
v_cvt_pk_bf16_f32 v20, v104, v105
v_cvt_pk_bf16_f32 v21, v106, v107
v_mfma_f32_16x16x32_bf16 v[132:135], a[244:247], a[136:139], v[132:135]
v_cvt_pk_bf16_f32 v22, v108, v109
v_cvt_pk_bf16_f32 v23, v110, v111
v_mfma_f32_16x16x32_bf16 v[132:135], a[248:251], a[204:207], v[132:135]
v_permlane16_swap_b32 v20, v22
v_permlane16_swap_b32 v21, v23
v_mfma_f32_16x16x32_bf16 v[136:139], a[180:183], a[228:231], v[136:139]
v_mul_f32 v112, s47, v112
v_mul_f32 v113, s47, v113
v_mfma_f32_16x16x32_bf16 v[136:139], a[188:191], a[232:235], v[136:139]
v_mul_f32 v114, s47, v114
v_mul_f32 v115, s47, v115
v_mfma_f32_16x16x32_bf16 v[136:139], a[192:195], a[216:219], v[136:139]
v_mul_f32 v116, s47, v116
v_mul_f32 v117, s47, v117
v_mfma_f32_16x16x32_bf16 v[136:139], a[200:203], a[220:223], v[136:139]
v_mul_f32 v118, s47, v118
v_mul_f32 v119, s47, v119
v_mfma_f32_16x16x32_bf16 v[140:143], a[212:215], a[228:231], v[140:143]
ds_read_b64_tr_b16 a[144:145], v205 offset:1536
ds_read_b64_tr_b16 a[146:147], v205 offset:1792
v_mfma_f32_16x16x32_bf16 v[140:143], a[240:243], a[232:235], v[140:143]
ds_read_b64_tr_b16 a[156:157], v205 offset:9728
ds_read_b64_tr_b16 a[158:159], v205 offset:9984
v_mfma_f32_16x16x32_bf16 v[140:143], a[244:247], a[216:219], v[140:143]
ds_read_b64_tr_b16 a[168:169], v205 offset:50688
ds_read_b64_tr_b16 a[170:171], v205 offset:50944
v_mfma_f32_16x16x32_bf16 v[140:143], a[248:251], a[220:223], v[140:143]
ds_read_b64_tr_b16 a[172:173], v205 offset:58880
ds_read_b64_tr_b16 a[174:175], v205 offset:59136
v_mfma_f32_16x16x32_bf16 v[144:147], a[180:183], a[148:151], v[144:147]
ds_read_b64_tr_b16 a[184:185], v208 offset:1536
ds_read_b64_tr_b16 a[186:187], v208 offset:1792
v_mfma_f32_16x16x32_bf16 v[144:147], a[188:191], a[160:163], v[144:147]
ds_read_b64_tr_b16 a[196:197], v208 offset:9728
ds_read_b64_tr_b16 a[198:199], v208 offset:9984
v_mfma_f32_16x16x32_bf16 v[144:147], a[192:195], a[132:135], v[144:147]
ds_read_b64_tr_b16 a[208:209], v208 offset:50688
ds_read_b64_tr_b16 a[210:211], v208 offset:50944
v_mfma_f32_16x16x32_bf16 v[144:147], a[200:203], a[252:255], v[144:147]
ds_read_b64_tr_b16 a[236:237], v208 offset:58880
ds_read_b64_tr_b16 a[238:239], v208 offset:59136
v_mfma_f32_16x16x32_bf16 v[148:151], a[212:215], a[148:151], v[148:151]
v_cvt_pk_bf16_f32 v36, v112, v113
v_cvt_pk_bf16_f32 v37, v114, v115
v_mfma_f32_16x16x32_bf16 v[148:151], a[240:243], a[160:163], v[148:151]
v_cvt_pk_bf16_f32 v38, v116, v117
v_cvt_pk_bf16_f32 v39, v118, v119
v_mfma_f32_16x16x32_bf16 v[148:151], a[244:247], a[132:135], v[148:151]
v_permlane16_swap_b32 v36, v38
v_permlane16_swap_b32 v37, v39
v_mfma_f32_16x16x32_bf16 v[148:151], a[248:251], a[252:255], v[148:151]
v_mul_f32 v120, s47, v120
v_mul_f32 v121, s47, v121
v_mfma_f32_16x16x32_bf16 v[152:155], a[180:183], a[164:167], v[152:155]
v_mul_f32 v122, s47, v122
v_mul_f32 v123, s47, v123
v_mfma_f32_16x16x32_bf16 v[152:155], a[188:191], a[176:179], v[152:155]
v_mul_f32 v124, s47, v124
v_mul_f32 v125, s47, v125
v_mfma_f32_16x16x32_bf16 v[152:155], a[192:195], a[140:143], v[152:155]
v_mul_f32 v126, s47, v126
v_mul_f32 v127, s47, v127
v_mfma_f32_16x16x32_bf16 v[152:155], a[200:203], a[152:155], v[152:155]
v_cvt_pk_bf16_f32 v52, v120, v121
v_cvt_pk_bf16_f32 v53, v122, v123
v_mfma_f32_16x16x32_bf16 v[156:159], a[212:215], a[164:167], v[156:159]
s_waitcnt lgkmcnt(14)
v_cvt_pk_bf16_f32 v54, v124, v125
v_cvt_pk_bf16_f32 v55, v126, v127
v_mfma_f32_16x16x32_bf16 v[156:159], a[240:243], a[176:179], v[156:159]
v_permlane16_swap_b32 v52, v54
v_permlane16_swap_b32 v53, v55
v_mfma_f32_16x16x32_bf16 v[156:159], a[244:247], a[140:143], v[156:159]
v_mul_f32 v128, s47, v128
v_mul_f32 v129, s47, v129
v_mfma_f32_16x16x32_bf16 v[156:159], a[248:251], a[152:155], v[156:159]
v_mul_f32 v130, s47, v130
v_mul_f32 v131, s47, v131
v_mfma_f32_16x16x32_bf16 v[160:163], a[144:147], a[128:131], v[160:163]
s_waitcnt lgkmcnt(6)
v_mul_f32 v132, s47, v132
v_mul_f32 v133, s47, v133
v_mfma_f32_16x16x32_bf16 v[160:163], a[156:159], a[224:227], v[160:163]
v_mul_f32 v134, s47, v134
v_mul_f32 v135, s47, v135
v_mfma_f32_16x16x32_bf16 v[160:163], a[168:171], a[136:139], v[160:163]
s_xor_b32 s78, s42, 32768
v_cvt_pk_bf16_f32 v8, v128, v129
v_cvt_pk_bf16_f32 v9, v130, v131
v_mfma_f32_16x16x32_bf16 v[160:163], a[172:175], a[204:207], v[160:163]
L_tch10_%=:
s_cbranch_execz L_tch14_%=
s_add_u32 m0, s40, 32768
v_cvt_pk_bf16_f32 v10, v132, v133
v_cvt_pk_bf16_f32 v11, v134, v135
v_mfma_f32_16x16x32_bf16 v[164:167], a[184:187], a[128:131], v[164:167]
s_waitcnt lgkmcnt(0)
buffer_load_dwordx4 v212, s[20:23], s78 offen lds
v_mfma_f32_16x16x32_bf16 v[164:167], a[196:199], a[224:227], v[164:167]
v_permlane16_swap_b32 v8, v10
v_permlane16_swap_b32 v9, v11
v_mfma_f32_16x16x32_bf16 v[164:167], a[208:211], a[136:139], v[164:167]
buffer_load_dwordx4 v212, s[20:23], s78 offen offset:2048 lds
v_mfma_f32_16x16x32_bf16 v[164:167], a[236:239], a[204:207], v[164:167]
v_mul_f32 v136, s47, v136
v_mul_f32 v137, s47, v137
v_mfma_f32_16x16x32_bf16 v[168:171], a[144:147], a[228:231], v[168:171]
s_barrier
v_mfma_f32_16x16x32_bf16 v[168:171], a[156:159], a[232:235], v[168:171]
v_mul_f32 v138, s47, v138
v_mul_f32 v139, s47, v139
v_mfma_f32_16x16x32_bf16 v[168:171], a[168:171], a[216:219], v[168:171]
v_mul_f32 v140, s47, v140
v_mul_f32 v141, s47, v141
v_mfma_f32_16x16x32_bf16 v[168:171], a[172:175], a[220:223], v[168:171]
v_mul_f32 v142, s47, v142
v_mul_f32 v143, s47, v143
v_mfma_f32_16x16x32_bf16 v[172:175], a[184:187], a[228:231], v[172:175]
ds_read_b128 a[128:131], v192 offset:49152
ds_read_b128 a[136:139], v192 offset:49664
v_mfma_f32_16x16x32_bf16 v[172:175], a[196:199], a[232:235], v[172:175]
v_cvt_pk_bf16_f32 v24, v136, v137
v_cvt_pk_bf16_f32 v25, v138, v139
v_mfma_f32_16x16x32_bf16 v[172:175], a[208:211], a[216:219], v[172:175]
v_cvt_pk_bf16_f32 v26, v140, v141
v_cvt_pk_bf16_f32 v27, v142, v143
v_mfma_f32_16x16x32_bf16 v[172:175], a[236:239], a[220:223], v[172:175]
v_permlane16_swap_b32 v24, v26
v_permlane16_swap_b32 v25, v27
v_mfma_f32_16x16x32_bf16 v[176:179], a[144:147], a[148:151], v[176:179]
ds_read_b128 a[180:183], v192 offset:53248
ds_read_b128 a[188:191], v192 offset:53760
v_mfma_f32_16x16x32_bf16 v[176:179], a[156:159], a[160:163], v[176:179]
v_mul_f32 v144, s47, v144
v_mul_f32 v145, s47, v145
v_mfma_f32_16x16x32_bf16 v[176:179], a[168:171], a[132:135], v[176:179]
v_mul_f32 v146, s47, v146
v_mul_f32 v147, s47, v147
v_mfma_f32_16x16x32_bf16 v[176:179], a[172:175], a[252:255], v[176:179]
v_mul_f32 v148, s47, v148
v_mul_f32 v149, s47, v149
v_mfma_f32_16x16x32_bf16 v[180:183], a[184:187], a[148:151], v[180:183]
v_mul_f32 v150, s47, v150
v_mul_f32 v151, s47, v151
v_mfma_f32_16x16x32_bf16 v[180:183], a[196:199], a[160:163], v[180:183]
v_cvt_pk_bf16_f32 v40, v144, v145
v_cvt_pk_bf16_f32 v41, v146, v147
v_mfma_f32_16x16x32_bf16 v[180:183], a[208:211], a[132:135], v[180:183]
v_cvt_pk_bf16_f32 v42, v148, v149
v_cvt_pk_bf16_f32 v43, v150, v151
v_mfma_f32_16x16x32_bf16 v[180:183], a[236:239], a[252:255], v[180:183]
v_permlane16_swap_b32 v40, v42
v_permlane16_swap_b32 v41, v43
v_mfma_f32_16x16x32_bf16 v[184:187], a[144:147], a[164:167], v[184:187]
v_mul_f32 v152, s47, v152
v_mul_f32 v153, s47, v153
v_mfma_f32_16x16x32_bf16 v[184:187], a[156:159], a[176:179], v[184:187]
v_mul_f32 v154, s47, v154
v_mul_f32 v155, s47, v155
v_mfma_f32_16x16x32_bf16 v[184:187], a[168:171], a[140:143], v[184:187]
v_mul_f32 v156, s47, v156
v_mul_f32 v157, s47, v157
v_mfma_f32_16x16x32_bf16 v[184:187], a[172:175], a[152:155], v[184:187]
v_mul_f32 v158, s47, v158
v_mul_f32 v159, s47, v159
v_mfma_f32_16x16x32_bf16 v[188:191], a[184:187], a[164:167], v[188:191]
s_waitcnt lgkmcnt(3)
ds_read_b128 a[132:135], v192 offset:50176
ds_read_b128 a[144:147], v192 offset:50688
v_mfma_f32_16x16x32_bf16 v[188:191], a[196:199], a[176:179], v[188:191]
v_cvt_pk_bf16_f32 v56, v152, v153
v_cvt_pk_bf16_f32 v57, v154, v155
v_mfma_f32_16x16x32_bf16 v[188:191], a[208:211], a[140:143], v[188:191]
v_cvt_pk_bf16_f32 v58, v156, v157
v_cvt_pk_bf16_f32 v59, v158, v159
v_mfma_f32_16x16x32_bf16 v[188:191], a[236:239], a[152:155], v[188:191]
v_permlane16_swap_b32 v56, v58
v_permlane16_swap_b32 v57, v59
v_mfma_f32_16x16x32_bf16 v[64:67], a[128:131], v[0:3], 0
s_waitcnt lgkmcnt(3)
ds_read_b128 a[140:143], v192 offset:54272
ds_read_b128 a[148:151], v192 offset:54784
v_mfma_f32_16x16x32_bf16 v[72:75], a[128:131], v[16:19], 0
v_mul_f32 v160, s47, v160
v_mul_f32 v161, s47, v161
v_mfma_f32_16x16x32_bf16 v[80:83], a[128:131], v[32:35], 0
v_mul_f32 v162, s47, v162
v_mul_f32 v163, s47, v163
v_mfma_f32_16x16x32_bf16 v[88:91], a[128:131], v[48:51], 0
v_mul_f32 v164, s47, v164
v_mul_f32 v165, s47, v165
v_mfma_f32_16x16x32_bf16 v[68:71], a[180:183], v[0:3], 0
v_mul_f32 v166, s47, v166
v_mul_f32 v167, s47, v167
v_mfma_f32_16x16x32_bf16 v[76:79], a[180:183], v[16:19], 0
v_cvt_pk_bf16_f32 v12, v160, v161
v_cvt_pk_bf16_f32 v13, v162, v163
v_mfma_f32_16x16x32_bf16 v[84:87], a[180:183], v[32:35], 0
v_cvt_pk_bf16_f32 v14, v164, v165
v_cvt_pk_bf16_f32 v15, v166, v167
v_mfma_f32_16x16x32_bf16 v[92:95], a[180:183], v[48:51], 0
v_permlane16_swap_b32 v12, v14
v_permlane16_swap_b32 v13, v15
v_mfma_f32_16x16x32_bf16 v[64:67], a[136:139], v[4:7], v[64:67]
s_waitcnt lgkmcnt(4)
v_mul_f32 v168, s47, v168
v_mul_f32 v169, s47, v169
v_mfma_f32_16x16x32_bf16 v[72:75], a[136:139], v[20:23], v[72:75]
v_mul_f32 v170, s47, v170
v_mul_f32 v171, s47, v171
v_mfma_f32_16x16x32_bf16 v[80:83], a[136:139], v[36:39], v[80:83]
v_mul_f32 v172, s47, v172
v_mul_f32 v173, s47, v173
v_mfma_f32_16x16x32_bf16 v[88:91], a[136:139], v[52:55], v[88:91]
s_add_u32 m0, s40, 33792
v_mul_f32 v174, s47, v174
v_mul_f32 v175, s47, v175
v_mfma_f32_16x16x32_bf16 v[68:71], a[188:191], v[4:7], v[68:71]
s_waitcnt lgkmcnt(3)
ds_read_b128 a[128:131], v192 offset:57344
ds_read_b128 a[136:139], v192 offset:57856
v_mfma_f32_16x16x32_bf16 v[76:79], a[188:191], v[20:23], v[76:79]
s_xor_b32 s79, s42, 32896
buffer_load_dwordx4 v212, s[20:23], s79 offen lds
v_mfma_f32_16x16x32_bf16 v[84:87], a[188:191], v[36:39], v[84:87]
v_cvt_pk_bf16_f32 v28, v168, v169
v_cvt_pk_bf16_f32 v29, v170, v171
v_mfma_f32_16x16x32_bf16 v[92:95], a[188:191], v[52:55], v[92:95]
buffer_load_dwordx4 v212, s[20:23], s79 offen offset:2048 lds
v_mfma_f32_16x16x32_bf16 v[64:67], a[132:135], v[8:11], v[64:67]
s_waitcnt lgkmcnt(3)
v_cvt_pk_bf16_f32 v30, v172, v173
v_cvt_pk_bf16_f32 v31, v174, v175
v_mfma_f32_16x16x32_bf16 v[72:75], a[132:135], v[24:27], v[72:75]
v_permlane16_swap_b32 v28, v30
v_permlane16_swap_b32 v29, v31
v_mfma_f32_16x16x32_bf16 v[80:83], a[132:135], v[40:43], v[80:83]
v_mul_f32 v176, s47, v176
v_mul_f32 v177, s47, v177
v_mfma_f32_16x16x32_bf16 v[88:91], a[132:135], v[56:59], v[88:91]
v_mul_f32 v178, s47, v178
v_mul_f32 v179, s47, v179
v_mfma_f32_16x16x32_bf16 v[68:71], a[140:143], v[8:11], v[68:71]
ds_read_b128 a[132:135], v192 offset:58368
ds_read_b128 a[152:155], v192 offset:58880
v_mfma_f32_16x16x32_bf16 v[76:79], a[140:143], v[24:27], v[76:79]
v_mul_f32 v180, s47, v180
v_mul_f32 v181, s47, v181
v_mfma_f32_16x16x32_bf16 v[84:87], a[140:143], v[40:43], v[84:87]
v_mul_f32 v182, s47, v182
v_mul_f32 v183, s47, v183
v_mfma_f32_16x16x32_bf16 v[92:95], a[140:143], v[56:59], v[92:95]
v_cvt_pk_bf16_f32 v44, v176, v177
v_cvt_pk_bf16_f32 v45, v178, v179
v_mfma_f32_16x16x32_bf16 v[64:67], a[144:147], v[12:15], v[64:67]
s_waitcnt lgkmcnt(4)
v_cvt_pk_bf16_f32 v46, v180, v181
v_cvt_pk_bf16_f32 v47, v182, v183
s_nop 0
v_permlane16_swap_b32 v44, v46
v_permlane16_swap_b32 v45, v47
v_mfma_f32_16x16x32_bf16 v[72:75], a[144:147], v[28:31], v[72:75]
v_mul_f32 v184, s47, v184
v_mul_f32 v185, s47, v185
v_mul_f32 v186, s47, v186
v_mul_f32 v187, s47, v187
v_mul_f32 v188, s47, v188
v_mul_f32 v189, s47, v189
v_mul_f32 v190, s47, v190
v_mul_f32 v191, s47, v191
v_cvt_pk_bf16_f32 v60, v184, v185
v_cvt_pk_bf16_f32 v61, v186, v187
v_cvt_pk_bf16_f32 v62, v188, v189
v_cvt_pk_bf16_f32 v63, v190, v191
s_nop 0
v_permlane16_swap_b32 v60, v62
v_permlane16_swap_b32 v61, v63
v_mfma_f32_16x16x32_bf16 v[80:83], a[144:147], v[44:47], v[80:83]
s_nop 0
v_mfma_f32_16x16x32_bf16 v[88:91], a[144:147], v[60:63], v[88:91]
v_mfma_f32_16x16x32_bf16 v[68:71], a[148:151], v[12:15], v[68:71]
s_waitcnt lgkmcnt(3)
ds_read_b128 a[140:143], v192 offset:61440
ds_read_b128 a[144:147], v192 offset:61952
v_mfma_f32_16x16x32_bf16 v[76:79], a[148:151], v[28:31], v[76:79]
s_add_u32 s60, s49, 0
v_mfma_f32_16x16x32_bf16 v[84:87], a[148:151], v[44:47], v[84:87]
s_waitcnt vmcnt(8)
buffer_store_dwordx2 v[254:255], v252, s[24:27], s60 offen sc1
v_mfma_f32_16x16x32_bf16 v[92:95], a[148:151], v[60:63], v[92:95]
ds_read_b128 a[148:151], v192 offset:62464
ds_read_b128 a[156:159], v192 offset:62976
v_mfma_f32_16x16x32_bf16 v[96:99], a[128:131], v[0:3], 0
s_waitcnt lgkmcnt(6)
v_mfma_f32_16x16x32_bf16 v[96:99], a[136:139], v[4:7], v[96:99]
v_mfma_f32_16x16x32_bf16 v[104:107], a[128:131], v[16:19], 0
v_mfma_f32_16x16x32_bf16 v[104:107], a[136:139], v[20:23], v[104:107]
v_max3_f32 v218, v64, v65, v66
v_max3_f32 v218, v218, v67, v68
v_mfma_f32_16x16x32_bf16 v[112:115], a[128:131], v[32:35], 0
s_waitcnt lgkmcnt(5)
v_max3_f32 v218, v218, v69, v70
v_max_f32 v218, v218, v71
v_mfma_f32_16x16x32_bf16 v[112:115], a[136:139], v[36:39], v[112:115]
v_max3_f32 v219, v72, v73, v74
v_max3_f32 v219, v219, v75, v76
v_mfma_f32_16x16x32_bf16 v[120:123], a[128:131], v[48:51], 0
v_max3_f32 v219, v219, v77, v78
v_max_f32 v219, v219, v79
v_mfma_f32_16x16x32_bf16 v[120:123], a[136:139], v[52:55], v[120:123]
v_max3_f32 v220, v80, v81, v82
v_max3_f32 v220, v220, v83, v84
v_mfma_f32_16x16x32_bf16 v[96:99], a[132:135], v[8:11], v[96:99]
s_waitcnt lgkmcnt(0)
s_barrier
v_mfma_f32_16x16x32_bf16 v[96:99], a[152:155], v[12:15], v[96:99]
v_max3_f32 v220, v220, v85, v86
v_max_f32 v220, v220, v87
v_mfma_f32_16x16x32_bf16 v[104:107], a[132:135], v[24:27], v[104:107]
v_max3_f32 v221, v88, v89, v90
v_max3_f32 v221, v221, v91, v92
v_mfma_f32_16x16x32_bf16 v[104:107], a[152:155], v[28:31], v[104:107]
v_max3_f32 v221, v221, v93, v94
v_max_f32 v221, v221, v95
v_mfma_f32_16x16x32_bf16 v[112:115], a[132:135], v[40:43], v[112:115]
ds_read_b128 a[128:131], v194 offset:0
ds_read_b128 a[136:139], v194 offset:512
v_mfma_f32_16x16x32_bf16 v[112:115], a[152:155], v[44:47], v[112:115]
v_mov_b32 v226, v218
v_mov_b32 v227, v219
v_mfma_f32_16x16x32_bf16 v[120:123], a[132:135], v[56:59], v[120:123]
v_mov_b32 v228, v220
v_mov_b32 v229, v221
v_mfma_f32_16x16x32_bf16 v[120:123], a[152:155], v[60:63], v[120:123]
v_permlane16_swap_b32 v218, v226
v_permlane16_swap_b32 v219, v227
v_mfma_f32_16x16x32_bf16 v[100:103], a[140:143], v[0:3], 0
v_permlane16_swap_b32 v220, v228
v_permlane16_swap_b32 v221, v229
v_mfma_f32_16x16x32_bf16 v[100:103], a[144:147], v[4:7], v[100:103]
v_max_f32 v218, v218, v226
v_max_f32 v219, v219, v227
v_mfma_f32_16x16x32_bf16 v[108:111], a[140:143], v[16:19], 0
v_max_f32 v220, v220, v228
v_max_f32 v221, v221, v229
v_mfma_f32_16x16x32_bf16 v[108:111], a[144:147], v[20:23], v[108:111]
s_add_u32 m0, s40, 81920
v_mov_b32 v226, v218
v_mov_b32 v227, v219
v_mfma_f32_16x16x32_bf16 v[116:119], a[140:143], v[32:35], 0
ds_read_b128 a[132:135], v194 offset:1024
ds_read_b128 a[152:155], v194 offset:1536
v_mfma_f32_16x16x32_bf16 v[116:119], a[144:147], v[36:39], v[116:119]
s_xor_b32 s78, s42, 65536
buffer_load_dwordx4 v210, s[16:19], s78 offen lds
v_mfma_f32_16x16x32_bf16 v[124:127], a[140:143], v[48:51], 0
v_mov_b32 v228, v220
v_mov_b32 v229, v221
v_mfma_f32_16x16x32_bf16 v[124:127], a[144:147], v[52:55], v[124:127]
buffer_load_dwordx4 v211, s[16:19], s78 offen offset:2048 lds
v_mfma_f32_16x16x32_bf16 v[100:103], a[148:151], v[8:11], v[100:103]
v_permlane32_swap_b32 v218, v226
v_permlane32_swap_b32 v219, v227
v_mfma_f32_16x16x32_bf16 v[100:103], a[156:159], v[12:15], v[100:103]
v_permlane32_swap_b32 v220, v228
v_permlane32_swap_b32 v221, v229
v_mfma_f32_16x16x32_bf16 v[108:111], a[148:151], v[24:27], v[108:111]
v_max_f32 v218, v218, v226
v_max_f32 v219, v219, v227
v_mfma_f32_16x16x32_bf16 v[108:111], a[156:159], v[28:31], v[108:111]
v_max_f32 v220, v220, v228
v_max_f32 v221, v221, v229
v_mfma_f32_16x16x32_bf16 v[116:119], a[148:151], v[40:43], v[116:119]
s_waitcnt lgkmcnt(3)
ds_read_b128 a[140:143], v194 offset:4096
ds_read_b128 a[144:147], v194 offset:4608
v_mfma_f32_16x16x32_bf16 v[116:119], a[156:159], v[44:47], v[116:119]
v_add_f32 v221, s99, v221
v_sub_f32 v160, 0, v218
v_mfma_f32_16x16x32_bf16 v[124:127], a[148:151], v[56:59], v[124:127]
v_sub_f32 v161, 0, v218
v_sub_f32 v162, 0, v218
v_mfma_f32_16x16x32_bf16 v[124:127], a[156:159], v[60:63], v[124:127]
v_sub_f32 v163, 0, v218
v_sub_f32 v164, 0, v219
v_sub_f32 v165, 0, v219
v_sub_f32 v166, 0, v219
v_sub_f32 v167, 0, v219
v_sub_f32 v168, 0, v220
v_sub_f32 v169, 0, v220
v_sub_f32 v170, 0, v220
v_sub_f32 v171, 0, v220
v_sub_f32 v172, 0, v221
v_sub_f32 v173, 0, v221
v_sub_f32 v174, 0, v221
v_sub_f32 v175, 0, v221
v_sub_f32 v64, v64, v218
v_sub_f32 v65, v65, v218
v_sub_f32 v66, v66, v218
v_sub_f32 v67, v67, v218
v_exp_f32 v64, v64
v_exp_f32 v65, v65
v_exp_f32 v66, v66
v_exp_f32 v67, v67
v_sub_f32 v68, v68, v218
v_sub_f32 v69, v69, v218
v_sub_f32 v70, v70, v218
v_sub_f32 v71, v71, v218
v_exp_f32 v68, v68
v_cvt_pk_bf16_f32 v128, v64, v65
v_cvt_pk_bf16_f32 v129, v66, v67
v_exp_f32 v69, v69
v_exp_f32 v70, v70
v_exp_f32 v71, v71
v_sub_f32 v72, v72, v219
v_sub_f32 v73, v73, v219
v_sub_f32 v74, v74, v219
v_sub_f32 v75, v75, v219
v_exp_f32 v72, v72
v_cvt_pk_bf16_f32 v130, v68, v69
v_cvt_pk_bf16_f32 v131, v70, v71
v_exp_f32 v73, v73
v_exp_f32 v74, v74
v_exp_f32 v75, v75
v_sub_f32 v76, v76, v219
v_sub_f32 v77, v77, v219
v_sub_f32 v78, v78, v219
v_sub_f32 v79, v79, v219
v_exp_f32 v76, v76
v_cvt_pk_bf16_f32 v132, v72, v73
v_cvt_pk_bf16_f32 v133, v74, v75
v_exp_f32 v77, v77
v_exp_f32 v78, v78
v_exp_f32 v79, v79
v_sub_f32 v80, v80, v220
v_sub_f32 v81, v81, v220
v_sub_f32 v82, v82, v220
v_sub_f32 v83, v83, v220
v_exp_f32 v80, v80
v_cvt_pk_bf16_f32 v134, v76, v77
v_cvt_pk_bf16_f32 v135, v78, v79
v_exp_f32 v81, v81
v_exp_f32 v82, v82
v_exp_f32 v83, v83
v_sub_f32 v84, v84, v220
v_sub_f32 v85, v85, v220
v_sub_f32 v86, v86, v220
v_sub_f32 v87, v87, v220
v_exp_f32 v84, v84
v_cvt_pk_bf16_f32 v136, v80, v81
v_cvt_pk_bf16_f32 v137, v82, v83
v_exp_f32 v85, v85
v_exp_f32 v86, v86
v_exp_f32 v87, v87
v_sub_f32 v88, v88, v221
v_sub_f32 v89, v89, v221
v_sub_f32 v90, v90, v221
v_sub_f32 v91, v91, v221
v_exp_f32 v88, v88
v_cvt_pk_bf16_f32 v138, v84, v85
v_cvt_pk_bf16_f32 v139, v86, v87
v_exp_f32 v89, v89
v_exp_f32 v90, v90
v_exp_f32 v91, v91
v_sub_f32 v92, v92, v221
v_sub_f32 v93, v93, v221
v_sub_f32 v94, v94, v221
v_sub_f32 v95, v95, v221
v_exp_f32 v92, v92
v_cvt_pk_bf16_f32 v140, v88, v89
v_cvt_pk_bf16_f32 v141, v90, v91
v_exp_f32 v93, v93
v_exp_f32 v94, v94
v_exp_f32 v95, v95
v_cvt_pk_bf16_f32 v142, v92, v93
v_cvt_pk_bf16_f32 v143, v94, v95
v_mfma_f32_16x16x32_bf16 v[64:67], a[128:131], v[0:3], v[160:163]
s_waitcnt lgkmcnt(4)
v_sub_f32 v96, v96, v218
v_sub_f32 v97, v97, v218
v_mfma_f32_16x16x32_bf16 v[64:67], a[136:139], v[4:7], v[64:67]
v_sub_f32 v98, v98, v218
v_sub_f32 v99, v99, v218
v_mfma_f32_16x16x32_bf16 v[72:75], a[128:131], v[16:19], v[164:167]
v_exp_f32 v96, v96
v_mfma_f32_16x16x32_bf16 v[72:75], a[136:139], v[20:23], v[72:75]
v_exp_f32 v97, v97
v_mfma_f32_16x16x32_bf16 v[80:83], a[128:131], v[32:35], v[168:171]
s_waitcnt lgkmcnt(3)
ds_read_b128 a[148:151], v194 offset:5120
ds_read_b128 a[156:159], v194 offset:5632
v_mfma_f32_16x16x32_bf16 v[80:83], a[136:139], v[36:39], v[80:83]
v_exp_f32 v98, v98
v_mfma_f32_16x16x32_bf16 v[88:91], a[128:131], v[48:51], v[172:175]
v_exp_f32 v99, v99
v_mfma_f32_16x16x32_bf16 v[88:91], a[136:139], v[52:55], v[88:91]
v_sub_f32 v100, v100, v218
v_sub_f32 v101, v101, v218
v_mfma_f32_16x16x32_bf16 v[64:67], a[132:135], v[8:11], v[64:67]
s_waitcnt lgkmcnt(4)
v_sub_f32 v102, v102, v218
v_sub_f32 v103, v103, v218
v_mfma_f32_16x16x32_bf16 v[64:67], a[152:155], v[12:15], v[64:67]
v_exp_f32 v100, v100
v_mfma_f32_16x16x32_bf16 v[72:75], a[132:135], v[24:27], v[72:75]
v_cvt_pk_bf16_f32 v144, v96, v97
v_cvt_pk_bf16_f32 v145, v98, v99
v_mfma_f32_16x16x32_bf16 v[72:75], a[152:155], v[28:31], v[72:75]
v_exp_f32 v101, v101
v_mfma_f32_16x16x32_bf16 v[80:83], a[132:135], v[40:43], v[80:83]
s_waitcnt lgkmcnt(3)
v_exp_f32 v102, v102
v_mfma_f32_16x16x32_bf16 v[80:83], a[152:155], v[44:47], v[80:83]
v_exp_f32 v103, v103
v_mfma_f32_16x16x32_bf16 v[88:91], a[132:135], v[56:59], v[88:91]
v_sub_f32 v104, v104, v219
v_sub_f32 v105, v105, v219
v_mfma_f32_16x16x32_bf16 v[88:91], a[152:155], v[60:63], v[88:91]
v_sub_f32 v106, v106, v219
v_sub_f32 v107, v107, v219
v_mfma_f32_16x16x32_bf16 v[68:71], a[140:143], v[0:3], v[160:163]
s_waitcnt lgkmcnt(2)
ds_read_b64_tr_b16 a[128:129], v199 offset:32768
ds_read_b64_tr_b16 a[130:131], v199 offset:36864
v_mfma_f32_16x16x32_bf16 v[68:71], a[144:147], v[4:7], v[68:71]
v_exp_f32 v104, v104
v_mfma_f32_16x16x32_bf16 v[76:79], a[140:143], v[16:19], v[164:167]
v_cvt_pk_bf16_f32 v146, v100, v101
v_cvt_pk_bf16_f32 v147, v102, v103
v_mfma_f32_16x16x32_bf16 v[76:79], a[144:147], v[20:23], v[76:79]
v_exp_f32 v105, v105
v_mfma_f32_16x16x32_bf16 v[84:87], a[140:143], v[32:35], v[168:171]
s_waitcnt lgkmcnt(3)
ds_read_b64_tr_b16 a[132:133], v202 offset:32768
ds_read_b64_tr_b16 a[134:135], v202 offset:36864
v_mfma_f32_16x16x32_bf16 v[84:87], a[144:147], v[36:39], v[84:87]
v_exp_f32 v106, v106
v_mfma_f32_16x16x32_bf16 v[92:95], a[140:143], v[48:51], v[172:175]
v_exp_f32 v107, v107
v_mfma_f32_16x16x32_bf16 v[92:95], a[144:147], v[52:55], v[92:95]
v_sub_f32 v108, v108, v219
v_sub_f32 v109, v109, v219
v_mfma_f32_16x16x32_bf16 v[68:71], a[148:151], v[8:11], v[68:71]
s_waitcnt lgkmcnt(4)
ds_read_b64_tr_b16 a[136:137], v199 offset:33280
ds_read_b64_tr_b16 a[138:139], v199 offset:37376
v_mfma_f32_16x16x32_bf16 v[68:71], a[156:159], v[12:15], v[68:71]
v_sub_f32 v110, v110, v219
v_sub_f32 v111, v111, v219
v_mfma_f32_16x16x32_bf16 v[76:79], a[148:151], v[24:27], v[76:79]
v_exp_f32 v108, v108
v_mfma_f32_16x16x32_bf16 v[76:79], a[156:159], v[28:31], v[76:79]
s_add_u32 m0, s40, 82944
v_cvt_pk_bf16_f32 v148, v104, v105
v_cvt_pk_bf16_f32 v149, v106, v107
v_mfma_f32_16x16x32_bf16 v[84:87], a[148:151], v[40:43], v[84:87]
ds_read_b64_tr_b16 a[140:141], v202 offset:33280
ds_read_b64_tr_b16 a[142:143], v202 offset:37376
v_mfma_f32_16x16x32_bf16 v[84:87], a[156:159], v[44:47], v[84:87]
s_xor_b32 s79, s42, 65664
buffer_load_dwordx4 v210, s[16:19], s79 offen lds
v_mfma_f32_16x16x32_bf16 v[92:95], a[148:151], v[56:59], v[92:95]
v_exp_f32 v109, v109
v_mfma_f32_16x16x32_bf16 v[92:95], a[156:159], v[60:63], v[92:95]
buffer_load_dwordx4 v211, s[16:19], s79 offen offset:2048 lds
v_mfma_f32_16x16x32_bf16 v[176:179], v[232:235], v[128:131], 0
s_waitcnt lgkmcnt(6)
ds_read_b64_tr_b16 a[144:145], v199 offset:33792
ds_read_b64_tr_b16 a[146:147], v199 offset:37888
v_mfma_f32_16x16x32_bf16 v[180:183], v[232:235], v[132:135], 0
v_exp_f32 v110, v110
v_mfma_f32_16x16x32_bf16 v[184:187], v[232:235], v[136:139], 0
v_exp_f32 v111, v111
v_mfma_f32_16x16x32_bf16 v[188:191], v[232:235], v[140:143], 0
v_sub_f32 v112, v112, v220
v_sub_f32 v113, v113, v220
v_mfma_f32_16x16x32_bf16 a[0:3], a[128:131], v[128:131], 0
s_waitcnt lgkmcnt(6)
ds_read_b64_tr_b16 a[148:149], v202 offset:33792
ds_read_b64_tr_b16 a[150:151], v202 offset:37888
v_mfma_f32_16x16x32_bf16 a[4:7], a[128:131], v[132:135], 0
v_sub_f32 v114, v114, v220
v_sub_f32 v115, v115, v220
v_mfma_f32_16x16x32_bf16 a[8:11], a[128:131], v[136:139], 0
v_exp_f32 v112, v112
v_mfma_f32_16x16x32_bf16 a[12:15], a[128:131], v[140:143], 0
v_cvt_pk_bf16_f32 v150, v108, v109
v_cvt_pk_bf16_f32 v151, v110, v111
v_mfma_f32_16x16x32_bf16 a[16:19], a[132:135], v[128:131], 0
s_waitcnt lgkmcnt(6)
ds_read_b64_tr_b16 a[128:129], v199 offset:34304
ds_read_b64_tr_b16 a[130:131], v199 offset:38400
v_mfma_f32_16x16x32_bf16 a[20:23], a[132:135], v[132:135], 0
v_exp_f32 v113, v113
v_mfma_f32_16x16x32_bf16 a[24:27], a[132:135], v[136:139], 0
v_exp_f32 v114, v114
v_mfma_f32_16x16x32_bf16 a[28:31], a[132:135], v[140:143], 0
v_exp_f32 v115, v115
v_mfma_f32_16x16x32_bf16 a[32:35], a[136:139], v[128:131], 0
s_waitcnt lgkmcnt(6)
ds_read_b64_tr_b16 a[132:133], v202 offset:34304
ds_read_b64_tr_b16 a[134:135], v202 offset:38400
v_mfma_f32_16x16x32_bf16 a[36:39], a[136:139], v[132:135], 0
v_sub_f32 v116, v116, v220
v_sub_f32 v117, v117, v220
v_mfma_f32_16x16x32_bf16 a[40:43], a[136:139], v[136:139], 0
v_sub_f32 v118, v118, v220
v_sub_f32 v119, v119, v220
v_mfma_f32_16x16x32_bf16 a[44:47], a[136:139], v[140:143], 0
v_exp_f32 v116, v116
v_mfma_f32_16x16x32_bf16 a[48:51], a[140:143], v[128:131], 0
s_waitcnt lgkmcnt(6)
ds_read_b128 a[136:139], v194 offset:8192
ds_read_b128 a[152:155], v194 offset:8704
v_mfma_f32_16x16x32_bf16 a[52:55], a[140:143], v[132:135], 0
v_cvt_pk_bf16_f32 v152, v112, v113
v_cvt_pk_bf16_f32 v153, v114, v115
v_mfma_f32_16x16x32_bf16 a[56:59], a[140:143], v[136:139], 0
v_exp_f32 v117, v117
v_mfma_f32_16x16x32_bf16 a[60:63], a[140:143], v[140:143], 0
v_exp_f32 v118, v118
v_mfma_f32_16x16x32_bf16 a[64:67], a[144:147], v[128:131], 0
s_waitcnt lgkmcnt(6)
v_exp_f32 v119, v119
v_mfma_f32_16x16x32_bf16 a[68:71], a[144:147], v[132:135], 0
v_sub_f32 v120, v120, v221
v_sub_f32 v121, v121, v221
v_mfma_f32_16x16x32_bf16 a[72:75], a[144:147], v[136:139], 0
v_sub_f32 v122, v122, v221
v_sub_f32 v123, v123, v221
v_mfma_f32_16x16x32_bf16 a[76:79], a[144:147], v[140:143], 0
v_exp_f32 v120, v120
v_mfma_f32_16x16x32_bf16 a[80:83], a[148:151], v[128:131], 0
s_waitcnt lgkmcnt(4)
ds_read_b128 a[140:143], v194 offset:9216
ds_read_b128 a[144:147], v194 offset:9728
v_mfma_f32_16x16x32_bf16 a[84:87], a[148:151], v[132:135], 0
v_cvt_pk_bf16_f32 v154, v116, v117
v_cvt_pk_bf16_f32 v155, v118, v119
v_mfma_f32_16x16x32_bf16 a[88:91], a[148:151], v[136:139], 0
v_exp_f32 v121, v121
v_mfma_f32_16x16x32_bf16 a[92:95], a[148:151], v[140:143], 0
v_exp_f32 v122, v122
v_mfma_f32_16x16x32_bf16 a[96:99], a[128:131], v[128:131], 0
s_waitcnt lgkmcnt(4)
v_exp_f32 v123, v123
v_mfma_f32_16x16x32_bf16 a[100:103], a[128:131], v[132:135], 0
v_sub_f32 v124, v124, v221
v_sub_f32 v125, v125, v221
v_mfma_f32_16x16x32_bf16 a[104:107], a[128:131], v[136:139], 0
v_sub_f32 v126, v126, v221
v_sub_f32 v127, v127, v221
v_mfma_f32_16x16x32_bf16 a[108:111], a[128:131], v[140:143], 0
v_exp_f32 v124, v124
v_mfma_f32_16x16x32_bf16 a[112:115], a[132:135], v[128:131], 0
s_waitcnt lgkmcnt(3)
ds_read_b128 a[128:131], v194 offset:12288
ds_read_b128 a[148:151], v194 offset:12800
v_mfma_f32_16x16x32_bf16 a[116:119], a[132:135], v[132:135], 0
v_cvt_pk_bf16_f32 v156, v120, v121
v_cvt_pk_bf16_f32 v157, v122, v123
v_mfma_f32_16x16x32_bf16 a[120:123], a[132:135], v[136:139], 0
v_exp_f32 v125, v125
v_mfma_f32_16x16x32_bf16 a[124:127], a[132:135], v[140:143], 0
s_add_u32 m0, s40, 65536
v_exp_f32 v126, v126
v_exp_f32 v127, v127
v_cvt_pk_bf16_f32 v158, v124, v125
v_cvt_pk_bf16_f32 v159, v126, v127
v_mfma_f32_16x16x32_bf16 v[96:99], a[136:139], v[0:3], v[160:163]
s_waitcnt lgkmcnt(4)
v_exp_f32 v64, v64
v_mfma_f32_16x16x32_bf16 v[96:99], a[152:155], v[4:7], v[96:99]
s_xor_b32 s78, s42, 49152
buffer_load_dwordx4 v212, s[20:23], s78 offen lds
v_mfma_f32_16x16x32_bf16 v[104:107], a[136:139], v[16:19], v[164:167]
v_mfma_f32_16x16x32_bf16 v[104:107], a[152:155], v[20:23], v[104:107]
buffer_load_dwordx4 v212, s[20:23], s78 offen offset:2048 lds
v_mfma_f32_16x16x32_bf16 v[112:115], a[136:139], v[32:35], v[168:171]
s_waitcnt lgkmcnt(3)
ds_read_b128 a[132:135], v194 offset:13312
ds_read_b128 a[156:159], v194 offset:13824
v_mfma_f32_16x16x32_bf16 v[112:115], a[152:155], v[36:39], v[112:115]
v_exp_f32 v65, v65
v_mfma_f32_16x16x32_bf16 v[120:123], a[136:139], v[48:51], v[172:175]
v_exp_f32 v66, v66
v_mfma_f32_16x16x32_bf16 v[120:123], a[152:155], v[52:55], v[120:123]
v_exp_f32 v67, v67
v_mfma_f32_16x16x32_bf16 v[96:99], a[140:143], v[8:11], v[96:99]
s_waitcnt lgkmcnt(4)
v_mfma_f32_16x16x32_bf16 v[96:99], a[144:147], v[12:15], v[96:99]
v_exp_f32 v68, v68
v_mfma_f32_16x16x32_bf16 v[104:107], a[140:143], v[24:27], v[104:107]
v_cvt_pk_bf16_f32 v128, v64, v65
v_cvt_pk_bf16_f32 v129, v66, v67
v_mfma_f32_16x16x32_bf16 v[104:107], a[144:147], v[28:31], v[104:107]
v_exp_f32 v69, v69
v_mfma_f32_16x16x32_bf16 v[112:115], a[140:143], v[40:43], v[112:115]
s_waitcnt lgkmcnt(3)
v_exp_f32 v70, v70
v_mfma_f32_16x16x32_bf16 v[112:115], a[144:147], v[44:47], v[112:115]
v_mfma_f32_16x16x32_bf16 v[120:123], a[140:143], v[56:59], v[120:123]
v_exp_f32 v71, v71
v_mfma_f32_16x16x32_bf16 v[120:123], a[144:147], v[60:63], v[120:123]
v_exp_f32 v72, v72
v_mfma_f32_16x16x32_bf16 v[100:103], a[128:131], v[0:3], v[160:163]
s_waitcnt lgkmcnt(2)
ds_read_b64_tr_b16 a[136:137], v199 offset:40960
ds_read_b64_tr_b16 a[138:139], v199 offset:45056
v_mfma_f32_16x16x32_bf16 v[100:103], a[148:151], v[4:7], v[100:103]
v_cvt_pk_bf16_f32 v130, v68, v69
v_cvt_pk_bf16_f32 v131, v70, v71
v_mfma_f32_16x16x32_bf16 v[108:111], a[128:131], v[16:19], v[164:167]
v_exp_f32 v73, v73
v_mfma_f32_16x16x32_bf16 v[108:111], a[148:151], v[20:23], v[108:111]
v_mfma_f32_16x16x32_bf16 v[116:119], a[128:131], v[32:35], v[168:171]
s_waitcnt lgkmcnt(3)
ds_read_b64_tr_b16 a[140:141], v202 offset:40960
ds_read_b64_tr_b16 a[142:143], v202 offset:45056
v_mfma_f32_16x16x32_bf16 v[116:119], a[148:151], v[36:39], v[116:119]
v_exp_f32 v74, v74
v_mfma_f32_16x16x32_bf16 v[124:127], a[128:131], v[48:51], v[172:175]
v_exp_f32 v75, v75
v_mfma_f32_16x16x32_bf16 v[124:127], a[148:151], v[52:55], v[124:127]
v_exp_f32 v76, v76
v_mfma_f32_16x16x32_bf16 v[100:103], a[132:135], v[8:11], v[100:103]
s_waitcnt lgkmcnt(4)
ds_read_b64_tr_b16 a[128:129], v199 offset:41472
ds_read_b64_tr_b16 a[130:131], v199 offset:45568
v_mfma_f32_16x16x32_bf16 v[100:103], a[156:159], v[12:15], v[100:103]
v_cvt_pk_bf16_f32 v132, v72, v73
v_mfma_f32_16x16x32_bf16 v[108:111], a[132:135], v[24:27], v[108:111]
v_cvt_pk_bf16_f32 v133, v74, v75
v_mfma_f32_16x16x32_bf16 v[108:111], a[156:159], v[28:31], v[108:111]
v_exp_f32 v77, v77
v_mfma_f32_16x16x32_bf16 v[116:119], a[132:135], v[40:43], v[116:119]
ds_read_b64_tr_b16 a[144:145], v202 offset:41472
ds_read_b64_tr_b16 a[146:147], v202 offset:45568
v_mfma_f32_16x16x32_bf16 v[116:119], a[156:159], v[44:47], v[116:119]
v_exp_f32 v78, v78
v_mfma_f32_16x16x32_bf16 v[124:127], a[132:135], v[56:59], v[124:127]
v_exp_f32 v79, v79
v_mfma_f32_16x16x32_bf16 v[124:127], a[156:159], v[60:63], v[124:127]
v_mfma_f32_16x16x32_bf16 v[176:179], v[232:235], v[144:147], v[176:179]
s_waitcnt lgkmcnt(6)
ds_read_b64_tr_b16 a[132:133], v199 offset:41984
ds_read_b64_tr_b16 a[134:135], v199 offset:46080
v_mfma_f32_16x16x32_bf16 v[180:183], v[232:235], v[148:151], v[180:183]
ds_read_b64_tr_b16 a[156:157], v202 offset:42496
ds_read_b64_tr_b16 a[158:159], v202 offset:46592
v_mfma_f32_16x16x32_bf16 v[184:187], v[232:235], v[152:155], v[184:187]
ds_read_b64_tr_b16 a[152:153], v199 offset:42496
ds_read_b64_tr_b16 a[154:155], v199 offset:46592
v_mfma_f32_16x16x32_bf16 v[188:191], v[232:235], v[156:159], v[188:191]
ds_read_b64_tr_b16 a[148:149], v202 offset:41984
ds_read_b64_tr_b16 a[150:151], v202 offset:46080
v_mfma_f32_16x16x32_bf16 a[0:3], a[136:139], v[144:147], a[0:3]
L_tch11_%=:
s_cbranch_execz L_tch15_%=
s_waitcnt lgkmcnt(12)
v_exp_f32 v80, v80
v_mfma_f32_16x16x32_bf16 a[4:7], a[136:139], v[148:151], a[4:7]
v_cvt_pk_bf16_f32 v134, v76, v77
v_cvt_pk_bf16_f32 v135, v78, v79
v_mfma_f32_16x16x32_bf16 a[8:11], a[136:139], v[152:155], a[8:11]
v_exp_f32 v81, v81
v_mfma_f32_16x16x32_bf16 a[12:15], a[136:139], v[156:159], a[12:15]
s_add_u32 m0, s40, 66560
v_mfma_f32_16x16x32_bf16 a[16:19], a[140:143], v[144:147], a[16:19]
s_waitcnt lgkmcnt(10)
v_exp_f32 v82, v82
v_mfma_f32_16x16x32_bf16 a[20:23], a[140:143], v[148:151], a[20:23]
s_xor_b32 s79, s42, 49280
buffer_load_dwordx4 v212, s[20:23], s79 offen lds
v_mfma_f32_16x16x32_bf16 a[24:27], a[140:143], v[152:155], a[24:27]
v_exp_f32 v83, v83
v_mfma_f32_16x16x32_bf16 a[28:31], a[140:143], v[156:159], a[28:31]
buffer_load_dwordx4 v212, s[20:23], s79 offen offset:2048 lds
v_mfma_f32_16x16x32_bf16 a[32:35], a[128:131], v[144:147], a[32:35]
s_waitcnt lgkmcnt(0)
s_barrier
v_mfma_f32_16x16x32_bf16 a[36:39], a[128:131], v[148:151], a[36:39]
v_exp_f32 v84, v84
v_mfma_f32_16x16x32_bf16 a[40:43], a[128:131], v[152:155], a[40:43]
v_cvt_pk_bf16_f32 v136, v80, v81
v_cvt_pk_bf16_f32 v137, v82, v83
v_mfma_f32_16x16x32_bf16 a[44:47], a[128:131], v[156:159], a[44:47]
v_mfma_f32_16x16x32_bf16 a[48:51], a[144:147], v[144:147], a[48:51]
ds_read_b128 a[128:131], v192 offset:0
ds_read_b128 a[136:139], v192 offset:512
v_mfma_f32_16x16x32_bf16 a[52:55], a[144:147], v[148:151], a[52:55]
v_exp_f32 v85, v85
v_mfma_f32_16x16x32_bf16 a[56:59], a[144:147], v[152:155], a[56:59]
v_exp_f32 v86, v86
v_mfma_f32_16x16x32_bf16 a[60:63], a[144:147], v[156:159], a[60:63]
v_exp_f32 v87, v87
v_mfma_f32_16x16x32_bf16 a[64:67], a[132:135], v[144:147], a[64:67]
v_exp_f32 v88, v88
v_mfma_f32_16x16x32_bf16 a[68:71], a[132:135], v[148:151], a[68:71]
v_cvt_pk_bf16_f32 v138, v84, v85
v_mfma_f32_16x16x32_bf16 a[72:75], a[132:135], v[152:155], a[72:75]
v_cvt_pk_bf16_f32 v139, v86, v87
v_mfma_f32_16x16x32_bf16 a[76:79], a[132:135], v[156:159], a[76:79]
v_exp_f32 v89, v89
v_mfma_f32_16x16x32_bf16 a[80:83], a[148:151], v[144:147], a[80:83]
ds_read_b128 a[132:135], v192 offset:1024
ds_read_b128 a[140:143], v192 offset:1536
v_mfma_f32_16x16x32_bf16 a[84:87], a[148:151], v[148:151], a[84:87]
v_exp_f32 v90, v90
v_mfma_f32_16x16x32_bf16 a[88:91], a[148:151], v[152:155], a[88:91]
v_mfma_f32_16x16x32_bf16 a[92:95], a[148:151], v[156:159], a[92:95]
v_exp_f32 v91, v91
v_mfma_f32_16x16x32_bf16 a[96:99], a[152:155], v[144:147], a[96:99]
v_exp_f32 v92, v92
v_mfma_f32_16x16x32_bf16 a[100:103], a[152:155], v[148:151], a[100:103]
v_cvt_pk_bf16_f32 v140, v88, v89
v_cvt_pk_bf16_f32 v141, v90, v91
v_mfma_f32_16x16x32_bf16 a[104:107], a[152:155], v[152:155], a[104:107]
v_exp_f32 v93, v93
v_mfma_f32_16x16x32_bf16 a[108:111], a[152:155], v[156:159], a[108:111]
v_mfma_f32_16x16x32_bf16 a[112:115], a[156:159], v[144:147], a[112:115]
s_waitcnt lgkmcnt(3)
ds_read_b128 a[144:147], v192 offset:4096
ds_read_b128 a[148:151], v192 offset:4608
v_mfma_f32_16x16x32_bf16 a[116:119], a[156:159], v[148:151], a[116:119]
v_exp_f32 v94, v94
v_mfma_f32_16x16x32_bf16 a[120:123], a[156:159], v[152:155], a[120:123]
v_exp_f32 v95, v95
v_mfma_f32_16x16x32_bf16 a[124:127], a[156:159], v[156:159], a[124:127]
v_cvt_pk_bf16_f32 v142, v92, v93
v_cvt_pk_bf16_f32 v143, v94, v95
v_mfma_f32_16x16x32_bf16 v[64:67], a[128:131], v[0:3], v[160:163]
s_waitcnt lgkmcnt(4)
s_and_b32 s54, s62, 1
v_mfma_f32_16x16x32_bf16 v[64:67], a[136:139], v[4:7], v[64:67]
v_exp_f32 v96, v96
v_mfma_f32_16x16x32_bf16 v[72:75], a[128:131], v[16:19], v[164:167]
s_lshl_b32 s54, s54, 10
v_mfma_f32_16x16x32_bf16 v[72:75], a[136:139], v[20:23], v[72:75]
v_exp_f32 v97, v97
v_mfma_f32_16x16x32_bf16 v[80:83], a[128:131], v[32:35], v[168:171]
s_waitcnt lgkmcnt(3)
ds_read_b128 a[152:155], v192 offset:5120
ds_read_b128 a[156:159], v192 offset:5632
v_mfma_f32_16x16x32_bf16 v[80:83], a[136:139], v[36:39], v[80:83]
v_add_u32 v204, s54, v198
v_mfma_f32_16x16x32_bf16 v[88:91], a[128:131], v[48:51], v[172:175]
v_add_u32 v205, s54, v199
v_mfma_f32_16x16x32_bf16 v[88:91], a[136:139], v[52:55], v[88:91]
v_exp_f32 v98, v98
v_mfma_f32_16x16x32_bf16 v[64:67], a[132:135], v[8:11], v[64:67]
s_waitcnt lgkmcnt(4)
v_add_u32 v206, s54, v200
v_mfma_f32_16x16x32_bf16 v[64:67], a[140:143], v[12:15], v[64:67]
v_add_u32 v207, s54, v201
v_mfma_f32_16x16x32_bf16 v[72:75], a[132:135], v[24:27], v[72:75]
v_exp_f32 v99, v99
v_mfma_f32_16x16x32_bf16 v[72:75], a[140:143], v[28:31], v[72:75]
s_add_u32 m0, s40, 114688
v_add_u32 v208, s54, v202
v_mfma_f32_16x16x32_bf16 v[80:83], a[132:135], v[40:43], v[80:83]
s_waitcnt lgkmcnt(3)
v_exp_f32 v100, v100
v_mfma_f32_16x16x32_bf16 v[80:83], a[140:143], v[44:47], v[80:83]
s_xor_b32 s78, s42, 81920
buffer_load_dwordx4 v210, s[16:19], s78 offen lds
v_mfma_f32_16x16x32_bf16 v[88:91], a[132:135], v[56:59], v[88:91]
v_add_u32 v209, s54, v203
v_cvt_pk_bf16_f32 v144, v96, v97
v_mfma_f32_16x16x32_bf16 v[88:91], a[140:143], v[60:63], v[88:91]
buffer_load_dwordx4 v211, s[16:19], s78 offen offset:2048 lds
v_mfma_f32_16x16x32_bf16 v[68:71], a[144:147], v[0:3], v[160:163]
s_waitcnt lgkmcnt(2)
ds_read_b64_tr_b16 a[128:129], v198 offset:16384
ds_read_b64_tr_b16 a[130:131], v198 offset:20480
v_mfma_f32_16x16x32_bf16 v[68:71], a[148:151], v[4:7], v[68:71]
s_lshr_b32 s54, s62, 1
v_cvt_pk_bf16_f32 v145, v98, v99
v_mfma_f32_16x16x32_bf16 v[76:79], a[144:147], v[16:19], v[164:167]
v_exp_f32 v101, v101
v_mfma_f32_16x16x32_bf16 v[76:79], a[148:151], v[20:23], v[76:79]
s_lshl_b32 s54, s54, 13
v_mfma_f32_16x16x32_bf16 v[84:87], a[144:147], v[32:35], v[168:171]
s_waitcnt lgkmcnt(3)
ds_read_b64_tr_b16 a[132:133], v201 offset:16384
ds_read_b64_tr_b16 a[134:135], v201 offset:20480
v_mfma_f32_16x16x32_bf16 v[84:87], a[148:151], v[36:39], v[84:87]
s_add_u32 s54, s54, s63
v_mfma_f32_16x16x32_bf16 v[92:95], a[144:147], v[48:51], v[172:175]
v_exp_f32 v102, v102
v_mfma_f32_16x16x32_bf16 v[92:95], a[148:151], v[52:55], v[92:95]
v_add_u32 v195, s54, v217
v_mfma_f32_16x16x32_bf16 v[68:71], a[152:155], v[8:11], v[68:71]
s_waitcnt lgkmcnt(4)
ds_read_b64_tr_b16 a[136:137], v198 offset:16896
ds_read_b64_tr_b16 a[138:139], v198 offset:20992
v_mfma_f32_16x16x32_bf16 v[68:71], a[156:159], v[12:15], v[68:71]
v_exp_f32 v103, v103
v_mfma_f32_16x16x32_bf16 v[76:79], a[152:155], v[24:27], v[76:79]
v_add_u32 v196, 0x10000, v195
v_mfma_f32_16x16x32_bf16 v[76:79], a[156:159], v[28:31], v[76:79]
v_add_u32 v197, 0x20000, v195
v_mfma_f32_16x16x32_bf16 v[84:87], a[152:155], v[40:43], v[84:87]
ds_read_b64_tr_b16 a[140:141], v201 offset:16896
ds_read_b64_tr_b16 a[142:143], v201 offset:20992
v_mfma_f32_16x16x32_bf16 v[84:87], a[156:159], v[44:47], v[84:87]
v_exp_f32 v104, v104
v_mfma_f32_16x16x32_bf16 v[92:95], a[152:155], v[56:59], v[92:95]
s_and_b32 s54, s61, 1
v_cvt_pk_bf16_f32 v146, v100, v101
v_mfma_f32_16x16x32_bf16 v[92:95], a[156:159], v[60:63], v[92:95]
s_lshl_b32 s54, s54, 6
v_cvt_pk_bf16_f32 v147, v102, v103
v_mfma_f32_16x16x32_bf16 v[176:179], v[232:235], v[128:131], v[176:179]
s_waitcnt lgkmcnt(6)
ds_read_b64_tr_b16 a[144:145], v198 offset:17408
ds_read_b64_tr_b16 a[146:147], v198 offset:21504
v_mfma_f32_16x16x32_bf16 v[180:183], v[232:235], v[132:135], v[180:183]
v_exp_f32 v105, v105
v_mfma_f32_16x16x32_bf16 v[184:187], v[232:235], v[136:139], v[184:187]
v_lshrrev_b32 v236, 8, v212
v_mfma_f32_16x16x32_bf16 v[188:191], v[232:235], v[140:143], v[188:191]
v_exp_f32 v106, v106
v_mfma_f32_16x16x32_bf16 a[0:3], a[128:131], v[128:131], a[0:3]
s_waitcnt lgkmcnt(6)
ds_read_b64_tr_b16 a[148:149], v201 offset:17408
ds_read_b64_tr_b16 a[150:151], v201 offset:21504
v_mfma_f32_16x16x32_bf16 a[4:7], a[128:131], v[132:135], a[4:7]
v_and_b32 v237, 0x7f, v212
v_mfma_f32_16x16x32_bf16 a[8:11], a[128:131], v[136:139], a[8:11]
v_xor_b32 v237, s54, v237
v_mfma_f32_16x16x32_bf16 a[12:15], a[128:131], v[140:143], a[12:15]
v_exp_f32 v107, v107
v_mfma_f32_16x16x32_bf16 a[16:19], a[132:135], v[128:131], a[16:19]
s_waitcnt lgkmcnt(6)
ds_read_b64_tr_b16 a[128:129], v198 offset:17920
ds_read_b64_tr_b16 a[130:131], v198 offset:22016
v_mfma_f32_16x16x32_bf16 a[20:23], a[132:135], v[132:135], a[20:23]
v_lshl_add_u32 v213, v236, 10, v237
v_mfma_f32_16x16x32_bf16 a[24:27], a[132:135], v[136:139], a[24:27]
v_add_u32 v214, 6144, v213
v_mfma_f32_16x16x32_bf16 a[28:31], a[132:135], v[140:143], a[28:31]
v_exp_f32 v108, v108
v_mfma_f32_16x16x32_bf16 a[32:35], a[136:139], v[128:131], a[32:35]
s_waitcnt lgkmcnt(6)
ds_read_b64_tr_b16 a[132:133], v201 offset:17920
ds_read_b64_tr_b16 a[134:135], v201 offset:22016
v_mfma_f32_16x16x32_bf16 a[36:39], a[136:139], v[132:135], a[36:39]
v_and_b32 v236, 0xf0, v217
v_cvt_pk_bf16_f32 v148, v104, v105
v_mfma_f32_16x16x32_bf16 a[40:43], a[136:139], v[136:139], a[40:43]
v_cvt_pk_bf16_f32 v149, v106, v107
v_mfma_f32_16x16x32_bf16 a[44:47], a[136:139], v[140:143], a[44:47]
v_lshlrev_b32 v236, 6, v236
v_mfma_f32_16x16x32_bf16 a[48:51], a[140:143], v[128:131], a[48:51]
s_waitcnt lgkmcnt(6)
ds_read_b128 a[136:139], v192 offset:8192
ds_read_b128 a[152:155], v192 offset:8704
v_mfma_f32_16x16x32_bf16 a[52:55], a[140:143], v[132:135], a[52:55]
v_bfe_u32 v237, v217, 8, 1
v_mfma_f32_16x16x32_bf16 a[56:59], a[140:143], v[136:139], a[56:59]
v_exp_f32 v109, v109
v_mfma_f32_16x16x32_bf16 a[60:63], a[140:143], v[140:143], a[60:63]
s_add_u32 m0, s40, 115712
v_lshl_add_u32 v236, v237, 5, v236
v_mfma_f32_16x16x32_bf16 a[64:67], a[144:147], v[128:131], a[64:67]
s_waitcnt lgkmcnt(6)
v_bfe_u32 v237, v217, 9, 1
v_mfma_f32_16x16x32_bf16 a[68:71], a[144:147], v[132:135], a[68:71]
s_xor_b32 s79, s42, 82048
buffer_load_dwordx4 v210, s[16:19], s79 offen lds
v_mfma_f32_16x16x32_bf16 a[72:75], a[144:147], v[136:139], a[72:75]
v_exp_f32 v110, v110
v_mfma_f32_16x16x32_bf16 a[76:79], a[144:147], v[140:143], a[76:79]
buffer_load_dwordx4 v211, s[16:19], s79 offen offset:2048 lds
v_mfma_f32_16x16x32_bf16 a[80:83], a[148:151], v[128:131], a[80:83]
s_waitcnt lgkmcnt(4)
ds_read_b128 a[140:143], v192 offset:9216
ds_read_b128 a[144:147], v192 offset:9728
v_mfma_f32_16x16x32_bf16 a[84:87], a[148:151], v[132:135], a[84:87]
v_lshl_add_u32 v236, v237, 4, v236
v_mfma_f32_16x16x32_bf16 a[88:91], a[148:151], v[136:139], a[88:91]
v_exp_f32 v111, v111
v_mfma_f32_16x16x32_bf16 a[92:95], a[148:151], v[140:143], a[92:95]
v_add_u32 v216, s51, v236
v_mfma_f32_16x16x32_bf16 a[96:99], a[128:131], v[128:131], a[96:99]
s_waitcnt lgkmcnt(4)
v_exp_f32 v112, v112
v_mfma_f32_16x16x32_bf16 a[100:103], a[128:131], v[132:135], a[100:103]
v_cvt_pk_bf16_f32 v150, v108, v109
v_cvt_pk_bf16_f32 v151, v110, v111
v_mfma_f32_16x16x32_bf16 a[104:107], a[128:131], v[136:139], a[104:107]
v_exp_f32 v113, v113
v_mfma_f32_16x16x32_bf16 a[108:111], a[128:131], v[140:143], a[108:111]
v_exp_f32 v114, v114
v_mfma_f32_16x16x32_bf16 a[112:115], a[132:135], v[128:131], a[112:115]
s_waitcnt lgkmcnt(3)
ds_read_b128 a[128:131], v192 offset:12288
ds_read_b128 a[148:151], v192 offset:12800
v_mfma_f32_16x16x32_bf16 a[116:119], a[132:135], v[132:135], a[116:119]
v_exp_f32 v115, v115
v_mfma_f32_16x16x32_bf16 a[120:123], a[132:135], v[136:139], a[120:123]
v_exp_f32 v116, v116
v_mfma_f32_16x16x32_bf16 a[124:127], a[132:135], v[140:143], a[124:127]
v_cvt_pk_bf16_f32 v152, v112, v113
v_cvt_pk_bf16_f32 v153, v114, v115
v_exp_f32 v117, v117
v_exp_f32 v118, v118
v_exp_f32 v119, v119
v_exp_f32 v120, v120
v_cvt_pk_bf16_f32 v154, v116, v117
v_cvt_pk_bf16_f32 v155, v118, v119
v_exp_f32 v121, v121
v_exp_f32 v122, v122
v_exp_f32 v123, v123
v_exp_f32 v124, v124
v_cvt_pk_bf16_f32 v156, v120, v121
v_cvt_pk_bf16_f32 v157, v122, v123
v_exp_f32 v125, v125
v_exp_f32 v126, v126
v_exp_f32 v127, v127
v_cvt_pk_bf16_f32 v158, v124, v125
v_cvt_pk_bf16_f32 v159, v126, v127
v_mfma_f32_16x16x32_bf16 v[96:99], a[136:139], v[0:3], v[160:163]
s_waitcnt lgkmcnt(4)
v_exp_f32 v64, v64
v_mfma_f32_16x16x32_bf16 v[96:99], a[152:155], v[4:7], v[96:99]
v_mfma_f32_16x16x32_bf16 v[104:107], a[136:139], v[16:19], v[164:167]
v_exp_f32 v65, v65
v_mfma_f32_16x16x32_bf16 v[104:107], a[152:155], v[20:23], v[104:107]
v_exp_f32 v66, v66
v_mfma_f32_16x16x32_bf16 v[112:115], a[136:139], v[32:35], v[168:171]
s_waitcnt lgkmcnt(3)
ds_read_b128 a[132:135], v192 offset:13312
ds_read_b128 a[156:159], v192 offset:13824
v_mfma_f32_16x16x32_bf16 v[112:115], a[152:155], v[36:39], v[112:115]
v_exp_f32 v67, v67
v_mfma_f32_16x16x32_bf16 v[120:123], a[136:139], v[48:51], v[172:175]
v_mfma_f32_16x16x32_bf16 v[120:123], a[152:155], v[52:55], v[120:123]
v_exp_f32 v68, v68
v_mfma_f32_16x16x32_bf16 v[96:99], a[140:143], v[8:11], v[96:99]
s_waitcnt lgkmcnt(4)
v_cvt_pk_bf16_f32 v128, v64, v65
v_cvt_pk_bf16_f32 v129, v66, v67
v_mfma_f32_16x16x32_bf16 v[96:99], a[144:147], v[12:15], v[96:99]
v_exp_f32 v69, v69
v_mfma_f32_16x16x32_bf16 v[104:107], a[140:143], v[24:27], v[104:107]
v_exp_f32 v70, v70
v_mfma_f32_16x16x32_bf16 v[104:107], a[144:147], v[28:31], v[104:107]
v_mfma_f32_16x16x32_bf16 v[112:115], a[140:143], v[40:43], v[112:115]
s_waitcnt lgkmcnt(3)
v_exp_f32 v71, v71
v_mfma_f32_16x16x32_bf16 v[112:115], a[144:147], v[44:47], v[112:115]
v_exp_f32 v72, v72
v_mfma_f32_16x16x32_bf16 v[120:123], a[140:143], v[56:59], v[120:123]
v_cvt_pk_bf16_f32 v130, v68, v69
v_cvt_pk_bf16_f32 v131, v70, v71
v_mfma_f32_16x16x32_bf16 v[120:123], a[144:147], v[60:63], v[120:123]
v_exp_f32 v73, v73
v_mfma_f32_16x16x32_bf16 v[100:103], a[128:131], v[0:3], v[160:163]
s_waitcnt lgkmcnt(2)
ds_read_b64_tr_b16 a[136:137], v198 offset:24576
ds_read_b64_tr_b16 a[138:139], v198 offset:28672
v_mfma_f32_16x16x32_bf16 v[100:103], a[148:151], v[4:7], v[100:103]
v_mfma_f32_16x16x32_bf16 v[108:111], a[128:131], v[16:19], v[164:167]
v_exp_f32 v74, v74
v_mfma_f32_16x16x32_bf16 v[108:111], a[148:151], v[20:23], v[108:111]
v_exp_f32 v75, v75
v_mfma_f32_16x16x32_bf16 v[116:119], a[128:131], v[32:35], v[168:171]
s_waitcnt lgkmcnt(3)
ds_read_b64_tr_b16 a[140:141], v201 offset:24576
ds_read_b64_tr_b16 a[142:143], v201 offset:28672
v_mfma_f32_16x16x32_bf16 v[116:119], a[148:151], v[36:39], v[116:119]
v_exp_f32 v76, v76
v_mfma_f32_16x16x32_bf16 v[124:127], a[128:131], v[48:51], v[172:175]
v_cvt_pk_bf16_f32 v132, v72, v73
v_mfma_f32_16x16x32_bf16 v[124:127], a[148:151], v[52:55], v[124:127]
s_add_u32 m0, s40, 49152
v_cvt_pk_bf16_f32 v133, v74, v75
v_mfma_f32_16x16x32_bf16 v[100:103], a[132:135], v[8:11], v[100:103]
s_waitcnt lgkmcnt(4)
ds_read_b64_tr_b16 a[128:129], v198 offset:25088
ds_read_b64_tr_b16 a[130:131], v198 offset:29184
v_mfma_f32_16x16x32_bf16 v[100:103], a[156:159], v[12:15], v[100:103]
s_xor_b32 s78, s42, 65536
buffer_load_dwordx4 v212, s[20:23], s78 offen lds
v_mfma_f32_16x16x32_bf16 v[108:111], a[132:135], v[24:27], v[108:111]
v_exp_f32 v77, v77
v_mfma_f32_16x16x32_bf16 v[108:111], a[156:159], v[28:31], v[108:111]
buffer_load_dwordx4 v212, s[20:23], s78 offen offset:2048 lds
v_mfma_f32_16x16x32_bf16 v[116:119], a[132:135], v[40:43], v[116:119]
ds_read_b64_tr_b16 a[144:145], v201 offset:25088
ds_read_b64_tr_b16 a[146:147], v201 offset:29184
v_mfma_f32_16x16x32_bf16 v[116:119], a[156:159], v[44:47], v[116:119]
v_exp_f32 v78, v78
v_mfma_f32_16x16x32_bf16 v[124:127], a[132:135], v[56:59], v[124:127]
v_exp_f32 v79, v79
v_mfma_f32_16x16x32_bf16 v[124:127], a[156:159], v[60:63], v[124:127]
v_mfma_f32_16x16x32_bf16 v[176:179], v[232:235], v[144:147], v[176:179]
s_waitcnt lgkmcnt(6)
ds_read_b64_tr_b16 a[132:133], v198 offset:25600
ds_read_b64_tr_b16 a[134:135], v198 offset:29696
v_mfma_f32_16x16x32_bf16 v[180:183], v[232:235], v[148:151], v[180:183]
ds_read_b64_tr_b16 a[156:157], v201 offset:26112
ds_read_b64_tr_b16 a[158:159], v201 offset:30208
v_mfma_f32_16x16x32_bf16 v[184:187], v[232:235], v[152:155], v[184:187]
ds_read_b64_tr_b16 a[152:153], v198 offset:26112
ds_read_b64_tr_b16 a[154:155], v198 offset:30208
v_mfma_f32_16x16x32_bf16 v[188:191], v[232:235], v[156:159], v[188:191]
ds_read_b64_tr_b16 a[148:149], v201 offset:25600
ds_read_b64_tr_b16 a[150:151], v201 offset:29696
v_mfma_f32_16x16x32_bf16 a[0:3], a[136:139], v[144:147], a[0:3]
s_waitcnt lgkmcnt(12)
v_exp_f32 v80, v80
v_mfma_f32_16x16x32_bf16 a[4:7], a[136:139], v[148:151], a[4:7]
v_cvt_pk_bf16_f32 v134, v76, v77
v_cvt_pk_bf16_f32 v135, v78, v79
v_mfma_f32_16x16x32_bf16 a[8:11], a[136:139], v[152:155], a[8:11]
v_exp_f32 v81, v81
v_mfma_f32_16x16x32_bf16 a[12:15], a[136:139], v[156:159], a[12:15]
v_mfma_f32_16x16x32_bf16 a[16:19], a[140:143], v[144:147], a[16:19]
s_waitcnt lgkmcnt(10)
v_exp_f32 v82, v82
v_mfma_f32_16x16x32_bf16 a[20:23], a[140:143], v[148:151], a[20:23]
v_exp_f32 v83, v83
v_mfma_f32_16x16x32_bf16 a[24:27], a[140:143], v[152:155], a[24:27]
v_exp_f32 v84, v84
v_mfma_f32_16x16x32_bf16 a[28:31], a[140:143], v[156:159], a[28:31]
v_cvt_pk_bf16_f32 v136, v80, v81
v_cvt_pk_bf16_f32 v137, v82, v83
v_mfma_f32_16x16x32_bf16 a[32:35], a[128:131], v[144:147], a[32:35]
s_waitcnt vmcnt(15) lgkmcnt(0)
s_barrier
v_mfma_f32_16x16x32_bf16 a[36:39], a[128:131], v[148:151], a[36:39]
v_mfma_f32_16x16x32_bf16 a[40:43], a[128:131], v[152:155], a[40:43]
v_exp_f32 v85, v85
v_mfma_f32_16x16x32_bf16 a[44:47], a[128:131], v[156:159], a[44:47]
v_exp_f32 v86, v86
v_mfma_f32_16x16x32_bf16 a[48:51], a[144:147], v[144:147], a[48:51]
ds_read_b128 a[128:131], v194 offset:16384
ds_read_b128 a[136:139], v194 offset:16896
v_mfma_f32_16x16x32_bf16 a[52:55], a[144:147], v[148:151], a[52:55]
v_exp_f32 v87, v87
v_mfma_f32_16x16x32_bf16 a[56:59], a[144:147], v[152:155], a[56:59]
v_exp_f32 v88, v88
v_mfma_f32_16x16x32_bf16 a[60:63], a[144:147], v[156:159], a[60:63]
v_cvt_pk_bf16_f32 v138, v84, v85
v_mfma_f32_16x16x32_bf16 a[64:67], a[132:135], v[144:147], a[64:67]
v_cvt_pk_bf16_f32 v139, v86, v87
v_mfma_f32_16x16x32_bf16 a[68:71], a[132:135], v[148:151], a[68:71]
v_exp_f32 v89, v89
v_mfma_f32_16x16x32_bf16 a[72:75], a[132:135], v[152:155], a[72:75]
v_exp_f32 v90, v90
v_mfma_f32_16x16x32_bf16 a[76:79], a[132:135], v[156:159], a[76:79]
v_mfma_f32_16x16x32_bf16 a[80:83], a[148:151], v[144:147], a[80:83]
ds_read_b128 a[132:135], v194 offset:17408
ds_read_b128 a[140:143], v194 offset:17920
v_mfma_f32_16x16x32_bf16 a[84:87], a[148:151], v[148:151], a[84:87]
v_exp_f32 v91, v91
v_mfma_f32_16x16x32_bf16 a[88:91], a[148:151], v[152:155], a[88:91]
v_exp_f32 v92, v92
v_mfma_f32_16x16x32_bf16 a[92:95], a[148:151], v[156:159], a[92:95]
v_cvt_pk_bf16_f32 v140, v88, v89
v_cvt_pk_bf16_f32 v141, v90, v91
v_mfma_f32_16x16x32_bf16 a[96:99], a[152:155], v[144:147], a[96:99]
v_exp_f32 v93, v93
v_mfma_f32_16x16x32_bf16 a[100:103], a[152:155], v[148:151], a[100:103]
v_mfma_f32_16x16x32_bf16 a[104:107], a[152:155], v[152:155], a[104:107]
v_exp_f32 v94, v94
v_mfma_f32_16x16x32_bf16 a[108:111], a[152:155], v[156:159], a[108:111]
s_add_u32 m0, s40, 50176
v_exp_f32 v95, v95
v_mfma_f32_16x16x32_bf16 a[112:115], a[156:159], v[144:147], a[112:115]
s_waitcnt lgkmcnt(3)
ds_read_b128 a[144:147], v194 offset:20480
ds_read_b128 a[148:151], v194 offset:20992
v_mfma_f32_16x16x32_bf16 a[116:119], a[156:159], v[148:151], a[116:119]
s_xor_b32 s79, s42, 65664
buffer_load_dwordx4 v212, s[20:23], s79 offen lds
v_mfma_f32_16x16x32_bf16 a[120:123], a[156:159], v[152:155], a[120:123]
v_cvt_pk_bf16_f32 v142, v92, v93
v_cvt_pk_bf16_f32 v143, v94, v95
v_mfma_f32_16x16x32_bf16 a[124:127], a[156:159], v[156:159], a[124:127]
buffer_load_dwordx4 v212, s[20:23], s79 offen offset:2048 lds
v_mfma_f32_16x16x32_bf16 v[64:67], a[128:131], v[0:3], v[160:163]
s_waitcnt lgkmcnt(4)
v_mfma_f32_16x16x32_bf16 v[64:67], a[136:139], v[4:7], v[64:67]
v_exp_f32 v96, v96
v_mfma_f32_16x16x32_bf16 v[72:75], a[128:131], v[16:19], v[164:167]
v_exp_f32 v97, v97
v_mfma_f32_16x16x32_bf16 v[72:75], a[136:139], v[20:23], v[72:75]
v_exp_f32 v98, v98
v_mfma_f32_16x16x32_bf16 v[80:83], a[128:131], v[32:35], v[168:171]
s_waitcnt lgkmcnt(3)
ds_read_b128 a[152:155], v194 offset:21504
ds_read_b128 a[156:159], v194 offset:22016
v_mfma_f32_16x16x32_bf16 v[80:83], a[136:139], v[36:39], v[80:83]
v_mfma_f32_16x16x32_bf16 v[88:91], a[128:131], v[48:51], v[172:175]
v_exp_f32 v99, v99
v_mfma_f32_16x16x32_bf16 v[88:91], a[136:139], v[52:55], v[88:91]
v_exp_f32 v100, v100
v_mfma_f32_16x16x32_bf16 v[64:67], a[132:135], v[8:11], v[64:67]
s_waitcnt lgkmcnt(4)
v_cvt_pk_bf16_f32 v144, v96, v97
v_cvt_pk_bf16_f32 v145, v98, v99
v_mfma_f32_16x16x32_bf16 v[64:67], a[140:143], v[12:15], v[64:67]
v_mfma_f32_16x16x32_bf16 v[72:75], a[132:135], v[24:27], v[72:75]
v_exp_f32 v101, v101
v_mfma_f32_16x16x32_bf16 v[72:75], a[140:143], v[28:31], v[72:75]
v_exp_f32 v102, v102
v_mfma_f32_16x16x32_bf16 v[80:83], a[132:135], v[40:43], v[80:83]
s_waitcnt lgkmcnt(3)
v_exp_f32 v103, v103
v_mfma_f32_16x16x32_bf16 v[80:83], a[140:143], v[44:47], v[80:83]
v_mfma_f32_16x16x32_bf16 v[88:91], a[132:135], v[56:59], v[88:91]
v_exp_f32 v104, v104
v_mfma_f32_16x16x32_bf16 v[88:91], a[140:143], v[60:63], v[88:91]
v_cvt_pk_bf16_f32 v146, v100, v101
v_cvt_pk_bf16_f32 v147, v102, v103
v_mfma_f32_16x16x32_bf16 v[68:71], a[144:147], v[0:3], v[160:163]
s_waitcnt lgkmcnt(2)
ds_read_b64_tr_b16 a[128:129], v198 offset:32768
ds_read_b64_tr_b16 a[130:131], v198 offset:36864
v_mfma_f32_16x16x32_bf16 v[68:71], a[148:151], v[4:7], v[68:71]
v_exp_f32 v105, v105
v_mfma_f32_16x16x32_bf16 v[76:79], a[144:147], v[16:19], v[164:167]
v_mfma_f32_16x16x32_bf16 v[76:79], a[148:151], v[20:23], v[76:79]
v_exp_f32 v106, v106
v_mfma_f32_16x16x32_bf16 v[84:87], a[144:147], v[32:35], v[168:171]
s_waitcnt lgkmcnt(3)
ds_read_b64_tr_b16 a[132:133], v201 offset:32768
ds_read_b64_tr_b16 a[134:135], v201 offset:36864
v_mfma_f32_16x16x32_bf16 v[84:87], a[148:151], v[36:39], v[84:87]
v_exp_f32 v107, v107
v_mfma_f32_16x16x32_bf16 v[92:95], a[144:147], v[48:51], v[172:175]
v_exp_f32 v108, v108
v_mfma_f32_16x16x32_bf16 v[92:95], a[148:151], v[52:55], v[92:95]
v_cvt_pk_bf16_f32 v148, v104, v105
v_mfma_f32_16x16x32_bf16 v[68:71], a[152:155], v[8:11], v[68:71]
s_waitcnt lgkmcnt(4)
ds_read_b64_tr_b16 a[136:137], v198 offset:33280
ds_read_b64_tr_b16 a[138:139], v198 offset:37376
v_mfma_f32_16x16x32_bf16 v[68:71], a[156:159], v[12:15], v[68:71]
v_cvt_pk_bf16_f32 v149, v106, v107
v_mfma_f32_16x16x32_bf16 v[76:79], a[152:155], v[24:27], v[76:79]
v_exp_f32 v109, v109
v_mfma_f32_16x16x32_bf16 v[76:79], a[156:159], v[28:31], v[76:79]
v_exp_f32 v110, v110
v_mfma_f32_16x16x32_bf16 v[84:87], a[152:155], v[40:43], v[84:87]
ds_read_b64_tr_b16 a[140:141], v201 offset:33280
ds_read_b64_tr_b16 a[142:143], v201 offset:37376
v_mfma_f32_16x16x32_bf16 v[84:87], a[156:159], v[44:47], v[84:87]
v_mfma_f32_16x16x32_bf16 v[92:95], a[152:155], v[56:59], v[92:95]
v_exp_f32 v111, v111
v_mfma_f32_16x16x32_bf16 v[92:95], a[156:159], v[60:63], v[92:95]
v_exp_f32 v112, v112
v_mfma_f32_16x16x32_bf16 v[176:179], v[232:235], v[128:131], v[176:179]
s_waitcnt lgkmcnt(6)
ds_read_b64_tr_b16 a[144:145], v198 offset:33792
ds_read_b64_tr_b16 a[146:147], v198 offset:37888
v_mfma_f32_16x16x32_bf16 v[180:183], v[232:235], v[132:135], v[180:183]
v_cvt_pk_bf16_f32 v150, v108, v109
v_mfma_f32_16x16x32_bf16 v[184:187], v[232:235], v[136:139], v[184:187]
v_cvt_pk_bf16_f32 v151, v110, v111
v_mfma_f32_16x16x32_bf16 v[188:191], v[232:235], v[140:143], v[188:191]
s_add_u32 m0, s40, 98304
v_exp_f32 v113, v113
v_mfma_f32_16x16x32_bf16 a[0:3], a[128:131], v[128:131], a[0:3]
s_waitcnt lgkmcnt(6)
ds_read_b64_tr_b16 a[148:149], v201 offset:33792
ds_read_b64_tr_b16 a[150:151], v201 offset:37888
v_mfma_f32_16x16x32_bf16 a[4:7], a[128:131], v[132:135], a[4:7]
s_xor_b32 s78, s42, 98304
buffer_load_dwordx4 v210, s[16:19], s78 offen lds
v_mfma_f32_16x16x32_bf16 a[8:11], a[128:131], v[136:139], a[8:11]
v_exp_f32 v114, v114
v_mfma_f32_16x16x32_bf16 a[12:15], a[128:131], v[140:143], a[12:15]
buffer_load_dwordx4 v211, s[16:19], s78 offen offset:2048 lds
v_mfma_f32_16x16x32_bf16 a[16:19], a[132:135], v[128:131], a[16:19]
s_waitcnt lgkmcnt(6)
ds_read_b64_tr_b16 a[128:129], v198 offset:34304
ds_read_b64_tr_b16 a[130:131], v198 offset:38400
v_mfma_f32_16x16x32_bf16 a[20:23], a[132:135], v[132:135], a[20:23]
v_mfma_f32_16x16x32_bf16 a[24:27], a[132:135], v[136:139], a[24:27]
v_exp_f32 v115, v115
v_mfma_f32_16x16x32_bf16 a[28:31], a[132:135], v[140:143], a[28:31]
v_exp_f32 v116, v116
v_mfma_f32_16x16x32_bf16 a[32:35], a[136:139], v[128:131], a[32:35]
s_waitcnt lgkmcnt(6)
ds_read_b64_tr_b16 a[132:133], v201 offset:34304
ds_read_b64_tr_b16 a[134:135], v201 offset:38400
v_mfma_f32_16x16x32_bf16 a[36:39], a[136:139], v[132:135], a[36:39]
v_cvt_pk_bf16_f32 v152, v112, v113
v_cvt_pk_bf16_f32 v153, v114, v115
v_mfma_f32_16x16x32_bf16 a[40:43], a[136:139], v[136:139], a[40:43]
v_mfma_f32_16x16x32_bf16 a[44:47], a[136:139], v[140:143], a[44:47]
v_exp_f32 v117, v117
v_mfma_f32_16x16x32_bf16 a[48:51], a[140:143], v[128:131], a[48:51]
s_waitcnt lgkmcnt(6)
ds_read_b128 a[136:139], v194 offset:24576
ds_read_b128 a[152:155], v194 offset:25088
v_mfma_f32_16x16x32_bf16 a[52:55], a[140:143], v[132:135], a[52:55]
v_exp_f32 v118, v118
v_mfma_f32_16x16x32_bf16 a[56:59], a[140:143], v[136:139], a[56:59]
v_exp_f32 v119, v119
v_mfma_f32_16x16x32_bf16 a[60:63], a[140:143], v[140:143], a[60:63]
v_mfma_f32_16x16x32_bf16 a[64:67], a[144:147], v[128:131], a[64:67]
s_waitcnt lgkmcnt(6)
v_exp_f32 v120, v120
v_mfma_f32_16x16x32_bf16 a[68:71], a[144:147], v[132:135], a[68:71]
v_cvt_pk_bf16_f32 v154, v116, v117
v_cvt_pk_bf16_f32 v155, v118, v119
v_mfma_f32_16x16x32_bf16 a[72:75], a[144:147], v[136:139], a[72:75]
v_exp_f32 v121, v121
v_mfma_f32_16x16x32_bf16 a[76:79], a[144:147], v[140:143], a[76:79]
v_mfma_f32_16x16x32_bf16 a[80:83], a[148:151], v[128:131], a[80:83]
s_waitcnt lgkmcnt(4)
ds_read_b128 a[140:143], v194 offset:25600
ds_read_b128 a[144:147], v194 offset:26112
v_mfma_f32_16x16x32_bf16 a[84:87], a[148:151], v[132:135], a[84:87]
v_exp_f32 v122, v122
v_mfma_f32_16x16x32_bf16 a[88:91], a[148:151], v[136:139], a[88:91]
v_exp_f32 v123, v123
v_mfma_f32_16x16x32_bf16 a[92:95], a[148:151], v[140:143], a[92:95]
v_exp_f32 v124, v124
v_mfma_f32_16x16x32_bf16 a[96:99], a[128:131], v[128:131], a[96:99]
s_waitcnt lgkmcnt(4)
v_cvt_pk_bf16_f32 v156, v120, v121
v_mfma_f32_16x16x32_bf16 a[100:103], a[128:131], v[132:135], a[100:103]
v_cvt_pk_bf16_f32 v157, v122, v123
v_mfma_f32_16x16x32_bf16 a[104:107], a[128:131], v[136:139], a[104:107]
v_exp_f32 v125, v125
v_mfma_f32_16x16x32_bf16 a[108:111], a[128:131], v[140:143], a[108:111]
v_exp_f32 v126, v126
v_mfma_f32_16x16x32_bf16 a[112:115], a[132:135], v[128:131], a[112:115]
s_waitcnt lgkmcnt(3)
ds_read_b128 a[128:131], v194 offset:28672
ds_read_b128 a[148:151], v194 offset:29184
v_mfma_f32_16x16x32_bf16 a[116:119], a[132:135], v[132:135], a[116:119]
v_mfma_f32_16x16x32_bf16 a[120:123], a[132:135], v[136:139], a[120:123]
v_exp_f32 v127, v127
v_mfma_f32_16x16x32_bf16 a[124:127], a[132:135], v[140:143], a[124:127]
v_cvt_pk_bf16_f32 v158, v124, v125
v_cvt_pk_bf16_f32 v159, v126, v127
v_mfma_f32_16x16x32_bf16 v[96:99], a[136:139], v[0:3], v[160:163]
s_waitcnt lgkmcnt(4)
v_exp_f32 v64, v64
v_mfma_f32_16x16x32_bf16 v[96:99], a[152:155], v[4:7], v[96:99]
v_mfma_f32_16x16x32_bf16 v[104:107], a[136:139], v[16:19], v[164:167]
v_exp_f32 v65, v65
v_mfma_f32_16x16x32_bf16 v[104:107], a[152:155], v[20:23], v[104:107]
v_exp_f32 v66, v66
v_mfma_f32_16x16x32_bf16 v[112:115], a[136:139], v[32:35], v[168:171]
s_waitcnt lgkmcnt(3)
ds_read_b128 a[132:135], v194 offset:29696
ds_read_b128 a[156:159], v194 offset:30208
v_mfma_f32_16x16x32_bf16 v[112:115], a[152:155], v[36:39], v[112:115]
v_exp_f32 v67, v67
v_mfma_f32_16x16x32_bf16 v[120:123], a[136:139], v[48:51], v[172:175]
v_mfma_f32_16x16x32_bf16 v[120:123], a[152:155], v[52:55], v[120:123]
s_add_u32 m0, s40, 99328
v_exp_f32 v68, v68
v_mfma_f32_16x16x32_bf16 v[96:99], a[140:143], v[8:11], v[96:99]
L_tch12_%=:
s_cbranch_execz L_tch16_%=
s_waitcnt lgkmcnt(4)
v_cvt_pk_bf16_f32 v128, v64, v65
v_cvt_pk_bf16_f32 v129, v66, v67
v_mfma_f32_16x16x32_bf16 v[96:99], a[144:147], v[12:15], v[96:99]
s_xor_b32 s79, s42, 98432
buffer_load_dwordx4 v210, s[16:19], s79 offen lds
v_mfma_f32_16x16x32_bf16 v[104:107], a[140:143], v[24:27], v[104:107]
v_exp_f32 v69, v69
v_mfma_f32_16x16x32_bf16 v[104:107], a[144:147], v[28:31], v[104:107]
buffer_load_dwordx4 v211, s[16:19], s79 offen offset:2048 lds
v_mfma_f32_16x16x32_bf16 v[112:115], a[140:143], v[40:43], v[112:115]
s_waitcnt lgkmcnt(3)
v_exp_f32 v70, v70
v_mfma_f32_16x16x32_bf16 v[112:115], a[144:147], v[44:47], v[112:115]
v_mfma_f32_16x16x32_bf16 v[120:123], a[140:143], v[56:59], v[120:123]
v_exp_f32 v71, v71
v_mfma_f32_16x16x32_bf16 v[120:123], a[144:147], v[60:63], v[120:123]
v_exp_f32 v72, v72
v_mfma_f32_16x16x32_bf16 v[100:103], a[128:131], v[0:3], v[160:163]
s_waitcnt lgkmcnt(2)
ds_read_b64_tr_b16 a[136:137], v198 offset:40960
ds_read_b64_tr_b16 a[138:139], v198 offset:45056
v_mfma_f32_16x16x32_bf16 v[100:103], a[148:151], v[4:7], v[100:103]
v_cvt_pk_bf16_f32 v130, v68, v69
v_cvt_pk_bf16_f32 v131, v70, v71
v_mfma_f32_16x16x32_bf16 v[108:111], a[128:131], v[16:19], v[164:167]
v_exp_f32 v73, v73
v_mfma_f32_16x16x32_bf16 v[108:111], a[148:151], v[20:23], v[108:111]
v_mfma_f32_16x16x32_bf16 v[116:119], a[128:131], v[32:35], v[168:171]
s_waitcnt lgkmcnt(3)
ds_read_b64_tr_b16 a[140:141], v201 offset:40960
ds_read_b64_tr_b16 a[142:143], v201 offset:45056
v_mfma_f32_16x16x32_bf16 v[116:119], a[148:151], v[36:39], v[116:119]
v_exp_f32 v74, v74
v_mfma_f32_16x16x32_bf16 v[124:127], a[128:131], v[48:51], v[172:175]
v_exp_f32 v75, v75
v_mfma_f32_16x16x32_bf16 v[124:127], a[148:151], v[52:55], v[124:127]
v_exp_f32 v76, v76
v_mfma_f32_16x16x32_bf16 v[100:103], a[132:135], v[8:11], v[100:103]
s_waitcnt lgkmcnt(4)
ds_read_b64_tr_b16 a[128:129], v198 offset:41472
ds_read_b64_tr_b16 a[130:131], v198 offset:45568
v_mfma_f32_16x16x32_bf16 v[100:103], a[156:159], v[12:15], v[100:103]
v_cvt_pk_bf16_f32 v132, v72, v73
v_mfma_f32_16x16x32_bf16 v[108:111], a[132:135], v[24:27], v[108:111]
v_cvt_pk_bf16_f32 v133, v74, v75
v_mfma_f32_16x16x32_bf16 v[108:111], a[156:159], v[28:31], v[108:111]
v_exp_f32 v77, v77
v_mfma_f32_16x16x32_bf16 v[116:119], a[132:135], v[40:43], v[116:119]
ds_read_b64_tr_b16 a[144:145], v201 offset:41472
ds_read_b64_tr_b16 a[146:147], v201 offset:45568
v_mfma_f32_16x16x32_bf16 v[116:119], a[156:159], v[44:47], v[116:119]
v_exp_f32 v78, v78
v_mfma_f32_16x16x32_bf16 v[124:127], a[132:135], v[56:59], v[124:127]
v_exp_f32 v79, v79
v_mfma_f32_16x16x32_bf16 v[124:127], a[156:159], v[60:63], v[124:127]
v_mfma_f32_16x16x32_bf16 v[176:179], v[232:235], v[144:147], v[176:179]
s_waitcnt lgkmcnt(6)
ds_read_b64_tr_b16 a[132:133], v198 offset:41984
ds_read_b64_tr_b16 a[134:135], v198 offset:46080
v_mfma_f32_16x16x32_bf16 v[180:183], v[232:235], v[148:151], v[180:183]
ds_read_b64_tr_b16 a[156:157], v201 offset:42496
ds_read_b64_tr_b16 a[158:159], v201 offset:46592
v_mfma_f32_16x16x32_bf16 v[184:187], v[232:235], v[152:155], v[184:187]
ds_read_b64_tr_b16 a[152:153], v198 offset:42496
ds_read_b64_tr_b16 a[154:155], v198 offset:46592
v_mfma_f32_16x16x32_bf16 v[188:191], v[232:235], v[156:159], v[188:191]
ds_read_b64_tr_b16 a[148:149], v201 offset:41984
ds_read_b64_tr_b16 a[150:151], v201 offset:46080
v_mfma_f32_16x16x32_bf16 a[0:3], a[136:139], v[144:147], a[0:3]
s_waitcnt lgkmcnt(12)
v_exp_f32 v80, v80
v_mfma_f32_16x16x32_bf16 a[4:7], a[136:139], v[148:151], a[4:7]
v_cvt_pk_bf16_f32 v134, v76, v77
v_cvt_pk_bf16_f32 v135, v78, v79
v_mfma_f32_16x16x32_bf16 a[8:11], a[136:139], v[152:155], a[8:11]
v_exp_f32 v81, v81
v_mfma_f32_16x16x32_bf16 a[12:15], a[136:139], v[156:159], a[12:15]
v_mfma_f32_16x16x32_bf16 a[16:19], a[140:143], v[144:147], a[16:19]
s_waitcnt lgkmcnt(10)
v_exp_f32 v82, v82
v_mfma_f32_16x16x32_bf16 a[20:23], a[140:143], v[148:151], a[20:23]
v_exp_f32 v83, v83
v_mfma_f32_16x16x32_bf16 a[24:27], a[140:143], v[152:155], a[24:27]
v_exp_f32 v84, v84
v_mfma_f32_16x16x32_bf16 a[28:31], a[140:143], v[156:159], a[28:31]
v_cvt_pk_bf16_f32 v136, v80, v81
v_cvt_pk_bf16_f32 v137, v82, v83
v_mfma_f32_16x16x32_bf16 a[32:35], a[128:131], v[144:147], a[32:35]
s_waitcnt vmcnt(12) lgkmcnt(0)
s_barrier
v_mfma_f32_16x16x32_bf16 a[36:39], a[128:131], v[148:151], a[36:39]
v_mfma_f32_16x16x32_bf16 a[40:43], a[128:131], v[152:155], a[40:43]
v_exp_f32 v85, v85
v_mfma_f32_16x16x32_bf16 a[44:47], a[128:131], v[156:159], a[44:47]
s_add_u32 m0, s40, 131072
v_exp_f32 v86, v86
v_mfma_f32_16x16x32_bf16 a[48:51], a[144:147], v[144:147], a[48:51]
ds_read_b128 a[128:131], v193 offset:16384
ds_read_b128 a[136:139], v193 offset:16896
v_mfma_f32_16x16x32_bf16 a[52:55], a[144:147], v[148:151], a[52:55]
s_xor_b32 s78, s42, 81920
buffer_load_dwordx4 v212, s[20:23], s78 offen lds
v_mfma_f32_16x16x32_bf16 a[56:59], a[144:147], v[152:155], a[56:59]
v_exp_f32 v87, v87
v_mfma_f32_16x16x32_bf16 a[60:63], a[144:147], v[156:159], a[60:63]
buffer_load_dwordx4 v212, s[20:23], s78 offen offset:2048 lds
v_mfma_f32_16x16x32_bf16 a[64:67], a[132:135], v[144:147], a[64:67]
v_exp_f32 v88, v88
v_mfma_f32_16x16x32_bf16 a[68:71], a[132:135], v[148:151], a[68:71]
v_cvt_pk_bf16_f32 v138, v84, v85
v_mfma_f32_16x16x32_bf16 a[72:75], a[132:135], v[152:155], a[72:75]
v_cvt_pk_bf16_f32 v139, v86, v87
v_mfma_f32_16x16x32_bf16 a[76:79], a[132:135], v[156:159], a[76:79]
v_exp_f32 v89, v89
v_mfma_f32_16x16x32_bf16 a[80:83], a[148:151], v[144:147], a[80:83]
ds_read_b128 a[132:135], v193 offset:17408
ds_read_b128 a[140:143], v193 offset:17920
v_mfma_f32_16x16x32_bf16 a[84:87], a[148:151], v[148:151], a[84:87]
v_exp_f32 v90, v90
v_mfma_f32_16x16x32_bf16 a[88:91], a[148:151], v[152:155], a[88:91]
v_mfma_f32_16x16x32_bf16 a[92:95], a[148:151], v[156:159], a[92:95]
v_exp_f32 v91, v91
v_mfma_f32_16x16x32_bf16 a[96:99], a[152:155], v[144:147], a[96:99]
v_exp_f32 v92, v92
v_mfma_f32_16x16x32_bf16 a[100:103], a[152:155], v[148:151], a[100:103]
v_cvt_pk_bf16_f32 v140, v88, v89
v_cvt_pk_bf16_f32 v141, v90, v91
v_mfma_f32_16x16x32_bf16 a[104:107], a[152:155], v[152:155], a[104:107]
v_exp_f32 v93, v93
v_mfma_f32_16x16x32_bf16 a[108:111], a[152:155], v[156:159], a[108:111]
v_mfma_f32_16x16x32_bf16 a[112:115], a[156:159], v[144:147], a[112:115]
s_waitcnt lgkmcnt(3)
ds_read_b128 a[144:147], v193 offset:20480
ds_read_b128 a[148:151], v193 offset:20992
v_mfma_f32_16x16x32_bf16 a[116:119], a[156:159], v[148:151], a[116:119]
v_exp_f32 v94, v94
v_mfma_f32_16x16x32_bf16 a[120:123], a[156:159], v[152:155], a[120:123]
v_exp_f32 v95, v95
v_mfma_f32_16x16x32_bf16 a[124:127], a[156:159], v[156:159], a[124:127]
v_cvt_pk_bf16_f32 v142, v92, v93
v_cvt_pk_bf16_f32 v143, v94, v95
v_mfma_f32_16x16x32_bf16 v[64:67], a[128:131], v[0:3], v[160:163]
s_waitcnt lgkmcnt(4)
v_exp_f32 v96, v96
v_mfma_f32_16x16x32_bf16 v[64:67], a[136:139], v[4:7], v[64:67]
v_mfma_f32_16x16x32_bf16 v[72:75], a[128:131], v[16:19], v[164:167]
v_exp_f32 v97, v97
v_mfma_f32_16x16x32_bf16 v[72:75], a[136:139], v[20:23], v[72:75]
v_exp_f32 v98, v98
v_mfma_f32_16x16x32_bf16 v[80:83], a[128:131], v[32:35], v[168:171]
s_waitcnt lgkmcnt(3)
ds_read_b128 a[152:155], v193 offset:21504
ds_read_b128 a[156:159], v193 offset:22016
v_mfma_f32_16x16x32_bf16 v[80:83], a[136:139], v[36:39], v[80:83]
v_exp_f32 v99, v99
v_mfma_f32_16x16x32_bf16 v[88:91], a[128:131], v[48:51], v[172:175]
v_mfma_f32_16x16x32_bf16 v[88:91], a[136:139], v[52:55], v[88:91]
v_exp_f32 v100, v100
v_mfma_f32_16x16x32_bf16 v[64:67], a[132:135], v[8:11], v[64:67]
s_waitcnt lgkmcnt(4)
v_cvt_pk_bf16_f32 v144, v96, v97
v_cvt_pk_bf16_f32 v145, v98, v99
v_mfma_f32_16x16x32_bf16 v[64:67], a[140:143], v[12:15], v[64:67]
v_exp_f32 v101, v101
v_mfma_f32_16x16x32_bf16 v[72:75], a[132:135], v[24:27], v[72:75]
v_mfma_f32_16x16x32_bf16 v[72:75], a[140:143], v[28:31], v[72:75]
v_exp_f32 v102, v102
v_mfma_f32_16x16x32_bf16 v[80:83], a[132:135], v[40:43], v[80:83]
s_waitcnt lgkmcnt(3)
v_exp_f32 v103, v103
v_mfma_f32_16x16x32_bf16 v[80:83], a[140:143], v[44:47], v[80:83]
v_exp_f32 v104, v104
v_mfma_f32_16x16x32_bf16 v[88:91], a[132:135], v[56:59], v[88:91]
v_cvt_pk_bf16_f32 v146, v100, v101
v_cvt_pk_bf16_f32 v147, v102, v103
v_mfma_f32_16x16x32_bf16 v[88:91], a[140:143], v[60:63], v[88:91]
v_mfma_f32_16x16x32_bf16 v[68:71], a[144:147], v[0:3], v[160:163]
s_waitcnt lgkmcnt(2)
ds_read_b64_tr_b16 a[128:129], v199 offset:0
ds_read_b64_tr_b16 a[130:131], v199 offset:4096
v_mfma_f32_16x16x32_bf16 v[68:71], a[148:151], v[4:7], v[68:71]
v_exp_f32 v105, v105
v_mfma_f32_16x16x32_bf16 v[76:79], a[144:147], v[16:19], v[164:167]
v_exp_f32 v106, v106
v_mfma_f32_16x16x32_bf16 v[76:79], a[148:151], v[20:23], v[76:79]
s_add_u32 m0, s40, 132096
v_exp_f32 v107, v107
v_mfma_f32_16x16x32_bf16 v[84:87], a[144:147], v[32:35], v[168:171]
s_waitcnt lgkmcnt(3)
ds_read_b64_tr_b16 a[132:133], v202 offset:0
ds_read_b64_tr_b16 a[134:135], v202 offset:4096
v_mfma_f32_16x16x32_bf16 v[84:87], a[148:151], v[36:39], v[84:87]
s_xor_b32 s79, s42, 82048
buffer_load_dwordx4 v212, s[20:23], s79 offen lds
v_mfma_f32_16x16x32_bf16 v[92:95], a[144:147], v[48:51], v[172:175]
v_mfma_f32_16x16x32_bf16 v[92:95], a[148:151], v[52:55], v[92:95]
buffer_load_dwordx4 v212, s[20:23], s79 offen offset:2048 lds
v_mfma_f32_16x16x32_bf16 v[68:71], a[152:155], v[8:11], v[68:71]
s_waitcnt lgkmcnt(4)
ds_read_b64_tr_b16 a[136:137], v199 offset:512
ds_read_b64_tr_b16 a[138:139], v199 offset:4608
v_mfma_f32_16x16x32_bf16 v[68:71], a[156:159], v[12:15], v[68:71]
v_exp_f32 v108, v108
v_mfma_f32_16x16x32_bf16 v[76:79], a[152:155], v[24:27], v[76:79]
v_cvt_pk_bf16_f32 v148, v104, v105
v_cvt_pk_bf16_f32 v149, v106, v107
v_mfma_f32_16x16x32_bf16 v[76:79], a[156:159], v[28:31], v[76:79]
v_exp_f32 v109, v109
v_mfma_f32_16x16x32_bf16 v[84:87], a[152:155], v[40:43], v[84:87]
ds_read_b64_tr_b16 a[140:141], v202 offset:512
ds_read_b64_tr_b16 a[142:143], v202 offset:4608
v_mfma_f32_16x16x32_bf16 v[84:87], a[156:159], v[44:47], v[84:87]
v_mfma_f32_16x16x32_bf16 v[92:95], a[152:155], v[56:59], v[92:95]
v_exp_f32 v110, v110
v_mfma_f32_16x16x32_bf16 v[92:95], a[156:159], v[60:63], v[92:95]
v_exp_f32 v111, v111
v_mfma_f32_16x16x32_bf16 v[176:179], v[232:235], v[128:131], v[176:179]
s_waitcnt lgkmcnt(6)
ds_read_b64_tr_b16 a[144:145], v199 offset:1024
ds_read_b64_tr_b16 a[146:147], v199 offset:5120
v_mfma_f32_16x16x32_bf16 v[180:183], v[232:235], v[132:135], v[180:183]
v_exp_f32 v112, v112
v_mfma_f32_16x16x32_bf16 v[184:187], v[232:235], v[136:139], v[184:187]
v_cvt_pk_bf16_f32 v150, v108, v109
v_cvt_pk_bf16_f32 v151, v110, v111
v_mfma_f32_16x16x32_bf16 v[188:191], v[232:235], v[140:143], v[188:191]
v_mfma_f32_16x16x32_bf16 a[0:3], a[128:131], v[128:131], a[0:3]
s_waitcnt lgkmcnt(6)
ds_read_b64_tr_b16 a[148:149], v202 offset:1024
ds_read_b64_tr_b16 a[150:151], v202 offset:5120
v_mfma_f32_16x16x32_bf16 a[4:7], a[128:131], v[132:135], a[4:7]
v_exp_f32 v113, v113
v_mfma_f32_16x16x32_bf16 a[8:11], a[128:131], v[136:139], a[8:11]
v_exp_f32 v114, v114
v_mfma_f32_16x16x32_bf16 a[12:15], a[128:131], v[140:143], a[12:15]
v_exp_f32 v115, v115
v_mfma_f32_16x16x32_bf16 a[16:19], a[132:135], v[128:131], a[16:19]
s_waitcnt lgkmcnt(6)
ds_read_b64_tr_b16 a[128:129], v199 offset:1536
ds_read_b64_tr_b16 a[130:131], v199 offset:5632
v_mfma_f32_16x16x32_bf16 a[20:23], a[132:135], v[132:135], a[20:23]
v_mfma_f32_16x16x32_bf16 a[24:27], a[132:135], v[136:139], a[24:27]
v_exp_f32 v116, v116
v_mfma_f32_16x16x32_bf16 a[28:31], a[132:135], v[140:143], a[28:31]
v_cvt_pk_bf16_f32 v152, v112, v113
v_cvt_pk_bf16_f32 v153, v114, v115
v_mfma_f32_16x16x32_bf16 a[32:35], a[136:139], v[128:131], a[32:35]
s_waitcnt lgkmcnt(6)
ds_read_b64_tr_b16 a[132:133], v202 offset:1536
ds_read_b64_tr_b16 a[134:135], v202 offset:5632
v_mfma_f32_16x16x32_bf16 a[36:39], a[136:139], v[132:135], a[36:39]
v_exp_f32 v117, v117
v_mfma_f32_16x16x32_bf16 a[40:43], a[136:139], v[136:139], a[40:43]
v_mfma_f32_16x16x32_bf16 a[44:47], a[136:139], v[140:143], a[44:47]
v_exp_f32 v118, v118
v_mfma_f32_16x16x32_bf16 a[48:51], a[140:143], v[128:131], a[48:51]
s_waitcnt lgkmcnt(6)
ds_read_b128 a[136:139], v193 offset:24576
ds_read_b128 a[152:155], v193 offset:25088
v_mfma_f32_16x16x32_bf16 a[52:55], a[140:143], v[132:135], a[52:55]
v_exp_f32 v119, v119
v_mfma_f32_16x16x32_bf16 a[56:59], a[140:143], v[136:139], a[56:59]
v_exp_f32 v120, v120
v_mfma_f32_16x16x32_bf16 a[60:63], a[140:143], v[140:143], a[60:63]
v_cvt_pk_bf16_f32 v154, v116, v117
v_cvt_pk_bf16_f32 v155, v118, v119
v_mfma_f32_16x16x32_bf16 a[64:67], a[144:147], v[128:131], a[64:67]
s_waitcnt lgkmcnt(6)
v_mfma_f32_16x16x32_bf16 a[68:71], a[144:147], v[132:135], a[68:71]
v_exp_f32 v121, v121
v_mfma_f32_16x16x32_bf16 a[72:75], a[144:147], v[136:139], a[72:75]
v_exp_f32 v122, v122
v_mfma_f32_16x16x32_bf16 a[76:79], a[144:147], v[140:143], a[76:79]
v_exp_f32 v123, v123
v_mfma_f32_16x16x32_bf16 a[80:83], a[148:151], v[128:131], a[80:83]
s_waitcnt lgkmcnt(4)
ds_read_b128 a[140:143], v193 offset:25600
ds_read_b128 a[144:147], v193 offset:26112
v_mfma_f32_16x16x32_bf16 a[84:87], a[148:151], v[132:135], a[84:87]
v_mfma_f32_16x16x32_bf16 a[88:91], a[148:151], v[136:139], a[88:91]
v_exp_f32 v124, v124
v_mfma_f32_16x16x32_bf16 a[92:95], a[148:151], v[140:143], a[92:95]
s_add_u32 m0, s40, 0
v_cvt_pk_bf16_f32 v156, v120, v121
v_cvt_pk_bf16_f32 v157, v122, v123
v_mfma_f32_16x16x32_bf16 a[96:99], a[128:131], v[128:131], a[96:99]
s_waitcnt lgkmcnt(4)
v_exp_f32 v125, v125
v_mfma_f32_16x16x32_bf16 a[100:103], a[128:131], v[132:135], a[100:103]
s_xor_b32 s78, s42, 114688
buffer_load_dwordx4 v210, s[16:19], s78 offen lds
v_mfma_f32_16x16x32_bf16 a[104:107], a[128:131], v[136:139], a[104:107]
v_mfma_f32_16x16x32_bf16 a[108:111], a[128:131], v[140:143], a[108:111]
buffer_load_dwordx4 v211, s[16:19], s78 offen offset:2048 lds
v_mfma_f32_16x16x32_bf16 a[112:115], a[132:135], v[128:131], a[112:115]
s_waitcnt lgkmcnt(3)
ds_read_b128 a[128:131], v193 offset:28672
ds_read_b128 a[148:151], v193 offset:29184
v_mfma_f32_16x16x32_bf16 a[116:119], a[132:135], v[132:135], a[116:119]
v_exp_f32 v126, v126
v_mfma_f32_16x16x32_bf16 a[120:123], a[132:135], v[136:139], a[120:123]
v_exp_f32 v127, v127
v_mfma_f32_16x16x32_bf16 a[124:127], a[132:135], v[140:143], a[124:127]
v_cvt_pk_bf16_f32 v158, v124, v125
v_cvt_pk_bf16_f32 v159, v126, v127
v_mfma_f32_16x16x32_bf16 v[96:99], a[136:139], v[0:3], v[160:163]
s_waitcnt lgkmcnt(4)
v_exp_f32 v64, v64
v_mfma_f32_16x16x32_bf16 v[96:99], a[152:155], v[4:7], v[96:99]
v_mfma_f32_16x16x32_bf16 v[104:107], a[136:139], v[16:19], v[164:167]
v_exp_f32 v65, v65
v_mfma_f32_16x16x32_bf16 v[104:107], a[152:155], v[20:23], v[104:107]
v_exp_f32 v66, v66
v_mfma_f32_16x16x32_bf16 v[112:115], a[136:139], v[32:35], v[168:171]
s_waitcnt lgkmcnt(3)
ds_read_b128 a[132:135], v193 offset:29696
ds_read_b128 a[156:159], v193 offset:30208
v_mfma_f32_16x16x32_bf16 v[112:115], a[152:155], v[36:39], v[112:115]
v_exp_f32 v67, v67
v_mfma_f32_16x16x32_bf16 v[120:123], a[136:139], v[48:51], v[172:175]
v_mfma_f32_16x16x32_bf16 v[120:123], a[152:155], v[52:55], v[120:123]
v_exp_f32 v68, v68
v_mfma_f32_16x16x32_bf16 v[96:99], a[140:143], v[8:11], v[96:99]
s_waitcnt lgkmcnt(4)
v_cvt_pk_bf16_f32 v128, v64, v65
v_cvt_pk_bf16_f32 v129, v66, v67
v_mfma_f32_16x16x32_bf16 v[96:99], a[144:147], v[12:15], v[96:99]
v_exp_f32 v69, v69
v_mfma_f32_16x16x32_bf16 v[104:107], a[140:143], v[24:27], v[104:107]
v_exp_f32 v70, v70
v_mfma_f32_16x16x32_bf16 v[104:107], a[144:147], v[28:31], v[104:107]
v_mfma_f32_16x16x32_bf16 v[112:115], a[140:143], v[40:43], v[112:115]
s_waitcnt lgkmcnt(3)
v_exp_f32 v71, v71
v_mfma_f32_16x16x32_bf16 v[112:115], a[144:147], v[44:47], v[112:115]
v_exp_f32 v72, v72
v_mfma_f32_16x16x32_bf16 v[120:123], a[140:143], v[56:59], v[120:123]
v_cvt_pk_bf16_f32 v130, v68, v69
v_cvt_pk_bf16_f32 v131, v70, v71
v_mfma_f32_16x16x32_bf16 v[120:123], a[144:147], v[60:63], v[120:123]
v_exp_f32 v73, v73
v_mfma_f32_16x16x32_bf16 v[100:103], a[128:131], v[0:3], v[160:163]
s_waitcnt lgkmcnt(2)
ds_read_b64_tr_b16 a[136:137], v199 offset:8192
ds_read_b64_tr_b16 a[138:139], v199 offset:12288
v_mfma_f32_16x16x32_bf16 v[100:103], a[148:151], v[4:7], v[100:103]
v_mfma_f32_16x16x32_bf16 v[108:111], a[128:131], v[16:19], v[164:167]
v_exp_f32 v74, v74
v_mfma_f32_16x16x32_bf16 v[108:111], a[148:151], v[20:23], v[108:111]
v_exp_f32 v75, v75
v_mfma_f32_16x16x32_bf16 v[116:119], a[128:131], v[32:35], v[168:171]
s_waitcnt lgkmcnt(3)
ds_read_b64_tr_b16 a[140:141], v202 offset:8192
ds_read_b64_tr_b16 a[142:143], v202 offset:12288
v_mfma_f32_16x16x32_bf16 v[116:119], a[148:151], v[36:39], v[116:119]
v_exp_f32 v76, v76
v_mfma_f32_16x16x32_bf16 v[124:127], a[128:131], v[48:51], v[172:175]
v_cvt_pk_bf16_f32 v132, v72, v73
v_mfma_f32_16x16x32_bf16 v[124:127], a[148:151], v[52:55], v[124:127]
v_cvt_pk_bf16_f32 v133, v74, v75
v_mfma_f32_16x16x32_bf16 v[100:103], a[132:135], v[8:11], v[100:103]
s_waitcnt lgkmcnt(4)
ds_read_b64_tr_b16 a[128:129], v199 offset:8704
ds_read_b64_tr_b16 a[130:131], v199 offset:12800
v_mfma_f32_16x16x32_bf16 v[100:103], a[156:159], v[12:15], v[100:103]
v_exp_f32 v77, v77
v_mfma_f32_16x16x32_bf16 v[108:111], a[132:135], v[24:27], v[108:111]
v_exp_f32 v78, v78
v_mfma_f32_16x16x32_bf16 v[108:111], a[156:159], v[28:31], v[108:111]
v_exp_f32 v79, v79
v_mfma_f32_16x16x32_bf16 v[116:119], a[132:135], v[40:43], v[116:119]
ds_read_b64_tr_b16 a[144:145], v202 offset:8704
ds_read_b64_tr_b16 a[146:147], v202 offset:12800
v_mfma_f32_16x16x32_bf16 v[116:119], a[156:159], v[44:47], v[116:119]
v_bfe_u32 v226, v217, 1, 6
v_and_b32 v227, 0x80, v217
v_and_b32 v226, 0x38, v226
v_or_b32 v226, v226, v227
v_mfma_f32_16x16x32_bf16 v[124:127], a[132:135], v[56:59], v[124:127]
s_add_u32 s54, s50, 0
buffer_load_dwordx2 v[230:231], v226, s[24:27], s54 offen sc1
v_mfma_f32_16x16x32_bf16 v[124:127], a[156:159], v[60:63], v[124:127]
v_mfma_f32_16x16x32_bf16 v[176:179], v[232:235], v[144:147], v[176:179]
s_waitcnt lgkmcnt(6)
ds_read_b64_tr_b16 a[132:133], v199 offset:9216
ds_read_b64_tr_b16 a[134:135], v199 offset:13312
v_mfma_f32_16x16x32_bf16 v[180:183], v[232:235], v[148:151], v[180:183]
ds_read_b64_tr_b16 a[156:157], v202 offset:9728
ds_read_b64_tr_b16 a[158:159], v202 offset:13824
v_mfma_f32_16x16x32_bf16 v[184:187], v[232:235], v[152:155], v[184:187]
s_xor_b32 s79, s42, 114816
ds_read_b64_tr_b16 a[152:153], v199 offset:9728
ds_read_b64_tr_b16 a[154:155], v199 offset:13824
v_mfma_f32_16x16x32_bf16 v[188:191], v[232:235], v[156:159], v[188:191]
s_add_u32 m0, s40, 1024
ds_read_b64_tr_b16 a[148:149], v202 offset:9216
ds_read_b64_tr_b16 a[150:151], v202 offset:13312
v_mfma_f32_16x16x32_bf16 a[0:3], a[136:139], v[144:147], a[0:3]
s_waitcnt lgkmcnt(12)
buffer_load_dwordx4 v210, s[16:19], s79 offen lds
v_mfma_f32_16x16x32_bf16 a[4:7], a[136:139], v[148:151], a[4:7]
v_exp_f32 v80, v80
v_mfma_f32_16x16x32_bf16 a[8:11], a[136:139], v[152:155], a[8:11]
buffer_load_dwordx4 v211, s[16:19], s79 offen offset:2048 lds
v_mfma_f32_16x16x32_bf16 a[12:15], a[136:139], v[156:159], a[12:15]
v_cvt_pk_bf16_f32 v134, v76, v77
v_cvt_pk_bf16_f32 v135, v78, v79
v_mfma_f32_16x16x32_bf16 a[16:19], a[140:143], v[144:147], a[16:19]
s_waitcnt lgkmcnt(10)
v_exp_f32 v81, v81
v_mfma_f32_16x16x32_bf16 a[20:23], a[140:143], v[148:151], a[20:23]
v_mfma_f32_16x16x32_bf16 a[24:27], a[140:143], v[152:155], a[24:27]
v_exp_f32 v82, v82
v_mfma_f32_16x16x32_bf16 a[28:31], a[140:143], v[156:159], a[28:31]
v_exp_f32 v83, v83
v_mfma_f32_16x16x32_bf16 a[32:35], a[128:131], v[144:147], a[32:35]
s_waitcnt vmcnt(13) lgkmcnt(0)
s_barrier
v_mfma_f32_16x16x32_bf16 a[36:39], a[128:131], v[148:151], a[36:39]
v_exp_f32 v84, v84
v_mfma_f32_16x16x32_bf16 a[40:43], a[128:131], v[152:155], a[40:43]
v_cvt_pk_bf16_f32 v136, v80, v81
v_cvt_pk_bf16_f32 v137, v82, v83
v_mfma_f32_16x16x32_bf16 a[44:47], a[128:131], v[156:159], a[44:47]
v_mfma_f32_16x16x32_bf16 a[48:51], a[144:147], v[144:147], a[48:51]
ds_read_b128 a[128:131], v193 offset:49152
ds_read_b128 a[136:139], v193 offset:49664
v_mfma_f32_16x16x32_bf16 a[52:55], a[144:147], v[148:151], a[52:55]
v_exp_f32 v85, v85
v_mfma_f32_16x16x32_bf16 a[56:59], a[144:147], v[152:155], a[56:59]
v_exp_f32 v86, v86
v_mfma_f32_16x16x32_bf16 a[60:63], a[144:147], v[156:159], a[60:63]
v_exp_f32 v87, v87
v_mfma_f32_16x16x32_bf16 a[64:67], a[132:135], v[144:147], a[64:67]
v_exp_f32 v88, v88
v_mfma_f32_16x16x32_bf16 a[68:71], a[132:135], v[148:151], a[68:71]
v_cvt_pk_bf16_f32 v138, v84, v85
v_mfma_f32_16x16x32_bf16 a[72:75], a[132:135], v[152:155], a[72:75]
v_cvt_pk_bf16_f32 v139, v86, v87
v_mfma_f32_16x16x32_bf16 a[76:79], a[132:135], v[156:159], a[76:79]
v_exp_f32 v89, v89
v_mfma_f32_16x16x32_bf16 a[80:83], a[148:151], v[144:147], a[80:83]
ds_read_b128 a[132:135], v193 offset:50176
ds_read_b128 a[140:143], v193 offset:50688
v_mfma_f32_16x16x32_bf16 a[84:87], a[148:151], v[148:151], a[84:87]
v_exp_f32 v90, v90
v_mfma_f32_16x16x32_bf16 a[88:91], a[148:151], v[152:155], a[88:91]
v_mfma_f32_16x16x32_bf16 a[92:95], a[148:151], v[156:159], a[92:95]
v_exp_f32 v91, v91
v_mfma_f32_16x16x32_bf16 a[96:99], a[152:155], v[144:147], a[96:99]
v_exp_f32 v92, v92
v_mfma_f32_16x16x32_bf16 a[100:103], a[152:155], v[148:151], a[100:103]
v_cvt_pk_bf16_f32 v140, v88, v89
v_cvt_pk_bf16_f32 v141, v90, v91
v_mfma_f32_16x16x32_bf16 a[104:107], a[152:155], v[152:155], a[104:107]
v_exp_f32 v93, v93
v_mfma_f32_16x16x32_bf16 a[108:111], a[152:155], v[156:159], a[108:111]
v_mfma_f32_16x16x32_bf16 a[112:115], a[156:159], v[144:147], a[112:115]
s_waitcnt lgkmcnt(3)
ds_read_b128 a[144:147], v193 offset:53248
ds_read_b128 a[148:151], v193 offset:53760
v_mfma_f32_16x16x32_bf16 a[116:119], a[156:159], v[148:151], a[116:119]
v_exp_f32 v94, v94
v_mfma_f32_16x16x32_bf16 a[120:123], a[156:159], v[152:155], a[120:123]
v_exp_f32 v95, v95
v_mfma_f32_16x16x32_bf16 a[124:127], a[156:159], v[156:159], a[124:127]
v_cvt_pk_bf16_f32 v142, v92, v93
v_cvt_pk_bf16_f32 v143, v94, v95
v_mfma_f32_16x16x32_bf16 v[64:67], a[128:131], v[0:3], v[160:163]
s_waitcnt lgkmcnt(4)
v_exp_f32 v96, v96
v_mfma_f32_16x16x32_bf16 v[64:67], a[136:139], v[4:7], v[64:67]
v_mfma_f32_16x16x32_bf16 v[72:75], a[128:131], v[16:19], v[164:167]
v_exp_f32 v97, v97
v_mfma_f32_16x16x32_bf16 v[72:75], a[136:139], v[20:23], v[72:75]
v_exp_f32 v98, v98
v_mfma_f32_16x16x32_bf16 v[80:83], a[128:131], v[32:35], v[168:171]
s_waitcnt lgkmcnt(3)
ds_read_b128 a[152:155], v193 offset:54272
ds_read_b128 a[156:159], v193 offset:54784
v_mfma_f32_16x16x32_bf16 v[80:83], a[136:139], v[36:39], v[80:83]
v_exp_f32 v99, v99
v_mfma_f32_16x16x32_bf16 v[88:91], a[128:131], v[48:51], v[172:175]
s_xor_b32 s78, s42, 98304
v_mfma_f32_16x16x32_bf16 v[88:91], a[136:139], v[52:55], v[88:91]
s_add_u32 m0, s40, 16384
v_exp_f32 v100, v100
v_mfma_f32_16x16x32_bf16 v[64:67], a[132:135], v[8:11], v[64:67]
s_waitcnt lgkmcnt(4)
buffer_load_dwordx4 v212, s[20:23], s78 offen lds
v_mfma_f32_16x16x32_bf16 v[64:67], a[140:143], v[12:15], v[64:67]
v_cvt_pk_bf16_f32 v144, v96, v97
v_cvt_pk_bf16_f32 v145, v98, v99
v_mfma_f32_16x16x32_bf16 v[72:75], a[132:135], v[24:27], v[72:75]
buffer_load_dwordx4 v212, s[20:23], s78 offen offset:2048 lds
v_mfma_f32_16x16x32_bf16 v[72:75], a[140:143], v[28:31], v[72:75]
v_exp_f32 v101, v101
v_mfma_f32_16x16x32_bf16 v[80:83], a[132:135], v[40:43], v[80:83]
s_waitcnt lgkmcnt(3)
v_mfma_f32_16x16x32_bf16 v[80:83], a[140:143], v[44:47], v[80:83]
v_exp_f32 v102, v102
v_mfma_f32_16x16x32_bf16 v[88:91], a[132:135], v[56:59], v[88:91]
v_exp_f32 v103, v103
v_mfma_f32_16x16x32_bf16 v[88:91], a[140:143], v[60:63], v[88:91]
v_exp_f32 v104, v104
v_mfma_f32_16x16x32_bf16 v[68:71], a[144:147], v[0:3], v[160:163]
s_waitcnt lgkmcnt(2)
ds_read_b64_tr_b16 a[128:129], v198 offset:49152
ds_read_b64_tr_b16 a[130:131], v198 offset:53248
v_mfma_f32_16x16x32_bf16 v[68:71], a[148:151], v[4:7], v[68:71]
v_cvt_pk_bf16_f32 v146, v100, v101
v_cvt_pk_bf16_f32 v147, v102, v103
v_mfma_f32_16x16x32_bf16 v[76:79], a[144:147], v[16:19], v[164:167]
v_mfma_f32_16x16x32_bf16 v[76:79], a[148:151], v[20:23], v[76:79]
v_exp_f32 v105, v105
v_mfma_f32_16x16x32_bf16 v[84:87], a[144:147], v[32:35], v[168:171]
s_waitcnt lgkmcnt(3)
ds_read_b64_tr_b16 a[132:133], v201 offset:49152
ds_read_b64_tr_b16 a[134:135], v201 offset:53248
v_mfma_f32_16x16x32_bf16 v[84:87], a[148:151], v[36:39], v[84:87]
v_exp_f32 v106, v106
v_mfma_f32_16x16x32_bf16 v[92:95], a[144:147], v[48:51], v[172:175]
v_exp_f32 v107, v107
v_mfma_f32_16x16x32_bf16 v[92:95], a[148:151], v[52:55], v[92:95]
v_mfma_f32_16x16x32_bf16 v[68:71], a[152:155], v[8:11], v[68:71]
s_waitcnt lgkmcnt(4)
ds_read_b64_tr_b16 a[136:137], v198 offset:49664
ds_read_b64_tr_b16 a[138:139], v198 offset:53760
v_mfma_f32_16x16x32_bf16 v[68:71], a[156:159], v[12:15], v[68:71]
v_exp_f32 v108, v108
v_mfma_f32_16x16x32_bf16 v[76:79], a[152:155], v[24:27], v[76:79]
v_cvt_pk_bf16_f32 v148, v104, v105
v_cvt_pk_bf16_f32 v149, v106, v107
v_mfma_f32_16x16x32_bf16 v[76:79], a[156:159], v[28:31], v[76:79]
v_exp_f32 v109, v109
v_mfma_f32_16x16x32_bf16 v[84:87], a[152:155], v[40:43], v[84:87]
ds_read_b64_tr_b16 a[140:141], v201 offset:49664
ds_read_b64_tr_b16 a[142:143], v201 offset:53760
v_mfma_f32_16x16x32_bf16 v[84:87], a[156:159], v[44:47], v[84:87]
v_mfma_f32_16x16x32_bf16 v[92:95], a[152:155], v[56:59], v[92:95]
v_exp_f32 v110, v110
v_mfma_f32_16x16x32_bf16 v[92:95], a[156:159], v[60:63], v[92:95]
v_exp_f32 v111, v111
v_mfma_f32_16x16x32_bf16 v[176:179], v[232:235], v[128:131], v[176:179]
s_waitcnt lgkmcnt(6)
ds_read_b64_tr_b16 a[144:145], v198 offset:50176
ds_read_b64_tr_b16 a[146:147], v198 offset:54272
v_mfma_f32_16x16x32_bf16 v[180:183], v[232:235], v[132:135], v[180:183]
v_exp_f32 v112, v112
v_mfma_f32_16x16x32_bf16 v[184:187], v[232:235], v[136:139], v[184:187]
v_cvt_pk_bf16_f32 v150, v108, v109
v_cvt_pk_bf16_f32 v151, v110, v111
v_mfma_f32_16x16x32_bf16 v[188:191], v[232:235], v[140:143], v[188:191]
v_mfma_f32_16x16x32_bf16 a[0:3], a[128:131], v[128:131], a[0:3]
s_waitcnt lgkmcnt(6)
ds_read_b64_tr_b16 a[148:149], v201 offset:50176
ds_read_b64_tr_b16 a[150:151], v201 offset:54272
v_mfma_f32_16x16x32_bf16 a[4:7], a[128:131], v[132:135], a[4:7]
v_exp_f32 v113, v113
v_mfma_f32_16x16x32_bf16 a[8:11], a[128:131], v[136:139], a[8:11]
v_exp_f32 v114, v114
v_mfma_f32_16x16x32_bf16 a[12:15], a[128:131], v[140:143], a[12:15]
v_exp_f32 v115, v115
v_mfma_f32_16x16x32_bf16 a[16:19], a[132:135], v[128:131], a[16:19]
s_waitcnt lgkmcnt(6)
ds_read_b64_tr_b16 a[128:129], v198 offset:50688
ds_read_b64_tr_b16 a[130:131], v198 offset:54784
v_mfma_f32_16x16x32_bf16 a[20:23], a[132:135], v[132:135], a[20:23]
v_mfma_f32_16x16x32_bf16 a[24:27], a[132:135], v[136:139], a[24:27]
v_exp_f32 v116, v116
v_mfma_f32_16x16x32_bf16 a[28:31], a[132:135], v[140:143], a[28:31]
v_cvt_pk_bf16_f32 v152, v112, v113
v_cvt_pk_bf16_f32 v153, v114, v115
v_mfma_f32_16x16x32_bf16 a[32:35], a[136:139], v[128:131], a[32:35]
s_waitcnt lgkmcnt(6)
ds_read_b64_tr_b16 a[132:133], v201 offset:50688
ds_read_b64_tr_b16 a[134:135], v201 offset:54784
v_mfma_f32_16x16x32_bf16 a[36:39], a[136:139], v[132:135], a[36:39]
v_exp_f32 v117, v117
v_mfma_f32_16x16x32_bf16 a[40:43], a[136:139], v[136:139], a[40:43]
v_mfma_f32_16x16x32_bf16 a[44:47], a[136:139], v[140:143], a[44:47]
s_add_u32 m0, s40, 17408
v_exp_f32 v118, v118
v_mfma_f32_16x16x32_bf16 a[48:51], a[140:143], v[128:131], a[48:51]
s_waitcnt lgkmcnt(6)
ds_read_b128 a[136:139], v193 offset:57344
ds_read_b128 a[152:155], v193 offset:57856
v_mfma_f32_16x16x32_bf16 a[52:55], a[140:143], v[132:135], a[52:55]
s_xor_b32 s79, s42, 98432
buffer_load_dwordx4 v212, s[20:23], s79 offen lds
v_mfma_f32_16x16x32_bf16 a[56:59], a[140:143], v[136:139], a[56:59]
v_exp_f32 v119, v119
v_mfma_f32_16x16x32_bf16 a[60:63], a[140:143], v[140:143], a[60:63]
buffer_load_dwordx4 v212, s[20:23], s79 offen offset:2048 lds
v_mfma_f32_16x16x32_bf16 a[64:67], a[144:147], v[128:131], a[64:67]
s_waitcnt lgkmcnt(6)
v_exp_f32 v120, v120
v_mfma_f32_16x16x32_bf16 a[68:71], a[144:147], v[132:135], a[68:71]
L_tch13_%=:
s_cbranch_execz L_tb1_%=
v_cvt_pk_bf16_f32 v154, v116, v117
v_cvt_pk_bf16_f32 v155, v118, v119
v_mfma_f32_16x16x32_bf16 a[72:75], a[144:147], v[136:139], a[72:75]
v_mfma_f32_16x16x32_bf16 a[76:79], a[144:147], v[140:143], a[76:79]
v_exp_f32 v121, v121
v_mfma_f32_16x16x32_bf16 a[80:83], a[148:151], v[128:131], a[80:83]
s_waitcnt lgkmcnt(4)
ds_read_b128 a[140:143], v193 offset:58368
ds_read_b128 a[144:147], v193 offset:58880
v_mfma_f32_16x16x32_bf16 a[84:87], a[148:151], v[132:135], a[84:87]
v_exp_f32 v122, v122
v_mfma_f32_16x16x32_bf16 a[88:91], a[148:151], v[136:139], a[88:91]
v_exp_f32 v123, v123
v_mfma_f32_16x16x32_bf16 a[92:95], a[148:151], v[140:143], a[92:95]
v_mfma_f32_16x16x32_bf16 a[96:99], a[128:131], v[128:131], a[96:99]
s_waitcnt lgkmcnt(4)
v_exp_f32 v124, v124
v_mfma_f32_16x16x32_bf16 a[100:103], a[128:131], v[132:135], a[100:103]
v_cvt_pk_bf16_f32 v156, v120, v121
v_cvt_pk_bf16_f32 v157, v122, v123
v_mfma_f32_16x16x32_bf16 a[104:107], a[128:131], v[136:139], a[104:107]
v_exp_f32 v125, v125
v_mfma_f32_16x16x32_bf16 a[108:111], a[128:131], v[140:143], a[108:111]
v_mfma_f32_16x16x32_bf16 a[112:115], a[132:135], v[128:131], a[112:115]
s_waitcnt lgkmcnt(3)
ds_read_b128 a[128:131], v193 offset:61440
ds_read_b128 a[148:151], v193 offset:61952
v_mfma_f32_16x16x32_bf16 a[116:119], a[132:135], v[132:135], a[116:119]
v_exp_f32 v126, v126
v_mfma_f32_16x16x32_bf16 a[120:123], a[132:135], v[136:139], a[120:123]
v_exp_f32 v127, v127
v_mfma_f32_16x16x32_bf16 a[124:127], a[132:135], v[140:143], a[124:127]
v_cvt_pk_bf16_f32 v158, v124, v125
v_cvt_pk_bf16_f32 v159, v126, v127
v_mfma_f32_16x16x32_bf16 v[96:99], a[136:139], v[0:3], v[160:163]
s_waitcnt lgkmcnt(4)
v_exp_f32 v64, v64
v_mfma_f32_16x16x32_bf16 v[96:99], a[152:155], v[4:7], v[96:99]
v_mfma_f32_16x16x32_bf16 v[104:107], a[136:139], v[16:19], v[164:167]
v_exp_f32 v65, v65
v_mfma_f32_16x16x32_bf16 v[104:107], a[152:155], v[20:23], v[104:107]
v_exp_f32 v66, v66
v_mfma_f32_16x16x32_bf16 v[112:115], a[136:139], v[32:35], v[168:171]
s_waitcnt lgkmcnt(3)
ds_read_b128 a[132:135], v193 offset:62464
ds_read_b128 a[156:159], v193 offset:62976
v_mfma_f32_16x16x32_bf16 v[112:115], a[152:155], v[36:39], v[112:115]
v_exp_f32 v67, v67
v_mfma_f32_16x16x32_bf16 v[120:123], a[136:139], v[48:51], v[172:175]
v_exp_f32 v68, v68
v_mfma_f32_16x16x32_bf16 v[120:123], a[152:155], v[52:55], v[120:123]
v_cvt_pk_bf16_f32 v128, v64, v65
v_mfma_f32_16x16x32_bf16 v[96:99], a[140:143], v[8:11], v[96:99]
s_waitcnt lgkmcnt(4)
v_cvt_pk_bf16_f32 v129, v66, v67
v_mfma_f32_16x16x32_bf16 v[96:99], a[144:147], v[12:15], v[96:99]
v_exp_f32 v69, v69
v_mfma_f32_16x16x32_bf16 v[104:107], a[140:143], v[24:27], v[104:107]
v_exp_f32 v70, v70
v_mfma_f32_16x16x32_bf16 v[104:107], a[144:147], v[28:31], v[104:107]
v_exp_f32 v71, v71
v_mfma_f32_16x16x32_bf16 v[112:115], a[140:143], v[40:43], v[112:115]
s_waitcnt lgkmcnt(3)
v_mfma_f32_16x16x32_bf16 v[112:115], a[144:147], v[44:47], v[112:115]
v_exp_f32 v72, v72
v_mfma_f32_16x16x32_bf16 v[120:123], a[140:143], v[56:59], v[120:123]
v_cvt_pk_bf16_f32 v130, v68, v69
v_cvt_pk_bf16_f32 v131, v70, v71
v_mfma_f32_16x16x32_bf16 v[120:123], a[144:147], v[60:63], v[120:123]
v_exp_f32 v73, v73
v_mfma_f32_16x16x32_bf16 v[100:103], a[128:131], v[0:3], v[160:163]
s_waitcnt lgkmcnt(2)
ds_read_b64_tr_b16 a[136:137], v198 offset:57344
ds_read_b64_tr_b16 a[138:139], v198 offset:61440
v_mfma_f32_16x16x32_bf16 v[100:103], a[148:151], v[4:7], v[100:103]
v_exp_f32 v74, v74
v_mfma_f32_16x16x32_bf16 v[108:111], a[128:131], v[16:19], v[164:167]
v_mfma_f32_16x16x32_bf16 v[108:111], a[148:151], v[20:23], v[108:111]
s_add_u32 m0, s40, 32768
v_exp_f32 v75, v75
v_mfma_f32_16x16x32_bf16 v[116:119], a[128:131], v[32:35], v[168:171]
s_waitcnt lgkmcnt(3)
ds_read_b64_tr_b16 a[140:141], v201 offset:57344
ds_read_b64_tr_b16 a[142:143], v201 offset:61440
v_mfma_f32_16x16x32_bf16 v[116:119], a[148:151], v[36:39], v[116:119]
s_xor_b32 s78, s42, 114688
buffer_load_dwordx4 v212, s[20:23], s78 offen lds
v_mfma_f32_16x16x32_bf16 v[124:127], a[128:131], v[48:51], v[172:175]
v_exp_f32 v76, v76
v_mfma_f32_16x16x32_bf16 v[124:127], a[148:151], v[52:55], v[124:127]
buffer_load_dwordx4 v212, s[20:23], s78 offen offset:2048 lds
v_mfma_f32_16x16x32_bf16 v[100:103], a[132:135], v[8:11], v[100:103]
s_waitcnt lgkmcnt(4)
ds_read_b64_tr_b16 a[128:129], v198 offset:57856
ds_read_b64_tr_b16 a[130:131], v198 offset:61952
v_mfma_f32_16x16x32_bf16 v[100:103], a[156:159], v[12:15], v[100:103]
v_cvt_pk_bf16_f32 v132, v72, v73
v_cvt_pk_bf16_f32 v133, v74, v75
v_mfma_f32_16x16x32_bf16 v[108:111], a[132:135], v[24:27], v[108:111]
v_exp_f32 v77, v77
v_mfma_f32_16x16x32_bf16 v[108:111], a[156:159], v[28:31], v[108:111]
v_mfma_f32_16x16x32_bf16 v[116:119], a[132:135], v[40:43], v[116:119]
ds_read_b64_tr_b16 a[144:145], v201 offset:57856
ds_read_b64_tr_b16 a[146:147], v201 offset:61952
v_mfma_f32_16x16x32_bf16 v[116:119], a[156:159], v[44:47], v[116:119]
v_exp_f32 v78, v78
v_mfma_f32_16x16x32_bf16 v[124:127], a[132:135], v[56:59], v[124:127]
s_waitcnt vmcnt(8)
v_cmp_ne_u64 vcc, s[52:53], v[230:231]
s_nop 4
s_cbranch_vccz L_pollok0_%=
s_mov_b32 s55, 0
L_poll0_%=:
s_sleep 2
buffer_load_dwordx2 v[230:231], v226, s[24:27], s54 offen sc1
s_add_u32 s55, s55, 1
s_waitcnt vmcnt(0)
v_cmp_ne_u64 vcc, s[52:53], v[230:231]
s_cmp_lt_u32 s55, 0x40000
s_cbranch_scc0 L_pollok0_%=
s_nop 4
s_cbranch_vccnz L_poll0_%=
L_pollok0_%=:
v_mfma_f32_16x16x32_bf16 v[124:127], a[156:159], v[60:63], v[124:127]
v_exp_f32 v79, v79
v_mfma_f32_16x16x32_bf16 v[176:179], v[232:235], v[144:147], v[176:179]
s_waitcnt lgkmcnt(6)
ds_read_b64_tr_b16 a[132:133], v198 offset:58368
ds_read_b64_tr_b16 a[134:135], v198 offset:62464
v_mfma_f32_16x16x32_bf16 v[180:183], v[232:235], v[148:151], v[180:183]
ds_read_b64_tr_b16 a[156:157], v201 offset:58880
ds_read_b64_tr_b16 a[158:159], v201 offset:62976
v_mfma_f32_16x16x32_bf16 v[184:187], v[232:235], v[152:155], v[184:187]
ds_read_b64_tr_b16 a[152:153], v198 offset:58880
ds_read_b64_tr_b16 a[154:155], v198 offset:62976
v_mfma_f32_16x16x32_bf16 v[188:191], v[232:235], v[156:159], v[188:191]
ds_read_b64_tr_b16 a[148:149], v201 offset:58368
ds_read_b64_tr_b16 a[150:151], v201 offset:62464
v_mfma_f32_16x16x32_bf16 a[0:3], a[136:139], v[144:147], a[0:3]
s_waitcnt lgkmcnt(12)
v_exp_f32 v80, v80
v_mfma_f32_16x16x32_bf16 a[4:7], a[136:139], v[148:151], a[4:7]
v_cvt_pk_bf16_f32 v134, v76, v77
v_cvt_pk_bf16_f32 v135, v78, v79
v_mfma_f32_16x16x32_bf16 a[8:11], a[136:139], v[152:155], a[8:11]
v_mfma_f32_16x16x32_bf16 a[12:15], a[136:139], v[156:159], a[12:15]
v_exp_f32 v81, v81
v_mfma_f32_16x16x32_bf16 a[16:19], a[140:143], v[144:147], a[16:19]
s_waitcnt lgkmcnt(10)
v_exp_f32 v82, v82
v_mfma_f32_16x16x32_bf16 a[20:23], a[140:143], v[148:151], a[20:23]
v_exp_f32 v83, v83
v_mfma_f32_16x16x32_bf16 a[24:27], a[140:143], v[152:155], a[24:27]
v_exp_f32 v84, v84
v_mfma_f32_16x16x32_bf16 a[28:31], a[140:143], v[156:159], a[28:31]
v_cvt_pk_bf16_f32 v136, v80, v81
v_mfma_f32_16x16x32_bf16 a[32:35], a[128:131], v[144:147], a[32:35]
s_waitcnt lgkmcnt(0)
s_barrier
v_mfma_f32_16x16x32_bf16 a[36:39], a[128:131], v[148:151], a[36:39]
v_cvt_pk_bf16_f32 v137, v82, v83
v_mfma_f32_16x16x32_bf16 a[40:43], a[128:131], v[152:155], a[40:43]
v_exp_f32 v85, v85
v_mfma_f32_16x16x32_bf16 a[44:47], a[128:131], v[156:159], a[44:47]
v_exp_f32 v86, v86
v_mfma_f32_16x16x32_bf16 a[48:51], a[144:147], v[144:147], a[48:51]
ds_read_b128 a[128:131], v193 offset:32768
ds_read_b128 a[136:139], v193 offset:33280
v_mfma_f32_16x16x32_bf16 a[52:55], a[144:147], v[148:151], a[52:55]
v_exp_f32 v87, v87
v_mfma_f32_16x16x32_bf16 a[56:59], a[144:147], v[152:155], a[56:59]
v_mfma_f32_16x16x32_bf16 a[60:63], a[144:147], v[156:159], a[60:63]
v_exp_f32 v88, v88
v_mfma_f32_16x16x32_bf16 a[64:67], a[132:135], v[144:147], a[64:67]
v_cvt_pk_bf16_f32 v138, v84, v85
v_cvt_pk_bf16_f32 v139, v86, v87
v_mfma_f32_16x16x32_bf16 a[68:71], a[132:135], v[148:151], a[68:71]
v_exp_f32 v89, v89
v_mfma_f32_16x16x32_bf16 a[72:75], a[132:135], v[152:155], a[72:75]
v_exp_f32 v90, v90
v_mfma_f32_16x16x32_bf16 a[76:79], a[132:135], v[156:159], a[76:79]
v_mfma_f32_16x16x32_bf16 a[80:83], a[148:151], v[144:147], a[80:83]
ds_read_b128 a[132:135], v193 offset:33792
ds_read_b128 a[140:143], v193 offset:34304
v_mfma_f32_16x16x32_bf16 a[84:87], a[148:151], v[148:151], a[84:87]
v_exp_f32 v91, v91
v_mfma_f32_16x16x32_bf16 a[88:91], a[148:151], v[152:155], a[88:91]
v_exp_f32 v92, v92
v_mfma_f32_16x16x32_bf16 a[92:95], a[148:151], v[156:159], a[92:95]
s_add_u32 m0, s40, 33792
v_cvt_pk_bf16_f32 v140, v88, v89
v_cvt_pk_bf16_f32 v141, v90, v91
v_mfma_f32_16x16x32_bf16 a[96:99], a[152:155], v[144:147], a[96:99]
v_exp_f32 v93, v93
v_mfma_f32_16x16x32_bf16 a[100:103], a[152:155], v[148:151], a[100:103]
s_xor_b32 s79, s42, 114816
buffer_load_dwordx4 v212, s[20:23], s79 offen lds
v_mfma_f32_16x16x32_bf16 a[104:107], a[152:155], v[152:155], a[104:107]
v_mfma_f32_16x16x32_bf16 a[108:111], a[152:155], v[156:159], a[108:111]
buffer_load_dwordx4 v212, s[20:23], s79 offen offset:2048 lds
v_mfma_f32_16x16x32_bf16 a[112:115], a[156:159], v[144:147], a[112:115]
s_waitcnt lgkmcnt(3)
ds_read_b128 a[144:147], v193 offset:36864
ds_read_b128 a[148:151], v193 offset:37376
v_mfma_f32_16x16x32_bf16 a[116:119], a[156:159], v[148:151], a[116:119]
v_exp_f32 v94, v94
v_mfma_f32_16x16x32_bf16 a[120:123], a[156:159], v[152:155], a[120:123]
s_add_u32 s56, s80, 0
v_exp_f32 v95, v95
v_mfma_f32_16x16x32_bf16 a[124:127], a[156:159], v[156:159], a[124:127]
s_add_u32 m0, s40, 147456
v_cvt_pk_bf16_f32 v142, v92, v93
v_cvt_pk_bf16_f32 v143, v94, v95
v_mfma_f32_16x16x32_bf16 v[64:67], a[128:131], v[0:3], v[160:163]
s_waitcnt lgkmcnt(4)
buffer_load_dwordx4 v213, s[28:31], s56 offen lds
v_mfma_f32_16x16x32_bf16 v[64:67], a[136:139], v[4:7], v[64:67]
v_exp_f32 v96, v96
v_mfma_f32_16x16x32_bf16 v[72:75], a[128:131], v[16:19], v[164:167]
buffer_load_dwordx4 v214, s[28:31], s56 offen offset:2048 lds
v_mfma_f32_16x16x32_bf16 v[72:75], a[136:139], v[20:23], v[72:75]
v_mfma_f32_16x16x32_bf16 v[80:83], a[128:131], v[32:35], v[168:171]
s_waitcnt lgkmcnt(3)
ds_read_b128 a[152:155], v193 offset:37888
ds_read_b128 a[156:159], v193 offset:38400
v_mfma_f32_16x16x32_bf16 v[80:83], a[136:139], v[36:39], v[80:83]
v_exp_f32 v97, v97
v_mfma_f32_16x16x32_bf16 v[88:91], a[128:131], v[48:51], v[172:175]
v_exp_f32 v98, v98
v_mfma_f32_16x16x32_bf16 v[88:91], a[136:139], v[52:55], v[88:91]
v_exp_f32 v99, v99
v_mfma_f32_16x16x32_bf16 v[64:67], a[132:135], v[8:11], v[64:67]
s_waitcnt lgkmcnt(4)
v_exp_f32 v100, v100
v_mfma_f32_16x16x32_bf16 v[64:67], a[140:143], v[12:15], v[64:67]
v_cvt_pk_bf16_f32 v144, v96, v97
v_cvt_pk_bf16_f32 v145, v98, v99
v_mfma_f32_16x16x32_bf16 v[72:75], a[132:135], v[24:27], v[72:75]
v_exp_f32 v101, v101
v_mfma_f32_16x16x32_bf16 v[72:75], a[140:143], v[28:31], v[72:75]
v_exp_f32 v102, v102
v_mfma_f32_16x16x32_bf16 v[80:83], a[132:135], v[40:43], v[80:83]
s_waitcnt lgkmcnt(3)
v_mfma_f32_16x16x32_bf16 v[80:83], a[140:143], v[44:47], v[80:83]
v_exp_f32 v103, v103
v_mfma_f32_16x16x32_bf16 v[88:91], a[132:135], v[56:59], v[88:91]
v_exp_f32 v104, v104
v_mfma_f32_16x16x32_bf16 v[88:91], a[140:143], v[60:63], v[88:91]
s_add_u32 m0, s40, 148480
v_cvt_pk_bf16_f32 v146, v100, v101
v_cvt_pk_bf16_f32 v147, v102, v103
v_mfma_f32_16x16x32_bf16 v[68:71], a[144:147], v[0:3], v[160:163]
s_waitcnt lgkmcnt(2)
ds_read_b64_tr_b16 a[128:129], v200 offset:0
ds_read_b64_tr_b16 a[130:131], v200 offset:4096
v_mfma_f32_16x16x32_bf16 v[68:71], a[148:151], v[4:7], v[68:71]
s_add_u32 s57, s80, 128
buffer_load_dwordx4 v213, s[28:31], s57 offen lds
v_mfma_f32_16x16x32_bf16 v[76:79], a[144:147], v[16:19], v[164:167]
v_exp_f32 v105, v105
v_mfma_f32_16x16x32_bf16 v[76:79], a[148:151], v[20:23], v[76:79]
buffer_load_dwordx4 v214, s[28:31], s57 offen offset:2048 lds
v_mfma_f32_16x16x32_bf16 v[84:87], a[144:147], v[32:35], v[168:171]
s_waitcnt lgkmcnt(3)
ds_read_b64_tr_b16 a[132:133], v203 offset:0
ds_read_b64_tr_b16 a[134:135], v203 offset:4096
v_mfma_f32_16x16x32_bf16 v[84:87], a[148:151], v[36:39], v[84:87]
v_exp_f32 v106, v106
v_mfma_f32_16x16x32_bf16 v[92:95], a[144:147], v[48:51], v[172:175]
v_exp_f32 v107, v107
v_mfma_f32_16x16x32_bf16 v[92:95], a[148:151], v[52:55], v[92:95]
s_add_u32 m0, s40, 65536
v_exp_f32 v108, v108
v_mfma_f32_16x16x32_bf16 v[68:71], a[152:155], v[8:11], v[68:71]
s_waitcnt lgkmcnt(4)
ds_read_b64_tr_b16 a[136:137], v200 offset:512
ds_read_b64_tr_b16 a[138:139], v200 offset:4608
v_mfma_f32_16x16x32_bf16 v[68:71], a[156:159], v[12:15], v[68:71]
s_xor_b32 s58, s72, 0
buffer_load_dwordx4 v217, s[24:27], s58 offen sc1 lds
v_mfma_f32_16x16x32_bf16 v[76:79], a[152:155], v[24:27], v[76:79]
v_cvt_pk_bf16_f32 v148, v104, v105
v_mfma_f32_16x16x32_bf16 v[76:79], a[156:159], v[28:31], v[76:79]
v_cvt_pk_bf16_f32 v149, v106, v107
v_mfma_f32_16x16x32_bf16 v[84:87], a[152:155], v[40:43], v[84:87]
ds_read_b64_tr_b16 a[140:141], v203 offset:512
ds_read_b64_tr_b16 a[142:143], v203 offset:4608
v_mfma_f32_16x16x32_bf16 v[84:87], a[156:159], v[44:47], v[84:87]
v_exp_f32 v109, v109
v_mfma_f32_16x16x32_bf16 v[92:95], a[152:155], v[56:59], v[92:95]
v_exp_f32 v110, v110
v_mfma_f32_16x16x32_bf16 v[92:95], a[156:159], v[60:63], v[92:95]
s_add_u32 m0, s40, 66560
v_exp_f32 v111, v111
v_mfma_f32_16x16x32_bf16 v[176:179], v[232:235], v[128:131], v[176:179]
s_waitcnt lgkmcnt(6)
ds_read_b64_tr_b16 a[144:145], v200 offset:1024
ds_read_b64_tr_b16 a[146:147], v200 offset:5120
v_mfma_f32_16x16x32_bf16 v[180:183], v[232:235], v[132:135], v[180:183]
s_xor_b32 s59, s72, 4096
buffer_load_dwordx4 v217, s[24:27], s59 offen sc1 lds
v_mfma_f32_16x16x32_bf16 v[184:187], v[232:235], v[136:139], v[184:187]
v_exp_f32 v112, v112
v_mfma_f32_16x16x32_bf16 v[188:191], v[232:235], v[140:143], v[188:191]
v_cvt_pk_bf16_f32 v150, v108, v109
v_cvt_pk_bf16_f32 v151, v110, v111
v_mfma_f32_16x16x32_bf16 a[0:3], a[128:131], v[128:131], a[0:3]
s_waitcnt lgkmcnt(6)
ds_read_b64_tr_b16 a[148:149], v203 offset:1024
ds_read_b64_tr_b16 a[150:151], v203 offset:5120
v_mfma_f32_16x16x32_bf16 a[4:7], a[128:131], v[132:135], a[4:7]
v_mfma_f32_16x16x32_bf16 a[8:11], a[128:131], v[136:139], a[8:11]
v_exp_f32 v113, v113
v_mfma_f32_16x16x32_bf16 a[12:15], a[128:131], v[140:143], a[12:15]
s_add_u32 m0, s40, 67584
v_exp_f32 v114, v114
v_mfma_f32_16x16x32_bf16 a[16:19], a[132:135], v[128:131], a[16:19]
s_waitcnt lgkmcnt(6)
ds_read_b64_tr_b16 a[128:129], v200 offset:1536
ds_read_b64_tr_b16 a[130:131], v200 offset:5632
v_mfma_f32_16x16x32_bf16 a[20:23], a[132:135], v[132:135], a[20:23]
s_xor_b32 s56, s72, 8192
buffer_load_dwordx4 v217, s[24:27], s56 offen sc1 lds
v_mfma_f32_16x16x32_bf16 a[24:27], a[132:135], v[136:139], a[24:27]
v_exp_f32 v115, v115
v_mfma_f32_16x16x32_bf16 a[28:31], a[132:135], v[140:143], a[28:31]
v_exp_f32 v116, v116
v_mfma_f32_16x16x32_bf16 a[32:35], a[136:139], v[128:131], a[32:35]
s_waitcnt lgkmcnt(6)
ds_read_b64_tr_b16 a[132:133], v203 offset:1536
ds_read_b64_tr_b16 a[134:135], v203 offset:5632
v_mfma_f32_16x16x32_bf16 a[36:39], a[136:139], v[132:135], a[36:39]
v_cvt_pk_bf16_f32 v152, v112, v113
v_cvt_pk_bf16_f32 v153, v114, v115
v_mfma_f32_16x16x32_bf16 a[40:43], a[136:139], v[136:139], a[40:43]
v_exp_f32 v117, v117
v_mfma_f32_16x16x32_bf16 a[44:47], a[136:139], v[140:143], a[44:47]
s_add_u32 m0, s40, 68608
v_exp_f32 v118, v118
v_mfma_f32_16x16x32_bf16 a[48:51], a[140:143], v[128:131], a[48:51]
s_waitcnt lgkmcnt(6)
ds_read_b128 a[136:139], v193 offset:40960
ds_read_b128 a[152:155], v193 offset:41472
v_mfma_f32_16x16x32_bf16 a[52:55], a[140:143], v[132:135], a[52:55]
s_xor_b32 s57, s72, 12288
buffer_load_dwordx4 v217, s[24:27], s57 offen sc1 lds
v_mfma_f32_16x16x32_bf16 a[56:59], a[140:143], v[136:139], a[56:59]
v_mfma_f32_16x16x32_bf16 a[60:63], a[140:143], v[140:143], a[60:63]
v_exp_f32 v119, v119
v_mfma_f32_16x16x32_bf16 a[64:67], a[144:147], v[128:131], a[64:67]
s_waitcnt lgkmcnt(6)
v_exp_f32 v120, v120
v_mfma_f32_16x16x32_bf16 a[68:71], a[144:147], v[132:135], a[68:71]
v_cvt_pk_bf16_f32 v154, v116, v117
v_cvt_pk_bf16_f32 v155, v118, v119
v_mfma_f32_16x16x32_bf16 a[72:75], a[144:147], v[136:139], a[72:75]
v_exp_f32 v121, v121
v_mfma_f32_16x16x32_bf16 a[76:79], a[144:147], v[140:143], a[76:79]
v_exp_f32 v122, v122
v_mfma_f32_16x16x32_bf16 a[80:83], a[148:151], v[128:131], a[80:83]
s_waitcnt lgkmcnt(4)
ds_read_b128 a[140:143], v193 offset:41984
ds_read_b128 a[144:147], v193 offset:42496
v_mfma_f32_16x16x32_bf16 a[84:87], a[148:151], v[132:135], a[84:87]
v_exp_f32 v123, v123
v_mfma_f32_16x16x32_bf16 a[88:91], a[148:151], v[136:139], a[88:91]
v_exp_f32 v124, v124
v_mfma_f32_16x16x32_bf16 a[92:95], a[148:151], v[140:143], a[92:95]
v_cvt_pk_bf16_f32 v156, v120, v121
v_mfma_f32_16x16x32_bf16 a[96:99], a[128:131], v[128:131], a[96:99]
s_waitcnt lgkmcnt(4)
v_cvt_pk_bf16_f32 v157, v122, v123
v_mfma_f32_16x16x32_bf16 a[100:103], a[128:131], v[132:135], a[100:103]
v_exp_f32 v125, v125
v_mfma_f32_16x16x32_bf16 a[104:107], a[128:131], v[136:139], a[104:107]
v_exp_f32 v126, v126
v_mfma_f32_16x16x32_bf16 a[108:111], a[128:131], v[140:143], a[108:111]
s_add_u32 m0, s40, 81920
v_exp_f32 v127, v127
v_mfma_f32_16x16x32_bf16 a[112:115], a[132:135], v[128:131], a[112:115]
s_waitcnt lgkmcnt(3)
ds_read_b128 a[128:131], v193 offset:45056
ds_read_b128 a[148:151], v193 offset:45568
v_mfma_f32_16x16x32_bf16 a[116:119], a[132:135], v[132:135], a[116:119]
s_add_u32 s58, s80, 65536
buffer_load_dwordx4 v213, s[28:31], s58 offen lds
v_mfma_f32_16x16x32_bf16 a[120:123], a[132:135], v[136:139], a[120:123]
v_cvt_pk_bf16_f32 v158, v124, v125
v_cvt_pk_bf16_f32 v159, v126, v127
v_mfma_f32_16x16x32_bf16 a[124:127], a[132:135], v[140:143], a[124:127]
buffer_load_dwordx4 v214, s[28:31], s58 offen offset:2048 lds
v_mfma_f32_16x16x32_bf16 v[96:99], a[136:139], v[0:3], v[160:163]
s_waitcnt lgkmcnt(4)
v_exp_f32 v64, v64
v_mfma_f32_16x16x32_bf16 v[96:99], a[152:155], v[4:7], v[96:99]
v_mfma_f32_16x16x32_bf16 v[104:107], a[136:139], v[16:19], v[164:167]
v_exp_f32 v65, v65
v_mfma_f32_16x16x32_bf16 v[104:107], a[152:155], v[20:23], v[104:107]
v_exp_f32 v66, v66
v_mfma_f32_16x16x32_bf16 v[112:115], a[136:139], v[32:35], v[168:171]
s_waitcnt lgkmcnt(3)
ds_read_b128 a[132:135], v193 offset:46080
ds_read_b128 a[156:159], v193 offset:46592
v_mfma_f32_16x16x32_bf16 v[112:115], a[152:155], v[36:39], v[112:115]
v_exp_f32 v67, v67
v_mfma_f32_16x16x32_bf16 v[120:123], a[136:139], v[48:51], v[172:175]
v_exp_f32 v68, v68
v_mfma_f32_16x16x32_bf16 v[120:123], a[152:155], v[52:55], v[120:123]
v_cvt_pk_bf16_f32 v128, v64, v65
v_cvt_pk_bf16_f32 v129, v66, v67
v_mfma_f32_16x16x32_bf16 v[96:99], a[140:143], v[8:11], v[96:99]
s_waitcnt lgkmcnt(4)
v_exp_f32 v69, v69
v_mfma_f32_16x16x32_bf16 v[96:99], a[144:147], v[12:15], v[96:99]
v_mfma_f32_16x16x32_bf16 v[104:107], a[140:143], v[24:27], v[104:107]
v_exp_f32 v70, v70
v_mfma_f32_16x16x32_bf16 v[104:107], a[144:147], v[28:31], v[104:107]
s_add_u32 m0, s40, 82944
v_exp_f32 v71, v71
v_mfma_f32_16x16x32_bf16 v[112:115], a[140:143], v[40:43], v[112:115]
s_waitcnt lgkmcnt(3)
v_exp_f32 v72, v72
v_mfma_f32_16x16x32_bf16 v[112:115], a[144:147], v[44:47], v[112:115]
s_add_u32 s59, s80, 65664
buffer_load_dwordx4 v213, s[28:31], s59 offen lds
v_mfma_f32_16x16x32_bf16 v[120:123], a[140:143], v[56:59], v[120:123]
v_cvt_pk_bf16_f32 v130, v68, v69
v_cvt_pk_bf16_f32 v131, v70, v71
v_mfma_f32_16x16x32_bf16 v[120:123], a[144:147], v[60:63], v[120:123]
buffer_load_dwordx4 v214, s[28:31], s59 offen offset:2048 lds
v_mfma_f32_16x16x32_bf16 v[100:103], a[128:131], v[0:3], v[160:163]
s_waitcnt lgkmcnt(2)
ds_read_b64_tr_b16 a[136:137], v200 offset:8192
ds_read_b64_tr_b16 a[138:139], v200 offset:12288
v_mfma_f32_16x16x32_bf16 v[100:103], a[148:151], v[4:7], v[100:103]
v_exp_f32 v73, v73
v_mfma_f32_16x16x32_bf16 v[108:111], a[128:131], v[16:19], v[164:167]
v_exp_f32 v74, v74
v_mfma_f32_16x16x32_bf16 v[108:111], a[148:151], v[20:23], v[108:111]
s_add_u32 m0, s40, 49152
v_mfma_f32_16x16x32_bf16 v[116:119], a[128:131], v[32:35], v[168:171]
s_waitcnt lgkmcnt(3)
ds_read_b64_tr_b16 a[140:141], v203 offset:8192
ds_read_b64_tr_b16 a[142:143], v203 offset:12288
v_mfma_f32_16x16x32_bf16 v[116:119], a[148:151], v[36:39], v[116:119]
s_xor_b32 s56, s72, 2048
buffer_load_dwordx4 v217, s[24:27], s56 offen sc1 lds
v_mfma_f32_16x16x32_bf16 v[124:127], a[128:131], v[48:51], v[172:175]
v_exp_f32 v75, v75
v_mfma_f32_16x16x32_bf16 v[124:127], a[148:151], v[52:55], v[124:127]
v_exp_f32 v76, v76
v_mfma_f32_16x16x32_bf16 v[100:103], a[132:135], v[8:11], v[100:103]
s_waitcnt lgkmcnt(4)
ds_read_b64_tr_b16 a[128:129], v200 offset:8704
ds_read_b64_tr_b16 a[130:131], v200 offset:12800
v_mfma_f32_16x16x32_bf16 v[100:103], a[156:159], v[12:15], v[100:103]
v_cvt_pk_bf16_f32 v132, v72, v73
v_cvt_pk_bf16_f32 v133, v74, v75
v_mfma_f32_16x16x32_bf16 v[108:111], a[132:135], v[24:27], v[108:111]
v_exp_f32 v77, v77
v_mfma_f32_16x16x32_bf16 v[108:111], a[156:159], v[28:31], v[108:111]
s_add_u32 m0, s40, 50176
v_exp_f32 v78, v78
v_mfma_f32_16x16x32_bf16 v[116:119], a[132:135], v[40:43], v[116:119]
ds_read_b64_tr_b16 a[144:145], v203 offset:8704
ds_read_b64_tr_b16 a[146:147], v203 offset:12800
v_mfma_f32_16x16x32_bf16 v[116:119], a[156:159], v[44:47], v[116:119]
s_xor_b32 s57, s72, 6144
buffer_load_dwordx4 v217, s[24:27], s57 offen sc1 lds
v_mfma_f32_16x16x32_bf16 v[124:127], a[132:135], v[56:59], v[124:127]
v_mfma_f32_16x16x32_bf16 v[124:127], a[156:159], v[60:63], v[124:127]
v_exp_f32 v79, v79
v_mfma_f32_16x16x32_bf16 v[176:179], v[232:235], v[144:147], v[176:179]
s_waitcnt lgkmcnt(6)
ds_read_b64_tr_b16 a[132:133], v200 offset:9216
ds_read_b64_tr_b16 a[134:135], v200 offset:13312
v_mfma_f32_16x16x32_bf16 v[180:183], v[232:235], v[148:151], v[180:183]
ds_read_b64_tr_b16 a[156:157], v203 offset:9728
ds_read_b64_tr_b16 a[158:159], v203 offset:13824
v_mfma_f32_16x16x32_bf16 v[184:187], v[232:235], v[152:155], v[184:187]
ds_read_b64_tr_b16 a[152:153], v200 offset:9728
ds_read_b64_tr_b16 a[154:155], v200 offset:13824
v_mfma_f32_16x16x32_bf16 v[188:191], v[232:235], v[156:159], v[188:191]
s_add_u32 m0, s40, 51200
ds_read_b64_tr_b16 a[148:149], v203 offset:9216
ds_read_b64_tr_b16 a[150:151], v203 offset:13312
v_mfma_f32_16x16x32_bf16 a[0:3], a[136:139], v[144:147], a[0:3]
s_waitcnt lgkmcnt(12)
v_exp_f32 v80, v80
v_mfma_f32_16x16x32_bf16 a[4:7], a[136:139], v[148:151], a[4:7]
s_xor_b32 s58, s72, 10240
buffer_load_dwordx4 v217, s[24:27], s58 offen sc1 lds
v_mfma_f32_16x16x32_bf16 a[8:11], a[136:139], v[152:155], a[8:11]
v_cvt_pk_bf16_f32 v134, v76, v77
v_cvt_pk_bf16_f32 v135, v78, v79
v_mfma_f32_16x16x32_bf16 a[12:15], a[136:139], v[156:159], a[12:15]
v_exp_f32 v81, v81
v_mfma_f32_16x16x32_bf16 a[16:19], a[140:143], v[144:147], a[16:19]
s_waitcnt lgkmcnt(10)
v_exp_f32 v82, v82
v_mfma_f32_16x16x32_bf16 a[20:23], a[140:143], v[148:151], a[20:23]
v_exp_f32 v83, v83
v_mfma_f32_16x16x32_bf16 a[24:27], a[140:143], v[152:155], a[24:27]
v_mfma_f32_16x16x32_bf16 a[28:31], a[140:143], v[156:159], a[28:31]
s_add_u32 m0, s40, 52224
v_exp_f32 v84, v84
v_mfma_f32_16x16x32_bf16 a[32:35], a[128:131], v[144:147], a[32:35]
s_waitcnt vmcnt(19) lgkmcnt(0)
s_barrier
v_mfma_f32_16x16x32_bf16 a[36:39], a[128:131], v[148:151], a[36:39]
s_xor_b32 s59, s72, 14336
buffer_load_dwordx4 v217, s[24:27], s59 offen sc1 lds
v_mfma_f32_16x16x32_bf16 a[40:43], a[128:131], v[152:155], a[40:43]
v_cvt_pk_bf16_f32 v136, v80, v81
v_cvt_pk_bf16_f32 v137, v82, v83
v_mfma_f32_16x16x32_bf16 a[44:47], a[128:131], v[156:159], a[44:47]
v_exp_f32 v85, v85
v_mfma_f32_16x16x32_bf16 a[48:51], a[144:147], v[144:147], a[48:51]
ds_read_b128 a[128:131], v192 offset:0
ds_read_b128 a[136:139], v192 offset:512
v_mfma_f32_16x16x32_bf16 a[52:55], a[144:147], v[148:151], a[52:55]
v_exp_f32 v86, v86
v_mfma_f32_16x16x32_bf16 a[56:59], a[144:147], v[152:155], a[56:59]
v_exp_f32 v87, v87
v_mfma_f32_16x16x32_bf16 a[60:63], a[144:147], v[156:159], a[60:63]
v_exp_f32 v88, v88
v_mfma_f32_16x16x32_bf16 a[64:67], a[132:135], v[144:147], a[64:67]
v_cvt_pk_bf16_f32 v138, v84, v85
v_mfma_f32_16x16x32_bf16 a[68:71], a[132:135], v[148:151], a[68:71]
v_cvt_pk_bf16_f32 v139, v86, v87
v_mfma_f32_16x16x32_bf16 a[72:75], a[132:135], v[152:155], a[72:75]
v_exp_f32 v89, v89
v_mfma_f32_16x16x32_bf16 a[76:79], a[132:135], v[156:159], a[76:79]
v_exp_f32 v90, v90
v_mfma_f32_16x16x32_bf16 a[80:83], a[148:151], v[144:147], a[80:83]
ds_read_b128 a[132:135], v192 offset:1024
ds_read_b128 a[140:143], v192 offset:1536
v_mfma_f32_16x16x32_bf16 a[84:87], a[148:151], v[148:151], a[84:87]
v_exp_f32 v91, v91
v_mfma_f32_16x16x32_bf16 a[88:91], a[148:151], v[152:155], a[88:91]
v_exp_f32 v92, v92
v_mfma_f32_16x16x32_bf16 a[92:95], a[148:151], v[156:159], a[92:95]
s_add_u32 m0, s40, 114688
v_cvt_pk_bf16_f32 v140, v88, v89
v_mfma_f32_16x16x32_bf16 a[96:99], a[152:155], v[144:147], a[96:99]
v_cvt_pk_bf16_f32 v141, v90, v91
v_mfma_f32_16x16x32_bf16 a[100:103], a[152:155], v[148:151], a[100:103]
s_add_u32 s56, s81, 0
buffer_load_dwordx4 v213, s[28:31], s56 offen lds
v_mfma_f32_16x16x32_bf16 a[104:107], a[152:155], v[152:155], a[104:107]
v_exp_f32 v93, v93
v_mfma_f32_16x16x32_bf16 a[108:111], a[152:155], v[156:159], a[108:111]
buffer_load_dwordx4 v214, s[28:31], s56 offen offset:2048 lds
v_mfma_f32_16x16x32_bf16 a[112:115], a[156:159], v[144:147], a[112:115]
s_waitcnt lgkmcnt(3)
ds_read_b128 a[144:147], v192 offset:4096
ds_read_b128 a[148:151], v192 offset:4608
v_mfma_f32_16x16x32_bf16 a[116:119], a[156:159], v[148:151], a[116:119]
v_exp_f32 v94, v94
v_mfma_f32_16x16x32_bf16 a[120:123], a[156:159], v[152:155], a[120:123]
v_exp_f32 v95, v95
v_mfma_f32_16x16x32_bf16 a[124:127], a[156:159], v[156:159], a[124:127]
v_cvt_pk_bf16_f32 v142, v92, v93
v_cvt_pk_bf16_f32 v143, v94, v95
v_mfma_f32_16x16x32_bf16 v[64:67], a[128:131], v[0:3], v[160:163]
s_waitcnt lgkmcnt(4)
v_exp_f32 v96, v96
v_mfma_f32_16x16x32_bf16 v[64:67], a[136:139], v[4:7], v[64:67]
v_mfma_f32_16x16x32_bf16 v[72:75], a[128:131], v[16:19], v[164:167]
v_exp_f32 v97, v97
v_mfma_f32_16x16x32_bf16 v[72:75], a[136:139], v[20:23], v[72:75]
v_exp_f32 v98, v98
v_mfma_f32_16x16x32_bf16 v[80:83], a[128:131], v[32:35], v[168:171]
s_waitcnt lgkmcnt(3)
ds_read_b128 a[152:155], v192 offset:5120
ds_read_b128 a[156:159], v192 offset:5632
v_mfma_f32_16x16x32_bf16 v[80:83], a[136:139], v[36:39], v[80:83]
v_exp_f32 v99, v99
v_mfma_f32_16x16x32_bf16 v[88:91], a[128:131], v[48:51], v[172:175]
v_exp_f32 v100, v100
v_mfma_f32_16x16x32_bf16 v[88:91], a[136:139], v[52:55], v[88:91]
s_add_u32 m0, s40, 115712
v_cvt_pk_bf16_f32 v144, v96, v97
v_cvt_pk_bf16_f32 v145, v98, v99
v_mfma_f32_16x16x32_bf16 v[64:67], a[132:135], v[8:11], v[64:67]
s_waitcnt lgkmcnt(4)
v_mfma_f32_16x16x32_bf16 v[64:67], a[140:143], v[12:15], v[64:67]
s_add_u32 s57, s81, 128
buffer_load_dwordx4 v213, s[28:31], s57 offen lds
v_mfma_f32_16x16x32_bf16 v[72:75], a[132:135], v[24:27], v[72:75]
v_exp_f32 v101, v101
v_mfma_f32_16x16x32_bf16 v[72:75], a[140:143], v[28:31], v[72:75]
buffer_load_dwordx4 v214, s[28:31], s57 offen offset:2048 lds
v_mfma_f32_16x16x32_bf16 v[80:83], a[132:135], v[40:43], v[80:83]
s_waitcnt lgkmcnt(3)
v_exp_f32 v102, v102
v_mfma_f32_16x16x32_bf16 v[80:83], a[140:143], v[44:47], v[80:83]
v_exp_f32 v103, v103
v_mfma_f32_16x16x32_bf16 v[88:91], a[132:135], v[56:59], v[88:91]
v_exp_f32 v104, v104
v_mfma_f32_16x16x32_bf16 v[88:91], a[140:143], v[60:63], v[88:91]
L_tch14_%=:
s_cbranch_execz L_tb1_%=
s_add_u32 m0, s40, 98304
v_cvt_pk_bf16_f32 v146, v100, v101
v_cvt_pk_bf16_f32 v147, v102, v103
v_mfma_f32_16x16x32_bf16 v[68:71], a[144:147], v[0:3], v[160:163]
s_waitcnt lgkmcnt(2)
ds_read_b64_tr_b16 a[128:129], v198 offset:16384
ds_read_b64_tr_b16 a[130:131], v198 offset:20480
v_mfma_f32_16x16x32_bf16 v[68:71], a[148:151], v[4:7], v[68:71]
s_xor_b32 s58, s73, 0
buffer_load_dwordx4 v217, s[24:27], s58 offen sc1 lds
v_mfma_f32_16x16x32_bf16 v[76:79], a[144:147], v[16:19], v[164:167]
v_mfma_f32_16x16x32_bf16 v[76:79], a[148:151], v[20:23], v[76:79]
v_exp_f32 v105, v105
v_mfma_f32_16x16x32_bf16 v[84:87], a[144:147], v[32:35], v[168:171]
s_waitcnt lgkmcnt(3)
ds_read_b64_tr_b16 a[132:133], v201 offset:16384
ds_read_b64_tr_b16 a[134:135], v201 offset:20480
v_mfma_f32_16x16x32_bf16 v[84:87], a[148:151], v[36:39], v[84:87]
v_exp_f32 v106, v106
v_mfma_f32_16x16x32_bf16 v[92:95], a[144:147], v[48:51], v[172:175]
v_exp_f32 v107, v107
v_mfma_f32_16x16x32_bf16 v[92:95], a[148:151], v[52:55], v[92:95]
s_add_u32 m0, s40, 99328
v_exp_f32 v108, v108
v_mfma_f32_16x16x32_bf16 v[68:71], a[152:155], v[8:11], v[68:71]
s_waitcnt lgkmcnt(4)
ds_read_b64_tr_b16 a[136:137], v198 offset:16896
ds_read_b64_tr_b16 a[138:139], v198 offset:20992
v_mfma_f32_16x16x32_bf16 v[68:71], a[156:159], v[12:15], v[68:71]
s_xor_b32 s59, s73, 4096
buffer_load_dwordx4 v217, s[24:27], s59 offen sc1 lds
v_mfma_f32_16x16x32_bf16 v[76:79], a[152:155], v[24:27], v[76:79]
v_cvt_pk_bf16_f32 v148, v104, v105
v_cvt_pk_bf16_f32 v149, v106, v107
v_mfma_f32_16x16x32_bf16 v[76:79], a[156:159], v[28:31], v[76:79]
v_mfma_f32_16x16x32_bf16 v[84:87], a[152:155], v[40:43], v[84:87]
ds_read_b64_tr_b16 a[140:141], v201 offset:16896
ds_read_b64_tr_b16 a[142:143], v201 offset:20992
v_mfma_f32_16x16x32_bf16 v[84:87], a[156:159], v[44:47], v[84:87]
v_exp_f32 v109, v109
v_mfma_f32_16x16x32_bf16 v[92:95], a[152:155], v[56:59], v[92:95]
v_exp_f32 v110, v110
v_mfma_f32_16x16x32_bf16 v[92:95], a[156:159], v[60:63], v[92:95]
s_add_u32 m0, s40, 100352
v_exp_f32 v111, v111
v_mfma_f32_16x16x32_bf16 v[176:179], v[232:235], v[128:131], v[176:179]
s_waitcnt lgkmcnt(6)
ds_read_b64_tr_b16 a[144:145], v198 offset:17408
ds_read_b64_tr_b16 a[146:147], v198 offset:21504
v_mfma_f32_16x16x32_bf16 v[180:183], v[232:235], v[132:135], v[180:183]
s_xor_b32 s56, s73, 8192
buffer_load_dwordx4 v217, s[24:27], s56 offen sc1 lds
v_mfma_f32_16x16x32_bf16 v[184:187], v[232:235], v[136:139], v[184:187]
v_exp_f32 v112, v112
v_mfma_f32_16x16x32_bf16 v[188:191], v[232:235], v[140:143], v[188:191]
v_cvt_pk_bf16_f32 v150, v108, v109
v_cvt_pk_bf16_f32 v151, v110, v111
v_mfma_f32_16x16x32_bf16 a[0:3], a[128:131], v[128:131], a[0:3]
s_waitcnt lgkmcnt(6)
ds_read_b64_tr_b16 a[148:149], v201 offset:17408
ds_read_b64_tr_b16 a[150:151], v201 offset:21504
v_mfma_f32_16x16x32_bf16 a[4:7], a[128:131], v[132:135], a[4:7]
v_mfma_f32_16x16x32_bf16 a[8:11], a[128:131], v[136:139], a[8:11]
v_exp_f32 v113, v113
v_mfma_f32_16x16x32_bf16 a[12:15], a[128:131], v[140:143], a[12:15]
s_add_u32 m0, s40, 101376
v_exp_f32 v114, v114
v_mfma_f32_16x16x32_bf16 a[16:19], a[132:135], v[128:131], a[16:19]
s_waitcnt lgkmcnt(6)
ds_read_b64_tr_b16 a[128:129], v198 offset:17920
ds_read_b64_tr_b16 a[130:131], v198 offset:22016
v_mfma_f32_16x16x32_bf16 a[20:23], a[132:135], v[132:135], a[20:23]
s_xor_b32 s57, s73, 12288
buffer_load_dwordx4 v217, s[24:27], s57 offen sc1 lds
v_mfma_f32_16x16x32_bf16 a[24:27], a[132:135], v[136:139], a[24:27]
v_exp_f32 v115, v115
v_mfma_f32_16x16x32_bf16 a[28:31], a[132:135], v[140:143], a[28:31]
v_exp_f32 v116, v116
v_mfma_f32_16x16x32_bf16 a[32:35], a[136:139], v[128:131], a[32:35]
s_waitcnt lgkmcnt(6)
ds_read_b64_tr_b16 a[132:133], v201 offset:17920
ds_read_b64_tr_b16 a[134:135], v201 offset:22016
v_mfma_f32_16x16x32_bf16 a[36:39], a[136:139], v[132:135], a[36:39]
v_cvt_pk_bf16_f32 v152, v112, v113
v_cvt_pk_bf16_f32 v153, v114, v115
v_mfma_f32_16x16x32_bf16 a[40:43], a[136:139], v[136:139], a[40:43]
v_mfma_f32_16x16x32_bf16 a[44:47], a[136:139], v[140:143], a[44:47]
v_exp_f32 v117, v117
v_mfma_f32_16x16x32_bf16 a[48:51], a[140:143], v[128:131], a[48:51]
s_waitcnt lgkmcnt(6)
ds_read_b128 a[136:139], v192 offset:8192
ds_read_b128 a[152:155], v192 offset:8704
v_mfma_f32_16x16x32_bf16 a[52:55], a[140:143], v[132:135], a[52:55]
v_exp_f32 v118, v118
v_mfma_f32_16x16x32_bf16 a[56:59], a[140:143], v[136:139], a[56:59]
v_exp_f32 v119, v119
v_mfma_f32_16x16x32_bf16 a[60:63], a[140:143], v[140:143], a[60:63]
v_exp_f32 v120, v120
v_mfma_f32_16x16x32_bf16 a[64:67], a[144:147], v[128:131], a[64:67]
s_waitcnt lgkmcnt(6)
v_cvt_pk_bf16_f32 v154, v116, v117
v_cvt_pk_bf16_f32 v155, v118, v119
v_mfma_f32_16x16x32_bf16 a[68:71], a[144:147], v[132:135], a[68:71]
v_mfma_f32_16x16x32_bf16 a[72:75], a[144:147], v[136:139], a[72:75]
v_exp_f32 v121, v121
v_mfma_f32_16x16x32_bf16 a[76:79], a[144:147], v[140:143], a[76:79]
s_add_u32 m0, s40, 131072
v_exp_f32 v122, v122
v_mfma_f32_16x16x32_bf16 a[80:83], a[148:151], v[128:131], a[80:83]
s_waitcnt lgkmcnt(4)
ds_read_b128 a[140:143], v192 offset:9216
ds_read_b128 a[144:147], v192 offset:9728
v_mfma_f32_16x16x32_bf16 a[84:87], a[148:151], v[132:135], a[84:87]
s_add_u32 s58, s81, 65536
buffer_load_dwordx4 v213, s[28:31], s58 offen lds
v_mfma_f32_16x16x32_bf16 a[88:91], a[148:151], v[136:139], a[88:91]
v_exp_f32 v123, v123
v_mfma_f32_16x16x32_bf16 a[92:95], a[148:151], v[140:143], a[92:95]
buffer_load_dwordx4 v214, s[28:31], s58 offen offset:2048 lds
v_mfma_f32_16x16x32_bf16 a[96:99], a[128:131], v[128:131], a[96:99]
s_waitcnt lgkmcnt(4)
v_exp_f32 v124, v124
v_mfma_f32_16x16x32_bf16 a[100:103], a[128:131], v[132:135], a[100:103]
v_cvt_pk_bf16_f32 v156, v120, v121
v_cvt_pk_bf16_f32 v157, v122, v123
v_mfma_f32_16x16x32_bf16 a[104:107], a[128:131], v[136:139], a[104:107]
v_mfma_f32_16x16x32_bf16 a[108:111], a[128:131], v[140:143], a[108:111]
v_exp_f32 v125, v125
v_mfma_f32_16x16x32_bf16 a[112:115], a[132:135], v[128:131], a[112:115]
s_waitcnt lgkmcnt(3)
ds_read_b128 a[128:131], v192 offset:12288
ds_read_b128 a[148:151], v192 offset:12800
v_mfma_f32_16x16x32_bf16 a[116:119], a[132:135], v[132:135], a[116:119]
v_exp_f32 v126, v126
v_mfma_f32_16x16x32_bf16 a[120:123], a[132:135], v[136:139], a[120:123]
v_exp_f32 v127, v127
v_mfma_f32_16x16x32_bf16 a[124:127], a[132:135], v[140:143], a[124:127]
v_cvt_pk_bf16_f32 v158, v124, v125
v_cvt_pk_bf16_f32 v159, v126, v127
v_mfma_f32_16x16x32_bf16 v[96:99], a[136:139], v[0:3], v[160:163]
s_waitcnt lgkmcnt(4)
v_exp_f32 v64, v64
v_mfma_f32_16x16x32_bf16 v[96:99], a[152:155], v[4:7], v[96:99]
v_mfma_f32_16x16x32_bf16 v[104:107], a[136:139], v[16:19], v[164:167]
v_exp_f32 v65, v65
v_mfma_f32_16x16x32_bf16 v[104:107], a[152:155], v[20:23], v[104:107]
s_add_u32 m0, s40, 132096
v_exp_f32 v66, v66
v_mfma_f32_16x16x32_bf16 v[112:115], a[136:139], v[32:35], v[168:171]
s_waitcnt lgkmcnt(3)
ds_read_b128 a[132:135], v192 offset:13312
ds_read_b128 a[156:159], v192 offset:13824
v_mfma_f32_16x16x32_bf16 v[112:115], a[152:155], v[36:39], v[112:115]
s_add_u32 s59, s81, 65664
buffer_load_dwordx4 v213, s[28:31], s59 offen lds
v_mfma_f32_16x16x32_bf16 v[120:123], a[136:139], v[48:51], v[172:175]
v_exp_f32 v67, v67
v_mfma_f32_16x16x32_bf16 v[120:123], a[152:155], v[52:55], v[120:123]
buffer_load_dwordx4 v214, s[28:31], s59 offen offset:2048 lds
v_mfma_f32_16x16x32_bf16 v[96:99], a[140:143], v[8:11], v[96:99]
s_waitcnt lgkmcnt(4)
v_exp_f32 v68, v68
v_mfma_f32_16x16x32_bf16 v[96:99], a[144:147], v[12:15], v[96:99]
v_cvt_pk_bf16_f32 v128, v64, v65
v_mfma_f32_16x16x32_bf16 v[104:107], a[140:143], v[24:27], v[104:107]
v_cvt_pk_bf16_f32 v129, v66, v67
v_mfma_f32_16x16x32_bf16 v[104:107], a[144:147], v[28:31], v[104:107]
v_exp_f32 v69, v69
v_mfma_f32_16x16x32_bf16 v[112:115], a[140:143], v[40:43], v[112:115]
s_waitcnt lgkmcnt(3)
v_exp_f32 v70, v70
v_mfma_f32_16x16x32_bf16 v[112:115], a[144:147], v[44:47], v[112:115]
v_exp_f32 v71, v71
v_mfma_f32_16x16x32_bf16 v[120:123], a[140:143], v[56:59], v[120:123]
v_exp_f32 v72, v72
v_mfma_f32_16x16x32_bf16 v[120:123], a[144:147], v[60:63], v[120:123]
v_cvt_pk_bf16_f32 v130, v68, v69
v_mfma_f32_16x16x32_bf16 v[100:103], a[128:131], v[0:3], v[160:163]
s_waitcnt lgkmcnt(2)
ds_read_b64_tr_b16 a[136:137], v198 offset:24576
ds_read_b64_tr_b16 a[138:139], v198 offset:28672
v_mfma_f32_16x16x32_bf16 v[100:103], a[148:151], v[4:7], v[100:103]
v_cvt_pk_bf16_f32 v131, v70, v71
v_mfma_f32_16x16x32_bf16 v[108:111], a[128:131], v[16:19], v[164:167]
v_exp_f32 v73, v73
v_mfma_f32_16x16x32_bf16 v[108:111], a[148:151], v[20:23], v[108:111]
v_exp_f32 v74, v74
v_mfma_f32_16x16x32_bf16 v[116:119], a[128:131], v[32:35], v[168:171]
s_waitcnt lgkmcnt(3)
ds_read_b64_tr_b16 a[140:141], v201 offset:24576
ds_read_b64_tr_b16 a[142:143], v201 offset:28672
v_mfma_f32_16x16x32_bf16 v[116:119], a[148:151], v[36:39], v[116:119]
v_exp_f32 v75, v75
v_mfma_f32_16x16x32_bf16 v[124:127], a[128:131], v[48:51], v[172:175]
v_mfma_f32_16x16x32_bf16 v[124:127], a[148:151], v[52:55], v[124:127]
v_exp_f32 v76, v76
v_mfma_f32_16x16x32_bf16 v[100:103], a[132:135], v[8:11], v[100:103]
s_waitcnt lgkmcnt(4)
ds_read_b64_tr_b16 a[128:129], v198 offset:25088
ds_read_b64_tr_b16 a[130:131], v198 offset:29184
v_mfma_f32_16x16x32_bf16 v[100:103], a[156:159], v[12:15], v[100:103]
v_cvt_pk_bf16_f32 v132, v72, v73
v_cvt_pk_bf16_f32 v133, v74, v75
v_mfma_f32_16x16x32_bf16 v[108:111], a[132:135], v[24:27], v[108:111]
v_exp_f32 v77, v77
v_mfma_f32_16x16x32_bf16 v[108:111], a[156:159], v[28:31], v[108:111]
v_exp_f32 v78, v78
v_mfma_f32_16x16x32_bf16 v[116:119], a[132:135], v[40:43], v[116:119]
ds_read_b64_tr_b16 a[144:145], v201 offset:25088
ds_read_b64_tr_b16 a[146:147], v201 offset:29184
v_mfma_f32_16x16x32_bf16 v[116:119], a[156:159], v[44:47], v[116:119]
v_exp_f32 v79, v79
v_mfma_f32_16x16x32_bf16 v[124:127], a[132:135], v[56:59], v[124:127]
v_mfma_f32_16x16x32_bf16 v[124:127], a[156:159], v[60:63], v[124:127]
v_exp_f32 v80, v80
v_mfma_f32_16x16x32_bf16 v[176:179], v[232:235], v[144:147], v[176:179]
s_waitcnt lgkmcnt(6)
ds_read_b64_tr_b16 a[132:133], v198 offset:25600
ds_read_b64_tr_b16 a[134:135], v198 offset:29696
v_mfma_f32_16x16x32_bf16 v[180:183], v[232:235], v[148:151], v[180:183]
v_cvt_pk_bf16_f32 v134, v76, v77
v_cvt_pk_bf16_f32 v135, v78, v79
v_mfma_f32_16x16x32_bf16 v[184:187], v[232:235], v[152:155], v[184:187]
v_exp_f32 v81, v81
v_mfma_f32_16x16x32_bf16 v[188:191], v[232:235], v[156:159], v[188:191]
v_exp_f32 v82, v82
v_mfma_f32_16x16x32_bf16 a[0:3], a[136:139], v[144:147], a[0:3]
s_waitcnt lgkmcnt(6)
ds_read_b64_tr_b16 a[148:149], v201 offset:25600
ds_read_b64_tr_b16 a[150:151], v201 offset:29696
v_mfma_f32_16x16x32_bf16 a[4:7], a[136:139], v[148:151], a[4:7]
v_mfma_f32_16x16x32_bf16 a[8:11], a[136:139], v[152:155], a[8:11]
ds_read_b64_tr_b16 a[152:153], v201 offset:26112
ds_read_b64_tr_b16 a[154:155], v201 offset:30208
v_mfma_f32_16x16x32_bf16 a[12:15], a[136:139], v[156:159], a[12:15]
ds_read_b64_tr_b16 a[136:137], v198 offset:26112
ds_read_b64_tr_b16 a[138:139], v198 offset:30208
v_mfma_f32_16x16x32_bf16 a[16:19], a[140:143], v[144:147], a[16:19]
s_waitcnt lgkmcnt(10)
v_exp_f32 v83, v83
v_mfma_f32_16x16x32_bf16 a[20:23], a[140:143], v[148:151], a[20:23]
v_exp_f32 v84, v84
v_mfma_f32_16x16x32_bf16 a[24:27], a[140:143], v[152:155], a[24:27]
v_cvt_pk_bf16_f32 v136, v80, v81
v_cvt_pk_bf16_f32 v137, v82, v83
v_mfma_f32_16x16x32_bf16 a[28:31], a[140:143], v[156:159], a[28:31]
v_exp_f32 v85, v85
v_mfma_f32_16x16x32_bf16 a[32:35], a[128:131], v[144:147], a[32:35]
s_waitcnt lgkmcnt(8)
v_exp_f32 v86, v86
v_mfma_f32_16x16x32_bf16 a[36:39], a[128:131], v[148:151], a[36:39]
v_mfma_f32_16x16x32_bf16 a[40:43], a[128:131], v[152:155], a[40:43]
v_exp_f32 v87, v87
v_mfma_f32_16x16x32_bf16 a[44:47], a[128:131], v[156:159], a[44:47]
s_add_u32 m0, s40, 0
v_exp_f32 v88, v88
v_mfma_f32_16x16x32_bf16 a[48:51], a[144:147], v[144:147], a[48:51]
s_waitcnt vmcnt(28) lgkmcnt(0)
s_barrier
v_mfma_f32_16x16x32_bf16 a[52:55], a[144:147], v[148:151], a[52:55]
s_xor_b32 s56, s73, 2048
buffer_load_dwordx4 v217, s[24:27], s56 offen sc1 lds
v_mfma_f32_16x16x32_bf16 a[56:59], a[144:147], v[152:155], a[56:59]
v_cvt_pk_bf16_f32 v138, v84, v85
v_cvt_pk_bf16_f32 v139, v86, v87
v_mfma_f32_16x16x32_bf16 a[60:63], a[144:147], v[156:159], a[60:63]
v_exp_f32 v89, v89
v_mfma_f32_16x16x32_bf16 a[64:67], a[132:135], v[144:147], a[64:67]
ds_read_b64_tr_b16 a[128:129], v198 offset:32768
ds_read_b64_tr_b16 a[130:131], v198 offset:36864
v_mfma_f32_16x16x32_bf16 a[68:71], a[132:135], v[148:151], a[68:71]
v_mfma_f32_16x16x32_bf16 a[72:75], a[132:135], v[152:155], a[72:75]
v_exp_f32 v90, v90
v_mfma_f32_16x16x32_bf16 a[76:79], a[132:135], v[156:159], a[76:79]
s_add_u32 m0, s40, 1024
v_exp_f32 v91, v91
v_mfma_f32_16x16x32_bf16 a[80:83], a[148:151], v[144:147], a[80:83]
ds_read_b64_tr_b16 a[132:133], v201 offset:32768
ds_read_b64_tr_b16 a[134:135], v201 offset:36864
v_mfma_f32_16x16x32_bf16 a[84:87], a[148:151], v[148:151], a[84:87]
s_xor_b32 s57, s73, 6144
buffer_load_dwordx4 v217, s[24:27], s57 offen sc1 lds
v_mfma_f32_16x16x32_bf16 a[88:91], a[148:151], v[152:155], a[88:91]
v_exp_f32 v92, v92
v_mfma_f32_16x16x32_bf16 a[92:95], a[148:151], v[156:159], a[92:95]
v_cvt_pk_bf16_f32 v140, v88, v89
v_cvt_pk_bf16_f32 v141, v90, v91
v_mfma_f32_16x16x32_bf16 a[96:99], a[136:139], v[144:147], a[96:99]
ds_read_b64_tr_b16 a[140:141], v198 offset:33280
ds_read_b64_tr_b16 a[142:143], v198 offset:37376
v_mfma_f32_16x16x32_bf16 a[100:103], a[136:139], v[148:151], a[100:103]
v_mfma_f32_16x16x32_bf16 a[104:107], a[136:139], v[152:155], a[104:107]
v_exp_f32 v93, v93
v_mfma_f32_16x16x32_bf16 a[108:111], a[136:139], v[156:159], a[108:111]
s_add_u32 m0, s40, 2048
v_exp_f32 v94, v94
v_mfma_f32_16x16x32_bf16 a[112:115], a[152:155], v[144:147], a[112:115]
ds_read_b64_tr_b16 a[136:137], v201 offset:33280
ds_read_b64_tr_b16 a[138:139], v201 offset:37376
v_mfma_f32_16x16x32_bf16 a[116:119], a[152:155], v[148:151], a[116:119]
s_xor_b32 s58, s73, 10240
buffer_load_dwordx4 v217, s[24:27], s58 offen sc1 lds
v_mfma_f32_16x16x32_bf16 a[120:123], a[152:155], v[152:155], a[120:123]
v_exp_f32 v95, v95
v_mfma_f32_16x16x32_bf16 a[124:127], a[152:155], v[156:159], a[124:127]
v_cvt_pk_bf16_f32 v142, v92, v93
v_cvt_pk_bf16_f32 v143, v94, v95
v_mfma_f32_16x16x32_bf16 v[176:179], v[232:235], v[128:131], v[176:179]
s_waitcnt lgkmcnt(6)
ds_read_b64_tr_b16 a[144:145], v198 offset:33792
ds_read_b64_tr_b16 a[146:147], v198 offset:37888
v_mfma_f32_16x16x32_bf16 v[180:183], v[232:235], v[132:135], v[180:183]
v_exp_f32 v96, v96
v_mfma_f32_16x16x32_bf16 v[184:187], v[232:235], v[136:139], v[184:187]
v_exp_f32 v97, v97
v_mfma_f32_16x16x32_bf16 v[188:191], v[232:235], v[140:143], v[188:191]
s_add_u32 m0, s40, 3072
v_exp_f32 v98, v98
v_mfma_f32_16x16x32_bf16 a[0:3], a[128:131], v[128:131], a[0:3]
s_waitcnt lgkmcnt(6)
ds_read_b64_tr_b16 a[148:149], v201 offset:33792
ds_read_b64_tr_b16 a[150:151], v201 offset:37888
v_mfma_f32_16x16x32_bf16 a[4:7], a[128:131], v[132:135], a[4:7]
s_xor_b32 s59, s73, 14336
buffer_load_dwordx4 v217, s[24:27], s59 offen sc1 lds
v_mfma_f32_16x16x32_bf16 a[8:11], a[128:131], v[136:139], a[8:11]
v_exp_f32 v99, v99
v_mfma_f32_16x16x32_bf16 a[12:15], a[128:131], v[140:143], a[12:15]
v_exp_f32 v100, v100
v_mfma_f32_16x16x32_bf16 a[16:19], a[132:135], v[128:131], a[16:19]
s_waitcnt lgkmcnt(6)
ds_read_b64_tr_b16 a[128:129], v198 offset:34304
ds_read_b64_tr_b16 a[130:131], v198 offset:38400
v_mfma_f32_16x16x32_bf16 a[20:23], a[132:135], v[132:135], a[20:23]
v_cvt_pk_bf16_f32 v144, v96, v97
v_cvt_pk_bf16_f32 v145, v98, v99
v_mfma_f32_16x16x32_bf16 a[24:27], a[132:135], v[136:139], a[24:27]
v_exp_f32 v101, v101
v_mfma_f32_16x16x32_bf16 a[28:31], a[132:135], v[140:143], a[28:31]
v_exp_f32 v102, v102
v_mfma_f32_16x16x32_bf16 a[32:35], a[140:143], v[128:131], a[32:35]
s_waitcnt lgkmcnt(6)
ds_read_b64_tr_b16 a[132:133], v201 offset:34304
ds_read_b64_tr_b16 a[134:135], v201 offset:38400
v_mfma_f32_16x16x32_bf16 a[36:39], a[140:143], v[132:135], a[36:39]
v_exp_f32 v103, v103
v_mfma_f32_16x16x32_bf16 a[40:43], a[140:143], v[136:139], a[40:43]
v_exp_f32 v104, v104
v_mfma_f32_16x16x32_bf16 a[44:47], a[140:143], v[140:143], a[44:47]
v_cvt_pk_bf16_f32 v146, v100, v101
v_cvt_pk_bf16_f32 v147, v102, v103
v_mfma_f32_16x16x32_bf16 a[48:51], a[136:139], v[128:131], a[48:51]
s_waitcnt lgkmcnt(6)
v_exp_f32 v105, v105
v_mfma_f32_16x16x32_bf16 a[52:55], a[136:139], v[132:135], a[52:55]
v_exp_f32 v106, v106
v_mfma_f32_16x16x32_bf16 a[56:59], a[136:139], v[136:139], a[56:59]
v_exp_f32 v107, v107
v_mfma_f32_16x16x32_bf16 a[60:63], a[136:139], v[140:143], a[60:63]
s_add_u32 m0, s40, 16384
v_exp_f32 v108, v108
v_mfma_f32_16x16x32_bf16 a[64:67], a[144:147], v[128:131], a[64:67]
s_waitcnt lgkmcnt(4)
ds_read_b64_tr_b16 a[136:137], v198 offset:40960
ds_read_b64_tr_b16 a[138:139], v198 offset:45056
v_mfma_f32_16x16x32_bf16 a[68:71], a[144:147], v[132:135], a[68:71]
s_add_u32 s56, s80, 262144
buffer_load_dwordx4 v213, s[28:31], s56 offen lds
v_mfma_f32_16x16x32_bf16 a[72:75], a[144:147], v[136:139], a[72:75]
v_cvt_pk_bf16_f32 v148, v104, v105
v_cvt_pk_bf16_f32 v149, v106, v107
v_mfma_f32_16x16x32_bf16 a[76:79], a[144:147], v[140:143], a[76:79]
buffer_load_dwordx4 v214, s[28:31], s56 offen offset:2048 lds
v_mfma_f32_16x16x32_bf16 a[80:83], a[148:151], v[128:131], a[80:83]
s_waitcnt lgkmcnt(4)
ds_read_b64_tr_b16 a[140:141], v201 offset:40960
ds_read_b64_tr_b16 a[142:143], v201 offset:45056
v_mfma_f32_16x16x32_bf16 a[84:87], a[148:151], v[132:135], a[84:87]
v_exp_f32 v109, v109
v_mfma_f32_16x16x32_bf16 a[88:91], a[148:151], v[136:139], a[88:91]
v_exp_f32 v110, v110
v_mfma_f32_16x16x32_bf16 a[92:95], a[148:151], v[140:143], a[92:95]
v_exp_f32 v111, v111
v_mfma_f32_16x16x32_bf16 a[96:99], a[128:131], v[128:131], a[96:99]
s_waitcnt lgkmcnt(4)
ds_read_b64_tr_b16 a[144:145], v198 offset:41472
ds_read_b64_tr_b16 a[146:147], v198 offset:45568
v_mfma_f32_16x16x32_bf16 a[100:103], a[128:131], v[132:135], a[100:103]
v_exp_f32 v112, v112
v_mfma_f32_16x16x32_bf16 a[104:107], a[128:131], v[136:139], a[104:107]
v_cvt_pk_bf16_f32 v150, v108, v109
v_cvt_pk_bf16_f32 v151, v110, v111
v_mfma_f32_16x16x32_bf16 a[108:111], a[128:131], v[140:143], a[108:111]
v_exp_f32 v113, v113
v_mfma_f32_16x16x32_bf16 a[112:115], a[132:135], v[128:131], a[112:115]
ds_read_b64_tr_b16 a[128:129], v201 offset:41472
ds_read_b64_tr_b16 a[130:131], v201 offset:45568
v_mfma_f32_16x16x32_bf16 a[116:119], a[132:135], v[132:135], a[116:119]
v_exp_f32 v114, v114
v_mfma_f32_16x16x32_bf16 a[120:123], a[132:135], v[136:139], a[120:123]
v_exp_f32 v115, v115
v_mfma_f32_16x16x32_bf16 a[124:127], a[132:135], v[140:143], a[124:127]
v_exp_f32 v116, v116
v_cvt_pk_bf16_f32 v152, v112, v113
v_cvt_pk_bf16_f32 v153, v114, v115
v_exp_f32 v117, v117
v_exp_f32 v118, v118
v_exp_f32 v119, v119
v_exp_f32 v120, v120
v_cvt_pk_bf16_f32 v154, v116, v117
v_cvt_pk_bf16_f32 v155, v118, v119
v_exp_f32 v121, v121
v_exp_f32 v122, v122
v_exp_f32 v123, v123
v_exp_f32 v124, v124
v_cvt_pk_bf16_f32 v156, v120, v121
v_cvt_pk_bf16_f32 v157, v122, v123
v_exp_f32 v125, v125
v_exp_f32 v126, v126
v_exp_f32 v127, v127
v_cvt_pk_bf16_f32 v158, v124, v125
v_cvt_pk_bf16_f32 v159, v126, v127
v_mfma_f32_16x16x32_bf16 v[176:179], v[232:235], v[144:147], v[176:179]
s_waitcnt lgkmcnt(6)
ds_read_b64_tr_b16 a[132:133], v198 offset:41984
ds_read_b64_tr_b16 a[134:135], v198 offset:46080
v_mfma_f32_16x16x32_bf16 v[180:183], v[232:235], v[148:151], v[180:183]
ds_read_b64_tr_b16 a[156:157], v201 offset:42496
ds_read_b64_tr_b16 a[158:159], v201 offset:46592
v_mfma_f32_16x16x32_bf16 v[184:187], v[232:235], v[152:155], v[184:187]
s_add_u32 s57, s80, 262272
ds_read_b64_tr_b16 a[152:153], v198 offset:42496
ds_read_b64_tr_b16 a[154:155], v198 offset:46592
v_mfma_f32_16x16x32_bf16 v[188:191], v[232:235], v[156:159], v[188:191]
s_add_u32 m0, s40, 17408
ds_read_b64_tr_b16 a[148:149], v201 offset:41984
ds_read_b64_tr_b16 a[150:151], v201 offset:46080
v_mfma_f32_16x16x32_bf16 a[0:3], a[136:139], v[144:147], a[0:3]
s_waitcnt lgkmcnt(12)
buffer_load_dwordx4 v213, s[28:31], s57 offen lds
v_mfma_f32_16x16x32_bf16 a[4:7], a[136:139], v[148:151], a[4:7]
v_mfma_f32_16x16x32_bf16 a[8:11], a[136:139], v[152:155], a[8:11]
buffer_load_dwordx4 v214, s[28:31], s57 offen offset:2048 lds
v_mfma_f32_16x16x32_bf16 a[12:15], a[136:139], v[156:159], a[12:15]
v_mfma_f32_16x16x32_bf16 a[16:19], a[140:143], v[144:147], a[16:19]
s_waitcnt lgkmcnt(10)
v_mfma_f32_16x16x32_bf16 a[20:23], a[140:143], v[148:151], a[20:23]
v_rcp_f32 v222, v176
v_mfma_f32_16x16x32_bf16 a[24:27], a[140:143], v[152:155], a[24:27]
v_rcp_f32 v223, v180
v_mfma_f32_16x16x32_bf16 a[28:31], a[140:143], v[156:159], a[28:31]
s_add_u32 m0, s40, 32768
v_rcp_f32 v224, v184
v_mfma_f32_16x16x32_bf16 a[32:35], a[144:147], v[144:147], a[32:35]
s_waitcnt vmcnt(28) lgkmcnt(0)
s_barrier
v_mfma_f32_16x16x32_bf16 a[36:39], a[144:147], v[148:151], a[36:39]
s_xor_b32 s58, s72, 40960
buffer_load_dwordx4 v217, s[24:27], s58 offen sc1 lds
v_mfma_f32_16x16x32_bf16 a[40:43], a[144:147], v[152:155], a[40:43]
v_mfma_f32_16x16x32_bf16 a[44:47], a[144:147], v[156:159], a[44:47]
ds_read_b128 a[140:143], v196 offset:0
ds_read_b128 a[144:147], v196 offset:4096
v_mfma_f32_16x16x32_bf16 a[48:51], a[128:131], v[144:147], a[48:51]
ds_read_b64_tr_b16 a[136:137], v206 offset:16384
ds_read_b64_tr_b16 a[138:139], v206 offset:20480
v_mfma_f32_16x16x32_bf16 a[52:55], a[128:131], v[148:151], a[52:55]
ds_read_b64_tr_b16 a[160:161], v206 offset:24576
ds_read_b64_tr_b16 a[162:163], v206 offset:28672
v_mfma_f32_16x16x32_bf16 a[56:59], a[128:131], v[152:155], a[56:59]
s_add_u32 m0, s40, 33792
ds_read_b128 a[164:167], v196 offset:1024
ds_read_b128 a[168:171], v196 offset:5120
v_mfma_f32_16x16x32_bf16 a[60:63], a[128:131], v[156:159], a[60:63]
s_xor_b32 s59, s72, 45056
buffer_load_dwordx4 v217, s[24:27], s59 offen sc1 lds
v_mfma_f32_16x16x32_bf16 a[64:67], a[132:135], v[144:147], a[64:67]
s_add_u32 m0, s40, 34816
ds_read_b128 a[128:131], v196 offset:2048
ds_read_b128 a[172:175], v196 offset:6144
v_mfma_f32_16x16x32_bf16 a[68:71], a[132:135], v[148:151], a[68:71]
s_xor_b32 s56, s72, 32768
buffer_load_dwordx4 v217, s[24:27], s56 offen sc1 lds
v_mfma_f32_16x16x32_bf16 a[72:75], a[132:135], v[152:155], a[72:75]
s_add_u32 m0, s40, 35840
ds_read_b128 a[176:179], v196 offset:3072
ds_read_b128 a[180:183], v196 offset:7168
v_mfma_f32_16x16x32_bf16 a[76:79], a[132:135], v[156:159], a[76:79]
s_xor_b32 s57, s72, 36864
buffer_load_dwordx4 v217, s[24:27], s57 offen sc1 lds
v_mfma_f32_16x16x32_bf16 a[80:83], a[148:151], v[144:147], a[80:83]
ds_read_b64_tr_b16 a[132:133], v209 offset:16384
ds_read_b64_tr_b16 a[134:135], v209 offset:20480
v_mfma_f32_16x16x32_bf16 a[84:87], a[148:151], v[148:151], a[84:87]
ds_read_b64_tr_b16 a[184:185], v209 offset:24576
ds_read_b64_tr_b16 a[186:187], v209 offset:28672
v_mfma_f32_16x16x32_bf16 a[88:91], a[148:151], v[152:155], a[88:91]
v_rcp_f32 v225, v188
v_mfma_f32_16x16x32_bf16 a[92:95], a[148:151], v[156:159], a[92:95]
v_add_f32 v227, v176, v180
v_min_f32 v225, s85, v225
v_mfma_f32_16x16x32_bf16 a[96:99], a[152:155], v[144:147], a[96:99]
v_add_f32 v228, v184, v188
v_add_f32 v227, v227, v228
v_mfma_f32_16x16x32_bf16 a[100:103], a[152:155], v[148:151], a[100:103]
v_cmp_ngt_f32 vcc, s48, v227
v_mfma_f32_16x16x32_bf16 a[104:107], a[152:155], v[152:155], a[104:107]
v_mfma_f32_16x16x32_bf16 a[108:111], a[152:155], v[156:159], a[108:111]
v_mfma_f32_16x16x32_bf16 a[112:115], a[156:159], v[144:147], a[112:115]
s_waitcnt lgkmcnt(12)
ds_read_b64_tr_b16 a[148:149], v206 offset:16896
ds_read_b64_tr_b16 a[150:151], v206 offset:20992
v_mfma_f32_16x16x32_bf16 a[116:119], a[156:159], v[148:151], a[116:119]
ds_read_b64_tr_b16 a[152:153], v206 offset:25088
ds_read_b64_tr_b16 a[154:155], v206 offset:29184
v_mfma_f32_16x16x32_bf16 a[120:123], a[156:159], v[152:155], a[120:123]
v_mfma_f32_16x16x32_bf16 a[124:127], a[156:159], v[156:159], a[124:127]
s_cbranch_vccnz L_slow1_%=
L_ret1_%=:
v_mfma_f32_16x16x32_bf16 v[64:67], a[136:139], a[140:143], 0
s_waitcnt lgkmcnt(11)
ds_read_b64_tr_b16 a[188:189], v209 offset:25088
ds_read_b64_tr_b16 a[190:191], v209 offset:29184
v_mfma_f32_16x16x32_bf16 v[64:67], a[160:163], a[144:147], v[64:67]
ds_read_b64_tr_b16 a[156:157], v209 offset:16896
ds_read_b64_tr_b16 a[158:159], v209 offset:20992
v_mfma_f32_16x16x32_bf16 v[68:71], a[136:139], a[164:167], 0
v_accvgpr_read_b32 v236, a0
v_accvgpr_read_b32 v237, a1
v_mfma_f32_16x16x32_bf16 v[68:71], a[160:163], a[168:171], v[68:71]
v_accvgpr_read_b32 v238, a2
v_accvgpr_read_b32 v239, a3
v_mfma_f32_16x16x32_bf16 v[72:75], a[136:139], a[128:131], 0
s_waitcnt lgkmcnt(10)
v_accvgpr_read_b32 v240, a16
v_accvgpr_read_b32 v241, a17
v_accvgpr_read_b32 v242, a18
v_accvgpr_read_b32 v243, a19
v_mul_f32 v236, v222, v236
v_mul_f32 v237, v222, v237
v_mul_f32 v238, v222, v238
v_mul_f32 v239, v222, v239
v_mul_f32 v240, v222, v240
v_mul_f32 v241, v222, v241
v_mul_f32 v242, v222, v242
v_mul_f32 v243, v222, v243
v_cvt_pk_bf16_f32 v0, v236, v237
v_cvt_pk_bf16_f32 v1, v238, v239
v_cvt_pk_bf16_f32 v2, v240, v241
v_cvt_pk_bf16_f32 v3, v242, v243
v_mfma_f32_16x16x32_bf16 v[72:75], a[160:163], a[172:175], v[72:75]
s_xor_b32 s60, s46, 0
buffer_store_dwordx4 v[0:3], v217, s[24:27], s60 offen sc1
v_accvgpr_read_b32 v244, a4
v_accvgpr_read_b32 v245, a5
v_mfma_f32_16x16x32_bf16 v[76:79], a[136:139], a[176:179], 0
s_xor_b32 s60, s46, 4096
v_accvgpr_read_b32 v246, a6
v_accvgpr_read_b32 v247, a7
v_mfma_f32_16x16x32_bf16 v[76:79], a[160:163], a[180:183], v[76:79]
s_add_u32 m0, s40, 65536
v_accvgpr_read_b32 v248, a20
v_accvgpr_read_b32 v249, a21
v_accvgpr_read_b32 v250, a22
v_accvgpr_read_b32 v251, a23
v_mul_f32 v244, v223, v244
v_mul_f32 v245, v223, v245
v_mul_f32 v246, v223, v246
v_mul_f32 v247, v223, v247
v_mul_f32 v248, v223, v248
v_mul_f32 v249, v223, v249
v_mul_f32 v250, v223, v250
v_mul_f32 v251, v223, v251
v_cvt_pk_bf16_f32 v16, v244, v245
v_cvt_pk_bf16_f32 v17, v246, v247
v_cvt_pk_bf16_f32 v18, v248, v249
v_cvt_pk_bf16_f32 v19, v250, v251
v_mfma_f32_16x16x32_bf16 v[80:83], a[132:135], a[140:143], 0
s_waitcnt vmcnt(25) lgkmcnt(0)
s_barrier
v_mfma_f32_16x16x32_bf16 v[80:83], a[184:187], a[144:147], v[80:83]
s_add_u32 s58, s80, 327680
buffer_load_dwordx4 v213, s[28:31], s58 offen lds
buffer_store_dwordx4 v[16:19], v217, s[24:27], s60 offen sc1
v_mfma_f32_16x16x32_bf16 v[84:87], a[132:135], a[164:167], 0
v_accvgpr_read_b32 v236, a8
v_accvgpr_read_b32 v237, a9
v_accvgpr_read_b32 v238, a10
v_accvgpr_read_b32 v239, a11
v_accvgpr_read_b32 v240, a24
v_accvgpr_read_b32 v241, a25
v_accvgpr_read_b32 v242, a26
v_accvgpr_read_b32 v243, a27
v_mul_f32 v236, v224, v236
v_mul_f32 v237, v224, v237
v_mul_f32 v238, v224, v238
v_mul_f32 v239, v224, v239
v_mul_f32 v240, v224, v240
v_mul_f32 v241, v224, v241
v_mul_f32 v242, v224, v242
v_mul_f32 v243, v224, v243
L_tch15_%=:
s_cbranch_execz L_tb1_%=
v_cvt_pk_bf16_f32 v32, v236, v237
v_cvt_pk_bf16_f32 v33, v238, v239
v_cvt_pk_bf16_f32 v34, v240, v241
v_cvt_pk_bf16_f32 v35, v242, v243
v_mfma_f32_16x16x32_bf16 v[84:87], a[184:187], a[168:171], v[84:87]
s_xor_b32 s60, s46, 8192
ds_read_b128 a[160:163], v195 offset:49152
ds_read_b128 a[192:195], v195 offset:53248
buffer_store_dwordx4 v[32:35], v217, s[24:27], s60 offen sc1
v_mfma_f32_16x16x32_bf16 v[88:91], a[132:135], a[128:131], 0
ds_read_b64_tr_b16 a[136:137], v205 offset:16384
ds_read_b64_tr_b16 a[138:139], v205 offset:20480
v_mfma_f32_16x16x32_bf16 v[88:91], a[184:187], a[172:175], v[88:91]
ds_read_b64_tr_b16 a[196:197], v205 offset:24576
ds_read_b64_tr_b16 a[198:199], v205 offset:28672
v_accvgpr_read_b32 v244, a12
v_accvgpr_read_b32 v245, a13
v_accvgpr_read_b32 v246, a14
v_accvgpr_read_b32 v247, a15
v_accvgpr_read_b32 v248, a28
v_accvgpr_read_b32 v249, a29
v_accvgpr_read_b32 v250, a30
v_accvgpr_read_b32 v251, a31
v_mul_f32 v244, v225, v244
v_mul_f32 v245, v225, v245
v_mul_f32 v246, v225, v246
v_mul_f32 v247, v225, v247
v_mul_f32 v248, v225, v248
v_mul_f32 v249, v225, v249
v_mul_f32 v250, v225, v250
v_mul_f32 v251, v225, v251
v_cvt_pk_bf16_f32 v48, v244, v245
v_cvt_pk_bf16_f32 v49, v246, v247
v_cvt_pk_bf16_f32 v50, v248, v249
v_cvt_pk_bf16_f32 v51, v250, v251
v_mfma_f32_16x16x32_bf16 v[92:95], a[132:135], a[176:179], 0
s_xor_b32 s60, s46, 12288
ds_read_b128 a[132:135], v195 offset:50176
ds_read_b128 a[200:203], v195 offset:54272
buffer_store_dwordx4 v[48:51], v217, s[24:27], s60 offen sc1
v_mfma_f32_16x16x32_bf16 v[92:95], a[184:187], a[180:183], v[92:95]
s_xor_b32 s60, s46, 1024
buffer_load_dwordx4 v214, s[28:31], s58 offen offset:2048 lds
v_mfma_f32_16x16x32_bf16 v[96:99], a[148:151], a[140:143], 0
s_add_u32 m0, s40, 66560
ds_read_b128 a[184:187], v195 offset:51200
ds_read_b128 a[204:207], v195 offset:55296
v_accvgpr_read_b32 v236, a32
v_accvgpr_read_b32 v237, a33
v_accvgpr_read_b32 v238, a34
v_accvgpr_read_b32 v239, a35
v_accvgpr_read_b32 v240, a48
v_accvgpr_read_b32 v241, a49
v_accvgpr_read_b32 v242, a50
v_accvgpr_read_b32 v243, a51
v_mul_f32 v236, v222, v236
v_mul_f32 v237, v222, v237
v_mul_f32 v238, v222, v238
v_mul_f32 v239, v222, v239
v_mul_f32 v240, v222, v240
v_mul_f32 v241, v222, v241
v_mul_f32 v242, v222, v242
v_mul_f32 v243, v222, v243
v_cvt_pk_bf16_f32 v4, v236, v237
v_cvt_pk_bf16_f32 v5, v238, v239
v_cvt_pk_bf16_f32 v6, v240, v241
v_cvt_pk_bf16_f32 v7, v242, v243
v_mfma_f32_16x16x32_bf16 v[96:99], a[152:155], a[144:147], v[96:99]
s_add_u32 s59, s80, 327808
buffer_load_dwordx4 v213, s[28:31], s59 offen lds
buffer_store_dwordx4 v[4:7], v217, s[24:27], s60 offen sc1
v_mfma_f32_16x16x32_bf16 v[100:103], a[148:151], a[164:167], 0
s_xor_b32 s60, s46, 5120
ds_read_b128 a[208:211], v195 offset:52224
ds_read_b128 a[212:215], v195 offset:56320
v_mfma_f32_16x16x32_bf16 v[100:103], a[152:155], a[168:171], v[100:103]
s_xor_b32 s57, s72, 47104
buffer_load_dwordx4 v214, s[28:31], s59 offen offset:2048 lds
v_accvgpr_read_b32 v244, a36
v_accvgpr_read_b32 v245, a37
v_accvgpr_read_b32 v246, a38
v_accvgpr_read_b32 v247, a39
v_accvgpr_read_b32 v248, a52
v_accvgpr_read_b32 v249, a53
v_accvgpr_read_b32 v250, a54
v_accvgpr_read_b32 v251, a55
v_mul_f32 v244, v223, v244
v_mul_f32 v245, v223, v245
v_mul_f32 v246, v223, v246
v_mul_f32 v247, v223, v247
v_mul_f32 v248, v223, v248
v_mul_f32 v249, v223, v249
v_mul_f32 v250, v223, v250
v_mul_f32 v251, v223, v251
v_cvt_pk_bf16_f32 v20, v244, v245
v_cvt_pk_bf16_f32 v21, v246, v247
v_cvt_pk_bf16_f32 v22, v248, v249
v_cvt_pk_bf16_f32 v23, v250, v251
v_mfma_f32_16x16x32_bf16 v[104:107], a[148:151], a[128:131], 0
ds_read_b64_tr_b16 a[216:217], v208 offset:16384
ds_read_b64_tr_b16 a[218:219], v208 offset:20480
buffer_store_dwordx4 v[20:23], v217, s[24:27], s60 offen sc1
v_mfma_f32_16x16x32_bf16 v[104:107], a[152:155], a[172:175], v[104:107]
s_add_u32 m0, s40, 147456
ds_read_b64_tr_b16 a[220:221], v208 offset:24576
ds_read_b64_tr_b16 a[222:223], v208 offset:28672
v_mfma_f32_16x16x32_bf16 v[108:111], a[148:151], a[176:179], 0
s_xor_b32 s56, s72, 43008
buffer_load_dwordx4 v217, s[24:27], s56 offen sc1 lds
v_accvgpr_read_b32 v236, a40
v_accvgpr_read_b32 v237, a41
v_accvgpr_read_b32 v238, a42
v_accvgpr_read_b32 v239, a43
v_accvgpr_read_b32 v240, a56
v_accvgpr_read_b32 v241, a57
v_accvgpr_read_b32 v242, a58
v_accvgpr_read_b32 v243, a59
v_mul_f32 v236, v224, v236
v_mul_f32 v237, v224, v237
v_mul_f32 v238, v224, v238
v_mul_f32 v239, v224, v239
v_mul_f32 v240, v224, v240
v_mul_f32 v241, v224, v241
v_mul_f32 v242, v224, v242
v_mul_f32 v243, v224, v243
v_cvt_pk_bf16_f32 v36, v236, v237
v_cvt_pk_bf16_f32 v37, v238, v239
v_cvt_pk_bf16_f32 v38, v240, v241
v_cvt_pk_bf16_f32 v39, v242, v243
v_mfma_f32_16x16x32_bf16 v[108:111], a[152:155], a[180:183], v[108:111]
s_add_u32 m0, s40, 148480
s_xor_b32 s60, s46, 9216
buffer_store_dwordx4 v[36:39], v217, s[24:27], s60 offen sc1
v_accvgpr_read_b32 v244, a44
v_accvgpr_read_b32 v245, a45
v_mfma_f32_16x16x32_bf16 v[112:115], a[156:159], a[140:143], 0
buffer_load_dwordx4 v217, s[24:27], s57 offen sc1 lds
v_mfma_f32_16x16x32_bf16 v[112:115], a[188:191], a[144:147], v[112:115]
s_add_u32 m0, s40, 149504
v_accvgpr_read_b32 v246, a46
v_accvgpr_read_b32 v247, a47
v_accvgpr_read_b32 v248, a60
v_accvgpr_read_b32 v249, a61
v_accvgpr_read_b32 v250, a62
v_accvgpr_read_b32 v251, a63
v_mul_f32 v244, v225, v244
v_mul_f32 v245, v225, v245
v_mul_f32 v246, v225, v246
v_mul_f32 v247, v225, v247
v_mul_f32 v248, v225, v248
v_mul_f32 v249, v225, v249
v_mul_f32 v250, v225, v250
v_mul_f32 v251, v225, v251
v_cvt_pk_bf16_f32 v52, v244, v245
v_cvt_pk_bf16_f32 v53, v246, v247
v_cvt_pk_bf16_f32 v54, v248, v249
v_cvt_pk_bf16_f32 v55, v250, v251
v_mfma_f32_16x16x32_bf16 v[116:119], a[156:159], a[164:167], 0
s_xor_b32 s58, s72, 34816
s_xor_b32 s60, s46, 13312
buffer_load_dwordx4 v217, s[24:27], s58 offen sc1 lds
buffer_store_dwordx4 v[52:55], v217, s[24:27], s60 offen sc1
v_mfma_f32_16x16x32_bf16 v[116:119], a[188:191], a[168:171], v[116:119]
s_xor_b32 s60, s46, 2048
v_accvgpr_read_b32 v236, a64
v_accvgpr_read_b32 v237, a65
v_mfma_f32_16x16x32_bf16 v[120:123], a[156:159], a[128:131], 0
s_waitcnt lgkmcnt(12)
ds_read_b64_tr_b16 a[128:129], v205 offset:16896
ds_read_b64_tr_b16 a[130:131], v205 offset:20992
v_accvgpr_read_b32 v238, a66
v_accvgpr_read_b32 v239, a67
v_accvgpr_read_b32 v240, a80
v_accvgpr_read_b32 v241, a81
v_accvgpr_read_b32 v242, a82
v_accvgpr_read_b32 v243, a83
v_mul_f32 v236, v222, v236
v_mul_f32 v237, v222, v237
v_mul_f32 v238, v222, v238
v_mul_f32 v239, v222, v239
v_mul_f32 v240, v222, v240
v_mul_f32 v241, v222, v241
v_mul_f32 v242, v222, v242
v_mul_f32 v243, v222, v243
v_cvt_pk_bf16_f32 v8, v236, v237
v_cvt_pk_bf16_f32 v9, v238, v239
v_cvt_pk_bf16_f32 v10, v240, v241
v_cvt_pk_bf16_f32 v11, v242, v243
v_mfma_f32_16x16x32_bf16 v[120:123], a[188:191], a[172:175], v[120:123]
s_add_u32 m0, s40, 150528
ds_read_b64_tr_b16 a[140:141], v205 offset:25088
ds_read_b64_tr_b16 a[142:143], v205 offset:29184
buffer_store_dwordx4 v[8:11], v217, s[24:27], s60 offen sc1
v_mfma_f32_16x16x32_bf16 v[124:127], a[156:159], a[176:179], 0
s_xor_b32 s59, s72, 38912
buffer_load_dwordx4 v217, s[24:27], s59 offen sc1 lds
v_mfma_f32_16x16x32_bf16 v[124:127], a[188:191], a[180:183], v[124:127]
s_xor_b32 s60, s46, 6144
v_accvgpr_read_b32 v244, a68
v_accvgpr_read_b32 v245, a69
v_accvgpr_read_b32 v246, a70
v_accvgpr_read_b32 v247, a71
v_accvgpr_read_b32 v248, a84
v_accvgpr_read_b32 v249, a85
v_accvgpr_read_b32 v250, a86
v_accvgpr_read_b32 v251, a87
v_mul_f32 v244, v223, v244
v_mul_f32 v245, v223, v245
v_mul_f32 v246, v223, v246
v_mul_f32 v247, v223, v247
v_mul_f32 v248, v223, v248
v_mul_f32 v249, v223, v249
v_mul_f32 v250, v223, v250
v_mul_f32 v251, v223, v251
v_cvt_pk_bf16_f32 v24, v244, v245
v_cvt_pk_bf16_f32 v25, v246, v247
v_cvt_pk_bf16_f32 v26, v248, v249
v_cvt_pk_bf16_f32 v27, v250, v251
v_mfma_f32_16x16x32_bf16 v[64:67], a[136:139], a[160:163], v[64:67]
s_waitcnt lgkmcnt(11)
ds_read_b64_tr_b16 a[148:149], v208 offset:25088
ds_read_b64_tr_b16 a[150:151], v208 offset:29184
buffer_store_dwordx4 v[24:27], v217, s[24:27], s60 offen sc1
v_mfma_f32_16x16x32_bf16 v[64:67], a[196:199], a[192:195], v[64:67]
ds_read_b64_tr_b16 a[144:145], v208 offset:16896
ds_read_b64_tr_b16 a[146:147], v208 offset:20992
v_mfma_f32_16x16x32_bf16 v[68:71], a[136:139], a[132:135], v[68:71]
v_accvgpr_read_b32 v236, a72
v_accvgpr_read_b32 v237, a73
v_accvgpr_read_b32 v238, a74
v_accvgpr_read_b32 v239, a75
v_accvgpr_read_b32 v240, a88
v_accvgpr_read_b32 v241, a89
v_accvgpr_read_b32 v242, a90
v_accvgpr_read_b32 v243, a91
v_mul_f32 v236, v224, v236
v_mul_f32 v237, v224, v237
v_mul_f32 v238, v224, v238
v_mul_f32 v239, v224, v239
v_mul_f32 v240, v224, v240
v_mul_f32 v241, v224, v241
v_mul_f32 v242, v224, v242
v_mul_f32 v243, v224, v243
v_cvt_pk_bf16_f32 v40, v236, v237
v_cvt_pk_bf16_f32 v41, v238, v239
v_cvt_pk_bf16_f32 v42, v240, v241
v_cvt_pk_bf16_f32 v43, v242, v243
v_mfma_f32_16x16x32_bf16 v[68:71], a[196:199], a[200:203], v[68:71]
s_xor_b32 s60, s46, 10240
buffer_store_dwordx4 v[40:43], v217, s[24:27], s60 offen sc1
v_accvgpr_read_b32 v244, a76
v_accvgpr_read_b32 v245, a77
v_mfma_f32_16x16x32_bf16 v[72:75], a[136:139], a[184:187], v[72:75]
s_waitcnt lgkmcnt(10)
v_accvgpr_read_b32 v246, a78
v_accvgpr_read_b32 v247, a79
v_mfma_f32_16x16x32_bf16 v[72:75], a[196:199], a[204:207], v[72:75]
v_accvgpr_read_b32 v248, a92
v_accvgpr_read_b32 v249, a93
v_accvgpr_read_b32 v250, a94
v_accvgpr_read_b32 v251, a95
v_mul_f32 v244, v225, v244
v_mul_f32 v245, v225, v245
v_mul_f32 v246, v225, v246
v_mul_f32 v247, v225, v247
v_mul_f32 v248, v225, v248
v_mul_f32 v249, v225, v249
v_mul_f32 v250, v225, v250
v_mul_f32 v251, v225, v251
v_cvt_pk_bf16_f32 v56, v244, v245
v_cvt_pk_bf16_f32 v57, v246, v247
v_cvt_pk_bf16_f32 v58, v248, v249
v_cvt_pk_bf16_f32 v59, v250, v251
v_mfma_f32_16x16x32_bf16 v[76:79], a[136:139], a[208:211], v[76:79]
s_xor_b32 s60, s46, 14336
buffer_store_dwordx4 v[56:59], v217, s[24:27], s60 offen sc1
v_accvgpr_read_b32 v236, a96
v_accvgpr_read_b32 v237, a97
v_mfma_f32_16x16x32_bf16 v[76:79], a[196:199], a[212:215], v[76:79]
s_add_u32 m0, s40, 49152
v_accvgpr_read_b32 v238, a98
v_accvgpr_read_b32 v239, a99
v_mfma_f32_16x16x32_bf16 v[80:83], a[216:219], a[160:163], v[80:83]
s_mov_b64 exec, 0
s_cmp_eq_u32 %2, 0
s_cbranch_scc1 L_tch17_%=
s_cmp_eq_u32 %2, 1
s_cbranch_scc1 L_tch18_%=
s_cmp_eq_u32 %2, 2
s_cbranch_scc1 L_tch19_%=
s_cmp_eq_u32 %2, 3
s_cbranch_scc1 L_tch20_%=
L_tb2_%=:
s_mov_b64 exec, -1
s_nop 1
s_waitcnt vmcnt(36) lgkmcnt(0)
s_barrier
v_accvgpr_read_b32 v240, a112
v_accvgpr_read_b32 v241, a113
v_accvgpr_read_b32 v242, a114
v_accvgpr_read_b32 v243, a115
v_mul_f32 v236, v222, v236
v_mul_f32 v237, v222, v237
v_mul_f32 v238, v222, v238
v_mul_f32 v239, v222, v239
v_mul_f32 v240, v222, v240
v_mul_f32 v241, v222, v241
v_mul_f32 v242, v222, v242
v_mul_f32 v243, v222, v243
v_cvt_pk_bf16_f32 v12, v236, v237
v_cvt_pk_bf16_f32 v13, v238, v239
v_cvt_pk_bf16_f32 v14, v240, v241
v_cvt_pk_bf16_f32 v15, v242, v243
v_mfma_f32_16x16x32_bf16 v[80:83], a[220:223], a[192:195], v[80:83]
s_add_u32 s56, s81, 262144
s_xor_b32 s60, s46, 3072
buffer_load_dwordx4 v213, s[28:31], s56 offen lds
buffer_store_dwordx4 v[12:15], v217, s[24:27], s60 offen sc1
v_mfma_f32_16x16x32_bf16 v[84:87], a[216:219], a[132:135], v[84:87]
v_accvgpr_read_b32 v244, a100
v_accvgpr_read_b32 v245, a101
v_mfma_f32_16x16x32_bf16 v[84:87], a[220:223], a[200:203], v[84:87]
s_xor_b32 s60, s46, 7168
ds_read_b128 a[152:155], v196 offset:32768
ds_read_b128 a[156:159], v196 offset:36864
v_accvgpr_read_b32 v246, a102
v_accvgpr_read_b32 v247, a103
v_accvgpr_read_b32 v248, a116
v_accvgpr_read_b32 v249, a117
v_accvgpr_read_b32 v250, a118
v_accvgpr_read_b32 v251, a119
v_mul_f32 v244, v223, v244
v_mul_f32 v245, v223, v245
v_mul_f32 v246, v223, v246
v_mul_f32 v247, v223, v247
v_mul_f32 v248, v223, v248
v_mul_f32 v249, v223, v249
v_mul_f32 v250, v223, v250
v_mul_f32 v251, v223, v251
v_cvt_pk_bf16_f32 v28, v244, v245
v_cvt_pk_bf16_f32 v29, v246, v247
v_cvt_pk_bf16_f32 v30, v248, v249
v_cvt_pk_bf16_f32 v31, v250, v251
v_mfma_f32_16x16x32_bf16 v[88:91], a[216:219], a[184:187], v[88:91]
ds_read_b64_tr_b16 a[136:137], v205 offset:49152
ds_read_b64_tr_b16 a[138:139], v205 offset:53248
buffer_store_dwordx4 v[28:31], v217, s[24:27], s60 offen sc1
v_mfma_f32_16x16x32_bf16 v[88:91], a[220:223], a[204:207], v[88:91]
ds_read_b64_tr_b16 a[164:165], v205 offset:57344
ds_read_b64_tr_b16 a[166:167], v205 offset:61440
v_mfma_f32_16x16x32_bf16 v[92:95], a[216:219], a[208:211], v[92:95]
ds_read_b128 a[168:171], v196 offset:33792
ds_read_b128 a[172:175], v196 offset:37888
v_accvgpr_read_b32 v236, a104
v_accvgpr_read_b32 v237, a105
v_accvgpr_read_b32 v238, a106
v_accvgpr_read_b32 v239, a107
v_accvgpr_read_b32 v240, a120
v_accvgpr_read_b32 v241, a121
v_accvgpr_read_b32 v242, a122
v_accvgpr_read_b32 v243, a123
v_mul_f32 v236, v224, v236
v_mul_f32 v237, v224, v237
v_mul_f32 v238, v224, v238
v_mul_f32 v239, v224, v239
v_mul_f32 v240, v224, v240
v_mul_f32 v241, v224, v241
v_mul_f32 v242, v224, v242
v_mul_f32 v243, v224, v243
v_cvt_pk_bf16_f32 v44, v236, v237
v_cvt_pk_bf16_f32 v45, v238, v239
v_cvt_pk_bf16_f32 v46, v240, v241
v_cvt_pk_bf16_f32 v47, v242, v243
v_mfma_f32_16x16x32_bf16 v[92:95], a[220:223], a[212:215], v[92:95]
s_xor_b32 s60, s46, 11264
buffer_load_dwordx4 v214, s[28:31], s56 offen offset:2048 lds
buffer_store_dwordx4 v[44:47], v217, s[24:27], s60 offen sc1
v_mfma_f32_16x16x32_bf16 v[96:99], a[128:131], a[160:163], v[96:99]
s_add_u32 m0, s40, 50176
ds_read_b128 a[176:179], v196 offset:34816
ds_read_b128 a[180:183], v196 offset:38912
v_mfma_f32_16x16x32_bf16 v[96:99], a[140:143], a[192:195], v[96:99]
s_add_u32 s57, s81, 262272
buffer_load_dwordx4 v213, s[28:31], s57 offen lds
v_accvgpr_read_b32 v244, a108
v_accvgpr_read_b32 v245, a109
v_accvgpr_read_b32 v246, a110
v_accvgpr_read_b32 v247, a111
v_accvgpr_read_b32 v248, a124
v_accvgpr_read_b32 v249, a125
v_accvgpr_read_b32 v250, a126
v_accvgpr_read_b32 v251, a127
v_mul_f32 v244, v225, v244
v_mul_f32 v245, v225, v245
v_mul_f32 v246, v225, v246
v_mul_f32 v247, v225, v247
v_mul_f32 v248, v225, v248
v_mul_f32 v249, v225, v249
v_mul_f32 v250, v225, v250
v_mul_f32 v251, v225, v251
v_cvt_pk_bf16_f32 v60, v244, v245
v_cvt_pk_bf16_f32 v61, v246, v247
v_cvt_pk_bf16_f32 v62, v248, v249
v_cvt_pk_bf16_f32 v63, v250, v251
v_mfma_f32_16x16x32_bf16 v[100:103], a[128:131], a[132:135], v[100:103]
s_xor_b32 s60, s46, 15360
ds_read_b128 a[188:191], v196 offset:35840
ds_read_b128 a[196:199], v196 offset:39936
buffer_store_dwordx4 v[60:63], v217, s[24:27], s60 offen sc1
v_mfma_f32_16x16x32_bf16 v[100:103], a[140:143], a[200:203], v[100:103]
s_xor_b32 s59, s73, 45056
buffer_load_dwordx4 v214, s[28:31], s57 offen offset:2048 lds
v_mfma_f32_16x16x32_bf16 v[104:107], a[128:131], a[184:187], v[104:107]
ds_read_b64_tr_b16 a[216:217], v208 offset:49152
ds_read_b64_tr_b16 a[218:219], v208 offset:53248
v_mfma_f32_16x16x32_bf16 v[104:107], a[140:143], a[204:207], v[104:107]
s_add_u32 m0, s40, 81920
ds_read_b64_tr_b16 a[220:221], v208 offset:57344
ds_read_b64_tr_b16 a[222:223], v208 offset:61440
v_mfma_f32_16x16x32_bf16 v[108:111], a[128:131], a[208:211], v[108:111]
s_xor_b32 s58, s73, 40960
buffer_load_dwordx4 v217, s[24:27], s58 offen sc1 lds
v_mfma_f32_16x16x32_bf16 v[108:111], a[140:143], a[212:215], v[108:111]
s_add_u32 m0, s40, 82944
v_mfma_f32_16x16x32_bf16 v[112:115], a[144:147], a[160:163], v[112:115]
buffer_load_dwordx4 v217, s[24:27], s59 offen sc1 lds
v_mfma_f32_16x16x32_bf16 v[112:115], a[148:151], a[192:195], v[112:115]
s_add_u32 m0, s40, 83968
v_mfma_f32_16x16x32_bf16 v[116:119], a[144:147], a[132:135], v[116:119]
s_xor_b32 s56, s73, 32768
buffer_load_dwordx4 v217, s[24:27], s56 offen sc1 lds
v_mfma_f32_16x16x32_bf16 v[116:119], a[148:151], a[200:203], v[116:119]
v_mfma_f32_16x16x32_bf16 v[120:123], a[144:147], a[184:187], v[120:123]
s_waitcnt lgkmcnt(12)
ds_read_b64_tr_b16 a[128:129], v205 offset:49664
ds_read_b64_tr_b16 a[130:131], v205 offset:53760
v_mfma_f32_16x16x32_bf16 v[120:123], a[148:151], a[204:207], v[120:123]
s_add_u32 m0, s40, 84992
ds_read_b64_tr_b16 a[132:133], v205 offset:57856
ds_read_b64_tr_b16 a[134:135], v205 offset:61952
v_mfma_f32_16x16x32_bf16 v[124:127], a[144:147], a[208:211], v[124:127]
s_xor_b32 s57, s73, 36864
buffer_load_dwordx4 v217, s[24:27], s57 offen sc1 lds
v_mfma_f32_16x16x32_bf16 v[124:127], a[148:151], a[212:215], v[124:127]
v_mfma_f32_16x16x32_bf16 v[64:67], a[136:139], a[152:155], v[64:67]
s_waitcnt lgkmcnt(11)
ds_read_b64_tr_b16 a[144:145], v208 offset:57856
ds_read_b64_tr_b16 a[146:147], v208 offset:61952
v_mfma_f32_16x16x32_bf16 v[64:67], a[164:167], a[156:159], v[64:67]
ds_read_b64_tr_b16 a[140:141], v208 offset:49664
ds_read_b64_tr_b16 a[142:143], v208 offset:53760
v_mfma_f32_16x16x32_bf16 v[68:71], a[136:139], a[168:171], v[68:71]
v_mfma_f32_16x16x32_bf16 v[68:71], a[164:167], a[172:175], v[68:71]
v_mfma_f32_16x16x32_bf16 v[72:75], a[136:139], a[176:179], v[72:75]
s_waitcnt lgkmcnt(10)
v_mfma_f32_16x16x32_bf16 v[72:75], a[164:167], a[180:183], v[72:75]
v_mfma_f32_16x16x32_bf16 v[76:79], a[136:139], a[188:191], v[76:79]
v_mfma_f32_16x16x32_bf16 v[76:79], a[164:167], a[196:199], v[76:79]
s_add_u32 m0, s40, 98304
v_mfma_f32_16x16x32_bf16 v[80:83], a[216:219], a[152:155], v[80:83]
s_waitcnt vmcnt(40) lgkmcnt(0)
s_barrier
v_mfma_f32_16x16x32_bf16 v[80:83], a[220:223], a[156:159], v[80:83]
s_add_u32 s58, s81, 327680
buffer_load_dwordx4 v213, s[28:31], s58 offen lds
v_mfma_f32_16x16x32_bf16 v[84:87], a[216:219], a[168:171], v[84:87]
v_mfma_f32_16x16x32_bf16 v[84:87], a[220:223], a[172:175], v[84:87]
ds_read_b128 a[148:151], v195 offset:0
ds_read_b128 a[160:163], v195 offset:4096
v_mfma_f32_16x16x32_bf16 v[88:91], a[216:219], a[176:179], v[88:91]
ds_read_b64_tr_b16 a[136:137], v206 offset:0
ds_read_b64_tr_b16 a[138:139], v206 offset:4096
v_mfma_f32_16x16x32_bf16 v[88:91], a[220:223], a[180:183], v[88:91]
ds_read_b64_tr_b16 a[164:165], v206 offset:8192
ds_read_b64_tr_b16 a[166:167], v206 offset:12288
v_mfma_f32_16x16x32_bf16 v[92:95], a[216:219], a[188:191], v[92:95]
ds_read_b128 a[184:187], v195 offset:1024
ds_read_b128 a[192:195], v195 offset:5120
v_mfma_f32_16x16x32_bf16 v[92:95], a[220:223], a[196:199], v[92:95]
buffer_load_dwordx4 v214, s[28:31], s58 offen offset:2048 lds
v_mfma_f32_16x16x32_bf16 v[96:99], a[128:131], a[152:155], v[96:99]
s_add_u32 m0, s40, 99328
ds_read_b128 a[200:203], v195 offset:2048
ds_read_b128 a[204:207], v195 offset:6144
v_mfma_f32_16x16x32_bf16 v[96:99], a[132:135], a[156:159], v[96:99]
s_add_u32 s59, s81, 327808
buffer_load_dwordx4 v213, s[28:31], s59 offen lds
v_mfma_f32_16x16x32_bf16 v[100:103], a[128:131], a[168:171], v[100:103]
ds_read_b128 a[208:211], v195 offset:3072
ds_read_b128 a[212:215], v195 offset:7168
v_mfma_f32_16x16x32_bf16 v[100:103], a[132:135], a[172:175], v[100:103]
s_xor_b32 s57, s73, 47104
buffer_load_dwordx4 v214, s[28:31], s59 offen offset:2048 lds
v_mfma_f32_16x16x32_bf16 v[104:107], a[128:131], a[176:179], v[104:107]
ds_read_b64_tr_b16 a[216:217], v209 offset:0
ds_read_b64_tr_b16 a[218:219], v209 offset:4096
v_mfma_f32_16x16x32_bf16 v[104:107], a[132:135], a[180:183], v[104:107]
s_add_u32 m0, s40, 114688
ds_read_b64_tr_b16 a[220:221], v209 offset:8192
ds_read_b64_tr_b16 a[222:223], v209 offset:12288
v_mfma_f32_16x16x32_bf16 v[108:111], a[128:131], a[188:191], v[108:111]
s_xor_b32 s56, s73, 43008
buffer_load_dwordx4 v217, s[24:27], s56 offen sc1 lds
v_mfma_f32_16x16x32_bf16 v[108:111], a[132:135], a[196:199], v[108:111]
s_add_u32 m0, s40, 115712
v_mfma_f32_16x16x32_bf16 v[112:115], a[140:143], a[152:155], v[112:115]
buffer_load_dwordx4 v217, s[24:27], s57 offen sc1 lds
v_mfma_f32_16x16x32_bf16 v[112:115], a[144:147], a[156:159], v[112:115]
s_add_u32 m0, s40, 116736
v_mfma_f32_16x16x32_bf16 v[116:119], a[140:143], a[168:171], v[116:119]
s_xor_b32 s58, s73, 34816
buffer_load_dwordx4 v217, s[24:27], s58 offen sc1 lds
v_mfma_f32_16x16x32_bf16 v[116:119], a[144:147], a[172:175], v[116:119]
v_mfma_f32_16x16x32_bf16 v[120:123], a[140:143], a[176:179], v[120:123]
s_waitcnt lgkmcnt(12)
ds_read_b64_tr_b16 a[128:129], v206 offset:512
ds_read_b64_tr_b16 a[130:131], v206 offset:4608
v_mfma_f32_16x16x32_bf16 v[120:123], a[144:147], a[180:183], v[120:123]
s_add_u32 m0, s40, 117760
ds_read_b64_tr_b16 a[132:133], v206 offset:8704
ds_read_b64_tr_b16 a[134:135], v206 offset:12800
v_mfma_f32_16x16x32_bf16 v[124:127], a[140:143], a[188:191], v[124:127]
s_xor_b32 s59, s73, 38912
buffer_load_dwordx4 v217, s[24:27], s59 offen sc1 lds
v_mfma_f32_16x16x32_bf16 v[124:127], a[144:147], a[196:199], v[124:127]
v_mfma_f32_16x16x32_bf16 v[64:67], a[136:139], a[148:151], v[64:67]
s_waitcnt lgkmcnt(11)
ds_read_b64_tr_b16 a[144:145], v209 offset:8704
ds_read_b64_tr_b16 a[146:147], v209 offset:12800
v_mfma_f32_16x16x32_bf16 v[64:67], a[164:167], a[160:163], v[64:67]
ds_read_b64_tr_b16 a[140:141], v209 offset:512
ds_read_b64_tr_b16 a[142:143], v209 offset:4608
v_mfma_f32_16x16x32_bf16 v[68:71], a[136:139], a[184:187], v[68:71]
v_mfma_f32_16x16x32_bf16 v[68:71], a[164:167], a[192:195], v[68:71]
v_mfma_f32_16x16x32_bf16 v[72:75], a[136:139], a[200:203], v[72:75]
s_waitcnt lgkmcnt(10)
v_mfma_f32_16x16x32_bf16 v[72:75], a[164:167], a[204:207], v[72:75]
v_mfma_f32_16x16x32_bf16 v[76:79], a[136:139], a[208:211], v[76:79]
v_mfma_f32_16x16x32_bf16 v[76:79], a[164:167], a[212:215], v[76:79]
s_add_u32 m0, s40, 0
v_mfma_f32_16x16x32_bf16 v[80:83], a[216:219], a[148:151], v[80:83]
s_waitcnt vmcnt(40) lgkmcnt(0)
s_barrier
v_mfma_f32_16x16x32_bf16 v[80:83], a[220:223], a[160:163], v[80:83]
s_add_u32 s56, s80, 524288
buffer_load_dwordx4 v213, s[28:31], s56 offen lds
v_mfma_f32_16x16x32_bf16 v[84:87], a[216:219], a[184:187], v[84:87]
v_mfma_f32_16x16x32_bf16 v[84:87], a[220:223], a[192:195], v[84:87]
ds_read_b128 a[152:155], v195 offset:32768
ds_read_b128 a[156:159], v195 offset:36864
v_mfma_f32_16x16x32_bf16 v[88:91], a[216:219], a[200:203], v[88:91]
ds_read_b64_tr_b16 a[136:137], v204 offset:16384
ds_read_b64_tr_b16 a[138:139], v204 offset:20480
v_mfma_f32_16x16x32_bf16 v[88:91], a[220:223], a[204:207], v[88:91]
ds_read_b64_tr_b16 a[164:165], v204 offset:24576
ds_read_b64_tr_b16 a[166:167], v204 offset:28672
v_mfma_f32_16x16x32_bf16 v[92:95], a[216:219], a[208:211], v[92:95]
ds_read_b128 a[168:171], v195 offset:33792
ds_read_b128 a[172:175], v195 offset:37888
v_mfma_f32_16x16x32_bf16 v[92:95], a[220:223], a[212:215], v[92:95]
buffer_load_dwordx4 v214, s[28:31], s56 offen offset:2048 lds
v_mfma_f32_16x16x32_bf16 v[96:99], a[128:131], a[148:151], v[96:99]
s_add_u32 m0, s40, 1024
ds_read_b128 a[176:179], v195 offset:34816
ds_read_b128 a[180:183], v195 offset:38912
v_mfma_f32_16x16x32_bf16 v[96:99], a[132:135], a[160:163], v[96:99]
s_add_u32 s57, s80, 524416
buffer_load_dwordx4 v213, s[28:31], s57 offen lds
v_mfma_f32_16x16x32_bf16 v[100:103], a[128:131], a[184:187], v[100:103]
ds_read_b128 a[188:191], v195 offset:35840
ds_read_b128 a[196:199], v195 offset:39936
v_mfma_f32_16x16x32_bf16 v[100:103], a[132:135], a[192:195], v[100:103]
L_tch16_%=:
s_cbranch_execz L_tb1_%=
s_xor_b32 s59, s72, 69632
buffer_load_dwordx4 v214, s[28:31], s57 offen offset:2048 lds
v_mfma_f32_16x16x32_bf16 v[104:107], a[128:131], a[200:203], v[104:107]
ds_read_b64_tr_b16 a[216:217], v207 offset:16384
ds_read_b64_tr_b16 a[218:219], v207 offset:20480
v_mfma_f32_16x16x32_bf16 v[104:107], a[132:135], a[204:207], v[104:107]
s_add_u32 m0, s40, 131072
ds_read_b64_tr_b16 a[220:221], v207 offset:24576
ds_read_b64_tr_b16 a[222:223], v207 offset:28672
v_mfma_f32_16x16x32_bf16 v[108:111], a[128:131], a[208:211], v[108:111]
s_xor_b32 s58, s72, 65536
buffer_load_dwordx4 v217, s[24:27], s58 offen sc1 lds
v_mfma_f32_16x16x32_bf16 v[108:111], a[132:135], a[212:215], v[108:111]
s_add_u32 m0, s40, 132096
v_mfma_f32_16x16x32_bf16 v[112:115], a[140:143], a[148:151], v[112:115]
buffer_load_dwordx4 v217, s[24:27], s59 offen sc1 lds
v_mfma_f32_16x16x32_bf16 v[112:115], a[144:147], a[160:163], v[112:115]
s_add_u32 m0, s40, 133120
v_mfma_f32_16x16x32_bf16 v[116:119], a[140:143], a[184:187], v[116:119]
s_xor_b32 s56, s72, 73728
buffer_load_dwordx4 v217, s[24:27], s56 offen sc1 lds
v_mfma_f32_16x16x32_bf16 v[116:119], a[144:147], a[192:195], v[116:119]
v_mfma_f32_16x16x32_bf16 v[120:123], a[140:143], a[200:203], v[120:123]
s_waitcnt lgkmcnt(12)
ds_read_b64_tr_b16 a[128:129], v204 offset:16896
ds_read_b64_tr_b16 a[130:131], v204 offset:20992
v_mfma_f32_16x16x32_bf16 v[120:123], a[144:147], a[204:207], v[120:123]
s_add_u32 m0, s40, 134144
ds_read_b64_tr_b16 a[132:133], v204 offset:25088
ds_read_b64_tr_b16 a[134:135], v204 offset:29184
v_mfma_f32_16x16x32_bf16 v[124:127], a[140:143], a[208:211], v[124:127]
s_xor_b32 s57, s72, 77824
buffer_load_dwordx4 v217, s[24:27], s57 offen sc1 lds
v_mfma_f32_16x16x32_bf16 v[124:127], a[144:147], a[212:215], v[124:127]
v_mfma_f32_16x16x32_bf16 v[64:67], a[136:139], a[152:155], v[64:67]
s_waitcnt lgkmcnt(11)
ds_read_b64_tr_b16 a[144:145], v207 offset:25088
ds_read_b64_tr_b16 a[146:147], v207 offset:29184
v_mfma_f32_16x16x32_bf16 v[64:67], a[164:167], a[156:159], v[64:67]
ds_read_b64_tr_b16 a[140:141], v207 offset:16896
ds_read_b64_tr_b16 a[142:143], v207 offset:20992
v_mfma_f32_16x16x32_bf16 v[68:71], a[136:139], a[168:171], v[68:71]
v_mfma_f32_16x16x32_bf16 v[68:71], a[164:167], a[172:175], v[68:71]
v_mfma_f32_16x16x32_bf16 v[72:75], a[136:139], a[176:179], v[72:75]
s_waitcnt lgkmcnt(10)
v_mfma_f32_16x16x32_bf16 v[72:75], a[164:167], a[180:183], v[72:75]
v_mfma_f32_16x16x32_bf16 v[76:79], a[136:139], a[188:191], v[76:79]
v_mfma_f32_16x16x32_bf16 v[76:79], a[164:167], a[196:199], v[76:79]
s_add_u32 m0, s40, 16384
v_mfma_f32_16x16x32_bf16 v[80:83], a[216:219], a[152:155], v[80:83]
s_waitcnt vmcnt(31) lgkmcnt(0)
s_barrier
v_mfma_f32_16x16x32_bf16 v[80:83], a[220:223], a[156:159], v[80:83]
s_add_u32 s58, s80, 589824
buffer_load_dwordx4 v213, s[28:31], s58 offen lds
v_mfma_f32_16x16x32_bf16 v[84:87], a[216:219], a[168:171], v[84:87]
v_mfma_f32_16x16x32_bf16 v[84:87], a[220:223], a[172:175], v[84:87]
s_add_u32 s60, s49, 8192
ds_read_b128 a[148:151], v197 offset:16384
ds_read_b128 a[160:163], v197 offset:20480
v_mfma_f32_16x16x32_bf16 v[88:91], a[216:219], a[176:179], v[88:91]
ds_read_b64_tr_b16 a[136:137], v205 offset:0
ds_read_b64_tr_b16 a[138:139], v205 offset:4096
s_waitcnt vmcnt(22)
buffer_store_dwordx2 v[254:255], v252, s[24:27], s60 offen sc1
v_mfma_f32_16x16x32_bf16 v[88:91], a[220:223], a[180:183], v[88:91]
ds_read_b64_tr_b16 a[164:165], v205 offset:8192
ds_read_b64_tr_b16 a[166:167], v205 offset:12288
v_mfma_f32_16x16x32_bf16 v[92:95], a[216:219], a[188:191], v[92:95]
ds_read_b128 a[184:187], v197 offset:17408
ds_read_b128 a[192:195], v197 offset:21504
v_mfma_f32_16x16x32_bf16 v[92:95], a[220:223], a[196:199], v[92:95]
buffer_load_dwordx4 v214, s[28:31], s58 offen offset:2048 lds
v_mfma_f32_16x16x32_bf16 v[96:99], a[128:131], a[152:155], v[96:99]
s_add_u32 m0, s40, 17408
ds_read_b128 a[200:203], v197 offset:18432
ds_read_b128 a[204:207], v197 offset:22528
v_mfma_f32_16x16x32_bf16 v[96:99], a[132:135], a[156:159], v[96:99]
s_add_u32 s59, s80, 589952
buffer_load_dwordx4 v213, s[28:31], s59 offen lds
v_mfma_f32_16x16x32_bf16 v[100:103], a[128:131], a[168:171], v[100:103]
ds_read_b128 a[208:211], v197 offset:19456
ds_read_b128 a[212:215], v197 offset:23552
v_mfma_f32_16x16x32_bf16 v[100:103], a[132:135], a[172:175], v[100:103]
s_xor_b32 s57, s72, 71680
buffer_load_dwordx4 v214, s[28:31], s59 offen offset:2048 lds
v_mfma_f32_16x16x32_bf16 v[104:107], a[128:131], a[176:179], v[104:107]
ds_read_b64_tr_b16 a[216:217], v208 offset:0
ds_read_b64_tr_b16 a[218:219], v208 offset:4096
v_mfma_f32_16x16x32_bf16 v[104:107], a[132:135], a[180:183], v[104:107]
s_add_u32 m0, s40, 32768
ds_read_b64_tr_b16 a[220:221], v208 offset:8192
ds_read_b64_tr_b16 a[222:223], v208 offset:12288
v_mfma_f32_16x16x32_bf16 v[108:111], a[128:131], a[188:191], v[108:111]
s_xor_b32 s56, s72, 67584
buffer_load_dwordx4 v217, s[24:27], s56 offen sc1 lds
v_mfma_f32_16x16x32_bf16 v[108:111], a[132:135], a[196:199], v[108:111]
s_add_u32 m0, s40, 33792
v_mfma_f32_16x16x32_bf16 v[112:115], a[140:143], a[152:155], v[112:115]
buffer_load_dwordx4 v217, s[24:27], s57 offen sc1 lds
v_mfma_f32_16x16x32_bf16 v[112:115], a[144:147], a[156:159], v[112:115]
s_add_u32 m0, s40, 34816
v_mfma_f32_16x16x32_bf16 v[116:119], a[140:143], a[168:171], v[116:119]
s_xor_b32 s58, s72, 75776
buffer_load_dwordx4 v217, s[24:27], s58 offen sc1 lds
v_mfma_f32_16x16x32_bf16 v[116:119], a[144:147], a[172:175], v[116:119]
v_mfma_f32_16x16x32_bf16 v[120:123], a[140:143], a[176:179], v[120:123]
s_waitcnt lgkmcnt(12)
ds_read_b64_tr_b16 a[128:129], v205 offset:512
ds_read_b64_tr_b16 a[130:131], v205 offset:4608
v_mfma_f32_16x16x32_bf16 v[120:123], a[144:147], a[180:183], v[120:123]
s_add_u32 m0, s40, 35840
ds_read_b64_tr_b16 a[132:133], v205 offset:8704
ds_read_b64_tr_b16 a[134:135], v205 offset:12800
v_mfma_f32_16x16x32_bf16 v[124:127], a[140:143], a[188:191], v[124:127]
s_xor_b32 s59, s72, 79872
buffer_load_dwordx4 v217, s[24:27], s59 offen sc1 lds
v_mfma_f32_16x16x32_bf16 v[124:127], a[144:147], a[196:199], v[124:127]
v_mfma_f32_16x16x32_bf16 v[64:67], a[136:139], a[148:151], v[64:67]
s_waitcnt lgkmcnt(11)
ds_read_b64_tr_b16 a[144:145], v208 offset:8704
ds_read_b64_tr_b16 a[146:147], v208 offset:12800
v_mfma_f32_16x16x32_bf16 v[64:67], a[164:167], a[160:163], v[64:67]
ds_read_b64_tr_b16 a[140:141], v208 offset:512
ds_read_b64_tr_b16 a[142:143], v208 offset:4608
v_mfma_f32_16x16x32_bf16 v[68:71], a[136:139], a[184:187], v[68:71]
v_mfma_f32_16x16x32_bf16 v[68:71], a[164:167], a[192:195], v[68:71]
v_mfma_f32_16x16x32_bf16 v[72:75], a[136:139], a[200:203], v[72:75]
s_waitcnt lgkmcnt(10)
v_mfma_f32_16x16x32_bf16 v[72:75], a[164:167], a[204:207], v[72:75]
v_mfma_f32_16x16x32_bf16 v[76:79], a[136:139], a[208:211], v[76:79]
v_mfma_f32_16x16x32_bf16 v[76:79], a[164:167], a[212:215], v[76:79]
s_add_u32 m0, s40, 65536
v_mfma_f32_16x16x32_bf16 v[80:83], a[216:219], a[148:151], v[80:83]
s_waitcnt vmcnt(25) lgkmcnt(0)
s_barrier
v_mfma_f32_16x16x32_bf16 v[80:83], a[220:223], a[160:163], v[80:83]
s_add_u32 s56, s81, 524288
buffer_load_dwordx4 v213, s[28:31], s56 offen lds
v_mfma_f32_16x16x32_bf16 v[84:87], a[216:219], a[184:187], v[84:87]
v_mfma_f32_16x16x32_bf16 v[84:87], a[220:223], a[192:195], v[84:87]
ds_read_b128 a[152:155], v196 offset:16384
ds_read_b128 a[156:159], v196 offset:20480
v_mfma_f32_16x16x32_bf16 v[88:91], a[216:219], a[200:203], v[88:91]
ds_read_b64_tr_b16 a[136:137], v204 offset:49152
ds_read_b64_tr_b16 a[138:139], v204 offset:53248
v_mfma_f32_16x16x32_bf16 v[88:91], a[220:223], a[204:207], v[88:91]
ds_read_b64_tr_b16 a[164:165], v204 offset:57344
ds_read_b64_tr_b16 a[166:167], v204 offset:61440
v_mfma_f32_16x16x32_bf16 v[92:95], a[216:219], a[208:211], v[92:95]
ds_read_b128 a[168:171], v196 offset:17408
ds_read_b128 a[172:175], v196 offset:21504
v_mfma_f32_16x16x32_bf16 v[92:95], a[220:223], a[212:215], v[92:95]
buffer_load_dwordx4 v214, s[28:31], s56 offen offset:2048 lds
v_mfma_f32_16x16x32_bf16 v[96:99], a[128:131], a[148:151], v[96:99]
s_add_u32 m0, s40, 66560
ds_read_b128 a[176:179], v196 offset:18432
ds_read_b128 a[180:183], v196 offset:22528
v_mfma_f32_16x16x32_bf16 v[96:99], a[132:135], a[160:163], v[96:99]
s_add_u32 s57, s81, 524416
buffer_load_dwordx4 v213, s[28:31], s57 offen lds
v_mfma_f32_16x16x32_bf16 v[100:103], a[128:131], a[184:187], v[100:103]
ds_read_b128 a[188:191], v196 offset:19456
ds_read_b128 a[196:199], v196 offset:23552
v_mfma_f32_16x16x32_bf16 v[100:103], a[132:135], a[192:195], v[100:103]
s_xor_b32 s59, s73, 69632
buffer_load_dwordx4 v214, s[28:31], s57 offen offset:2048 lds
v_mfma_f32_16x16x32_bf16 v[104:107], a[128:131], a[200:203], v[104:107]
ds_read_b64_tr_b16 a[216:217], v207 offset:49152
ds_read_b64_tr_b16 a[218:219], v207 offset:53248
v_mfma_f32_16x16x32_bf16 v[104:107], a[132:135], a[204:207], v[104:107]
s_add_u32 m0, s40, 147456
ds_read_b64_tr_b16 a[220:221], v207 offset:57344
ds_read_b64_tr_b16 a[222:223], v207 offset:61440
v_mfma_f32_16x16x32_bf16 v[108:111], a[128:131], a[208:211], v[108:111]
s_xor_b32 s58, s73, 65536
buffer_load_dwordx4 v217, s[24:27], s58 offen sc1 lds
v_mfma_f32_16x16x32_bf16 v[108:111], a[132:135], a[212:215], v[108:111]
s_add_u32 m0, s40, 148480
v_mfma_f32_16x16x32_bf16 v[112:115], a[140:143], a[148:151], v[112:115]
buffer_load_dwordx4 v217, s[24:27], s59 offen sc1 lds
v_mfma_f32_16x16x32_bf16 v[112:115], a[144:147], a[160:163], v[112:115]
s_add_u32 m0, s40, 149504
v_mfma_f32_16x16x32_bf16 v[116:119], a[140:143], a[184:187], v[116:119]
s_xor_b32 s56, s73, 73728
buffer_load_dwordx4 v217, s[24:27], s56 offen sc1 lds
v_mfma_f32_16x16x32_bf16 v[116:119], a[144:147], a[192:195], v[116:119]
v_mfma_f32_16x16x32_bf16 v[120:123], a[140:143], a[200:203], v[120:123]
s_waitcnt lgkmcnt(12)
ds_read_b64_tr_b16 a[128:129], v204 offset:49664
ds_read_b64_tr_b16 a[130:131], v204 offset:53760
v_mfma_f32_16x16x32_bf16 v[120:123], a[144:147], a[204:207], v[120:123]
s_add_u32 m0, s40, 150528
ds_read_b64_tr_b16 a[132:133], v204 offset:57856
ds_read_b64_tr_b16 a[134:135], v204 offset:61952
v_mfma_f32_16x16x32_bf16 v[124:127], a[140:143], a[208:211], v[124:127]
s_xor_b32 s57, s73, 77824
buffer_load_dwordx4 v217, s[24:27], s57 offen sc1 lds
v_mfma_f32_16x16x32_bf16 v[124:127], a[144:147], a[212:215], v[124:127]
v_mfma_f32_16x16x32_bf16 v[64:67], a[136:139], a[152:155], v[64:67]
s_waitcnt lgkmcnt(11)
ds_read_b64_tr_b16 a[144:145], v207 offset:57856
ds_read_b64_tr_b16 a[146:147], v207 offset:61952
v_mfma_f32_16x16x32_bf16 v[64:67], a[164:167], a[156:159], v[64:67]
ds_read_b64_tr_b16 a[140:141], v207 offset:49664
ds_read_b64_tr_b16 a[142:143], v207 offset:53760
v_mfma_f32_16x16x32_bf16 v[68:71], a[136:139], a[168:171], v[68:71]
v_mfma_f32_16x16x32_bf16 v[68:71], a[164:167], a[172:175], v[68:71]
v_mfma_f32_16x16x32_bf16 v[72:75], a[136:139], a[176:179], v[72:75]
s_waitcnt lgkmcnt(10)
v_mfma_f32_16x16x32_bf16 v[72:75], a[164:167], a[180:183], v[72:75]
v_mfma_f32_16x16x32_bf16 v[76:79], a[136:139], a[188:191], v[76:79]
v_mfma_f32_16x16x32_bf16 v[76:79], a[164:167], a[196:199], v[76:79]
s_add_u32 m0, s40, 49152
v_mfma_f32_16x16x32_bf16 v[80:83], a[216:219], a[152:155], v[80:83]
s_waitcnt vmcnt(25) lgkmcnt(0)
s_barrier
v_mfma_f32_16x16x32_bf16 v[80:83], a[220:223], a[156:159], v[80:83]
s_add_u32 s58, s81, 589824
buffer_load_dwordx4 v213, s[28:31], s58 offen lds
v_mfma_f32_16x16x32_bf16 v[84:87], a[216:219], a[168:171], v[84:87]
v_mfma_f32_16x16x32_bf16 v[84:87], a[220:223], a[172:175], v[84:87]
ds_read_b128 a[148:151], v196 offset:49152
ds_read_b128 a[160:163], v196 offset:53248
v_mfma_f32_16x16x32_bf16 v[88:91], a[216:219], a[176:179], v[88:91]
ds_read_b64_tr_b16 a[136:137], v205 offset:32768
ds_read_b64_tr_b16 a[138:139], v205 offset:36864
v_mfma_f32_16x16x32_bf16 v[88:91], a[220:223], a[180:183], v[88:91]
ds_read_b64_tr_b16 a[164:165], v205 offset:40960
ds_read_b64_tr_b16 a[166:167], v205 offset:45056
v_mfma_f32_16x16x32_bf16 v[92:95], a[216:219], a[188:191], v[92:95]
ds_read_b128 a[184:187], v196 offset:50176
ds_read_b128 a[192:195], v196 offset:54272
v_mfma_f32_16x16x32_bf16 v[92:95], a[220:223], a[196:199], v[92:95]
buffer_load_dwordx4 v214, s[28:31], s58 offen offset:2048 lds
v_mfma_f32_16x16x32_bf16 v[96:99], a[128:131], a[152:155], v[96:99]
s_add_u32 m0, s40, 50176
ds_read_b128 a[200:203], v196 offset:51200
ds_read_b128 a[204:207], v196 offset:55296
v_mfma_f32_16x16x32_bf16 v[96:99], a[132:135], a[156:159], v[96:99]
s_add_u32 s59, s81, 589952
buffer_load_dwordx4 v213, s[28:31], s59 offen lds
v_mfma_f32_16x16x32_bf16 v[100:103], a[128:131], a[168:171], v[100:103]
ds_read_b128 a[208:211], v196 offset:52224
ds_read_b128 a[212:215], v196 offset:56320
v_mfma_f32_16x16x32_bf16 v[100:103], a[132:135], a[172:175], v[100:103]
s_xor_b32 s57, s73, 71680
buffer_load_dwordx4 v214, s[28:31], s59 offen offset:2048 lds
v_mfma_f32_16x16x32_bf16 v[104:107], a[128:131], a[176:179], v[104:107]
ds_read_b64_tr_b16 a[216:217], v208 offset:32768
ds_read_b64_tr_b16 a[218:219], v208 offset:36864
v_mfma_f32_16x16x32_bf16 v[104:107], a[132:135], a[180:183], v[104:107]
s_add_u32 m0, s40, 81920
ds_read_b64_tr_b16 a[220:221], v208 offset:40960
ds_read_b64_tr_b16 a[222:223], v208 offset:45056
v_mfma_f32_16x16x32_bf16 v[108:111], a[128:131], a[188:191], v[108:111]
s_xor_b32 s56, s73, 67584
buffer_load_dwordx4 v217, s[24:27], s56 offen sc1 lds
v_mfma_f32_16x16x32_bf16 v[108:111], a[132:135], a[196:199], v[108:111]
s_add_u32 m0, s40, 82944
v_mfma_f32_16x16x32_bf16 v[112:115], a[140:143], a[152:155], v[112:115]
buffer_load_dwordx4 v217, s[24:27], s57 offen sc1 lds
v_mfma_f32_16x16x32_bf16 v[112:115], a[144:147], a[156:159], v[112:115]
s_add_u32 m0, s40, 83968
v_mfma_f32_16x16x32_bf16 v[116:119], a[140:143], a[168:171], v[116:119]
s_xor_b32 s58, s73, 75776
buffer_load_dwordx4 v217, s[24:27], s58 offen sc1 lds
v_mfma_f32_16x16x32_bf16 v[116:119], a[144:147], a[172:175], v[116:119]
v_mfma_f32_16x16x32_bf16 v[120:123], a[140:143], a[176:179], v[120:123]
s_waitcnt lgkmcnt(12)
ds_read_b64_tr_b16 a[128:129], v205 offset:33280
ds_read_b64_tr_b16 a[130:131], v205 offset:37376
v_mfma_f32_16x16x32_bf16 v[120:123], a[144:147], a[180:183], v[120:123]
s_add_u32 m0, s40, 84992
ds_read_b64_tr_b16 a[132:133], v205 offset:41472
ds_read_b64_tr_b16 a[134:135], v205 offset:45568
v_mfma_f32_16x16x32_bf16 v[124:127], a[140:143], a[188:191], v[124:127]
s_xor_b32 s59, s73, 79872
buffer_load_dwordx4 v217, s[24:27], s59 offen sc1 lds
v_mfma_f32_16x16x32_bf16 v[124:127], a[144:147], a[196:199], v[124:127]
v_mfma_f32_16x16x32_bf16 v[64:67], a[136:139], a[148:151], v[64:67]
s_waitcnt lgkmcnt(11)
ds_read_b64_tr_b16 a[144:145], v208 offset:41472
ds_read_b64_tr_b16 a[146:147], v208 offset:45568
v_mfma_f32_16x16x32_bf16 v[64:67], a[164:167], a[160:163], v[64:67]
ds_read_b64_tr_b16 a[140:141], v208 offset:33280
ds_read_b64_tr_b16 a[142:143], v208 offset:37376
v_mfma_f32_16x16x32_bf16 v[68:71], a[136:139], a[184:187], v[68:71]
v_mfma_f32_16x16x32_bf16 v[68:71], a[164:167], a[192:195], v[68:71]
v_mfma_f32_16x16x32_bf16 v[72:75], a[136:139], a[200:203], v[72:75]
s_waitcnt lgkmcnt(10)
v_mfma_f32_16x16x32_bf16 v[72:75], a[164:167], a[204:207], v[72:75]
v_mfma_f32_16x16x32_bf16 v[76:79], a[136:139], a[208:211], v[76:79]
v_mfma_f32_16x16x32_bf16 v[76:79], a[164:167], a[212:215], v[76:79]
s_add_u32 m0, s40, 98304
v_mfma_f32_16x16x32_bf16 v[80:83], a[216:219], a[148:151], v[80:83]
s_waitcnt vmcnt(25) lgkmcnt(0)
s_barrier
v_mfma_f32_16x16x32_bf16 v[80:83], a[220:223], a[160:163], v[80:83]
s_add_u32 s56, s80, 786432
buffer_load_dwordx4 v213, s[28:31], s56 offen lds
v_mfma_f32_16x16x32_bf16 v[84:87], a[216:219], a[184:187], v[84:87]
v_mfma_f32_16x16x32_bf16 v[84:87], a[220:223], a[192:195], v[84:87]
ds_read_b128 a[152:155], v197 offset:0
ds_read_b128 a[156:159], v197 offset:4096
v_mfma_f32_16x16x32_bf16 v[88:91], a[216:219], a[200:203], v[88:91]
ds_read_b64_tr_b16 a[136:137], v204 offset:0
ds_read_b64_tr_b16 a[138:139], v204 offset:4096
v_mfma_f32_16x16x32_bf16 v[88:91], a[220:223], a[204:207], v[88:91]
ds_read_b64_tr_b16 a[164:165], v204 offset:8192
ds_read_b64_tr_b16 a[166:167], v204 offset:12288
v_mfma_f32_16x16x32_bf16 v[92:95], a[216:219], a[208:211], v[92:95]
ds_read_b128 a[168:171], v197 offset:1024
ds_read_b128 a[172:175], v197 offset:5120
v_mfma_f32_16x16x32_bf16 v[92:95], a[220:223], a[212:215], v[92:95]
buffer_load_dwordx4 v214, s[28:31], s56 offen offset:2048 lds
v_mfma_f32_16x16x32_bf16 v[96:99], a[128:131], a[148:151], v[96:99]
s_add_u32 m0, s40, 99328
ds_read_b128 a[176:179], v197 offset:2048
ds_read_b128 a[180:183], v197 offset:6144
v_mfma_f32_16x16x32_bf16 v[96:99], a[132:135], a[160:163], v[96:99]
s_add_u32 s57, s80, 786560
buffer_load_dwordx4 v213, s[28:31], s57 offen lds
v_mfma_f32_16x16x32_bf16 v[100:103], a[128:131], a[184:187], v[100:103]
ds_read_b128 a[188:191], v197 offset:3072
ds_read_b128 a[196:199], v197 offset:7168
v_mfma_f32_16x16x32_bf16 v[100:103], a[132:135], a[192:195], v[100:103]
s_xor_b32 s59, s72, 110592
buffer_load_dwordx4 v214, s[28:31], s57 offen offset:2048 lds
v_mfma_f32_16x16x32_bf16 v[104:107], a[128:131], a[200:203], v[104:107]
ds_read_b64_tr_b16 a[216:217], v207 offset:0
ds_read_b64_tr_b16 a[218:219], v207 offset:4096
v_mfma_f32_16x16x32_bf16 v[104:107], a[132:135], a[204:207], v[104:107]
s_add_u32 m0, s40, 114688
ds_read_b64_tr_b16 a[220:221], v207 offset:8192
ds_read_b64_tr_b16 a[222:223], v207 offset:12288
v_mfma_f32_16x16x32_bf16 v[108:111], a[128:131], a[208:211], v[108:111]
s_xor_b32 s58, s72, 106496
buffer_load_dwordx4 v217, s[24:27], s58 offen sc1 lds
v_mfma_f32_16x16x32_bf16 v[108:111], a[132:135], a[212:215], v[108:111]
s_add_u32 m0, s40, 115712
v_mfma_f32_16x16x32_bf16 v[112:115], a[140:143], a[148:151], v[112:115]
buffer_load_dwordx4 v217, s[24:27], s59 offen sc1 lds
v_mfma_f32_16x16x32_bf16 v[112:115], a[144:147], a[160:163], v[112:115]
s_add_u32 m0, s40, 116736
v_mfma_f32_16x16x32_bf16 v[116:119], a[140:143], a[184:187], v[116:119]
s_xor_b32 s56, s72, 98304
buffer_load_dwordx4 v217, s[24:27], s56 offen sc1 lds
v_mfma_f32_16x16x32_bf16 v[116:119], a[144:147], a[192:195], v[116:119]
v_mfma_f32_16x16x32_bf16 v[120:123], a[140:143], a[200:203], v[120:123]
s_waitcnt lgkmcnt(12)
ds_read_b64_tr_b16 a[128:129], v204 offset:512
ds_read_b64_tr_b16 a[130:131], v204 offset:4608
v_mfma_f32_16x16x32_bf16 v[120:123], a[144:147], a[204:207], v[120:123]
s_add_u32 m0, s40, 117760
ds_read_b64_tr_b16 a[132:133], v204 offset:8704
ds_read_b64_tr_b16 a[134:135], v204 offset:12800
v_mfma_f32_16x16x32_bf16 v[124:127], a[140:143], a[208:211], v[124:127]
s_xor_b32 s57, s72, 102400
buffer_load_dwordx4 v217, s[24:27], s57 offen sc1 lds
v_mfma_f32_16x16x32_bf16 v[124:127], a[144:147], a[212:215], v[124:127]
v_mfma_f32_16x16x32_bf16 v[64:67], a[136:139], a[152:155], v[64:67]
s_waitcnt lgkmcnt(11)
ds_read_b64_tr_b16 a[144:145], v207 offset:8704
ds_read_b64_tr_b16 a[146:147], v207 offset:12800
v_mfma_f32_16x16x32_bf16 v[64:67], a[164:167], a[156:159], v[64:67]
ds_read_b64_tr_b16 a[140:141], v207 offset:512
ds_read_b64_tr_b16 a[142:143], v207 offset:4608
v_mfma_f32_16x16x32_bf16 v[68:71], a[136:139], a[168:171], v[68:71]
v_mfma_f32_16x16x32_bf16 v[68:71], a[164:167], a[172:175], v[68:71]
v_mfma_f32_16x16x32_bf16 v[72:75], a[136:139], a[176:179], v[72:75]
s_waitcnt lgkmcnt(10)
v_mfma_f32_16x16x32_bf16 v[72:75], a[164:167], a[180:183], v[72:75]
v_mfma_f32_16x16x32_bf16 v[76:79], a[136:139], a[188:191], v[76:79]
v_mfma_f32_16x16x32_bf16 v[76:79], a[164:167], a[196:199], v[76:79]
s_add_u32 m0, s40, 0
v_mfma_f32_16x16x32_bf16 v[80:83], a[216:219], a[152:155], v[80:83]
s_waitcnt vmcnt(24) lgkmcnt(0)
s_barrier
v_mfma_f32_16x16x32_bf16 v[80:83], a[220:223], a[156:159], v[80:83]
s_add_u32 s58, s80, 851968
buffer_load_dwordx4 v213, s[28:31], s58 offen lds
v_mfma_f32_16x16x32_bf16 v[84:87], a[216:219], a[168:171], v[84:87]
v_mfma_f32_16x16x32_bf16 v[84:87], a[220:223], a[172:175], v[84:87]
ds_read_b128 a[148:151], v195 offset:32768
ds_read_b128 a[160:163], v195 offset:36864
v_mfma_f32_16x16x32_bf16 v[88:91], a[216:219], a[176:179], v[88:91]
ds_read_b64_tr_b16 a[136:137], v204 offset:16384
ds_read_b64_tr_b16 a[138:139], v204 offset:20480
v_mfma_f32_16x16x32_bf16 v[88:91], a[220:223], a[180:183], v[88:91]
ds_read_b64_tr_b16 a[164:165], v204 offset:24576
ds_read_b64_tr_b16 a[166:167], v204 offset:28672
v_mfma_f32_16x16x32_bf16 v[92:95], a[216:219], a[188:191], v[92:95]
ds_read_b128 a[184:187], v195 offset:33792
ds_read_b128 a[192:195], v195 offset:37888
v_mfma_f32_16x16x32_bf16 v[92:95], a[220:223], a[196:199], v[92:95]
buffer_load_dwordx4 v214, s[28:31], s58 offen offset:2048 lds
v_mfma_f32_16x16x32_bf16 v[96:99], a[128:131], a[152:155], v[96:99]
s_add_u32 m0, s40, 1024
ds_read_b128 a[200:203], v195 offset:34816
ds_read_b128 a[204:207], v195 offset:38912
v_mfma_f32_16x16x32_bf16 v[96:99], a[132:135], a[156:159], v[96:99]
s_add_u32 s59, s80, 852096
buffer_load_dwordx4 v213, s[28:31], s59 offen lds
v_mfma_f32_16x16x32_bf16 v[100:103], a[128:131], a[168:171], v[100:103]
ds_read_b128 a[208:211], v195 offset:35840
ds_read_b128 a[212:215], v195 offset:39936
v_mfma_f32_16x16x32_bf16 v[100:103], a[132:135], a[172:175], v[100:103]
s_xor_b32 s57, s72, 112640
buffer_load_dwordx4 v214, s[28:31], s59 offen offset:2048 lds
v_mfma_f32_16x16x32_bf16 v[104:107], a[128:131], a[176:179], v[104:107]
ds_read_b64_tr_b16 a[216:217], v207 offset:16384
ds_read_b64_tr_b16 a[218:219], v207 offset:20480
v_mfma_f32_16x16x32_bf16 v[104:107], a[132:135], a[180:183], v[104:107]
s_add_u32 m0, s40, 131072
ds_read_b64_tr_b16 a[220:221], v207 offset:24576
ds_read_b64_tr_b16 a[222:223], v207 offset:28672
v_mfma_f32_16x16x32_bf16 v[108:111], a[128:131], a[188:191], v[108:111]
s_xor_b32 s56, s72, 108544
buffer_load_dwordx4 v217, s[24:27], s56 offen sc1 lds
v_mfma_f32_16x16x32_bf16 v[108:111], a[132:135], a[196:199], v[108:111]
s_add_u32 m0, s40, 132096
v_mfma_f32_16x16x32_bf16 v[112:115], a[140:143], a[152:155], v[112:115]
buffer_load_dwordx4 v217, s[24:27], s57 offen sc1 lds
v_mfma_f32_16x16x32_bf16 v[112:115], a[144:147], a[156:159], v[112:115]
s_add_u32 m0, s40, 133120
v_mfma_f32_16x16x32_bf16 v[116:119], a[140:143], a[168:171], v[116:119]
s_xor_b32 s58, s72, 100352
buffer_load_dwordx4 v217, s[24:27], s58 offen sc1 lds
v_mfma_f32_16x16x32_bf16 v[116:119], a[144:147], a[172:175], v[116:119]
v_mfma_f32_16x16x32_bf16 v[120:123], a[140:143], a[176:179], v[120:123]
s_waitcnt lgkmcnt(12)
ds_read_b64_tr_b16 a[128:129], v204 offset:16896
ds_read_b64_tr_b16 a[130:131], v204 offset:20992
v_mfma_f32_16x16x32_bf16 v[120:123], a[144:147], a[180:183], v[120:123]
s_add_u32 m0, s40, 134144
ds_read_b64_tr_b16 a[132:133], v204 offset:25088
ds_read_b64_tr_b16 a[134:135], v204 offset:29184
v_mfma_f32_16x16x32_bf16 v[124:127], a[140:143], a[188:191], v[124:127]
s_xor_b32 s59, s72, 104448
buffer_load_dwordx4 v217, s[24:27], s59 offen sc1 lds
v_mfma_f32_16x16x32_bf16 v[124:127], a[144:147], a[196:199], v[124:127]
v_mfma_f32_16x16x32_bf16 v[64:67], a[136:139], a[148:151], v[64:67]
s_waitcnt lgkmcnt(11)
ds_read_b64_tr_b16 a[144:145], v207 offset:25088
ds_read_b64_tr_b16 a[146:147], v207 offset:29184
v_mfma_f32_16x16x32_bf16 v[64:67], a[164:167], a[160:163], v[64:67]
ds_read_b64_tr_b16 a[140:141], v207 offset:16896
ds_read_b64_tr_b16 a[142:143], v207 offset:20992
v_mfma_f32_16x16x32_bf16 v[68:71], a[136:139], a[184:187], v[68:71]
v_mfma_f32_16x16x32_bf16 v[68:71], a[164:167], a[192:195], v[68:71]
v_mfma_f32_16x16x32_bf16 v[72:75], a[136:139], a[200:203], v[72:75]
s_waitcnt lgkmcnt(10)
v_mfma_f32_16x16x32_bf16 v[72:75], a[164:167], a[204:207], v[72:75]
v_mfma_f32_16x16x32_bf16 v[76:79], a[136:139], a[208:211], v[76:79]
v_mfma_f32_16x16x32_bf16 v[76:79], a[164:167], a[212:215], v[76:79]
s_add_u32 m0, s40, 16384
v_mfma_f32_16x16x32_bf16 v[80:83], a[216:219], a[148:151], v[80:83]
s_waitcnt vmcnt(24) lgkmcnt(0)
s_barrier
v_mfma_f32_16x16x32_bf16 v[80:83], a[220:223], a[160:163], v[80:83]
s_add_u32 s56, s81, 786432
buffer_load_dwordx4 v213, s[28:31], s56 offen lds
v_mfma_f32_16x16x32_bf16 v[84:87], a[216:219], a[184:187], v[84:87]
v_mfma_f32_16x16x32_bf16 v[84:87], a[220:223], a[192:195], v[84:87]
ds_read_b128 a[152:155], v197 offset:16384
ds_read_b128 a[156:159], v197 offset:20480
v_mfma_f32_16x16x32_bf16 v[88:91], a[216:219], a[200:203], v[88:91]
ds_read_b64_tr_b16 a[136:137], v205 offset:0
ds_read_b64_tr_b16 a[138:139], v205 offset:4096
v_mfma_f32_16x16x32_bf16 v[88:91], a[220:223], a[204:207], v[88:91]
ds_read_b64_tr_b16 a[164:165], v205 offset:8192
ds_read_b64_tr_b16 a[166:167], v205 offset:12288
v_mfma_f32_16x16x32_bf16 v[92:95], a[216:219], a[208:211], v[92:95]
ds_read_b128 a[168:171], v197 offset:17408
ds_read_b128 a[172:175], v197 offset:21504
v_mfma_f32_16x16x32_bf16 v[92:95], a[220:223], a[212:215], v[92:95]
buffer_load_dwordx4 v214, s[28:31], s56 offen offset:2048 lds
v_mfma_f32_16x16x32_bf16 v[96:99], a[128:131], a[148:151], v[96:99]
s_add_u32 m0, s40, 17408
ds_read_b128 a[176:179], v197 offset:18432
ds_read_b128 a[180:183], v197 offset:22528
v_mfma_f32_16x16x32_bf16 v[96:99], a[132:135], a[160:163], v[96:99]
s_add_u32 s57, s81, 786560
buffer_load_dwordx4 v213, s[28:31], s57 offen lds
v_mfma_f32_16x16x32_bf16 v[100:103], a[128:131], a[184:187], v[100:103]
ds_read_b128 a[188:191], v197 offset:19456
ds_read_b128 a[196:199], v197 offset:23552
v_mfma_f32_16x16x32_bf16 v[100:103], a[132:135], a[192:195], v[100:103]
s_xor_b32 s59, s73, 110592
buffer_load_dwordx4 v214, s[28:31], s57 offen offset:2048 lds
v_mfma_f32_16x16x32_bf16 v[104:107], a[128:131], a[200:203], v[104:107]
ds_read_b64_tr_b16 a[216:217], v208 offset:0
ds_read_b64_tr_b16 a[218:219], v208 offset:4096
v_mfma_f32_16x16x32_bf16 v[104:107], a[132:135], a[204:207], v[104:107]
s_add_u32 m0, s40, 32768
ds_read_b64_tr_b16 a[220:221], v208 offset:8192
ds_read_b64_tr_b16 a[222:223], v208 offset:12288
v_mfma_f32_16x16x32_bf16 v[108:111], a[128:131], a[208:211], v[108:111]
s_xor_b32 s58, s73, 106496
buffer_load_dwordx4 v217, s[24:27], s58 offen sc1 lds
v_mfma_f32_16x16x32_bf16 v[108:111], a[132:135], a[212:215], v[108:111]
s_add_u32 m0, s40, 33792
v_mfma_f32_16x16x32_bf16 v[112:115], a[140:143], a[148:151], v[112:115]
buffer_load_dwordx4 v217, s[24:27], s59 offen sc1 lds
v_mfma_f32_16x16x32_bf16 v[112:115], a[144:147], a[160:163], v[112:115]
s_add_u32 m0, s40, 34816
v_mfma_f32_16x16x32_bf16 v[116:119], a[140:143], a[184:187], v[116:119]
s_xor_b32 s56, s73, 98304
buffer_load_dwordx4 v217, s[24:27], s56 offen sc1 lds
v_mfma_f32_16x16x32_bf16 v[116:119], a[144:147], a[192:195], v[116:119]
v_mfma_f32_16x16x32_bf16 v[120:123], a[140:143], a[200:203], v[120:123]
s_waitcnt lgkmcnt(12)
ds_read_b64_tr_b16 a[128:129], v205 offset:512
ds_read_b64_tr_b16 a[130:131], v205 offset:4608
v_mfma_f32_16x16x32_bf16 v[120:123], a[144:147], a[204:207], v[120:123]
s_add_u32 m0, s40, 35840
ds_read_b64_tr_b16 a[132:133], v205 offset:8704
ds_read_b64_tr_b16 a[134:135], v205 offset:12800
v_mfma_f32_16x16x32_bf16 v[124:127], a[140:143], a[208:211], v[124:127]
s_xor_b32 s57, s73, 102400
buffer_load_dwordx4 v217, s[24:27], s57 offen sc1 lds
v_mfma_f32_16x16x32_bf16 v[124:127], a[144:147], a[212:215], v[124:127]
v_mfma_f32_16x16x32_bf16 v[64:67], a[136:139], a[152:155], v[64:67]
s_waitcnt lgkmcnt(11)
ds_read_b64_tr_b16 a[144:145], v208 offset:8704
ds_read_b64_tr_b16 a[146:147], v208 offset:12800
v_mfma_f32_16x16x32_bf16 v[64:67], a[164:167], a[156:159], v[64:67]
ds_read_b64_tr_b16 a[140:141], v208 offset:512
ds_read_b64_tr_b16 a[142:143], v208 offset:4608
v_mfma_f32_16x16x32_bf16 v[68:71], a[136:139], a[168:171], v[68:71]
L_tch17_%=:
s_cbranch_execz L_tb2_%=
v_mfma_f32_16x16x32_bf16 v[68:71], a[164:167], a[172:175], v[68:71]
v_mfma_f32_16x16x32_bf16 v[72:75], a[136:139], a[176:179], v[72:75]
s_waitcnt lgkmcnt(10)
v_mfma_f32_16x16x32_bf16 v[72:75], a[164:167], a[180:183], v[72:75]
v_mfma_f32_16x16x32_bf16 v[76:79], a[136:139], a[188:191], v[76:79]
v_mfma_f32_16x16x32_bf16 v[76:79], a[164:167], a[196:199], v[76:79]
s_add_u32 m0, s40, 65536
v_mfma_f32_16x16x32_bf16 v[80:83], a[216:219], a[152:155], v[80:83]
s_waitcnt vmcnt(24) lgkmcnt(0)
s_barrier
v_mfma_f32_16x16x32_bf16 v[80:83], a[220:223], a[156:159], v[80:83]
s_add_u32 s58, s81, 851968
buffer_load_dwordx4 v213, s[28:31], s58 offen lds
v_mfma_f32_16x16x32_bf16 v[84:87], a[216:219], a[168:171], v[84:87]
v_mfma_f32_16x16x32_bf16 v[84:87], a[220:223], a[172:175], v[84:87]
ds_read_b128 a[148:151], v196 offset:16384
ds_read_b128 a[160:163], v196 offset:20480
v_mfma_f32_16x16x32_bf16 v[88:91], a[216:219], a[176:179], v[88:91]
ds_read_b64_tr_b16 a[136:137], v204 offset:49152
ds_read_b64_tr_b16 a[138:139], v204 offset:53248
v_mfma_f32_16x16x32_bf16 v[88:91], a[220:223], a[180:183], v[88:91]
ds_read_b64_tr_b16 a[164:165], v204 offset:57344
ds_read_b64_tr_b16 a[166:167], v204 offset:61440
v_mfma_f32_16x16x32_bf16 v[92:95], a[216:219], a[188:191], v[92:95]
ds_read_b128 a[184:187], v196 offset:17408
ds_read_b128 a[192:195], v196 offset:21504
v_mfma_f32_16x16x32_bf16 v[92:95], a[220:223], a[196:199], v[92:95]
buffer_load_dwordx4 v214, s[28:31], s58 offen offset:2048 lds
v_mfma_f32_16x16x32_bf16 v[96:99], a[128:131], a[152:155], v[96:99]
s_add_u32 m0, s40, 66560
ds_read_b128 a[200:203], v196 offset:18432
ds_read_b128 a[204:207], v196 offset:22528
v_mfma_f32_16x16x32_bf16 v[96:99], a[132:135], a[156:159], v[96:99]
s_add_u32 s59, s81, 852096
buffer_load_dwordx4 v213, s[28:31], s59 offen lds
v_mfma_f32_16x16x32_bf16 v[100:103], a[128:131], a[168:171], v[100:103]
ds_read_b128 a[208:211], v196 offset:19456
ds_read_b128 a[212:215], v196 offset:23552
v_mfma_f32_16x16x32_bf16 v[100:103], a[132:135], a[172:175], v[100:103]
s_add_u32 s54, s50, 8192
buffer_load_dwordx4 v214, s[28:31], s59 offen offset:2048 lds
v_mfma_f32_16x16x32_bf16 v[104:107], a[128:131], a[176:179], v[104:107]
ds_read_b64_tr_b16 a[216:217], v207 offset:49152
ds_read_b64_tr_b16 a[218:219], v207 offset:53248
v_mfma_f32_16x16x32_bf16 v[104:107], a[132:135], a[180:183], v[104:107]
s_add_u32 m0, s40, 147456
ds_read_b64_tr_b16 a[220:221], v207 offset:57344
ds_read_b64_tr_b16 a[222:223], v207 offset:61440
v_mfma_f32_16x16x32_bf16 v[108:111], a[128:131], a[188:191], v[108:111]
s_xor_b32 s56, s73, 108544
buffer_load_dwordx4 v217, s[24:27], s56 offen sc1 lds
v_mfma_f32_16x16x32_bf16 v[108:111], a[132:135], a[196:199], v[108:111]
s_add_u32 m0, s40, 148480
v_bfe_u32 v226, v217, 1, 6
v_and_b32 v227, 0x80, v217
v_and_b32 v226, 0x38, v226
v_or_b32 v226, v226, v227
v_mfma_f32_16x16x32_bf16 v[112:115], a[140:143], a[152:155], v[112:115]
buffer_load_dwordx2 v[230:231], v226, s[24:27], s54 offen sc1
v_mfma_f32_16x16x32_bf16 v[112:115], a[144:147], a[156:159], v[112:115]
s_xor_b32 s57, s73, 112640
buffer_load_dwordx4 v217, s[24:27], s57 offen sc1 lds
v_mfma_f32_16x16x32_bf16 v[116:119], a[140:143], a[168:171], v[116:119]
s_add_u32 m0, s40, 149504
v_mfma_f32_16x16x32_bf16 v[116:119], a[144:147], a[172:175], v[116:119]
s_xor_b32 s58, s73, 100352
buffer_load_dwordx4 v217, s[24:27], s58 offen sc1 lds
v_mfma_f32_16x16x32_bf16 v[120:123], a[140:143], a[176:179], v[120:123]
s_waitcnt lgkmcnt(12)
ds_read_b64_tr_b16 a[128:129], v204 offset:49664
ds_read_b64_tr_b16 a[130:131], v204 offset:53760
v_mfma_f32_16x16x32_bf16 v[120:123], a[144:147], a[180:183], v[120:123]
s_add_u32 m0, s40, 150528
ds_read_b64_tr_b16 a[132:133], v204 offset:57856
ds_read_b64_tr_b16 a[134:135], v204 offset:61952
v_mfma_f32_16x16x32_bf16 v[124:127], a[140:143], a[188:191], v[124:127]
s_xor_b32 s59, s73, 104448
buffer_load_dwordx4 v217, s[24:27], s59 offen sc1 lds
v_mfma_f32_16x16x32_bf16 v[124:127], a[144:147], a[196:199], v[124:127]
v_mfma_f32_16x16x32_bf16 v[64:67], a[136:139], a[148:151], v[64:67]
s_waitcnt lgkmcnt(11)
ds_read_b64_tr_b16 a[144:145], v207 offset:57856
ds_read_b64_tr_b16 a[146:147], v207 offset:61952
v_mfma_f32_16x16x32_bf16 v[64:67], a[164:167], a[160:163], v[64:67]
ds_read_b64_tr_b16 a[140:141], v207 offset:49664
ds_read_b64_tr_b16 a[142:143], v207 offset:53760
v_mfma_f32_16x16x32_bf16 v[68:71], a[136:139], a[184:187], v[68:71]
v_mfma_f32_16x16x32_bf16 v[68:71], a[164:167], a[192:195], v[68:71]
v_mfma_f32_16x16x32_bf16 v[72:75], a[136:139], a[200:203], v[72:75]
s_waitcnt lgkmcnt(10)
v_mfma_f32_16x16x32_bf16 v[72:75], a[164:167], a[204:207], v[72:75]
v_mfma_f32_16x16x32_bf16 v[76:79], a[136:139], a[208:211], v[76:79]
v_mfma_f32_16x16x32_bf16 v[76:79], a[164:167], a[212:215], v[76:79]
s_add_u32 m0, s40, 49152
v_mfma_f32_16x16x32_bf16 v[80:83], a[216:219], a[148:151], v[80:83]
s_waitcnt vmcnt(25) lgkmcnt(0)
s_barrier
v_mfma_f32_16x16x32_bf16 v[80:83], a[220:223], a[160:163], v[80:83]
s_add_u32 s56, s80, 1048576
buffer_load_dwordx4 v213, s[28:31], s56 offen lds
v_mfma_f32_16x16x32_bf16 v[84:87], a[216:219], a[184:187], v[84:87]
v_mfma_f32_16x16x32_bf16 v[84:87], a[220:223], a[192:195], v[84:87]
ds_read_b128 a[152:155], v196 offset:49152
ds_read_b128 a[156:159], v196 offset:53248
v_mfma_f32_16x16x32_bf16 v[88:91], a[216:219], a[200:203], v[88:91]
ds_read_b64_tr_b16 a[136:137], v205 offset:32768
ds_read_b64_tr_b16 a[138:139], v205 offset:36864
v_mfma_f32_16x16x32_bf16 v[88:91], a[220:223], a[204:207], v[88:91]
ds_read_b64_tr_b16 a[164:165], v205 offset:40960
ds_read_b64_tr_b16 a[166:167], v205 offset:45056
v_mfma_f32_16x16x32_bf16 v[92:95], a[216:219], a[208:211], v[92:95]
ds_read_b128 a[168:171], v196 offset:50176
ds_read_b128 a[172:175], v196 offset:54272
v_mfma_f32_16x16x32_bf16 v[92:95], a[220:223], a[212:215], v[92:95]
buffer_load_dwordx4 v214, s[28:31], s56 offen offset:2048 lds
v_mfma_f32_16x16x32_bf16 v[96:99], a[128:131], a[148:151], v[96:99]
s_add_u32 m0, s40, 50176
ds_read_b128 a[176:179], v196 offset:51200
ds_read_b128 a[180:183], v196 offset:55296
v_mfma_f32_16x16x32_bf16 v[96:99], a[132:135], a[160:163], v[96:99]
s_add_u32 s57, s80, 1048704
buffer_load_dwordx4 v213, s[28:31], s57 offen lds
v_mfma_f32_16x16x32_bf16 v[100:103], a[128:131], a[184:187], v[100:103]
ds_read_b128 a[188:191], v196 offset:52224
ds_read_b128 a[196:199], v196 offset:56320
v_mfma_f32_16x16x32_bf16 v[100:103], a[132:135], a[192:195], v[100:103]
buffer_load_dwordx4 v214, s[28:31], s57 offen offset:2048 lds
v_mfma_f32_16x16x32_bf16 v[104:107], a[128:131], a[200:203], v[104:107]
ds_read_b64_tr_b16 a[216:217], v208 offset:32768
ds_read_b64_tr_b16 a[218:219], v208 offset:36864
v_mfma_f32_16x16x32_bf16 v[104:107], a[132:135], a[204:207], v[104:107]
ds_read_b64_tr_b16 a[220:221], v208 offset:40960
ds_read_b64_tr_b16 a[222:223], v208 offset:45056
v_mfma_f32_16x16x32_bf16 v[108:111], a[128:131], a[208:211], v[108:111]
v_mfma_f32_16x16x32_bf16 v[108:111], a[132:135], a[212:215], v[108:111]
v_mfma_f32_16x16x32_bf16 v[112:115], a[140:143], a[148:151], v[112:115]
v_mfma_f32_16x16x32_bf16 v[112:115], a[144:147], a[160:163], v[112:115]
v_mfma_f32_16x16x32_bf16 v[116:119], a[140:143], a[184:187], v[116:119]
v_mfma_f32_16x16x32_bf16 v[116:119], a[144:147], a[192:195], v[116:119]
v_mfma_f32_16x16x32_bf16 v[120:123], a[140:143], a[200:203], v[120:123]
s_waitcnt lgkmcnt(12)
ds_read_b64_tr_b16 a[128:129], v205 offset:33280
ds_read_b64_tr_b16 a[130:131], v205 offset:37376
v_mfma_f32_16x16x32_bf16 v[120:123], a[144:147], a[204:207], v[120:123]
ds_read_b64_tr_b16 a[132:133], v205 offset:41472
ds_read_b64_tr_b16 a[134:135], v205 offset:45568
v_mfma_f32_16x16x32_bf16 v[124:127], a[140:143], a[208:211], v[124:127]
v_mfma_f32_16x16x32_bf16 v[124:127], a[144:147], a[212:215], v[124:127]
v_mfma_f32_16x16x32_bf16 v[64:67], a[136:139], a[152:155], v[64:67]
s_waitcnt lgkmcnt(11)
ds_read_b64_tr_b16 a[144:145], v208 offset:41472
ds_read_b64_tr_b16 a[146:147], v208 offset:45568
v_mfma_f32_16x16x32_bf16 v[64:67], a[164:167], a[156:159], v[64:67]
ds_read_b64_tr_b16 a[140:141], v208 offset:33280
ds_read_b64_tr_b16 a[142:143], v208 offset:37376
v_mfma_f32_16x16x32_bf16 v[68:71], a[136:139], a[168:171], v[68:71]
s_waitcnt vmcnt(7)
s_add_u32 m0, s40, 81920
v_cmp_ne_u64 vcc, s[52:53], v[230:231]
s_nop 4
s_cbranch_vccz L_pollok1_%=
s_mov_b32 s55, 0
L_poll1_%=:
s_sleep 2
buffer_load_dwordx2 v[230:231], v226, s[24:27], s54 offen sc1
s_add_u32 s55, s55, 1
s_waitcnt vmcnt(0)
v_cmp_ne_u64 vcc, s[52:53], v[230:231]
s_cmp_lt_u32 s55, 0x40000
s_cbranch_scc0 L_pollok1_%=
s_nop 4
s_cbranch_vccnz L_poll1_%=
L_pollok1_%=:
v_mfma_f32_16x16x32_bf16 v[68:71], a[164:167], a[172:175], v[68:71]
s_xor_b32 s58, s72, 131072
buffer_load_dwordx4 v217, s[24:27], s58 offen sc1 lds
v_mfma_f32_16x16x32_bf16 v[72:75], a[136:139], a[176:179], v[72:75]
s_waitcnt lgkmcnt(10)
s_add_u32 m0, s40, 82944
v_mfma_f32_16x16x32_bf16 v[72:75], a[164:167], a[180:183], v[72:75]
s_xor_b32 s59, s72, 135168
buffer_load_dwordx4 v217, s[24:27], s59 offen sc1 lds
v_mfma_f32_16x16x32_bf16 v[76:79], a[136:139], a[188:191], v[76:79]
s_add_u32 m0, s40, 83968
v_mfma_f32_16x16x32_bf16 v[76:79], a[164:167], a[196:199], v[76:79]
s_xor_b32 s56, s72, 139264
buffer_load_dwordx4 v217, s[24:27], s56 offen sc1 lds
v_mfma_f32_16x16x32_bf16 v[80:83], a[216:219], a[152:155], v[80:83]
s_waitcnt lgkmcnt(0)
s_barrier
s_add_u32 m0, s40, 84992
v_mfma_f32_16x16x32_bf16 v[80:83], a[220:223], a[156:159], v[80:83]
s_xor_b32 s57, s72, 143360
buffer_load_dwordx4 v217, s[24:27], s57 offen sc1 lds
v_mfma_f32_16x16x32_bf16 v[84:87], a[216:219], a[168:171], v[84:87]
v_mfma_f32_16x16x32_bf16 v[84:87], a[220:223], a[172:175], v[84:87]
ds_read_b128 a[148:151], v197 offset:0
ds_read_b128 a[160:163], v197 offset:4096
v_mfma_f32_16x16x32_bf16 v[88:91], a[216:219], a[176:179], v[88:91]
ds_read_b64_tr_b16 a[136:137], v204 offset:0
ds_read_b64_tr_b16 a[138:139], v204 offset:4096
v_mfma_f32_16x16x32_bf16 v[88:91], a[220:223], a[180:183], v[88:91]
ds_read_b64_tr_b16 a[164:165], v204 offset:8192
ds_read_b64_tr_b16 a[166:167], v204 offset:12288
v_mfma_f32_16x16x32_bf16 v[92:95], a[216:219], a[188:191], v[92:95]
s_add_u32 m0, s40, 98304
ds_read_b128 a[184:187], v197 offset:1024
ds_read_b128 a[192:195], v197 offset:5120
v_mfma_f32_16x16x32_bf16 v[92:95], a[220:223], a[196:199], v[92:95]
s_add_u32 s58, s80, 1114112
buffer_load_dwordx4 v213, s[28:31], s58 offen lds
v_mfma_f32_16x16x32_bf16 v[96:99], a[128:131], a[152:155], v[96:99]
ds_read_b128 a[200:203], v197 offset:2048
ds_read_b128 a[204:207], v197 offset:6144
v_mfma_f32_16x16x32_bf16 v[96:99], a[132:135], a[156:159], v[96:99]
buffer_load_dwordx4 v214, s[28:31], s58 offen offset:2048 lds
v_mfma_f32_16x16x32_bf16 v[100:103], a[128:131], a[168:171], v[100:103]
s_add_u32 m0, s40, 99328
ds_read_b128 a[208:211], v197 offset:3072
ds_read_b128 a[212:215], v197 offset:7168
v_mfma_f32_16x16x32_bf16 v[100:103], a[132:135], a[172:175], v[100:103]
s_add_u32 s59, s80, 1114240
buffer_load_dwordx4 v213, s[28:31], s59 offen lds
v_mfma_f32_16x16x32_bf16 v[104:107], a[128:131], a[176:179], v[104:107]
ds_read_b64_tr_b16 a[216:217], v207 offset:0
ds_read_b64_tr_b16 a[218:219], v207 offset:4096
v_mfma_f32_16x16x32_bf16 v[104:107], a[132:135], a[180:183], v[104:107]
ds_read_b64_tr_b16 a[220:221], v207 offset:8192
ds_read_b64_tr_b16 a[222:223], v207 offset:12288
v_mfma_f32_16x16x32_bf16 v[108:111], a[128:131], a[188:191], v[108:111]
s_xor_b32 s56, s72, 133120
buffer_load_dwordx4 v214, s[28:31], s59 offen offset:2048 lds
v_mfma_f32_16x16x32_bf16 v[108:111], a[132:135], a[196:199], v[108:111]
s_add_u32 m0, s40, 114688
v_mfma_f32_16x16x32_bf16 v[112:115], a[140:143], a[152:155], v[112:115]
buffer_load_dwordx4 v217, s[24:27], s56 offen sc1 lds
v_mfma_f32_16x16x32_bf16 v[112:115], a[144:147], a[156:159], v[112:115]
s_add_u32 m0, s40, 115712
v_mfma_f32_16x16x32_bf16 v[116:119], a[140:143], a[168:171], v[116:119]
s_xor_b32 s57, s72, 137216
buffer_load_dwordx4 v217, s[24:27], s57 offen sc1 lds
v_mfma_f32_16x16x32_bf16 v[116:119], a[144:147], a[172:175], v[116:119]
v_mfma_f32_16x16x32_bf16 v[120:123], a[140:143], a[176:179], v[120:123]
s_waitcnt lgkmcnt(12)
ds_read_b64_tr_b16 a[128:129], v204 offset:512
ds_read_b64_tr_b16 a[130:131], v204 offset:4608
v_mfma_f32_16x16x32_bf16 v[120:123], a[144:147], a[180:183], v[120:123]
s_add_u32 m0, s40, 116736
ds_read_b64_tr_b16 a[132:133], v204 offset:8704
ds_read_b64_tr_b16 a[134:135], v204 offset:12800
v_mfma_f32_16x16x32_bf16 v[124:127], a[140:143], a[188:191], v[124:127]
s_xor_b32 s58, s72, 141312
buffer_load_dwordx4 v217, s[24:27], s58 offen sc1 lds
v_mfma_f32_16x16x32_bf16 v[124:127], a[144:147], a[196:199], v[124:127]
v_mfma_f32_16x16x32_bf16 v[64:67], a[136:139], a[148:151], v[64:67]
s_waitcnt lgkmcnt(11)
ds_read_b64_tr_b16 a[144:145], v207 offset:8704
ds_read_b64_tr_b16 a[146:147], v207 offset:12800
v_mfma_f32_16x16x32_bf16 v[64:67], a[164:167], a[160:163], v[64:67]
s_add_u32 m0, s40, 117760
ds_read_b64_tr_b16 a[140:141], v207 offset:512
ds_read_b64_tr_b16 a[142:143], v207 offset:4608
v_mfma_f32_16x16x32_bf16 v[68:71], a[136:139], a[184:187], v[68:71]
s_xor_b32 s59, s72, 145408
buffer_load_dwordx4 v217, s[24:27], s59 offen sc1 lds
v_mfma_f32_16x16x32_bf16 v[68:71], a[164:167], a[192:195], v[68:71]
v_mfma_f32_16x16x32_bf16 v[72:75], a[136:139], a[200:203], v[72:75]
s_waitcnt lgkmcnt(10)
v_mfma_f32_16x16x32_bf16 v[72:75], a[164:167], a[204:207], v[72:75]
v_mfma_f32_16x16x32_bf16 v[76:79], a[136:139], a[208:211], v[76:79]
v_mfma_f32_16x16x32_bf16 v[76:79], a[164:167], a[212:215], v[76:79]
s_add_u32 m0, s40, 0
v_mfma_f32_16x16x32_bf16 v[80:83], a[216:219], a[148:151], v[80:83]
s_waitcnt lgkmcnt(0)
s_barrier
v_mfma_f32_16x16x32_bf16 v[80:83], a[220:223], a[160:163], v[80:83]
s_add_u32 s56, s81, 1048576
buffer_load_dwordx4 v213, s[28:31], s56 offen lds
v_mfma_f32_16x16x32_bf16 v[84:87], a[216:219], a[184:187], v[84:87]
v_mfma_f32_16x16x32_bf16 v[84:87], a[220:223], a[192:195], v[84:87]
ds_read_b128 a[152:155], v195 offset:32768
ds_read_b128 a[156:159], v195 offset:36864
v_mfma_f32_16x16x32_bf16 v[88:91], a[216:219], a[200:203], v[88:91]
ds_read_b64_tr_b16 a[136:137], v204 offset:16384
ds_read_b64_tr_b16 a[138:139], v204 offset:20480
v_mfma_f32_16x16x32_bf16 v[88:91], a[220:223], a[204:207], v[88:91]
ds_read_b64_tr_b16 a[164:165], v204 offset:24576
ds_read_b64_tr_b16 a[166:167], v204 offset:28672
v_mfma_f32_16x16x32_bf16 v[92:95], a[216:219], a[208:211], v[92:95]
ds_read_b128 a[168:171], v195 offset:33792
ds_read_b128 a[172:175], v195 offset:37888
v_mfma_f32_16x16x32_bf16 v[92:95], a[220:223], a[212:215], v[92:95]
buffer_load_dwordx4 v214, s[28:31], s56 offen offset:2048 lds
v_mfma_f32_16x16x32_bf16 v[96:99], a[128:131], a[148:151], v[96:99]
s_add_u32 m0, s40, 1024
ds_read_b128 a[176:179], v195 offset:34816
ds_read_b128 a[180:183], v195 offset:38912
v_mfma_f32_16x16x32_bf16 v[96:99], a[132:135], a[160:163], v[96:99]
s_add_u32 s57, s81, 1048704
buffer_load_dwordx4 v213, s[28:31], s57 offen lds
v_mfma_f32_16x16x32_bf16 v[100:103], a[128:131], a[184:187], v[100:103]
ds_read_b128 a[188:191], v195 offset:35840
ds_read_b128 a[196:199], v195 offset:39936
v_mfma_f32_16x16x32_bf16 v[100:103], a[132:135], a[192:195], v[100:103]
s_xor_b32 s59, s73, 135168
buffer_load_dwordx4 v214, s[28:31], s57 offen offset:2048 lds
v_mfma_f32_16x16x32_bf16 v[104:107], a[128:131], a[200:203], v[104:107]
ds_read_b64_tr_b16 a[216:217], v207 offset:16384
ds_read_b64_tr_b16 a[218:219], v207 offset:20480
v_mfma_f32_16x16x32_bf16 v[104:107], a[132:135], a[204:207], v[104:107]
s_add_u32 m0, s40, 131072
ds_read_b64_tr_b16 a[220:221], v207 offset:24576
ds_read_b64_tr_b16 a[222:223], v207 offset:28672
v_mfma_f32_16x16x32_bf16 v[108:111], a[128:131], a[208:211], v[108:111]
s_xor_b32 s58, s73, 131072
buffer_load_dwordx4 v217, s[24:27], s58 offen sc1 lds
v_mfma_f32_16x16x32_bf16 v[108:111], a[132:135], a[212:215], v[108:111]
s_add_u32 m0, s40, 132096
v_mfma_f32_16x16x32_bf16 v[112:115], a[140:143], a[148:151], v[112:115]
buffer_load_dwordx4 v217, s[24:27], s59 offen sc1 lds
v_mfma_f32_16x16x32_bf16 v[112:115], a[144:147], a[160:163], v[112:115]
s_add_u32 m0, s40, 133120
v_mfma_f32_16x16x32_bf16 v[116:119], a[140:143], a[184:187], v[116:119]
s_xor_b32 s56, s73, 139264
buffer_load_dwordx4 v217, s[24:27], s56 offen sc1 lds
v_mfma_f32_16x16x32_bf16 v[116:119], a[144:147], a[192:195], v[116:119]
v_mfma_f32_16x16x32_bf16 v[120:123], a[140:143], a[200:203], v[120:123]
s_waitcnt lgkmcnt(12)
ds_read_b64_tr_b16 a[128:129], v204 offset:16896
ds_read_b64_tr_b16 a[130:131], v204 offset:20992
v_mfma_f32_16x16x32_bf16 v[120:123], a[144:147], a[204:207], v[120:123]
s_add_u32 m0, s40, 134144
ds_read_b64_tr_b16 a[132:133], v204 offset:25088
ds_read_b64_tr_b16 a[134:135], v204 offset:29184
v_mfma_f32_16x16x32_bf16 v[124:127], a[140:143], a[208:211], v[124:127]
s_xor_b32 s57, s73, 143360
buffer_load_dwordx4 v217, s[24:27], s57 offen sc1 lds
v_mfma_f32_16x16x32_bf16 v[124:127], a[144:147], a[212:215], v[124:127]
v_mfma_f32_16x16x32_bf16 v[64:67], a[136:139], a[152:155], v[64:67]
s_waitcnt lgkmcnt(11)
ds_read_b64_tr_b16 a[144:145], v207 offset:25088
ds_read_b64_tr_b16 a[146:147], v207 offset:29184
v_mfma_f32_16x16x32_bf16 v[64:67], a[164:167], a[156:159], v[64:67]
ds_read_b64_tr_b16 a[140:141], v207 offset:16896
ds_read_b64_tr_b16 a[142:143], v207 offset:20992
v_mfma_f32_16x16x32_bf16 v[68:71], a[136:139], a[168:171], v[68:71]
v_mfma_f32_16x16x32_bf16 v[68:71], a[164:167], a[172:175], v[68:71]
v_mfma_f32_16x16x32_bf16 v[72:75], a[136:139], a[176:179], v[72:75]
s_waitcnt lgkmcnt(10)
v_mfma_f32_16x16x32_bf16 v[72:75], a[164:167], a[180:183], v[72:75]
v_mfma_f32_16x16x32_bf16 v[76:79], a[136:139], a[188:191], v[76:79]
v_mfma_f32_16x16x32_bf16 v[76:79], a[164:167], a[196:199], v[76:79]
s_add_u32 m0, s40, 16384
v_mfma_f32_16x16x32_bf16 v[80:83], a[216:219], a[152:155], v[80:83]
s_waitcnt vmcnt(24) lgkmcnt(0)
s_barrier
v_mfma_f32_16x16x32_bf16 v[80:83], a[220:223], a[156:159], v[80:83]
s_add_u32 s58, s81, 1114112
buffer_load_dwordx4 v213, s[28:31], s58 offen lds
v_mfma_f32_16x16x32_bf16 v[84:87], a[216:219], a[168:171], v[84:87]
s_or_b32 s54, s61, s62
s_cmp_eq_u32 s54, 0
s_cbranch_scc0 L_done_%=
v_lshrrev_b32 v1, 6, v217
v_lshlrev_b32 v1, 9, v1
v_and_b32 v2, 0x30, v217
v_lshl_add_u32 v1, v2, 1, v1
s_mov_b32 s54, 33554432
s_mov_b32 s55, 0
L_ep_%=:
buffer_load_dwordx4 v[4:7], v1, s[24:27], s54 offen sc1
buffer_load_dwordx4 v[8:11], v1, s[24:27], s54 offen offset:16 sc1
s_add_u32 s55, s55, 1
s_waitcnt vmcnt(0)
v_cmp_ne_u64 s[88:89], s[52:53], v[4:5]
v_cmp_ne_u64 s[90:91], s[52:53], v[6:7]
v_cmp_ne_u64 s[92:93], s[52:53], v[8:9]
v_cmp_ne_u64 vcc, s[52:53], v[10:11]
s_nop 4
s_or_b64 s[88:89], s[88:89], s[90:91]
s_or_b64 s[92:93], s[92:93], vcc
s_or_b64 s[88:89], s[88:89], s[92:93]
s_cbranch_scc0 L_epok_%=
s_sleep 2
s_cmp_lt_u32 s55, 0x40000
s_cbranch_scc1 L_ep_%=
L_epok_%=:
s_mov_b32 s54, 33619968
buffer_store_dwordx2 v[254:255], v252, s[24:27], s54 offen sc1
L_done_%=:
v_mfma_f32_16x16x32_bf16 v[84:87], a[220:223], a[172:175], v[84:87]
ds_read_b128 a[148:151], v197 offset:16384
ds_read_b128 a[160:163], v197 offset:20480
v_mfma_f32_16x16x32_bf16 v[88:91], a[216:219], a[176:179], v[88:91]
ds_read_b64_tr_b16 a[136:137], v205 offset:0
ds_read_b64_tr_b16 a[138:139], v205 offset:4096
v_mfma_f32_16x16x32_bf16 v[88:91], a[220:223], a[180:183], v[88:91]
ds_read_b64_tr_b16 a[164:165], v205 offset:8192
ds_read_b64_tr_b16 a[166:167], v205 offset:12288
v_mfma_f32_16x16x32_bf16 v[92:95], a[216:219], a[188:191], v[92:95]
ds_read_b128 a[184:187], v197 offset:17408
ds_read_b128 a[192:195], v197 offset:21504
v_mfma_f32_16x16x32_bf16 v[92:95], a[220:223], a[196:199], v[92:95]
buffer_load_dwordx4 v214, s[28:31], s58 offen offset:2048 lds
v_mfma_f32_16x16x32_bf16 v[96:99], a[128:131], a[152:155], v[96:99]
s_add_u32 m0, s40, 17408
ds_read_b128 a[200:203], v197 offset:18432
ds_read_b128 a[204:207], v197 offset:22528
v_mfma_f32_16x16x32_bf16 v[96:99], a[132:135], a[156:159], v[96:99]
s_add_u32 s59, s81, 1114240
buffer_load_dwordx4 v213, s[28:31], s59 offen lds
v_mfma_f32_16x16x32_bf16 v[100:103], a[128:131], a[168:171], v[100:103]
ds_read_b128 a[208:211], v197 offset:19456
ds_read_b128 a[212:215], v197 offset:23552
v_mfma_f32_16x16x32_bf16 v[100:103], a[132:135], a[172:175], v[100:103]
s_xor_b32 s57, s73, 137216
buffer_load_dwordx4 v214, s[28:31], s59 offen offset:2048 lds
v_mfma_f32_16x16x32_bf16 v[104:107], a[128:131], a[176:179], v[104:107]
ds_read_b64_tr_b16 a[216:217], v208 offset:0
ds_read_b64_tr_b16 a[218:219], v208 offset:4096
v_mfma_f32_16x16x32_bf16 v[104:107], a[132:135], a[180:183], v[104:107]
s_add_u32 m0, s40, 32768
ds_read_b64_tr_b16 a[220:221], v208 offset:8192
ds_read_b64_tr_b16 a[222:223], v208 offset:12288
v_mfma_f32_16x16x32_bf16 v[108:111], a[128:131], a[188:191], v[108:111]
s_xor_b32 s56, s73, 133120
buffer_load_dwordx4 v217, s[24:27], s56 offen sc1 lds
v_mfma_f32_16x16x32_bf16 v[108:111], a[132:135], a[196:199], v[108:111]
s_add_u32 m0, s40, 33792
v_mfma_f32_16x16x32_bf16 v[112:115], a[140:143], a[152:155], v[112:115]
buffer_load_dwordx4 v217, s[24:27], s57 offen sc1 lds
v_mfma_f32_16x16x32_bf16 v[112:115], a[144:147], a[156:159], v[112:115]
s_add_u32 m0, s40, 34816
v_mfma_f32_16x16x32_bf16 v[116:119], a[140:143], a[168:171], v[116:119]
s_xor_b32 s58, s73, 141312
buffer_load_dwordx4 v217, s[24:27], s58 offen sc1 lds
v_mfma_f32_16x16x32_bf16 v[116:119], a[144:147], a[172:175], v[116:119]
v_mfma_f32_16x16x32_bf16 v[120:123], a[140:143], a[176:179], v[120:123]
s_waitcnt lgkmcnt(12)
ds_read_b64_tr_b16 a[128:129], v205 offset:512
ds_read_b64_tr_b16 a[130:131], v205 offset:4608
v_mfma_f32_16x16x32_bf16 v[120:123], a[144:147], a[180:183], v[120:123]
s_add_u32 m0, s40, 35840
ds_read_b64_tr_b16 a[132:133], v205 offset:8704
ds_read_b64_tr_b16 a[134:135], v205 offset:12800
v_mfma_f32_16x16x32_bf16 v[124:127], a[140:143], a[188:191], v[124:127]
s_xor_b32 s59, s73, 145408
buffer_load_dwordx4 v217, s[24:27], s59 offen sc1 lds
v_mfma_f32_16x16x32_bf16 v[124:127], a[144:147], a[196:199], v[124:127]
v_mfma_f32_16x16x32_bf16 v[64:67], a[136:139], a[148:151], v[64:67]
s_waitcnt lgkmcnt(11)
ds_read_b64_tr_b16 a[144:145], v208 offset:8704
ds_read_b64_tr_b16 a[146:147], v208 offset:12800
v_mfma_f32_16x16x32_bf16 v[64:67], a[164:167], a[160:163], v[64:67]
ds_read_b64_tr_b16 a[140:141], v208 offset:512
ds_read_b64_tr_b16 a[142:143], v208 offset:4608
v_mfma_f32_16x16x32_bf16 v[68:71], a[136:139], a[184:187], v[68:71]
v_mfma_f32_16x16x32_bf16 v[68:71], a[164:167], a[192:195], v[68:71]
v_mfma_f32_16x16x32_bf16 v[72:75], a[136:139], a[200:203], v[72:75]
s_waitcnt lgkmcnt(10)
v_mfma_f32_16x16x32_bf16 v[72:75], a[164:167], a[204:207], v[72:75]
v_mfma_f32_16x16x32_bf16 v[76:79], a[136:139], a[208:211], v[76:79]
v_mfma_f32_16x16x32_bf16 v[76:79], a[164:167], a[212:215], v[76:79]
s_add_u32 m0, s40, 65536
v_mfma_f32_16x16x32_bf16 v[80:83], a[216:219], a[148:151], v[80:83]
s_waitcnt vmcnt(24) lgkmcnt(0)
s_barrier
v_mfma_f32_16x16x32_bf16 v[80:83], a[220:223], a[160:163], v[80:83]
s_add_u32 s56, s80, 1310720
buffer_load_dwordx4 v213, s[28:31], s56 offen lds
v_mfma_f32_16x16x32_bf16 v[84:87], a[216:219], a[184:187], v[84:87]
v_mfma_f32_16x16x32_bf16 v[84:87], a[220:223], a[192:195], v[84:87]
ds_read_b128 a[152:155], v196 offset:16384
ds_read_b128 a[156:159], v196 offset:20480
v_mfma_f32_16x16x32_bf16 v[88:91], a[216:219], a[200:203], v[88:91]
ds_read_b64_tr_b16 a[136:137], v204 offset:49152
ds_read_b64_tr_b16 a[138:139], v204 offset:53248
v_mfma_f32_16x16x32_bf16 v[88:91], a[220:223], a[204:207], v[88:91]
ds_read_b64_tr_b16 a[164:165], v204 offset:57344
ds_read_b64_tr_b16 a[166:167], v204 offset:61440
v_mfma_f32_16x16x32_bf16 v[92:95], a[216:219], a[208:211], v[92:95]
ds_read_b128 a[168:171], v196 offset:17408
ds_read_b128 a[172:175], v196 offset:21504
v_mfma_f32_16x16x32_bf16 v[92:95], a[220:223], a[212:215], v[92:95]
buffer_load_dwordx4 v214, s[28:31], s56 offen offset:2048 lds
v_mfma_f32_16x16x32_bf16 v[96:99], a[128:131], a[148:151], v[96:99]
s_add_u32 m0, s40, 66560
ds_read_b128 a[176:179], v196 offset:18432
ds_read_b128 a[180:183], v196 offset:22528
v_mfma_f32_16x16x32_bf16 v[96:99], a[132:135], a[160:163], v[96:99]
s_add_u32 s57, s80, 1310848
buffer_load_dwordx4 v213, s[28:31], s57 offen lds
v_mfma_f32_16x16x32_bf16 v[100:103], a[128:131], a[184:187], v[100:103]
ds_read_b128 a[188:191], v196 offset:19456
ds_read_b128 a[196:199], v196 offset:23552
v_mfma_f32_16x16x32_bf16 v[100:103], a[132:135], a[192:195], v[100:103]
s_xor_b32 s59, s72, 176128
buffer_load_dwordx4 v214, s[28:31], s57 offen offset:2048 lds
v_mfma_f32_16x16x32_bf16 v[104:107], a[128:131], a[200:203], v[104:107]
ds_read_b64_tr_b16 a[216:217], v207 offset:49152
ds_read_b64_tr_b16 a[218:219], v207 offset:53248
v_mfma_f32_16x16x32_bf16 v[104:107], a[132:135], a[204:207], v[104:107]
s_add_u32 m0, s40, 147456
ds_read_b64_tr_b16 a[220:221], v207 offset:57344
ds_read_b64_tr_b16 a[222:223], v207 offset:61440
v_mfma_f32_16x16x32_bf16 v[108:111], a[128:131], a[208:211], v[108:111]
s_xor_b32 s58, s72, 172032
buffer_load_dwordx4 v217, s[24:27], s58 offen sc1 lds
v_mfma_f32_16x16x32_bf16 v[108:111], a[132:135], a[212:215], v[108:111]
s_add_u32 m0, s40, 148480
v_mfma_f32_16x16x32_bf16 v[112:115], a[140:143], a[148:151], v[112:115]
buffer_load_dwordx4 v217, s[24:27], s59 offen sc1 lds
v_mfma_f32_16x16x32_bf16 v[112:115], a[144:147], a[160:163], v[112:115]
s_add_u32 m0, s40, 149504
v_mfma_f32_16x16x32_bf16 v[116:119], a[140:143], a[184:187], v[116:119]
s_xor_b32 s56, s72, 163840
buffer_load_dwordx4 v217, s[24:27], s56 offen sc1 lds
v_mfma_f32_16x16x32_bf16 v[116:119], a[144:147], a[192:195], v[116:119]
v_mfma_f32_16x16x32_bf16 v[120:123], a[140:143], a[200:203], v[120:123]
s_waitcnt lgkmcnt(12)
ds_read_b64_tr_b16 a[128:129], v204 offset:49664
ds_read_b64_tr_b16 a[130:131], v204 offset:53760
v_mfma_f32_16x16x32_bf16 v[120:123], a[144:147], a[204:207], v[120:123]
s_add_u32 m0, s40, 150528
ds_read_b64_tr_b16 a[132:133], v204 offset:57856
ds_read_b64_tr_b16 a[134:135], v204 offset:61952
v_mfma_f32_16x16x32_bf16 v[124:127], a[140:143], a[208:211], v[124:127]
s_xor_b32 s57, s72, 167936
buffer_load_dwordx4 v217, s[24:27], s57 offen sc1 lds
v_mfma_f32_16x16x32_bf16 v[124:127], a[144:147], a[212:215], v[124:127]
v_mfma_f32_16x16x32_bf16 v[64:67], a[136:139], a[152:155], v[64:67]
s_waitcnt lgkmcnt(11)
ds_read_b64_tr_b16 a[144:145], v207 offset:57856
ds_read_b64_tr_b16 a[146:147], v207 offset:61952
v_mfma_f32_16x16x32_bf16 v[64:67], a[164:167], a[156:159], v[64:67]
ds_read_b64_tr_b16 a[140:141], v207 offset:49664
ds_read_b64_tr_b16 a[142:143], v207 offset:53760
v_mfma_f32_16x16x32_bf16 v[68:71], a[136:139], a[168:171], v[68:71]
v_mfma_f32_16x16x32_bf16 v[68:71], a[164:167], a[172:175], v[68:71]
v_mfma_f32_16x16x32_bf16 v[72:75], a[136:139], a[176:179], v[72:75]
s_waitcnt lgkmcnt(10)
v_mfma_f32_16x16x32_bf16 v[72:75], a[164:167], a[180:183], v[72:75]
v_mfma_f32_16x16x32_bf16 v[76:79], a[136:139], a[188:191], v[76:79]
v_mfma_f32_16x16x32_bf16 v[76:79], a[164:167], a[196:199], v[76:79]
s_add_u32 m0, s40, 49152
v_mfma_f32_16x16x32_bf16 v[80:83], a[216:219], a[152:155], v[80:83]
L_tch18_%=:
s_cbranch_execz L_tb2_%=
s_waitcnt vmcnt(24) lgkmcnt(0)
s_barrier
v_mfma_f32_16x16x32_bf16 v[80:83], a[220:223], a[156:159], v[80:83]
s_add_u32 s58, s80, 1376256
buffer_load_dwordx4 v213, s[28:31], s58 offen lds
v_mfma_f32_16x16x32_bf16 v[84:87], a[216:219], a[168:171], v[84:87]
v_mfma_f32_16x16x32_bf16 v[84:87], a[220:223], a[172:175], v[84:87]
ds_read_b128 a[148:151], v196 offset:49152
ds_read_b128 a[160:163], v196 offset:53248
v_mfma_f32_16x16x32_bf16 v[88:91], a[216:219], a[176:179], v[88:91]
ds_read_b64_tr_b16 a[136:137], v205 offset:32768
ds_read_b64_tr_b16 a[138:139], v205 offset:36864
v_mfma_f32_16x16x32_bf16 v[88:91], a[220:223], a[180:183], v[88:91]
ds_read_b64_tr_b16 a[164:165], v205 offset:40960
ds_read_b64_tr_b16 a[166:167], v205 offset:45056
v_mfma_f32_16x16x32_bf16 v[92:95], a[216:219], a[188:191], v[92:95]
ds_read_b128 a[184:187], v196 offset:50176
ds_read_b128 a[192:195], v196 offset:54272
v_mfma_f32_16x16x32_bf16 v[92:95], a[220:223], a[196:199], v[92:95]
buffer_load_dwordx4 v214, s[28:31], s58 offen offset:2048 lds
v_mfma_f32_16x16x32_bf16 v[96:99], a[128:131], a[152:155], v[96:99]
s_add_u32 m0, s40, 50176
ds_read_b128 a[200:203], v196 offset:51200
ds_read_b128 a[204:207], v196 offset:55296
v_mfma_f32_16x16x32_bf16 v[96:99], a[132:135], a[156:159], v[96:99]
s_add_u32 s59, s80, 1376384
buffer_load_dwordx4 v213, s[28:31], s59 offen lds
v_mfma_f32_16x16x32_bf16 v[100:103], a[128:131], a[168:171], v[100:103]
ds_read_b128 a[208:211], v196 offset:52224
ds_read_b128 a[212:215], v196 offset:56320
v_mfma_f32_16x16x32_bf16 v[100:103], a[132:135], a[172:175], v[100:103]
s_xor_b32 s57, s72, 178176
buffer_load_dwordx4 v214, s[28:31], s59 offen offset:2048 lds
v_mfma_f32_16x16x32_bf16 v[104:107], a[128:131], a[176:179], v[104:107]
ds_read_b64_tr_b16 a[216:217], v208 offset:32768
ds_read_b64_tr_b16 a[218:219], v208 offset:36864
v_mfma_f32_16x16x32_bf16 v[104:107], a[132:135], a[180:183], v[104:107]
s_add_u32 m0, s40, 81920
ds_read_b64_tr_b16 a[220:221], v208 offset:40960
ds_read_b64_tr_b16 a[222:223], v208 offset:45056
v_mfma_f32_16x16x32_bf16 v[108:111], a[128:131], a[188:191], v[108:111]
s_xor_b32 s56, s72, 174080
buffer_load_dwordx4 v217, s[24:27], s56 offen sc1 lds
v_mfma_f32_16x16x32_bf16 v[108:111], a[132:135], a[196:199], v[108:111]
s_add_u32 m0, s40, 82944
v_mfma_f32_16x16x32_bf16 v[112:115], a[140:143], a[152:155], v[112:115]
buffer_load_dwordx4 v217, s[24:27], s57 offen sc1 lds
v_mfma_f32_16x16x32_bf16 v[112:115], a[144:147], a[156:159], v[112:115]
s_add_u32 m0, s40, 83968
v_mfma_f32_16x16x32_bf16 v[116:119], a[140:143], a[168:171], v[116:119]
s_xor_b32 s58, s72, 165888
buffer_load_dwordx4 v217, s[24:27], s58 offen sc1 lds
v_mfma_f32_16x16x32_bf16 v[116:119], a[144:147], a[172:175], v[116:119]
v_mfma_f32_16x16x32_bf16 v[120:123], a[140:143], a[176:179], v[120:123]
s_waitcnt lgkmcnt(12)
ds_read_b64_tr_b16 a[128:129], v205 offset:33280
ds_read_b64_tr_b16 a[130:131], v205 offset:37376
v_mfma_f32_16x16x32_bf16 v[120:123], a[144:147], a[180:183], v[120:123]
s_add_u32 m0, s40, 84992
ds_read_b64_tr_b16 a[132:133], v205 offset:41472
ds_read_b64_tr_b16 a[134:135], v205 offset:45568
v_mfma_f32_16x16x32_bf16 v[124:127], a[140:143], a[188:191], v[124:127]
s_xor_b32 s59, s72, 169984
buffer_load_dwordx4 v217, s[24:27], s59 offen sc1 lds
v_mfma_f32_16x16x32_bf16 v[124:127], a[144:147], a[196:199], v[124:127]
v_mfma_f32_16x16x32_bf16 v[64:67], a[136:139], a[148:151], v[64:67]
s_waitcnt lgkmcnt(11)
ds_read_b64_tr_b16 a[144:145], v208 offset:41472
ds_read_b64_tr_b16 a[146:147], v208 offset:45568
v_mfma_f32_16x16x32_bf16 v[64:67], a[164:167], a[160:163], v[64:67]
ds_read_b64_tr_b16 a[140:141], v208 offset:33280
ds_read_b64_tr_b16 a[142:143], v208 offset:37376
v_mfma_f32_16x16x32_bf16 v[68:71], a[136:139], a[184:187], v[68:71]
v_mfma_f32_16x16x32_bf16 v[68:71], a[164:167], a[192:195], v[68:71]
v_mfma_f32_16x16x32_bf16 v[72:75], a[136:139], a[200:203], v[72:75]
s_waitcnt lgkmcnt(10)
v_mfma_f32_16x16x32_bf16 v[72:75], a[164:167], a[204:207], v[72:75]
v_mfma_f32_16x16x32_bf16 v[76:79], a[136:139], a[208:211], v[76:79]
v_mfma_f32_16x16x32_bf16 v[76:79], a[164:167], a[212:215], v[76:79]
s_add_u32 m0, s40, 98304
v_mfma_f32_16x16x32_bf16 v[80:83], a[216:219], a[148:151], v[80:83]
s_waitcnt vmcnt(24) lgkmcnt(0)
s_barrier
v_mfma_f32_16x16x32_bf16 v[80:83], a[220:223], a[160:163], v[80:83]
s_add_u32 s56, s81, 1310720
buffer_load_dwordx4 v213, s[28:31], s56 offen lds
v_mfma_f32_16x16x32_bf16 v[84:87], a[216:219], a[184:187], v[84:87]
v_mfma_f32_16x16x32_bf16 v[84:87], a[220:223], a[192:195], v[84:87]
ds_read_b128 a[152:155], v197 offset:0
ds_read_b128 a[156:159], v197 offset:4096
v_mfma_f32_16x16x32_bf16 v[88:91], a[216:219], a[200:203], v[88:91]
ds_read_b64_tr_b16 a[136:137], v204 offset:0
ds_read_b64_tr_b16 a[138:139], v204 offset:4096
v_mfma_f32_16x16x32_bf16 v[88:91], a[220:223], a[204:207], v[88:91]
ds_read_b64_tr_b16 a[164:165], v204 offset:8192
ds_read_b64_tr_b16 a[166:167], v204 offset:12288
v_mfma_f32_16x16x32_bf16 v[92:95], a[216:219], a[208:211], v[92:95]
ds_read_b128 a[168:171], v197 offset:1024
ds_read_b128 a[172:175], v197 offset:5120
v_mfma_f32_16x16x32_bf16 v[92:95], a[220:223], a[212:215], v[92:95]
buffer_load_dwordx4 v214, s[28:31], s56 offen offset:2048 lds
v_mfma_f32_16x16x32_bf16 v[96:99], a[128:131], a[148:151], v[96:99]
s_add_u32 m0, s40, 99328
ds_read_b128 a[176:179], v197 offset:2048
ds_read_b128 a[180:183], v197 offset:6144
v_mfma_f32_16x16x32_bf16 v[96:99], a[132:135], a[160:163], v[96:99]
s_add_u32 s57, s81, 1310848
buffer_load_dwordx4 v213, s[28:31], s57 offen lds
v_mfma_f32_16x16x32_bf16 v[100:103], a[128:131], a[184:187], v[100:103]
ds_read_b128 a[188:191], v197 offset:3072
ds_read_b128 a[196:199], v197 offset:7168
v_mfma_f32_16x16x32_bf16 v[100:103], a[132:135], a[192:195], v[100:103]
s_xor_b32 s59, s73, 176128
buffer_load_dwordx4 v214, s[28:31], s57 offen offset:2048 lds
v_mfma_f32_16x16x32_bf16 v[104:107], a[128:131], a[200:203], v[104:107]
ds_read_b64_tr_b16 a[216:217], v207 offset:0
ds_read_b64_tr_b16 a[218:219], v207 offset:4096
v_mfma_f32_16x16x32_bf16 v[104:107], a[132:135], a[204:207], v[104:107]
s_add_u32 m0, s40, 114688
ds_read_b64_tr_b16 a[220:221], v207 offset:8192
ds_read_b64_tr_b16 a[222:223], v207 offset:12288
v_mfma_f32_16x16x32_bf16 v[108:111], a[128:131], a[208:211], v[108:111]
s_xor_b32 s58, s73, 172032
buffer_load_dwordx4 v217, s[24:27], s58 offen sc1 lds
v_mfma_f32_16x16x32_bf16 v[108:111], a[132:135], a[212:215], v[108:111]
s_add_u32 m0, s40, 115712
v_mfma_f32_16x16x32_bf16 v[112:115], a[140:143], a[148:151], v[112:115]
buffer_load_dwordx4 v217, s[24:27], s59 offen sc1 lds
v_mfma_f32_16x16x32_bf16 v[112:115], a[144:147], a[160:163], v[112:115]
s_add_u32 m0, s40, 116736
v_mfma_f32_16x16x32_bf16 v[116:119], a[140:143], a[184:187], v[116:119]
s_xor_b32 s56, s73, 163840
buffer_load_dwordx4 v217, s[24:27], s56 offen sc1 lds
v_mfma_f32_16x16x32_bf16 v[116:119], a[144:147], a[192:195], v[116:119]
v_mfma_f32_16x16x32_bf16 v[120:123], a[140:143], a[200:203], v[120:123]
s_waitcnt lgkmcnt(12)
ds_read_b64_tr_b16 a[128:129], v204 offset:512
ds_read_b64_tr_b16 a[130:131], v204 offset:4608
v_mfma_f32_16x16x32_bf16 v[120:123], a[144:147], a[204:207], v[120:123]
s_add_u32 m0, s40, 117760
ds_read_b64_tr_b16 a[132:133], v204 offset:8704
ds_read_b64_tr_b16 a[134:135], v204 offset:12800
v_mfma_f32_16x16x32_bf16 v[124:127], a[140:143], a[208:211], v[124:127]
s_xor_b32 s57, s73, 167936
buffer_load_dwordx4 v217, s[24:27], s57 offen sc1 lds
v_mfma_f32_16x16x32_bf16 v[124:127], a[144:147], a[212:215], v[124:127]
v_mfma_f32_16x16x32_bf16 v[64:67], a[136:139], a[152:155], v[64:67]
s_waitcnt lgkmcnt(11)
ds_read_b64_tr_b16 a[144:145], v207 offset:8704
ds_read_b64_tr_b16 a[146:147], v207 offset:12800
v_mfma_f32_16x16x32_bf16 v[64:67], a[164:167], a[156:159], v[64:67]
ds_read_b64_tr_b16 a[140:141], v207 offset:512
ds_read_b64_tr_b16 a[142:143], v207 offset:4608
v_mfma_f32_16x16x32_bf16 v[68:71], a[136:139], a[168:171], v[68:71]
v_mfma_f32_16x16x32_bf16 v[68:71], a[164:167], a[172:175], v[68:71]
v_mfma_f32_16x16x32_bf16 v[72:75], a[136:139], a[176:179], v[72:75]
s_waitcnt lgkmcnt(10)
v_mfma_f32_16x16x32_bf16 v[72:75], a[164:167], a[180:183], v[72:75]
v_mfma_f32_16x16x32_bf16 v[76:79], a[136:139], a[188:191], v[76:79]
v_mfma_f32_16x16x32_bf16 v[76:79], a[164:167], a[196:199], v[76:79]
s_add_u32 m0, s40, 0
v_mfma_f32_16x16x32_bf16 v[80:83], a[216:219], a[152:155], v[80:83]
s_waitcnt vmcnt(24) lgkmcnt(0)
s_barrier
v_mfma_f32_16x16x32_bf16 v[80:83], a[220:223], a[156:159], v[80:83]
s_add_u32 s58, s81, 1376256
buffer_load_dwordx4 v213, s[28:31], s58 offen lds
v_mfma_f32_16x16x32_bf16 v[84:87], a[216:219], a[168:171], v[84:87]
v_mfma_f32_16x16x32_bf16 v[84:87], a[220:223], a[172:175], v[84:87]
ds_read_b128 a[148:151], v195 offset:32768
ds_read_b128 a[160:163], v195 offset:36864
v_mfma_f32_16x16x32_bf16 v[88:91], a[216:219], a[176:179], v[88:91]
ds_read_b64_tr_b16 a[136:137], v204 offset:16384
ds_read_b64_tr_b16 a[138:139], v204 offset:20480
v_mfma_f32_16x16x32_bf16 v[88:91], a[220:223], a[180:183], v[88:91]
ds_read_b64_tr_b16 a[164:165], v204 offset:24576
ds_read_b64_tr_b16 a[166:167], v204 offset:28672
v_mfma_f32_16x16x32_bf16 v[92:95], a[216:219], a[188:191], v[92:95]
ds_read_b128 a[184:187], v195 offset:33792
ds_read_b128 a[192:195], v195 offset:37888
v_mfma_f32_16x16x32_bf16 v[92:95], a[220:223], a[196:199], v[92:95]
buffer_load_dwordx4 v214, s[28:31], s58 offen offset:2048 lds
v_mfma_f32_16x16x32_bf16 v[96:99], a[128:131], a[152:155], v[96:99]
s_add_u32 m0, s40, 1024
ds_read_b128 a[200:203], v195 offset:34816
ds_read_b128 a[204:207], v195 offset:38912
v_mfma_f32_16x16x32_bf16 v[96:99], a[132:135], a[156:159], v[96:99]
s_add_u32 s59, s81, 1376384
buffer_load_dwordx4 v213, s[28:31], s59 offen lds
v_mfma_f32_16x16x32_bf16 v[100:103], a[128:131], a[168:171], v[100:103]
ds_read_b128 a[208:211], v195 offset:35840
ds_read_b128 a[212:215], v195 offset:39936
v_mfma_f32_16x16x32_bf16 v[100:103], a[132:135], a[172:175], v[100:103]
s_xor_b32 s57, s73, 178176
buffer_load_dwordx4 v214, s[28:31], s59 offen offset:2048 lds
v_mfma_f32_16x16x32_bf16 v[104:107], a[128:131], a[176:179], v[104:107]
ds_read_b64_tr_b16 a[216:217], v207 offset:16384
ds_read_b64_tr_b16 a[218:219], v207 offset:20480
v_mfma_f32_16x16x32_bf16 v[104:107], a[132:135], a[180:183], v[104:107]
s_add_u32 m0, s40, 131072
ds_read_b64_tr_b16 a[220:221], v207 offset:24576
ds_read_b64_tr_b16 a[222:223], v207 offset:28672
v_mfma_f32_16x16x32_bf16 v[108:111], a[128:131], a[188:191], v[108:111]
s_xor_b32 s56, s73, 174080
buffer_load_dwordx4 v217, s[24:27], s56 offen sc1 lds
v_mfma_f32_16x16x32_bf16 v[108:111], a[132:135], a[196:199], v[108:111]
s_add_u32 m0, s40, 132096
v_mfma_f32_16x16x32_bf16 v[112:115], a[140:143], a[152:155], v[112:115]
buffer_load_dwordx4 v217, s[24:27], s57 offen sc1 lds
v_mfma_f32_16x16x32_bf16 v[112:115], a[144:147], a[156:159], v[112:115]
s_add_u32 m0, s40, 133120
v_mfma_f32_16x16x32_bf16 v[116:119], a[140:143], a[168:171], v[116:119]
s_xor_b32 s58, s73, 165888
buffer_load_dwordx4 v217, s[24:27], s58 offen sc1 lds
v_mfma_f32_16x16x32_bf16 v[116:119], a[144:147], a[172:175], v[116:119]
v_mfma_f32_16x16x32_bf16 v[120:123], a[140:143], a[176:179], v[120:123]
s_waitcnt lgkmcnt(12)
ds_read_b64_tr_b16 a[128:129], v204 offset:16896
ds_read_b64_tr_b16 a[130:131], v204 offset:20992
v_mfma_f32_16x16x32_bf16 v[120:123], a[144:147], a[180:183], v[120:123]
s_add_u32 m0, s40, 134144
ds_read_b64_tr_b16 a[132:133], v204 offset:25088
ds_read_b64_tr_b16 a[134:135], v204 offset:29184
v_mfma_f32_16x16x32_bf16 v[124:127], a[140:143], a[188:191], v[124:127]
s_xor_b32 s59, s73, 169984
buffer_load_dwordx4 v217, s[24:27], s59 offen sc1 lds
v_mfma_f32_16x16x32_bf16 v[124:127], a[144:147], a[196:199], v[124:127]
v_mfma_f32_16x16x32_bf16 v[64:67], a[136:139], a[148:151], v[64:67]
s_waitcnt lgkmcnt(11)
ds_read_b64_tr_b16 a[144:145], v207 offset:25088
ds_read_b64_tr_b16 a[146:147], v207 offset:29184
v_mfma_f32_16x16x32_bf16 v[64:67], a[164:167], a[160:163], v[64:67]
ds_read_b64_tr_b16 a[140:141], v207 offset:16896
ds_read_b64_tr_b16 a[142:143], v207 offset:20992
v_mfma_f32_16x16x32_bf16 v[68:71], a[136:139], a[184:187], v[68:71]
v_mfma_f32_16x16x32_bf16 v[68:71], a[164:167], a[192:195], v[68:71]
v_mfma_f32_16x16x32_bf16 v[72:75], a[136:139], a[200:203], v[72:75]
s_waitcnt lgkmcnt(10)
v_mfma_f32_16x16x32_bf16 v[72:75], a[164:167], a[204:207], v[72:75]
v_mfma_f32_16x16x32_bf16 v[76:79], a[136:139], a[208:211], v[76:79]
v_mfma_f32_16x16x32_bf16 v[76:79], a[164:167], a[212:215], v[76:79]
s_add_u32 m0, s40, 16384
v_mfma_f32_16x16x32_bf16 v[80:83], a[216:219], a[148:151], v[80:83]
s_waitcnt vmcnt(24) lgkmcnt(0)
s_barrier
v_mfma_f32_16x16x32_bf16 v[80:83], a[220:223], a[160:163], v[80:83]
s_add_u32 s56, s80, 1572864
buffer_load_dwordx4 v213, s[28:31], s56 offen lds
v_mfma_f32_16x16x32_bf16 v[84:87], a[216:219], a[184:187], v[84:87]
v_mfma_f32_16x16x32_bf16 v[84:87], a[220:223], a[192:195], v[84:87]
ds_read_b128 a[152:155], v197 offset:16384
ds_read_b128 a[156:159], v197 offset:20480
v_mfma_f32_16x16x32_bf16 v[88:91], a[216:219], a[200:203], v[88:91]
ds_read_b64_tr_b16 a[136:137], v205 offset:0
ds_read_b64_tr_b16 a[138:139], v205 offset:4096
v_mfma_f32_16x16x32_bf16 v[88:91], a[220:223], a[204:207], v[88:91]
ds_read_b64_tr_b16 a[164:165], v205 offset:8192
ds_read_b64_tr_b16 a[166:167], v205 offset:12288
v_mfma_f32_16x16x32_bf16 v[92:95], a[216:219], a[208:211], v[92:95]
ds_read_b128 a[168:171], v197 offset:17408
ds_read_b128 a[172:175], v197 offset:21504
v_mfma_f32_16x16x32_bf16 v[92:95], a[220:223], a[212:215], v[92:95]
buffer_load_dwordx4 v214, s[28:31], s56 offen offset:2048 lds
v_mfma_f32_16x16x32_bf16 v[96:99], a[128:131], a[148:151], v[96:99]
s_add_u32 m0, s40, 17408
ds_read_b128 a[176:179], v197 offset:18432
ds_read_b128 a[180:183], v197 offset:22528
v_mfma_f32_16x16x32_bf16 v[96:99], a[132:135], a[160:163], v[96:99]
s_add_u32 s57, s80, 1572992
buffer_load_dwordx4 v213, s[28:31], s57 offen lds
v_mfma_f32_16x16x32_bf16 v[100:103], a[128:131], a[184:187], v[100:103]
ds_read_b128 a[188:191], v197 offset:19456
ds_read_b128 a[196:199], v197 offset:23552
v_mfma_f32_16x16x32_bf16 v[100:103], a[132:135], a[192:195], v[100:103]
s_xor_b32 s59, s72, 200704
buffer_load_dwordx4 v214, s[28:31], s57 offen offset:2048 lds
v_mfma_f32_16x16x32_bf16 v[104:107], a[128:131], a[200:203], v[104:107]
ds_read_b64_tr_b16 a[216:217], v208 offset:0
ds_read_b64_tr_b16 a[218:219], v208 offset:4096
v_mfma_f32_16x16x32_bf16 v[104:107], a[132:135], a[204:207], v[104:107]
s_add_u32 m0, s40, 32768
ds_read_b64_tr_b16 a[220:221], v208 offset:8192
ds_read_b64_tr_b16 a[222:223], v208 offset:12288
v_mfma_f32_16x16x32_bf16 v[108:111], a[128:131], a[208:211], v[108:111]
s_xor_b32 s58, s72, 196608
buffer_load_dwordx4 v217, s[24:27], s58 offen sc1 lds
v_mfma_f32_16x16x32_bf16 v[108:111], a[132:135], a[212:215], v[108:111]
s_add_u32 m0, s40, 33792
v_mfma_f32_16x16x32_bf16 v[112:115], a[140:143], a[148:151], v[112:115]
buffer_load_dwordx4 v217, s[24:27], s59 offen sc1 lds
v_mfma_f32_16x16x32_bf16 v[112:115], a[144:147], a[160:163], v[112:115]
s_add_u32 m0, s40, 34816
v_mfma_f32_16x16x32_bf16 v[116:119], a[140:143], a[184:187], v[116:119]
s_xor_b32 s56, s72, 204800
buffer_load_dwordx4 v217, s[24:27], s56 offen sc1 lds
v_mfma_f32_16x16x32_bf16 v[116:119], a[144:147], a[192:195], v[116:119]
v_mfma_f32_16x16x32_bf16 v[120:123], a[140:143], a[200:203], v[120:123]
s_waitcnt lgkmcnt(12)
ds_read_b64_tr_b16 a[128:129], v205 offset:512
ds_read_b64_tr_b16 a[130:131], v205 offset:4608
v_mfma_f32_16x16x32_bf16 v[120:123], a[144:147], a[204:207], v[120:123]
s_add_u32 m0, s40, 35840
ds_read_b64_tr_b16 a[132:133], v205 offset:8704
ds_read_b64_tr_b16 a[134:135], v205 offset:12800
v_mfma_f32_16x16x32_bf16 v[124:127], a[140:143], a[208:211], v[124:127]
s_xor_b32 s57, s72, 208896
buffer_load_dwordx4 v217, s[24:27], s57 offen sc1 lds
v_mfma_f32_16x16x32_bf16 v[124:127], a[144:147], a[212:215], v[124:127]
v_mfma_f32_16x16x32_bf16 v[64:67], a[136:139], a[152:155], v[64:67]
s_waitcnt lgkmcnt(11)
ds_read_b64_tr_b16 a[144:145], v208 offset:8704
ds_read_b64_tr_b16 a[146:147], v208 offset:12800
v_mfma_f32_16x16x32_bf16 v[64:67], a[164:167], a[156:159], v[64:67]
ds_read_b64_tr_b16 a[140:141], v208 offset:512
ds_read_b64_tr_b16 a[142:143], v208 offset:4608
v_mfma_f32_16x16x32_bf16 v[68:71], a[136:139], a[168:171], v[68:71]
v_mfma_f32_16x16x32_bf16 v[68:71], a[164:167], a[172:175], v[68:71]
v_mfma_f32_16x16x32_bf16 v[72:75], a[136:139], a[176:179], v[72:75]
s_waitcnt lgkmcnt(10)
v_mfma_f32_16x16x32_bf16 v[72:75], a[164:167], a[180:183], v[72:75]
v_mfma_f32_16x16x32_bf16 v[76:79], a[136:139], a[188:191], v[76:79]
v_mfma_f32_16x16x32_bf16 v[76:79], a[164:167], a[196:199], v[76:79]
s_add_u32 m0, s40, 65536
v_mfma_f32_16x16x32_bf16 v[80:83], a[216:219], a[152:155], v[80:83]
s_waitcnt vmcnt(24) lgkmcnt(0)
s_barrier
v_mfma_f32_16x16x32_bf16 v[80:83], a[220:223], a[156:159], v[80:83]
s_add_u32 s58, s80, 1638400
buffer_load_dwordx4 v213, s[28:31], s58 offen lds
v_mfma_f32_16x16x32_bf16 v[84:87], a[216:219], a[168:171], v[84:87]
v_mfma_f32_16x16x32_bf16 v[84:87], a[220:223], a[172:175], v[84:87]
ds_read_b128 a[148:151], v196 offset:16384
ds_read_b128 a[160:163], v196 offset:20480
v_mfma_f32_16x16x32_bf16 v[88:91], a[216:219], a[176:179], v[88:91]
ds_read_b64_tr_b16 a[136:137], v204 offset:49152
ds_read_b64_tr_b16 a[138:139], v204 offset:53248
v_mfma_f32_16x16x32_bf16 v[88:91], a[220:223], a[180:183], v[88:91]
ds_read_b64_tr_b16 a[164:165], v204 offset:57344
ds_read_b64_tr_b16 a[166:167], v204 offset:61440
v_mfma_f32_16x16x32_bf16 v[92:95], a[216:219], a[188:191], v[92:95]
ds_read_b128 a[184:187], v196 offset:17408
ds_read_b128 a[192:195], v196 offset:21504
v_mfma_f32_16x16x32_bf16 v[92:95], a[220:223], a[196:199], v[92:95]
buffer_load_dwordx4 v214, s[28:31], s58 offen offset:2048 lds
v_mfma_f32_16x16x32_bf16 v[96:99], a[128:131], a[152:155], v[96:99]
s_add_u32 m0, s40, 66560
ds_read_b128 a[200:203], v196 offset:18432
ds_read_b128 a[204:207], v196 offset:22528
v_mfma_f32_16x16x32_bf16 v[96:99], a[132:135], a[156:159], v[96:99]
s_add_u32 s59, s80, 1638528
buffer_load_dwordx4 v213, s[28:31], s59 offen lds
v_mfma_f32_16x16x32_bf16 v[100:103], a[128:131], a[168:171], v[100:103]
ds_read_b128 a[208:211], v196 offset:19456
ds_read_b128 a[212:215], v196 offset:23552
v_mfma_f32_16x16x32_bf16 v[100:103], a[132:135], a[172:175], v[100:103]
s_xor_b32 s57, s72, 202752
buffer_load_dwordx4 v214, s[28:31], s59 offen offset:2048 lds
v_mfma_f32_16x16x32_bf16 v[104:107], a[128:131], a[176:179], v[104:107]
ds_read_b64_tr_b16 a[216:217], v207 offset:49152
ds_read_b64_tr_b16 a[218:219], v207 offset:53248
v_mfma_f32_16x16x32_bf16 v[104:107], a[132:135], a[180:183], v[104:107]
s_add_u32 m0, s40, 147456
ds_read_b64_tr_b16 a[220:221], v207 offset:57344
ds_read_b64_tr_b16 a[222:223], v207 offset:61440
v_mfma_f32_16x16x32_bf16 v[108:111], a[128:131], a[188:191], v[108:111]
s_xor_b32 s56, s72, 198656
buffer_load_dwordx4 v217, s[24:27], s56 offen sc1 lds
v_mfma_f32_16x16x32_bf16 v[108:111], a[132:135], a[196:199], v[108:111]
s_add_u32 m0, s40, 148480
v_mfma_f32_16x16x32_bf16 v[112:115], a[140:143], a[152:155], v[112:115]
buffer_load_dwordx4 v217, s[24:27], s57 offen sc1 lds
v_mfma_f32_16x16x32_bf16 v[112:115], a[144:147], a[156:159], v[112:115]
s_add_u32 m0, s40, 149504
v_mfma_f32_16x16x32_bf16 v[116:119], a[140:143], a[168:171], v[116:119]
s_xor_b32 s58, s72, 206848
buffer_load_dwordx4 v217, s[24:27], s58 offen sc1 lds
v_mfma_f32_16x16x32_bf16 v[116:119], a[144:147], a[172:175], v[116:119]
v_mfma_f32_16x16x32_bf16 v[120:123], a[140:143], a[176:179], v[120:123]
s_waitcnt lgkmcnt(12)
ds_read_b64_tr_b16 a[128:129], v204 offset:49664
ds_read_b64_tr_b16 a[130:131], v204 offset:53760
v_mfma_f32_16x16x32_bf16 v[120:123], a[144:147], a[180:183], v[120:123]
s_add_u32 m0, s40, 150528
ds_read_b64_tr_b16 a[132:133], v204 offset:57856
ds_read_b64_tr_b16 a[134:135], v204 offset:61952
v_mfma_f32_16x16x32_bf16 v[124:127], a[140:143], a[188:191], v[124:127]
s_xor_b32 s59, s72, 210944
buffer_load_dwordx4 v217, s[24:27], s59 offen sc1 lds
v_mfma_f32_16x16x32_bf16 v[124:127], a[144:147], a[196:199], v[124:127]
v_mfma_f32_16x16x32_bf16 v[64:67], a[136:139], a[148:151], v[64:67]
s_waitcnt lgkmcnt(11)
ds_read_b64_tr_b16 a[144:145], v207 offset:57856
ds_read_b64_tr_b16 a[146:147], v207 offset:61952
v_mfma_f32_16x16x32_bf16 v[64:67], a[164:167], a[160:163], v[64:67]
ds_read_b64_tr_b16 a[140:141], v207 offset:49664
ds_read_b64_tr_b16 a[142:143], v207 offset:53760
v_mfma_f32_16x16x32_bf16 v[68:71], a[136:139], a[184:187], v[68:71]
v_mfma_f32_16x16x32_bf16 v[68:71], a[164:167], a[192:195], v[68:71]
v_mfma_f32_16x16x32_bf16 v[72:75], a[136:139], a[200:203], v[72:75]
s_waitcnt lgkmcnt(10)
v_mfma_f32_16x16x32_bf16 v[72:75], a[164:167], a[204:207], v[72:75]
v_mfma_f32_16x16x32_bf16 v[76:79], a[136:139], a[208:211], v[76:79]
v_mfma_f32_16x16x32_bf16 v[76:79], a[164:167], a[212:215], v[76:79]
s_add_u32 m0, s40, 49152
v_mfma_f32_16x16x32_bf16 v[80:83], a[216:219], a[148:151], v[80:83]
s_waitcnt vmcnt(24) lgkmcnt(0)
s_barrier
v_mfma_f32_16x16x32_bf16 v[80:83], a[220:223], a[160:163], v[80:83]
s_add_u32 s56, s81, 1572864
buffer_load_dwordx4 v213, s[28:31], s56 offen lds
v_mfma_f32_16x16x32_bf16 v[84:87], a[216:219], a[184:187], v[84:87]
v_mfma_f32_16x16x32_bf16 v[84:87], a[220:223], a[192:195], v[84:87]
ds_read_b128 a[152:155], v196 offset:49152
ds_read_b128 a[156:159], v196 offset:53248
v_mfma_f32_16x16x32_bf16 v[88:91], a[216:219], a[200:203], v[88:91]
ds_read_b64_tr_b16 a[136:137], v205 offset:32768
ds_read_b64_tr_b16 a[138:139], v205 offset:36864
v_mfma_f32_16x16x32_bf16 v[88:91], a[220:223], a[204:207], v[88:91]
ds_read_b64_tr_b16 a[164:165], v205 offset:40960
ds_read_b64_tr_b16 a[166:167], v205 offset:45056
v_mfma_f32_16x16x32_bf16 v[92:95], a[216:219], a[208:211], v[92:95]
ds_read_b128 a[168:171], v196 offset:50176
ds_read_b128 a[172:175], v196 offset:54272
v_mfma_f32_16x16x32_bf16 v[92:95], a[220:223], a[212:215], v[92:95]
buffer_load_dwordx4 v214, s[28:31], s56 offen offset:2048 lds
v_mfma_f32_16x16x32_bf16 v[96:99], a[128:131], a[148:151], v[96:99]
s_add_u32 m0, s40, 50176
ds_read_b128 a[176:179], v196 offset:51200
ds_read_b128 a[180:183], v196 offset:55296
v_mfma_f32_16x16x32_bf16 v[96:99], a[132:135], a[160:163], v[96:99]
s_add_u32 s57, s81, 1572992
buffer_load_dwordx4 v213, s[28:31], s57 offen lds
v_mfma_f32_16x16x32_bf16 v[100:103], a[128:131], a[184:187], v[100:103]
ds_read_b128 a[188:191], v196 offset:52224
ds_read_b128 a[196:199], v196 offset:56320
v_mfma_f32_16x16x32_bf16 v[100:103], a[132:135], a[192:195], v[100:103]
s_xor_b32 s59, s73, 200704
buffer_load_dwordx4 v214, s[28:31], s57 offen offset:2048 lds
v_mfma_f32_16x16x32_bf16 v[104:107], a[128:131], a[200:203], v[104:107]
ds_read_b64_tr_b16 a[216:217], v208 offset:32768
ds_read_b64_tr_b16 a[218:219], v208 offset:36864
v_mfma_f32_16x16x32_bf16 v[104:107], a[132:135], a[204:207], v[104:107]
s_add_u32 m0, s40, 81920
ds_read_b64_tr_b16 a[220:221], v208 offset:40960
ds_read_b64_tr_b16 a[222:223], v208 offset:45056
v_mfma_f32_16x16x32_bf16 v[108:111], a[128:131], a[208:211], v[108:111]
s_xor_b32 s58, s73, 196608
buffer_load_dwordx4 v217, s[24:27], s58 offen sc1 lds
v_mfma_f32_16x16x32_bf16 v[108:111], a[132:135], a[212:215], v[108:111]
s_add_u32 m0, s40, 82944
v_mfma_f32_16x16x32_bf16 v[112:115], a[140:143], a[148:151], v[112:115]
buffer_load_dwordx4 v217, s[24:27], s59 offen sc1 lds
v_mfma_f32_16x16x32_bf16 v[112:115], a[144:147], a[160:163], v[112:115]
s_add_u32 m0, s40, 83968
v_mfma_f32_16x16x32_bf16 v[116:119], a[140:143], a[184:187], v[116:119]
s_xor_b32 s56, s73, 204800
buffer_load_dwordx4 v217, s[24:27], s56 offen sc1 lds
v_mfma_f32_16x16x32_bf16 v[116:119], a[144:147], a[192:195], v[116:119]
v_mfma_f32_16x16x32_bf16 v[120:123], a[140:143], a[200:203], v[120:123]
s_waitcnt lgkmcnt(12)
ds_read_b64_tr_b16 a[128:129], v205 offset:33280
ds_read_b64_tr_b16 a[130:131], v205 offset:37376
v_mfma_f32_16x16x32_bf16 v[120:123], a[144:147], a[204:207], v[120:123]
s_add_u32 m0, s40, 84992
ds_read_b64_tr_b16 a[132:133], v205 offset:41472
ds_read_b64_tr_b16 a[134:135], v205 offset:45568
v_mfma_f32_16x16x32_bf16 v[124:127], a[140:143], a[208:211], v[124:127]
s_xor_b32 s57, s73, 208896
buffer_load_dwordx4 v217, s[24:27], s57 offen sc1 lds
v_mfma_f32_16x16x32_bf16 v[124:127], a[144:147], a[212:215], v[124:127]
v_mfma_f32_16x16x32_bf16 v[64:67], a[136:139], a[152:155], v[64:67]
s_waitcnt lgkmcnt(11)
ds_read_b64_tr_b16 a[144:145], v208 offset:41472
ds_read_b64_tr_b16 a[146:147], v208 offset:45568
v_mfma_f32_16x16x32_bf16 v[64:67], a[164:167], a[156:159], v[64:67]
ds_read_b64_tr_b16 a[140:141], v208 offset:33280
ds_read_b64_tr_b16 a[142:143], v208 offset:37376
v_mfma_f32_16x16x32_bf16 v[68:71], a[136:139], a[168:171], v[68:71]
v_mfma_f32_16x16x32_bf16 v[68:71], a[164:167], a[172:175], v[68:71]
v_mfma_f32_16x16x32_bf16 v[72:75], a[136:139], a[176:179], v[72:75]
s_waitcnt lgkmcnt(10)
v_mfma_f32_16x16x32_bf16 v[72:75], a[164:167], a[180:183], v[72:75]
v_mfma_f32_16x16x32_bf16 v[76:79], a[136:139], a[188:191], v[76:79]
v_mfma_f32_16x16x32_bf16 v[76:79], a[164:167], a[196:199], v[76:79]
s_add_u32 m0, s40, 98304
v_mfma_f32_16x16x32_bf16 v[80:83], a[216:219], a[152:155], v[80:83]
s_waitcnt vmcnt(24) lgkmcnt(0)
s_barrier
v_mfma_f32_16x16x32_bf16 v[80:83], a[220:223], a[156:159], v[80:83]
s_add_u32 s58, s81, 1638400
buffer_load_dwordx4 v213, s[28:31], s58 offen lds
v_mfma_f32_16x16x32_bf16 v[84:87], a[216:219], a[168:171], v[84:87]
v_mfma_f32_16x16x32_bf16 v[84:87], a[220:223], a[172:175], v[84:87]
ds_read_b128 a[148:151], v197 offset:0
ds_read_b128 a[160:163], v197 offset:4096
v_mfma_f32_16x16x32_bf16 v[88:91], a[216:219], a[176:179], v[88:91]
ds_read_b64_tr_b16 a[136:137], v204 offset:0
ds_read_b64_tr_b16 a[138:139], v204 offset:4096
v_mfma_f32_16x16x32_bf16 v[88:91], a[220:223], a[180:183], v[88:91]
ds_read_b64_tr_b16 a[164:165], v204 offset:8192
ds_read_b64_tr_b16 a[166:167], v204 offset:12288
v_mfma_f32_16x16x32_bf16 v[92:95], a[216:219], a[188:191], v[92:95]
ds_read_b128 a[184:187], v197 offset:1024
ds_read_b128 a[192:195], v197 offset:5120
v_mfma_f32_16x16x32_bf16 v[92:95], a[220:223], a[196:199], v[92:95]
buffer_load_dwordx4 v214, s[28:31], s58 offen offset:2048 lds
v_mfma_f32_16x16x32_bf16 v[96:99], a[128:131], a[152:155], v[96:99]
s_add_u32 m0, s40, 99328
ds_read_b128 a[200:203], v197 offset:2048
ds_read_b128 a[204:207], v197 offset:6144
v_mfma_f32_16x16x32_bf16 v[96:99], a[132:135], a[156:159], v[96:99]
s_add_u32 s59, s81, 1638528
buffer_load_dwordx4 v213, s[28:31], s59 offen lds
v_mfma_f32_16x16x32_bf16 v[100:103], a[128:131], a[168:171], v[100:103]
ds_read_b128 a[208:211], v197 offset:3072
ds_read_b128 a[212:215], v197 offset:7168
v_mfma_f32_16x16x32_bf16 v[100:103], a[132:135], a[172:175], v[100:103]
s_xor_b32 s57, s73, 202752
buffer_load_dwordx4 v214, s[28:31], s59 offen offset:2048 lds
v_mfma_f32_16x16x32_bf16 v[104:107], a[128:131], a[176:179], v[104:107]
ds_read_b64_tr_b16 a[216:217], v207 offset:0
ds_read_b64_tr_b16 a[218:219], v207 offset:4096
v_mfma_f32_16x16x32_bf16 v[104:107], a[132:135], a[180:183], v[104:107]
s_add_u32 m0, s40, 114688
ds_read_b64_tr_b16 a[220:221], v207 offset:8192
ds_read_b64_tr_b16 a[222:223], v207 offset:12288
v_mfma_f32_16x16x32_bf16 v[108:111], a[128:131], a[188:191], v[108:111]
s_xor_b32 s56, s73, 198656
buffer_load_dwordx4 v217, s[24:27], s56 offen sc1 lds
v_mfma_f32_16x16x32_bf16 v[108:111], a[132:135], a[196:199], v[108:111]
L_tch19_%=:
s_cbranch_execz L_tb2_%=
s_add_u32 m0, s40, 115712
v_mfma_f32_16x16x32_bf16 v[112:115], a[140:143], a[152:155], v[112:115]
buffer_load_dwordx4 v217, s[24:27], s57 offen sc1 lds
v_mfma_f32_16x16x32_bf16 v[112:115], a[144:147], a[156:159], v[112:115]
s_add_u32 m0, s40, 116736
v_mfma_f32_16x16x32_bf16 v[116:119], a[140:143], a[168:171], v[116:119]
s_xor_b32 s58, s73, 206848
buffer_load_dwordx4 v217, s[24:27], s58 offen sc1 lds
v_mfma_f32_16x16x32_bf16 v[116:119], a[144:147], a[172:175], v[116:119]
v_mfma_f32_16x16x32_bf16 v[120:123], a[140:143], a[176:179], v[120:123]
s_waitcnt lgkmcnt(12)
ds_read_b64_tr_b16 a[128:129], v204 offset:512
ds_read_b64_tr_b16 a[130:131], v204 offset:4608
v_mfma_f32_16x16x32_bf16 v[120:123], a[144:147], a[180:183], v[120:123]
s_add_u32 m0, s40, 117760
ds_read_b64_tr_b16 a[132:133], v204 offset:8704
ds_read_b64_tr_b16 a[134:135], v204 offset:12800
v_mfma_f32_16x16x32_bf16 v[124:127], a[140:143], a[188:191], v[124:127]
s_xor_b32 s59, s73, 210944
buffer_load_dwordx4 v217, s[24:27], s59 offen sc1 lds
v_mfma_f32_16x16x32_bf16 v[124:127], a[144:147], a[196:199], v[124:127]
v_mfma_f32_16x16x32_bf16 v[64:67], a[136:139], a[148:151], v[64:67]
s_waitcnt lgkmcnt(11)
ds_read_b64_tr_b16 a[144:145], v207 offset:8704
ds_read_b64_tr_b16 a[146:147], v207 offset:12800
v_mfma_f32_16x16x32_bf16 v[64:67], a[164:167], a[160:163], v[64:67]
ds_read_b64_tr_b16 a[140:141], v207 offset:512
ds_read_b64_tr_b16 a[142:143], v207 offset:4608
v_mfma_f32_16x16x32_bf16 v[68:71], a[136:139], a[184:187], v[68:71]
v_mfma_f32_16x16x32_bf16 v[68:71], a[164:167], a[192:195], v[68:71]
v_mfma_f32_16x16x32_bf16 v[72:75], a[136:139], a[200:203], v[72:75]
s_waitcnt lgkmcnt(10)
v_mfma_f32_16x16x32_bf16 v[72:75], a[164:167], a[204:207], v[72:75]
v_mfma_f32_16x16x32_bf16 v[76:79], a[136:139], a[208:211], v[76:79]
v_mfma_f32_16x16x32_bf16 v[76:79], a[164:167], a[212:215], v[76:79]
s_add_u32 m0, s40, 0
v_mfma_f32_16x16x32_bf16 v[80:83], a[216:219], a[148:151], v[80:83]
s_waitcnt vmcnt(24) lgkmcnt(0)
s_barrier
v_mfma_f32_16x16x32_bf16 v[80:83], a[220:223], a[160:163], v[80:83]
s_add_u32 s56, s80, 1835008
buffer_load_dwordx4 v213, s[28:31], s56 offen lds
v_mfma_f32_16x16x32_bf16 v[84:87], a[216:219], a[184:187], v[84:87]
v_mfma_f32_16x16x32_bf16 v[84:87], a[220:223], a[192:195], v[84:87]
ds_read_b128 a[152:155], v195 offset:32768
ds_read_b128 a[156:159], v195 offset:36864
v_mfma_f32_16x16x32_bf16 v[88:91], a[216:219], a[200:203], v[88:91]
ds_read_b64_tr_b16 a[136:137], v204 offset:16384
ds_read_b64_tr_b16 a[138:139], v204 offset:20480
v_mfma_f32_16x16x32_bf16 v[88:91], a[220:223], a[204:207], v[88:91]
ds_read_b64_tr_b16 a[164:165], v204 offset:24576
ds_read_b64_tr_b16 a[166:167], v204 offset:28672
v_mfma_f32_16x16x32_bf16 v[92:95], a[216:219], a[208:211], v[92:95]
ds_read_b128 a[168:171], v195 offset:33792
ds_read_b128 a[172:175], v195 offset:37888
v_mfma_f32_16x16x32_bf16 v[92:95], a[220:223], a[212:215], v[92:95]
buffer_load_dwordx4 v214, s[28:31], s56 offen offset:2048 lds
v_mfma_f32_16x16x32_bf16 v[96:99], a[128:131], a[148:151], v[96:99]
s_add_u32 m0, s40, 1024
ds_read_b128 a[176:179], v195 offset:34816
ds_read_b128 a[180:183], v195 offset:38912
v_mfma_f32_16x16x32_bf16 v[96:99], a[132:135], a[160:163], v[96:99]
s_add_u32 s57, s80, 1835136
buffer_load_dwordx4 v213, s[28:31], s57 offen lds
v_mfma_f32_16x16x32_bf16 v[100:103], a[128:131], a[184:187], v[100:103]
ds_read_b128 a[188:191], v195 offset:35840
ds_read_b128 a[196:199], v195 offset:39936
v_mfma_f32_16x16x32_bf16 v[100:103], a[132:135], a[192:195], v[100:103]
s_xor_b32 s59, s72, 241664
buffer_load_dwordx4 v214, s[28:31], s57 offen offset:2048 lds
v_mfma_f32_16x16x32_bf16 v[104:107], a[128:131], a[200:203], v[104:107]
ds_read_b64_tr_b16 a[216:217], v207 offset:16384
ds_read_b64_tr_b16 a[218:219], v207 offset:20480
v_mfma_f32_16x16x32_bf16 v[104:107], a[132:135], a[204:207], v[104:107]
s_add_u32 m0, s40, 131072
ds_read_b64_tr_b16 a[220:221], v207 offset:24576
ds_read_b64_tr_b16 a[222:223], v207 offset:28672
v_mfma_f32_16x16x32_bf16 v[108:111], a[128:131], a[208:211], v[108:111]
s_xor_b32 s58, s72, 237568
buffer_load_dwordx4 v217, s[24:27], s58 offen sc1 lds
v_mfma_f32_16x16x32_bf16 v[108:111], a[132:135], a[212:215], v[108:111]
s_add_u32 m0, s40, 132096
v_mfma_f32_16x16x32_bf16 v[112:115], a[140:143], a[148:151], v[112:115]
buffer_load_dwordx4 v217, s[24:27], s59 offen sc1 lds
v_mfma_f32_16x16x32_bf16 v[112:115], a[144:147], a[160:163], v[112:115]
s_add_u32 m0, s40, 133120
v_mfma_f32_16x16x32_bf16 v[116:119], a[140:143], a[184:187], v[116:119]
s_xor_b32 s56, s72, 229376
buffer_load_dwordx4 v217, s[24:27], s56 offen sc1 lds
v_mfma_f32_16x16x32_bf16 v[116:119], a[144:147], a[192:195], v[116:119]
v_mfma_f32_16x16x32_bf16 v[120:123], a[140:143], a[200:203], v[120:123]
s_waitcnt lgkmcnt(12)
ds_read_b64_tr_b16 a[128:129], v204 offset:16896
ds_read_b64_tr_b16 a[130:131], v204 offset:20992
v_mfma_f32_16x16x32_bf16 v[120:123], a[144:147], a[204:207], v[120:123]
s_add_u32 m0, s40, 134144
ds_read_b64_tr_b16 a[132:133], v204 offset:25088
ds_read_b64_tr_b16 a[134:135], v204 offset:29184
v_mfma_f32_16x16x32_bf16 v[124:127], a[140:143], a[208:211], v[124:127]
s_xor_b32 s57, s72, 233472
buffer_load_dwordx4 v217, s[24:27], s57 offen sc1 lds
v_mfma_f32_16x16x32_bf16 v[124:127], a[144:147], a[212:215], v[124:127]
v_mfma_f32_16x16x32_bf16 v[64:67], a[136:139], a[152:155], v[64:67]
s_waitcnt lgkmcnt(11)
ds_read_b64_tr_b16 a[144:145], v207 offset:25088
ds_read_b64_tr_b16 a[146:147], v207 offset:29184
v_mfma_f32_16x16x32_bf16 v[64:67], a[164:167], a[156:159], v[64:67]
ds_read_b64_tr_b16 a[140:141], v207 offset:16896
ds_read_b64_tr_b16 a[142:143], v207 offset:20992
v_mfma_f32_16x16x32_bf16 v[68:71], a[136:139], a[168:171], v[68:71]
v_mfma_f32_16x16x32_bf16 v[68:71], a[164:167], a[172:175], v[68:71]
v_mfma_f32_16x16x32_bf16 v[72:75], a[136:139], a[176:179], v[72:75]
s_waitcnt lgkmcnt(10)
v_mfma_f32_16x16x32_bf16 v[72:75], a[164:167], a[180:183], v[72:75]
v_mfma_f32_16x16x32_bf16 v[76:79], a[136:139], a[188:191], v[76:79]
v_mfma_f32_16x16x32_bf16 v[76:79], a[164:167], a[196:199], v[76:79]
s_add_u32 m0, s40, 16384
v_mfma_f32_16x16x32_bf16 v[80:83], a[216:219], a[152:155], v[80:83]
s_waitcnt vmcnt(24) lgkmcnt(0)
s_barrier
v_mfma_f32_16x16x32_bf16 v[80:83], a[220:223], a[156:159], v[80:83]
s_add_u32 s58, s80, 1900544
buffer_load_dwordx4 v213, s[28:31], s58 offen lds
v_mfma_f32_16x16x32_bf16 v[84:87], a[216:219], a[168:171], v[84:87]
v_mfma_f32_16x16x32_bf16 v[84:87], a[220:223], a[172:175], v[84:87]
ds_read_b128 a[148:151], v197 offset:16384
ds_read_b128 a[160:163], v197 offset:20480
v_mfma_f32_16x16x32_bf16 v[88:91], a[216:219], a[176:179], v[88:91]
ds_read_b64_tr_b16 a[136:137], v205 offset:0
ds_read_b64_tr_b16 a[138:139], v205 offset:4096
v_mfma_f32_16x16x32_bf16 v[88:91], a[220:223], a[180:183], v[88:91]
ds_read_b64_tr_b16 a[164:165], v205 offset:8192
ds_read_b64_tr_b16 a[166:167], v205 offset:12288
v_mfma_f32_16x16x32_bf16 v[92:95], a[216:219], a[188:191], v[92:95]
ds_read_b128 a[184:187], v197 offset:17408
ds_read_b128 a[192:195], v197 offset:21504
v_mfma_f32_16x16x32_bf16 v[92:95], a[220:223], a[196:199], v[92:95]
buffer_load_dwordx4 v214, s[28:31], s58 offen offset:2048 lds
v_mfma_f32_16x16x32_bf16 v[96:99], a[128:131], a[152:155], v[96:99]
s_add_u32 m0, s40, 17408
ds_read_b128 a[200:203], v197 offset:18432
ds_read_b128 a[204:207], v197 offset:22528
v_mfma_f32_16x16x32_bf16 v[96:99], a[132:135], a[156:159], v[96:99]
s_add_u32 s59, s80, 1900672
buffer_load_dwordx4 v213, s[28:31], s59 offen lds
v_mfma_f32_16x16x32_bf16 v[100:103], a[128:131], a[168:171], v[100:103]
ds_read_b128 a[208:211], v197 offset:19456
ds_read_b128 a[212:215], v197 offset:23552
v_mfma_f32_16x16x32_bf16 v[100:103], a[132:135], a[172:175], v[100:103]
s_xor_b32 s57, s72, 243712
buffer_load_dwordx4 v214, s[28:31], s59 offen offset:2048 lds
v_mfma_f32_16x16x32_bf16 v[104:107], a[128:131], a[176:179], v[104:107]
ds_read_b64_tr_b16 a[216:217], v208 offset:0
ds_read_b64_tr_b16 a[218:219], v208 offset:4096
v_mfma_f32_16x16x32_bf16 v[104:107], a[132:135], a[180:183], v[104:107]
s_add_u32 m0, s40, 32768
ds_read_b64_tr_b16 a[220:221], v208 offset:8192
ds_read_b64_tr_b16 a[222:223], v208 offset:12288
v_mfma_f32_16x16x32_bf16 v[108:111], a[128:131], a[188:191], v[108:111]
s_xor_b32 s56, s72, 239616
buffer_load_dwordx4 v217, s[24:27], s56 offen sc1 lds
v_mfma_f32_16x16x32_bf16 v[108:111], a[132:135], a[196:199], v[108:111]
s_add_u32 m0, s40, 33792
v_mfma_f32_16x16x32_bf16 v[112:115], a[140:143], a[152:155], v[112:115]
buffer_load_dwordx4 v217, s[24:27], s57 offen sc1 lds
v_mfma_f32_16x16x32_bf16 v[112:115], a[144:147], a[156:159], v[112:115]
s_add_u32 m0, s40, 34816
v_mfma_f32_16x16x32_bf16 v[116:119], a[140:143], a[168:171], v[116:119]
s_xor_b32 s58, s72, 231424
buffer_load_dwordx4 v217, s[24:27], s58 offen sc1 lds
v_mfma_f32_16x16x32_bf16 v[116:119], a[144:147], a[172:175], v[116:119]
v_mfma_f32_16x16x32_bf16 v[120:123], a[140:143], a[176:179], v[120:123]
s_waitcnt lgkmcnt(12)
ds_read_b64_tr_b16 a[128:129], v205 offset:512
ds_read_b64_tr_b16 a[130:131], v205 offset:4608
v_mfma_f32_16x16x32_bf16 v[120:123], a[144:147], a[180:183], v[120:123]
s_add_u32 m0, s40, 35840
ds_read_b64_tr_b16 a[132:133], v205 offset:8704
ds_read_b64_tr_b16 a[134:135], v205 offset:12800
v_mfma_f32_16x16x32_bf16 v[124:127], a[140:143], a[188:191], v[124:127]
s_xor_b32 s59, s72, 235520
buffer_load_dwordx4 v217, s[24:27], s59 offen sc1 lds
v_mfma_f32_16x16x32_bf16 v[124:127], a[144:147], a[196:199], v[124:127]
v_mfma_f32_16x16x32_bf16 v[64:67], a[136:139], a[148:151], v[64:67]
s_waitcnt lgkmcnt(11)
ds_read_b64_tr_b16 a[144:145], v208 offset:8704
ds_read_b64_tr_b16 a[146:147], v208 offset:12800
v_mfma_f32_16x16x32_bf16 v[64:67], a[164:167], a[160:163], v[64:67]
ds_read_b64_tr_b16 a[140:141], v208 offset:512
ds_read_b64_tr_b16 a[142:143], v208 offset:4608
v_mfma_f32_16x16x32_bf16 v[68:71], a[136:139], a[184:187], v[68:71]
v_mfma_f32_16x16x32_bf16 v[68:71], a[164:167], a[192:195], v[68:71]
v_mfma_f32_16x16x32_bf16 v[72:75], a[136:139], a[200:203], v[72:75]
s_waitcnt lgkmcnt(10)
v_mfma_f32_16x16x32_bf16 v[72:75], a[164:167], a[204:207], v[72:75]
v_mfma_f32_16x16x32_bf16 v[76:79], a[136:139], a[208:211], v[76:79]
v_mfma_f32_16x16x32_bf16 v[76:79], a[164:167], a[212:215], v[76:79]
s_add_u32 m0, s40, 65536
v_mfma_f32_16x16x32_bf16 v[80:83], a[216:219], a[148:151], v[80:83]
s_waitcnt vmcnt(24) lgkmcnt(0)
s_barrier
v_mfma_f32_16x16x32_bf16 v[80:83], a[220:223], a[160:163], v[80:83]
s_add_u32 s56, s81, 1835008
buffer_load_dwordx4 v213, s[28:31], s56 offen lds
v_mfma_f32_16x16x32_bf16 v[84:87], a[216:219], a[184:187], v[84:87]
v_mfma_f32_16x16x32_bf16 v[84:87], a[220:223], a[192:195], v[84:87]
ds_read_b128 a[152:155], v196 offset:16384
ds_read_b128 a[156:159], v196 offset:20480
v_mfma_f32_16x16x32_bf16 v[88:91], a[216:219], a[200:203], v[88:91]
ds_read_b64_tr_b16 a[136:137], v204 offset:49152
ds_read_b64_tr_b16 a[138:139], v204 offset:53248
v_mfma_f32_16x16x32_bf16 v[88:91], a[220:223], a[204:207], v[88:91]
ds_read_b64_tr_b16 a[164:165], v204 offset:57344
ds_read_b64_tr_b16 a[166:167], v204 offset:61440
v_mfma_f32_16x16x32_bf16 v[92:95], a[216:219], a[208:211], v[92:95]
ds_read_b128 a[168:171], v196 offset:17408
ds_read_b128 a[172:175], v196 offset:21504
v_mfma_f32_16x16x32_bf16 v[92:95], a[220:223], a[212:215], v[92:95]
buffer_load_dwordx4 v214, s[28:31], s56 offen offset:2048 lds
v_mfma_f32_16x16x32_bf16 v[96:99], a[128:131], a[148:151], v[96:99]
s_add_u32 m0, s40, 66560
ds_read_b128 a[176:179], v196 offset:18432
ds_read_b128 a[180:183], v196 offset:22528
v_mfma_f32_16x16x32_bf16 v[96:99], a[132:135], a[160:163], v[96:99]
s_add_u32 s57, s81, 1835136
buffer_load_dwordx4 v213, s[28:31], s57 offen lds
v_mfma_f32_16x16x32_bf16 v[100:103], a[128:131], a[184:187], v[100:103]
ds_read_b128 a[188:191], v196 offset:19456
ds_read_b128 a[196:199], v196 offset:23552
v_mfma_f32_16x16x32_bf16 v[100:103], a[132:135], a[192:195], v[100:103]
s_xor_b32 s59, s73, 241664
buffer_load_dwordx4 v214, s[28:31], s57 offen offset:2048 lds
v_mfma_f32_16x16x32_bf16 v[104:107], a[128:131], a[200:203], v[104:107]
ds_read_b64_tr_b16 a[216:217], v207 offset:49152
ds_read_b64_tr_b16 a[218:219], v207 offset:53248
v_mfma_f32_16x16x32_bf16 v[104:107], a[132:135], a[204:207], v[104:107]
s_add_u32 m0, s40, 147456
ds_read_b64_tr_b16 a[220:221], v207 offset:57344
ds_read_b64_tr_b16 a[222:223], v207 offset:61440
v_mfma_f32_16x16x32_bf16 v[108:111], a[128:131], a[208:211], v[108:111]
s_xor_b32 s58, s73, 237568
buffer_load_dwordx4 v217, s[24:27], s58 offen sc1 lds
v_mfma_f32_16x16x32_bf16 v[108:111], a[132:135], a[212:215], v[108:111]
s_add_u32 m0, s40, 148480
v_mfma_f32_16x16x32_bf16 v[112:115], a[140:143], a[148:151], v[112:115]
buffer_load_dwordx4 v217, s[24:27], s59 offen sc1 lds
v_mfma_f32_16x16x32_bf16 v[112:115], a[144:147], a[160:163], v[112:115]
s_add_u32 m0, s40, 149504
v_mfma_f32_16x16x32_bf16 v[116:119], a[140:143], a[184:187], v[116:119]
s_xor_b32 s56, s73, 229376
buffer_load_dwordx4 v217, s[24:27], s56 offen sc1 lds
v_mfma_f32_16x16x32_bf16 v[116:119], a[144:147], a[192:195], v[116:119]
v_mfma_f32_16x16x32_bf16 v[120:123], a[140:143], a[200:203], v[120:123]
s_waitcnt lgkmcnt(12)
ds_read_b64_tr_b16 a[128:129], v204 offset:49664
ds_read_b64_tr_b16 a[130:131], v204 offset:53760
v_mfma_f32_16x16x32_bf16 v[120:123], a[144:147], a[204:207], v[120:123]
s_add_u32 m0, s40, 150528
ds_read_b64_tr_b16 a[132:133], v204 offset:57856
ds_read_b64_tr_b16 a[134:135], v204 offset:61952
v_mfma_f32_16x16x32_bf16 v[124:127], a[140:143], a[208:211], v[124:127]
s_xor_b32 s57, s73, 233472
buffer_load_dwordx4 v217, s[24:27], s57 offen sc1 lds
v_mfma_f32_16x16x32_bf16 v[124:127], a[144:147], a[212:215], v[124:127]
v_mfma_f32_16x16x32_bf16 v[64:67], a[136:139], a[152:155], v[64:67]
s_waitcnt lgkmcnt(11)
ds_read_b64_tr_b16 a[144:145], v207 offset:57856
ds_read_b64_tr_b16 a[146:147], v207 offset:61952
v_mfma_f32_16x16x32_bf16 v[64:67], a[164:167], a[156:159], v[64:67]
ds_read_b64_tr_b16 a[140:141], v207 offset:49664
ds_read_b64_tr_b16 a[142:143], v207 offset:53760
v_mfma_f32_16x16x32_bf16 v[68:71], a[136:139], a[168:171], v[68:71]
v_mfma_f32_16x16x32_bf16 v[68:71], a[164:167], a[172:175], v[68:71]
v_mfma_f32_16x16x32_bf16 v[72:75], a[136:139], a[176:179], v[72:75]
s_waitcnt lgkmcnt(10)
v_mfma_f32_16x16x32_bf16 v[72:75], a[164:167], a[180:183], v[72:75]
v_mfma_f32_16x16x32_bf16 v[76:79], a[136:139], a[188:191], v[76:79]
v_mfma_f32_16x16x32_bf16 v[76:79], a[164:167], a[196:199], v[76:79]
v_mfma_f32_16x16x32_bf16 v[80:83], a[216:219], a[152:155], v[80:83]
s_waitcnt vmcnt(16) lgkmcnt(0)
s_barrier
v_mfma_f32_16x16x32_bf16 v[80:83], a[220:223], a[156:159], v[80:83]
ds_read_b64_tr_b16 a[224:225], v208 offset:32768
ds_read_b64_tr_b16 a[226:227], v208 offset:36864
v_mfma_f32_16x16x32_bf16 v[84:87], a[216:219], a[168:171], v[84:87]
ds_read_b64_tr_b16 a[212:213], v205 offset:40960
ds_read_b64_tr_b16 a[214:215], v205 offset:45056
v_mfma_f32_16x16x32_bf16 v[84:87], a[220:223], a[172:175], v[84:87]
ds_read_b128 a[148:151], v196 offset:49152
ds_read_b128 a[160:163], v196 offset:53248
v_mfma_f32_16x16x32_bf16 v[88:91], a[216:219], a[176:179], v[88:91]
ds_read_b64_tr_b16 a[136:137], v205 offset:33280
ds_read_b64_tr_b16 a[138:139], v205 offset:37376
v_mfma_f32_16x16x32_bf16 v[88:91], a[220:223], a[180:183], v[88:91]
ds_read_b64_tr_b16 a[164:165], v205 offset:41472
ds_read_b64_tr_b16 a[166:167], v205 offset:45568
v_mfma_f32_16x16x32_bf16 v[92:95], a[216:219], a[188:191], v[92:95]
ds_read_b128 a[184:187], v196 offset:50176
ds_read_b128 a[192:195], v196 offset:54272
v_mfma_f32_16x16x32_bf16 v[92:95], a[220:223], a[196:199], v[92:95]
ds_read_b64_tr_b16 a[208:209], v205 offset:32768
ds_read_b64_tr_b16 a[210:211], v205 offset:36864
v_mfma_f32_16x16x32_bf16 v[96:99], a[128:131], a[152:155], v[96:99]
ds_read_b128 a[200:203], v196 offset:51200
ds_read_b128 a[204:207], v196 offset:55296
v_mfma_f32_16x16x32_bf16 v[96:99], a[132:135], a[156:159], v[96:99]
ds_read_b64_tr_b16 a[216:217], v208 offset:40960
ds_read_b64_tr_b16 a[218:219], v208 offset:45056
v_mfma_f32_16x16x32_bf16 v[100:103], a[128:131], a[168:171], v[100:103]
s_add_u32 m0, s40, 49152
ds_read_b128 a[220:223], v196 offset:52224
ds_read_b128 a[228:231], v196 offset:56320
v_mfma_f32_16x16x32_bf16 v[100:103], a[132:135], a[172:175], v[100:103]
s_add_u32 s58, s81, 1900544
buffer_load_dwordx4 v213, s[28:31], s58 offen lds
v_mfma_f32_16x16x32_bf16 v[104:107], a[128:131], a[176:179], v[104:107]
ds_read_b64_tr_b16 a[232:233], v208 offset:33280
ds_read_b64_tr_b16 a[234:235], v208 offset:37376
v_mfma_f32_16x16x32_bf16 v[104:107], a[132:135], a[180:183], v[104:107]
ds_read_b64_tr_b16 a[236:237], v208 offset:41472
ds_read_b64_tr_b16 a[238:239], v208 offset:45568
v_mfma_f32_16x16x32_bf16 v[108:111], a[128:131], a[188:191], v[108:111]
s_add_u32 s59, s81, 1900672
buffer_load_dwordx4 v214, s[28:31], s58 offen offset:2048 lds
v_mfma_f32_16x16x32_bf16 v[108:111], a[132:135], a[196:199], v[108:111]
s_add_u32 m0, s40, 50176
v_mfma_f32_16x16x32_bf16 v[112:115], a[140:143], a[152:155], v[112:115]
buffer_load_dwordx4 v213, s[28:31], s59 offen lds
v_mfma_f32_16x16x32_bf16 v[112:115], a[144:147], a[156:159], v[112:115]
v_mfma_f32_16x16x32_bf16 v[116:119], a[140:143], a[168:171], v[116:119]
ds_read_b64_tr_b16 a[240:241], v207 offset:8704
ds_read_b64_tr_b16 a[242:243], v207 offset:12800
v_mfma_f32_16x16x32_bf16 v[116:119], a[144:147], a[172:175], v[116:119]
ds_read_b128 a[132:135], v197 offset:0
ds_read_b128 a[152:155], v197 offset:4096
v_mfma_f32_16x16x32_bf16 v[120:123], a[140:143], a[176:179], v[120:123]
s_waitcnt lgkmcnt(15)
ds_read_b64_tr_b16 a[128:129], v204 offset:512
ds_read_b64_tr_b16 a[130:131], v204 offset:4608
v_mfma_f32_16x16x32_bf16 v[120:123], a[144:147], a[180:183], v[120:123]
ds_read_b64_tr_b16 a[156:157], v204 offset:8704
ds_read_b64_tr_b16 a[158:159], v204 offset:12800
v_mfma_f32_16x16x32_bf16 v[124:127], a[140:143], a[188:191], v[124:127]
ds_read_b128 a[140:143], v197 offset:1024
ds_read_b128 a[168:171], v197 offset:5120
v_mfma_f32_16x16x32_bf16 v[124:127], a[144:147], a[196:199], v[124:127]
ds_read_b64_tr_b16 a[188:189], v207 offset:512
ds_read_b64_tr_b16 a[190:191], v207 offset:4608
v_mfma_f32_16x16x32_bf16 v[96:99], a[136:139], a[148:151], v[96:99]
s_waitcnt lgkmcnt(15)
ds_read_b128 a[144:147], v197 offset:2048
ds_read_b128 a[172:175], v197 offset:6144
v_mfma_f32_16x16x32_bf16 v[96:99], a[164:167], a[160:163], v[96:99]
ds_read_b128 a[176:179], v197 offset:3072
ds_read_b128 a[180:183], v197 offset:7168
v_mfma_f32_16x16x32_bf16 v[100:103], a[136:139], a[184:187], v[100:103]
s_xor_b32 s56, s73, 239616
buffer_load_dwordx4 v214, s[28:31], s59 offen offset:2048 lds
v_mfma_f32_16x16x32_bf16 v[100:103], a[164:167], a[192:195], v[100:103]
s_add_u32 m0, s40, 81920
v_mfma_f32_16x16x32_bf16 v[104:107], a[136:139], a[200:203], v[104:107]
s_waitcnt lgkmcnt(15)
buffer_load_dwordx4 v217, s[24:27], s56 offen sc1 lds
v_mfma_f32_16x16x32_bf16 v[104:107], a[164:167], a[204:207], v[104:107]
s_add_u32 m0, s40, 82944
v_mfma_f32_16x16x32_bf16 v[108:111], a[136:139], a[220:223], v[108:111]
s_xor_b32 s57, s73, 243712
buffer_load_dwordx4 v217, s[24:27], s57 offen sc1 lds
v_mfma_f32_16x16x32_bf16 v[108:111], a[164:167], a[228:231], v[108:111]
s_add_u32 m0, s40, 83968
v_mfma_f32_16x16x32_bf16 v[112:115], a[232:235], a[148:151], v[112:115]
s_waitcnt vmcnt(6) lgkmcnt(0)
s_barrier
v_mfma_f32_16x16x32_bf16 v[112:115], a[236:239], a[160:163], v[112:115]
s_xor_b32 s58, s73, 231424
buffer_load_dwordx4 v217, s[24:27], s58 offen sc1 lds
v_mfma_f32_16x16x32_bf16 v[116:119], a[232:235], a[184:187], v[116:119]
v_mfma_f32_16x16x32_bf16 v[116:119], a[236:239], a[192:195], v[116:119]
ds_read_b128 a[164:167], v195 offset:32768
ds_read_b128 a[196:199], v195 offset:36864
v_mfma_f32_16x16x32_bf16 v[120:123], a[232:235], a[200:203], v[120:123]
ds_read_b64_tr_b16 a[136:137], v204 offset:16896
ds_read_b64_tr_b16 a[138:139], v204 offset:20992
v_mfma_f32_16x16x32_bf16 v[120:123], a[236:239], a[204:207], v[120:123]
ds_read_b64_tr_b16 a[244:245], v204 offset:25088
ds_read_b64_tr_b16 a[246:247], v204 offset:29184
v_mfma_f32_16x16x32_bf16 v[124:127], a[232:235], a[220:223], v[124:127]
s_add_u32 m0, s40, 84992
ds_read_b128 a[232:235], v195 offset:33792
ds_read_b128 a[248:251], v195 offset:37888
v_mfma_f32_16x16x32_bf16 v[124:127], a[236:239], a[228:231], v[124:127]
s_xor_b32 s59, s73, 235520
buffer_load_dwordx4 v217, s[24:27], s59 offen sc1 lds
v_mfma_f32_16x16x32_bf16 v[96:99], a[128:131], a[132:135], v[96:99]
ds_read_b128 a[236:239], v195 offset:34816
ds_read_b128 a[252:255], v195 offset:38912
v_mfma_f32_16x16x32_bf16 v[96:99], a[156:159], a[152:155], v[96:99]
v_mfma_f32_16x16x32_bf16 v[100:103], a[128:131], a[140:143], v[100:103]
ds_read_b128 a[0:3], v195 offset:35840
ds_read_b128 a[4:7], v195 offset:39936
v_mfma_f32_16x16x32_bf16 v[100:103], a[156:159], a[168:171], v[100:103]
v_mfma_f32_16x16x32_bf16 v[104:107], a[128:131], a[144:147], v[104:107]
ds_read_b64_tr_b16 a[8:9], v207 offset:16896
ds_read_b64_tr_b16 a[10:11], v207 offset:20992
v_mfma_f32_16x16x32_bf16 v[104:107], a[156:159], a[172:175], v[104:107]
ds_read_b64_tr_b16 a[12:13], v207 offset:25088
ds_read_b64_tr_b16 a[14:15], v207 offset:29184
v_mfma_f32_16x16x32_bf16 v[108:111], a[128:131], a[176:179], v[108:111]
v_mfma_f32_16x16x32_bf16 v[108:111], a[156:159], a[180:183], v[108:111]
v_mfma_f32_16x16x32_bf16 v[112:115], a[188:191], a[132:135], v[112:115]
v_mfma_f32_16x16x32_bf16 v[112:115], a[240:243], a[152:155], v[112:115]
v_mfma_f32_16x16x32_bf16 v[116:119], a[188:191], a[140:143], v[116:119]
ds_read_b64_tr_b16 a[44:45], v208 offset:8704
ds_read_b64_tr_b16 a[46:47], v208 offset:12800
v_mfma_f32_16x16x32_bf16 v[116:119], a[240:243], a[168:171], v[116:119]
ds_read_b128 a[156:159], v197 offset:16384
ds_read_b128 a[16:19], v197 offset:20480
v_mfma_f32_16x16x32_bf16 v[120:123], a[188:191], a[144:147], v[120:123]
s_waitcnt lgkmcnt(15)
ds_read_b64_tr_b16 a[128:129], v205 offset:512
ds_read_b64_tr_b16 a[130:131], v205 offset:4608
v_mfma_f32_16x16x32_bf16 v[120:123], a[240:243], a[172:175], v[120:123]
ds_read_b64_tr_b16 a[20:21], v205 offset:8704
ds_read_b64_tr_b16 a[22:23], v205 offset:12800
v_mfma_f32_16x16x32_bf16 v[124:127], a[188:191], a[176:179], v[124:127]
ds_read_b128 a[188:191], v197 offset:17408
ds_read_b128 a[24:27], v197 offset:21504
v_mfma_f32_16x16x32_bf16 v[124:127], a[240:243], a[180:183], v[124:127]
ds_read_b64_tr_b16 a[40:41], v208 offset:512
ds_read_b64_tr_b16 a[42:43], v208 offset:4608
v_mfma_f32_16x16x32_bf16 v[96:99], a[136:139], a[164:167], v[96:99]
s_waitcnt lgkmcnt(15)
ds_read_b128 a[240:243], v197 offset:18432
ds_read_b128 a[28:31], v197 offset:22528
v_mfma_f32_16x16x32_bf16 v[96:99], a[244:247], a[196:199], v[96:99]
ds_read_b128 a[32:35], v197 offset:19456
ds_read_b128 a[36:39], v197 offset:23552
v_mfma_f32_16x16x32_bf16 v[100:103], a[136:139], a[232:235], v[100:103]
v_mfma_f32_16x16x32_bf16 v[100:103], a[244:247], a[248:251], v[100:103]
v_mfma_f32_16x16x32_bf16 v[104:107], a[136:139], a[236:239], v[104:107]
s_waitcnt lgkmcnt(15)
v_mfma_f32_16x16x32_bf16 v[104:107], a[244:247], a[252:255], v[104:107]
v_mfma_f32_16x16x32_bf16 v[108:111], a[136:139], a[0:3], v[108:111]
v_mfma_f32_16x16x32_bf16 v[108:111], a[244:247], a[4:7], v[108:111]
v_mfma_f32_16x16x32_bf16 v[112:115], a[8:11], a[164:167], v[112:115]
s_waitcnt vmcnt(0) lgkmcnt(0)
s_barrier
v_mfma_f32_16x16x32_bf16 v[112:115], a[12:15], a[196:199], v[112:115]
v_mfma_f32_16x16x32_bf16 v[116:119], a[8:11], a[232:235], v[116:119]
v_mfma_f32_16x16x32_bf16 v[116:119], a[12:15], a[248:251], v[116:119]
ds_read_b128 a[244:247], v196 offset:16384
ds_read_b128 a[48:51], v196 offset:20480
v_mfma_f32_16x16x32_bf16 v[120:123], a[8:11], a[236:239], v[120:123]
ds_read_b64_tr_b16 a[136:137], v204 offset:49664
ds_read_b64_tr_b16 a[138:139], v204 offset:53760
v_mfma_f32_16x16x32_bf16 v[120:123], a[12:15], a[252:255], v[120:123]
ds_read_b64_tr_b16 a[52:53], v204 offset:57856
ds_read_b64_tr_b16 a[54:55], v204 offset:61952
v_mfma_f32_16x16x32_bf16 v[124:127], a[8:11], a[0:3], v[124:127]
ds_read_b128 a[8:11], v196 offset:17408
ds_read_b128 a[56:59], v196 offset:21504
v_mfma_f32_16x16x32_bf16 v[124:127], a[12:15], a[4:7], v[124:127]
v_mfma_f32_16x16x32_bf16 v[96:99], a[128:131], a[156:159], v[96:99]
ds_read_b128 a[12:15], v196 offset:18432
ds_read_b128 a[60:63], v196 offset:22528
v_mfma_f32_16x16x32_bf16 v[96:99], a[20:23], a[16:19], v[96:99]
v_mfma_f32_16x16x32_bf16 v[100:103], a[128:131], a[188:191], v[100:103]
ds_read_b128 a[64:67], v196 offset:19456
ds_read_b128 a[68:71], v196 offset:23552
v_mfma_f32_16x16x32_bf16 v[100:103], a[20:23], a[24:27], v[100:103]
v_mfma_f32_16x16x32_bf16 v[104:107], a[128:131], a[240:243], v[104:107]
ds_read_b64_tr_b16 a[72:73], v207 offset:49664
ds_read_b64_tr_b16 a[74:75], v207 offset:53760
v_mfma_f32_16x16x32_bf16 v[104:107], a[20:23], a[28:31], v[104:107]
ds_read_b64_tr_b16 a[76:77], v207 offset:57856
ds_read_b64_tr_b16 a[78:79], v207 offset:61952
v_mfma_f32_16x16x32_bf16 v[108:111], a[128:131], a[32:35], v[108:111]
v_mfma_f32_16x16x32_bf16 v[108:111], a[20:23], a[36:39], v[108:111]
v_mfma_f32_16x16x32_bf16 v[112:115], a[40:43], a[156:159], v[112:115]
v_mfma_f32_16x16x32_bf16 v[112:115], a[44:47], a[16:19], v[112:115]
v_mfma_f32_16x16x32_bf16 v[116:119], a[40:43], a[188:191], v[116:119]
v_mfma_f32_16x16x32_bf16 v[116:119], a[44:47], a[24:27], v[116:119]
v_mfma_f32_16x16x32_bf16 v[120:123], a[40:43], a[240:243], v[120:123]
s_waitcnt lgkmcnt(12)
v_mfma_f32_16x16x32_bf16 v[120:123], a[44:47], a[28:31], v[120:123]
v_mfma_f32_16x16x32_bf16 v[124:127], a[40:43], a[32:35], v[124:127]
v_mfma_f32_16x16x32_bf16 v[124:127], a[44:47], a[36:39], v[124:127]
v_mfma_f32_16x16x32_bf16 v[96:99], a[136:139], a[244:247], v[96:99]
s_waitcnt lgkmcnt(7)
v_mfma_f32_16x16x32_bf16 v[96:99], a[52:55], a[48:51], v[96:99]
v_mfma_f32_16x16x32_bf16 v[100:103], a[136:139], a[8:11], v[100:103]
v_mfma_f32_16x16x32_bf16 v[100:103], a[52:55], a[56:59], v[100:103]
v_mfma_f32_16x16x32_bf16 v[104:107], a[136:139], a[12:15], v[104:107]
s_waitcnt lgkmcnt(2)
v_mfma_f32_16x16x32_bf16 v[104:107], a[52:55], a[60:63], v[104:107]
v_mfma_f32_16x16x32_bf16 v[108:111], a[136:139], a[64:67], v[108:111]
v_mov_b32 v230, 0xff
s_nop 0
v_bfi_b32 v96, v230, v254, v96
v_bfi_b32 v97, v230, v254, v97
v_bfi_b32 v98, v230, v254, v98
v_bfi_b32 v99, v230, v254, v99
v_mfma_f32_16x16x32_bf16 v[108:111], a[52:55], a[68:71], v[108:111]
s_xor_b32 s60, s98, 0
buffer_store_dwordx4 v[96:99], v217, s[24:27], s60 offen sc1
v_mfma_f32_16x16x32_bf16 v[112:115], a[72:75], a[244:247], v[112:115]
s_waitcnt lgkmcnt(0)
v_bfi_b32 v100, v230, v254, v100
v_bfi_b32 v101, v230, v254, v101
v_bfi_b32 v102, v230, v254, v102
v_bfi_b32 v103, v230, v254, v103
v_mfma_f32_16x16x32_bf16 v[112:115], a[76:79], a[48:51], v[112:115]
s_xor_b32 s60, s98, 1024
buffer_store_dwordx4 v[100:103], v217, s[24:27], s60 offen sc1
v_mfma_f32_16x16x32_bf16 v[116:119], a[72:75], a[8:11], v[116:119]
v_bfi_b32 v104, v230, v254, v104
v_bfi_b32 v105, v230, v254, v105
v_bfi_b32 v106, v230, v254, v106
v_bfi_b32 v107, v230, v254, v107
v_mfma_f32_16x16x32_bf16 v[116:119], a[76:79], a[56:59], v[116:119]
s_xor_b32 s60, s98, 2048
buffer_store_dwordx4 v[104:107], v217, s[24:27], s60 offen sc1
v_mfma_f32_16x16x32_bf16 v[120:123], a[72:75], a[12:15], v[120:123]
ds_read_b64_tr_b16 a[128:129], v204 offset:0
ds_read_b64_tr_b16 a[130:131], v204 offset:4096
v_bfi_b32 v108, v230, v254, v108
v_bfi_b32 v109, v230, v254, v109
v_bfi_b32 v110, v230, v254, v110
v_bfi_b32 v111, v230, v254, v111
v_mfma_f32_16x16x32_bf16 v[120:123], a[76:79], a[60:63], v[120:123]
L_tch20_%=:
s_cbranch_execz L_tb2_%=
s_xor_b32 s60, s98, 3072
ds_read_b64_tr_b16 a[136:137], v204 offset:8192
ds_read_b64_tr_b16 a[138:139], v204 offset:12288
buffer_store_dwordx4 v[108:111], v217, s[24:27], s60 offen sc1
v_mfma_f32_16x16x32_bf16 v[124:127], a[72:75], a[64:67], v[124:127]
v_bfi_b32 v112, v230, v254, v112
v_bfi_b32 v113, v230, v254, v113
v_bfi_b32 v114, v230, v254, v114
v_bfi_b32 v115, v230, v254, v115
v_mfma_f32_16x16x32_bf16 v[124:127], a[76:79], a[68:71], v[124:127]
s_xor_b32 s60, s98, 4096
buffer_store_dwordx4 v[112:115], v217, s[24:27], s60 offen sc1
v_mfma_f32_16x16x32_bf16 v[64:67], a[208:211], a[148:151], v[64:67]
v_bfi_b32 v116, v230, v254, v116
v_bfi_b32 v117, v230, v254, v117
v_bfi_b32 v118, v230, v254, v118
v_bfi_b32 v119, v230, v254, v119
v_mfma_f32_16x16x32_bf16 v[64:67], a[212:215], a[160:163], v[64:67]
s_xor_b32 s60, s98, 5120
buffer_store_dwordx4 v[116:119], v217, s[24:27], s60 offen sc1
v_mfma_f32_16x16x32_bf16 v[68:71], a[208:211], a[184:187], v[68:71]
v_bfi_b32 v120, v230, v254, v120
v_bfi_b32 v121, v230, v254, v121
v_bfi_b32 v122, v230, v254, v122
v_bfi_b32 v123, v230, v254, v123
v_mfma_f32_16x16x32_bf16 v[68:71], a[212:215], a[192:195], v[68:71]
s_xor_b32 s60, s98, 6144
buffer_store_dwordx4 v[120:123], v217, s[24:27], s60 offen sc1
v_mfma_f32_16x16x32_bf16 v[72:75], a[208:211], a[200:203], v[72:75]
ds_read_b64_tr_b16 a[20:21], v207 offset:0
ds_read_b64_tr_b16 a[22:23], v207 offset:4096
v_bfi_b32 v124, v230, v254, v124
v_bfi_b32 v125, v230, v254, v125
v_bfi_b32 v126, v230, v254, v126
v_bfi_b32 v127, v230, v254, v127
v_mfma_f32_16x16x32_bf16 v[72:75], a[212:215], a[204:207], v[72:75]
s_xor_b32 s60, s98, 7168
ds_read_b64_tr_b16 a[40:41], v207 offset:8192
ds_read_b64_tr_b16 a[42:43], v207 offset:12288
buffer_store_dwordx4 v[124:127], v217, s[24:27], s60 offen sc1
v_mfma_f32_16x16x32_bf16 v[76:79], a[208:211], a[220:223], v[76:79]
v_mfma_f32_16x16x32_bf16 v[76:79], a[212:215], a[228:231], v[76:79]
v_mfma_f32_16x16x32_bf16 v[80:83], a[224:227], a[148:151], v[80:83]
v_mfma_f32_16x16x32_bf16 v[80:83], a[216:219], a[160:163], v[80:83]
v_mfma_f32_16x16x32_bf16 v[84:87], a[224:227], a[184:187], v[84:87]
v_mfma_f32_16x16x32_bf16 v[84:87], a[216:219], a[192:195], v[84:87]
v_mfma_f32_16x16x32_bf16 v[88:91], a[224:227], a[200:203], v[88:91]
s_waitcnt lgkmcnt(6)
ds_read_b64_tr_b16 a[148:149], v204 offset:16384
ds_read_b64_tr_b16 a[150:151], v204 offset:20480
v_mfma_f32_16x16x32_bf16 v[88:91], a[216:219], a[204:207], v[88:91]
ds_read_b64_tr_b16 a[160:161], v204 offset:24576
ds_read_b64_tr_b16 a[162:163], v204 offset:28672
v_mfma_f32_16x16x32_bf16 v[92:95], a[224:227], a[220:223], v[92:95]
v_mfma_f32_16x16x32_bf16 v[92:95], a[216:219], a[228:231], v[92:95]
v_mfma_f32_16x16x32_bf16 v[64:67], a[128:131], a[132:135], v[64:67]
s_waitcnt lgkmcnt(8)
v_mfma_f32_16x16x32_bf16 v[64:67], a[136:139], a[152:155], v[64:67]
v_mfma_f32_16x16x32_bf16 v[68:71], a[128:131], a[140:143], v[68:71]
v_mfma_f32_16x16x32_bf16 v[68:71], a[136:139], a[168:171], v[68:71]
v_mfma_f32_16x16x32_bf16 v[72:75], a[128:131], a[144:147], v[72:75]
s_waitcnt lgkmcnt(6)
ds_read_b64_tr_b16 a[184:185], v207 offset:16384
ds_read_b64_tr_b16 a[186:187], v207 offset:20480
v_mfma_f32_16x16x32_bf16 v[72:75], a[136:139], a[172:175], v[72:75]
ds_read_b64_tr_b16 a[192:193], v207 offset:24576
ds_read_b64_tr_b16 a[194:195], v207 offset:28672
v_mfma_f32_16x16x32_bf16 v[76:79], a[128:131], a[176:179], v[76:79]
v_mfma_f32_16x16x32_bf16 v[76:79], a[136:139], a[180:183], v[76:79]
v_mfma_f32_16x16x32_bf16 v[80:83], a[20:23], a[132:135], v[80:83]
s_waitcnt lgkmcnt(8)
v_mfma_f32_16x16x32_bf16 v[80:83], a[40:43], a[152:155], v[80:83]
v_mfma_f32_16x16x32_bf16 v[84:87], a[20:23], a[140:143], v[84:87]
v_mfma_f32_16x16x32_bf16 v[84:87], a[40:43], a[168:171], v[84:87]
v_mfma_f32_16x16x32_bf16 v[88:91], a[20:23], a[144:147], v[88:91]
s_waitcnt lgkmcnt(6)
ds_read_b64_tr_b16 a[128:129], v205 offset:0
ds_read_b64_tr_b16 a[130:131], v205 offset:4096
v_mfma_f32_16x16x32_bf16 v[88:91], a[40:43], a[172:175], v[88:91]
ds_read_b64_tr_b16 a[132:133], v205 offset:8192
ds_read_b64_tr_b16 a[134:135], v205 offset:12288
v_mfma_f32_16x16x32_bf16 v[92:95], a[20:23], a[176:179], v[92:95]
v_mfma_f32_16x16x32_bf16 v[92:95], a[40:43], a[180:183], v[92:95]
v_mfma_f32_16x16x32_bf16 v[64:67], a[148:151], a[164:167], v[64:67]
s_waitcnt lgkmcnt(8)
v_mfma_f32_16x16x32_bf16 v[64:67], a[160:163], a[196:199], v[64:67]
v_mfma_f32_16x16x32_bf16 v[68:71], a[148:151], a[232:235], v[68:71]
v_mfma_f32_16x16x32_bf16 v[68:71], a[160:163], a[248:251], v[68:71]
v_mfma_f32_16x16x32_bf16 v[72:75], a[148:151], a[236:239], v[72:75]
s_waitcnt lgkmcnt(6)
ds_read_b64_tr_b16 a[136:137], v208 offset:0
ds_read_b64_tr_b16 a[138:139], v208 offset:4096
v_mfma_f32_16x16x32_bf16 v[72:75], a[160:163], a[252:255], v[72:75]
ds_read_b64_tr_b16 a[140:141], v208 offset:8192
ds_read_b64_tr_b16 a[142:143], v208 offset:12288
v_mfma_f32_16x16x32_bf16 v[76:79], a[148:151], a[0:3], v[76:79]
v_mfma_f32_16x16x32_bf16 v[76:79], a[160:163], a[4:7], v[76:79]
v_mfma_f32_16x16x32_bf16 v[80:83], a[184:187], a[164:167], v[80:83]
s_waitcnt lgkmcnt(8)
v_mfma_f32_16x16x32_bf16 v[80:83], a[192:195], a[196:199], v[80:83]
v_mfma_f32_16x16x32_bf16 v[84:87], a[184:187], a[232:235], v[84:87]
v_mfma_f32_16x16x32_bf16 v[84:87], a[192:195], a[248:251], v[84:87]
v_mfma_f32_16x16x32_bf16 v[88:91], a[184:187], a[236:239], v[88:91]
s_waitcnt lgkmcnt(6)
ds_read_b64_tr_b16 a[144:145], v204 offset:49152
ds_read_b64_tr_b16 a[146:147], v204 offset:53248
v_mfma_f32_16x16x32_bf16 v[88:91], a[192:195], a[252:255], v[88:91]
ds_read_b64_tr_b16 a[148:149], v204 offset:57344
ds_read_b64_tr_b16 a[150:151], v204 offset:61440
v_mfma_f32_16x16x32_bf16 v[92:95], a[184:187], a[0:3], v[92:95]
v_mfma_f32_16x16x32_bf16 v[92:95], a[192:195], a[4:7], v[92:95]
v_mfma_f32_16x16x32_bf16 v[64:67], a[128:131], a[156:159], v[64:67]
s_waitcnt lgkmcnt(8)
v_mfma_f32_16x16x32_bf16 v[64:67], a[132:135], a[16:19], v[64:67]
v_mfma_f32_16x16x32_bf16 v[68:71], a[128:131], a[188:191], v[68:71]
v_mfma_f32_16x16x32_bf16 v[68:71], a[132:135], a[24:27], v[68:71]
v_mfma_f32_16x16x32_bf16 v[72:75], a[128:131], a[240:243], v[72:75]
s_waitcnt lgkmcnt(6)
ds_read_b64_tr_b16 a[152:153], v207 offset:49152
ds_read_b64_tr_b16 a[154:155], v207 offset:53248
v_mfma_f32_16x16x32_bf16 v[72:75], a[132:135], a[28:31], v[72:75]
ds_read_b64_tr_b16 a[160:161], v207 offset:57344
ds_read_b64_tr_b16 a[162:163], v207 offset:61440
v_mfma_f32_16x16x32_bf16 v[76:79], a[128:131], a[32:35], v[76:79]
v_mfma_f32_16x16x32_bf16 v[76:79], a[132:135], a[36:39], v[76:79]
v_mfma_f32_16x16x32_bf16 v[80:83], a[136:139], a[156:159], v[80:83]
s_waitcnt lgkmcnt(8)
v_mfma_f32_16x16x32_bf16 v[80:83], a[140:143], a[16:19], v[80:83]
v_mfma_f32_16x16x32_bf16 v[84:87], a[136:139], a[188:191], v[84:87]
v_mfma_f32_16x16x32_bf16 v[84:87], a[140:143], a[24:27], v[84:87]
v_mfma_f32_16x16x32_bf16 v[88:91], a[136:139], a[240:243], v[88:91]
s_waitcnt lgkmcnt(6)
v_mfma_f32_16x16x32_bf16 v[88:91], a[140:143], a[28:31], v[88:91]
v_mfma_f32_16x16x32_bf16 v[92:95], a[136:139], a[32:35], v[92:95]
v_mfma_f32_16x16x32_bf16 v[92:95], a[140:143], a[36:39], v[92:95]
v_mfma_f32_16x16x32_bf16 v[64:67], a[144:147], a[244:247], v[64:67]
s_waitcnt lgkmcnt(4)
v_mfma_f32_16x16x32_bf16 v[64:67], a[148:151], a[48:51], v[64:67]
v_mfma_f32_16x16x32_bf16 v[68:71], a[144:147], a[8:11], v[68:71]
v_mfma_f32_16x16x32_bf16 v[68:71], a[148:151], a[56:59], v[68:71]
v_mfma_f32_16x16x32_bf16 v[72:75], a[144:147], a[12:15], v[72:75]
s_waitcnt lgkmcnt(2)
v_mfma_f32_16x16x32_bf16 v[72:75], a[148:151], a[60:63], v[72:75]
v_mfma_f32_16x16x32_bf16 v[76:79], a[144:147], a[64:67], v[76:79]
v_mfma_f32_16x16x32_bf16 v[76:79], a[148:151], a[68:71], v[76:79]
v_mfma_f32_16x16x32_bf16 v[80:83], a[152:155], a[244:247], v[80:83]
s_waitcnt lgkmcnt(0)
v_mfma_f32_16x16x32_bf16 v[80:83], a[160:163], a[48:51], v[80:83]
v_mfma_f32_16x16x32_bf16 v[84:87], a[152:155], a[8:11], v[84:87]
v_mfma_f32_16x16x32_bf16 v[84:87], a[160:163], a[56:59], v[84:87]
v_mfma_f32_16x16x32_bf16 v[88:91], a[152:155], a[12:15], v[88:91]
v_mfma_f32_16x16x32_bf16 v[88:91], a[160:163], a[60:63], v[88:91]
v_mfma_f32_16x16x32_bf16 v[92:95], a[152:155], a[64:67], v[92:95]
v_mfma_f32_16x16x32_bf16 v[92:95], a[160:163], a[68:71], v[92:95]
s_xor_b32 s98, s98, 0x8000
s_mov_b32 s55, 0
v_and_b32 v2, 0xff, v254
v_mov_b32 v3, 0
s_xor_b32 s60, s98, 7168
L_xl_%=:
buffer_load_dword v4, v3, s[24:27], s60 offen sc1
s_add_u32 s55, s55, 1
s_waitcnt vmcnt(0)
v_xor_b32 v4, v2, v4
v_and_b32 v4, 0xff, v4
v_cmp_ne_u32 vcc, 0, v4
s_cmp_lt_u32 s55, 0x40000
s_cbranch_scc0 L_xp_%=
s_nop 4
s_cbranch_vccnz L_xl_%=
L_xp_%=:
s_xor_b32 s60, s98, 0
buffer_load_dwordx4 v[128:131], v217, s[24:27], s60 offen sc1
s_xor_b32 s60, s98, 1024
buffer_load_dwordx4 v[132:135], v217, s[24:27], s60 offen sc1
s_xor_b32 s60, s98, 2048
buffer_load_dwordx4 v[136:139], v217, s[24:27], s60 offen sc1
s_xor_b32 s60, s98, 3072
buffer_load_dwordx4 v[140:143], v217, s[24:27], s60 offen sc1
s_xor_b32 s60, s98, 4096
buffer_load_dwordx4 v[144:147], v217, s[24:27], s60 offen sc1
s_xor_b32 s60, s98, 5120
buffer_load_dwordx4 v[148:151], v217, s[24:27], s60 offen sc1
s_xor_b32 s60, s98, 6144
buffer_load_dwordx4 v[152:155], v217, s[24:27], s60 offen sc1
s_xor_b32 s60, s98, 7168
buffer_load_dwordx4 v[156:159], v217, s[24:27], s60 offen sc1
s_add_u32 s55, s55, 1
s_waitcnt vmcnt(0)
L_xc_%=:
v_mov_b32 v1, 0
v_xor_b32 v128, v2, v128
v_xor_b32 v129, v2, v129
v_xor_b32 v130, v2, v130
v_xor_b32 v131, v2, v131
v_or3_b32 v1, v1, v128, v129
v_or3_b32 v1, v1, v130, v131
v_xor_b32 v132, v2, v132
v_xor_b32 v133, v2, v133
v_xor_b32 v134, v2, v134
v_xor_b32 v135, v2, v135
v_or3_b32 v1, v1, v132, v133
v_or3_b32 v1, v1, v134, v135
v_xor_b32 v136, v2, v136
v_xor_b32 v137, v2, v137
v_xor_b32 v138, v2, v138
v_xor_b32 v139, v2, v139
v_or3_b32 v1, v1, v136, v137
v_or3_b32 v1, v1, v138, v139
v_xor_b32 v140, v2, v140
v_xor_b32 v141, v2, v141
v_xor_b32 v142, v2, v142
v_xor_b32 v143, v2, v143
v_or3_b32 v1, v1, v140, v141
v_or3_b32 v1, v1, v142, v143
v_xor_b32 v144, v2, v144
v_xor_b32 v145, v2, v145
v_xor_b32 v146, v2, v146
v_xor_b32 v147, v2, v147
v_or3_b32 v1, v1, v144, v145
v_or3_b32 v1, v1, v146, v147
v_xor_b32 v148, v2, v148
v_xor_b32 v149, v2, v149
v_xor_b32 v150, v2, v150
v_xor_b32 v151, v2, v151
v_or3_b32 v1, v1, v148, v149
v_or3_b32 v1, v1, v150, v151
v_xor_b32 v152, v2, v152
v_xor_b32 v153, v2, v153
v_xor_b32 v154, v2, v154
v_xor_b32 v155, v2, v155
v_or3_b32 v1, v1, v152, v153
v_or3_b32 v1, v1, v154, v155
v_xor_b32 v156, v2, v156
v_xor_b32 v157, v2, v157
v_xor_b32 v158, v2, v158
v_xor_b32 v159, v2, v159
v_or3_b32 v1, v1, v156, v157
v_or3_b32 v1, v1, v158, v159
v_and_b32 v1, 0xff, v1
v_cmp_ne_u32 vcc, 0, v1
s_cmp_lt_u32 s55, 0x40000
s_cbranch_scc0 L_xok_%=
s_nop 4
s_cbranch_vccnz L_xp_%=
L_xok_%=:
v_add_f32 v64, v64, v128
v_add_f32 v65, v65, v129
v_add_f32 v66, v66, v130
v_add_f32 v67, v67, v131
v_add_f32 v80, v80, v144
v_add_f32 v81, v81, v145
v_add_f32 v82, v82, v146
v_add_f32 v83, v83, v147
v_cvt_pk_bf16_f32 v236, v64, v65
v_cvt_pk_bf16_f32 v237, v66, v67
v_cvt_pk_bf16_f32 v238, v80, v81
v_cvt_pk_bf16_f32 v239, v82, v83
s_mov_b32 s60, 0
s_nop 0
v_permlane16_swap_b32 v236, v238
v_permlane16_swap_b32 v237, v239
buffer_store_dwordx4 v[236:239], v216, s[36:39], s60 offen sc1
v_add_f32 v68, v68, v132
v_add_f32 v69, v69, v133
v_add_f32 v70, v70, v134
v_add_f32 v71, v71, v135
v_add_f32 v84, v84, v148
v_add_f32 v85, v85, v149
v_add_f32 v86, v86, v150
v_add_f32 v87, v87, v151
v_cvt_pk_bf16_f32 v240, v68, v69
v_cvt_pk_bf16_f32 v241, v70, v71
v_cvt_pk_bf16_f32 v242, v84, v85
v_cvt_pk_bf16_f32 v243, v86, v87
s_mov_b32 s60, 16384
s_nop 0
v_permlane16_swap_b32 v240, v242
v_permlane16_swap_b32 v241, v243
buffer_store_dwordx4 v[240:243], v216, s[36:39], s60 offen sc1
v_add_f32 v72, v72, v136
v_add_f32 v73, v73, v137
v_add_f32 v74, v74, v138
v_add_f32 v75, v75, v139
v_add_f32 v88, v88, v152
v_add_f32 v89, v89, v153
v_add_f32 v90, v90, v154
v_add_f32 v91, v91, v155
v_cvt_pk_bf16_f32 v244, v72, v73
v_cvt_pk_bf16_f32 v245, v74, v75
v_cvt_pk_bf16_f32 v246, v88, v89
v_cvt_pk_bf16_f32 v247, v90, v91
s_mov_b32 s60, 32768
s_nop 0
v_permlane16_swap_b32 v244, v246
v_permlane16_swap_b32 v245, v247
buffer_store_dwordx4 v[244:247], v216, s[36:39], s60 offen sc1
v_add_f32 v76, v76, v140
v_add_f32 v77, v77, v141
v_add_f32 v78, v78, v142
v_add_f32 v79, v79, v143
v_add_f32 v92, v92, v156
v_add_f32 v93, v93, v157
v_add_f32 v94, v94, v158
v_add_f32 v95, v95, v159
v_cvt_pk_bf16_f32 v248, v76, v77
v_cvt_pk_bf16_f32 v249, v78, v79
v_cvt_pk_bf16_f32 v250, v92, v93
v_cvt_pk_bf16_f32 v251, v94, v95
s_mov_b32 s60, 49152
s_nop 0
v_permlane16_swap_b32 v248, v250
v_permlane16_swap_b32 v249, v251
buffer_store_dwordx4 v[248:251], v216, s[36:39], s60 offen sc1
s_branch L_end_%=
L_slow0_%=:
s_mov_b32 s88, s41
s_mov_b32 s89, 0
s_branch L_slow_%=
L_slow1_%=:
s_mov_b32 s88, s42
s_mov_b32 s89, 1
L_slow_%=:
s_and_b32 s88, s88, 0xfffe0000
s_nop 15
v_and_b32 v236, 0xf0, v217
v_lshrrev_b32 v237, 3, v236
v_lshlrev_b32 v236, 4, v236
v_bfe_u32 v226, v217, 8, 1
v_lshl_add_u32 v236, v226, 5, v236
v_bfe_u32 v226, v217, 9, 1
v_lshl_add_u32 v236, v226, 4, v236
v_lshrrev_b32 v226, 8, v217
v_lshl_add_u32 v237, v226, 10, v237
v_mov_b32 v218, 0xff800000
v_mov_b32 v219, 0xff800000
v_mov_b32 v220, 0xff800000
v_mov_b32 v221, 0xff800000
s_mov_b32 s90, s88
s_mov_b32 s91, 0
L_sl1_%=:
buffer_load_dwordx4 v[112:115], v236, s[16:19], s90 offen offset:0
buffer_load_dwordx4 v[116:119], v236, s[16:19], s90 offen offset:64
buffer_load_dwordx4 v[120:123], v236, s[16:19], s90 offen offset:128
buffer_load_dwordx4 v[124:127], v236, s[16:19], s90 offen offset:192
s_waitcnt vmcnt(0)
v_mfma_f32_16x16x32_bf16 v[64:67], v[112:115], v[0:3], 0
v_mfma_f32_16x16x32_bf16 v[68:71], v[112:115], v[16:19], 0
v_mfma_f32_16x16x32_bf16 v[72:75], v[112:115], v[32:35], 0
v_mfma_f32_16x16x32_bf16 v[76:79], v[112:115], v[48:51], 0
v_mfma_f32_16x16x32_bf16 v[64:67], v[116:119], v[4:7], v[64:67]
v_mfma_f32_16x16x32_bf16 v[68:71], v[116:119], v[20:23], v[68:71]
v_mfma_f32_16x16x32_bf16 v[72:75], v[116:119], v[36:39], v[72:75]
v_mfma_f32_16x16x32_bf16 v[76:79], v[116:119], v[52:55], v[76:79]
v_mfma_f32_16x16x32_bf16 v[64:67], v[120:123], v[8:11], v[64:67]
v_mfma_f32_16x16x32_bf16 v[68:71], v[120:123], v[24:27], v[68:71]
v_mfma_f32_16x16x32_bf16 v[72:75], v[120:123], v[40:43], v[72:75]
v_mfma_f32_16x16x32_bf16 v[76:79], v[120:123], v[56:59], v[76:79]
v_mfma_f32_16x16x32_bf16 v[64:67], v[124:127], v[12:15], v[64:67]
v_mfma_f32_16x16x32_bf16 v[68:71], v[124:127], v[28:31], v[68:71]
v_mfma_f32_16x16x32_bf16 v[72:75], v[124:127], v[44:47], v[72:75]
v_mfma_f32_16x16x32_bf16 v[76:79], v[124:127], v[60:63], v[76:79]
s_nop 15
v_max3_f32 v218, v218, v64, v65
v_max3_f32 v218, v218, v66, v67
v_max3_f32 v219, v219, v68, v69
v_max3_f32 v219, v219, v70, v71
v_max3_f32 v220, v220, v72, v73
v_max3_f32 v220, v220, v74, v75
v_max3_f32 v221, v221, v76, v77
v_max3_f32 v221, v221, v78, v79
s_add_u32 s90, s90, 4096
s_add_u32 s91, s91, 1
s_cmp_lt_u32 s91, 32
s_cbranch_scc1 L_sl1_%=
v_mov_b32 v226, v218
v_mov_b32 v227, v219
v_mov_b32 v228, v220
v_mov_b32 v229, v221
s_nop 1
v_permlane16_swap_b32 v218, v226
v_permlane16_swap_b32 v219, v227
v_permlane16_swap_b32 v220, v228
v_permlane16_swap_b32 v221, v229
s_nop 1
v_max_f32 v218, v218, v226
v_max_f32 v219, v219, v227
v_max_f32 v220, v220, v228
v_max_f32 v221, v221, v229
v_mov_b32 v226, v218
v_mov_b32 v227, v219
v_mov_b32 v228, v220
v_mov_b32 v229, v221
s_nop 1
v_permlane32_swap_b32 v218, v226
v_permlane32_swap_b32 v219, v227
v_permlane32_swap_b32 v220, v228
v_permlane32_swap_b32 v221, v229
s_nop 1
v_max_f32 v218, v218, v226
v_max_f32 v219, v219, v227
v_max_f32 v220, v220, v228
v_max_f32 v221, v221, v229
v_sub_f32 v160, 0, v218
v_sub_f32 v161, 0, v218
v_sub_f32 v162, 0, v218
v_sub_f32 v163, 0, v218
v_sub_f32 v164, 0, v219
v_sub_f32 v165, 0, v219
v_sub_f32 v166, 0, v219
v_sub_f32 v167, 0, v219
v_sub_f32 v168, 0, v220
v_sub_f32 v169, 0, v220
v_sub_f32 v170, 0, v220
v_sub_f32 v171, 0, v220
v_sub_f32 v172, 0, v221
v_sub_f32 v173, 0, v221
v_sub_f32 v174, 0, v221
v_sub_f32 v175, 0, v221
v_mov_b32 v176, 0
v_mov_b32 v177, 0
v_mov_b32 v178, 0
v_mov_b32 v179, 0
v_mov_b32 v180, 0
v_mov_b32 v181, 0
v_mov_b32 v182, 0
v_mov_b32 v183, 0
v_mov_b32 v184, 0
v_mov_b32 v185, 0
v_mov_b32 v186, 0
v_mov_b32 v187, 0
v_mov_b32 v188, 0
v_mov_b32 v189, 0
v_mov_b32 v190, 0
v_mov_b32 v191, 0
v_accvgpr_write_b32 a0, 0
v_accvgpr_write_b32 a1, 0
v_accvgpr_write_b32 a2, 0
v_accvgpr_write_b32 a3, 0
v_accvgpr_write_b32 a4, 0
v_accvgpr_write_b32 a5, 0
v_accvgpr_write_b32 a6, 0
v_accvgpr_write_b32 a7, 0
v_accvgpr_write_b32 a8, 0
v_accvgpr_write_b32 a9, 0
v_accvgpr_write_b32 a10, 0
v_accvgpr_write_b32 a11, 0
v_accvgpr_write_b32 a12, 0
v_accvgpr_write_b32 a13, 0
v_accvgpr_write_b32 a14, 0
v_accvgpr_write_b32 a15, 0
v_accvgpr_write_b32 a16, 0
v_accvgpr_write_b32 a17, 0
v_accvgpr_write_b32 a18, 0
v_accvgpr_write_b32 a19, 0
v_accvgpr_write_b32 a20, 0
v_accvgpr_write_b32 a21, 0
v_accvgpr_write_b32 a22, 0
v_accvgpr_write_b32 a23, 0
v_accvgpr_write_b32 a24, 0
v_accvgpr_write_b32 a25, 0
v_accvgpr_write_b32 a26, 0
v_accvgpr_write_b32 a27, 0
v_accvgpr_write_b32 a28, 0
v_accvgpr_write_b32 a29, 0
v_accvgpr_write_b32 a30, 0
v_accvgpr_write_b32 a31, 0
v_accvgpr_write_b32 a32, 0
v_accvgpr_write_b32 a33, 0
v_accvgpr_write_b32 a34, 0
v_accvgpr_write_b32 a35, 0
v_accvgpr_write_b32 a36, 0
v_accvgpr_write_b32 a37, 0
v_accvgpr_write_b32 a38, 0
v_accvgpr_write_b32 a39, 0
v_accvgpr_write_b32 a40, 0
v_accvgpr_write_b32 a41, 0
v_accvgpr_write_b32 a42, 0
v_accvgpr_write_b32 a43, 0
v_accvgpr_write_b32 a44, 0
v_accvgpr_write_b32 a45, 0
v_accvgpr_write_b32 a46, 0
v_accvgpr_write_b32 a47, 0
v_accvgpr_write_b32 a48, 0
v_accvgpr_write_b32 a49, 0
v_accvgpr_write_b32 a50, 0
v_accvgpr_write_b32 a51, 0
v_accvgpr_write_b32 a52, 0
v_accvgpr_write_b32 a53, 0
v_accvgpr_write_b32 a54, 0
v_accvgpr_write_b32 a55, 0
v_accvgpr_write_b32 a56, 0
v_accvgpr_write_b32 a57, 0
v_accvgpr_write_b32 a58, 0
v_accvgpr_write_b32 a59, 0
v_accvgpr_write_b32 a60, 0
v_accvgpr_write_b32 a61, 0
v_accvgpr_write_b32 a62, 0
v_accvgpr_write_b32 a63, 0
v_accvgpr_write_b32 a64, 0
v_accvgpr_write_b32 a65, 0
v_accvgpr_write_b32 a66, 0
v_accvgpr_write_b32 a67, 0
v_accvgpr_write_b32 a68, 0
v_accvgpr_write_b32 a69, 0
v_accvgpr_write_b32 a70, 0
v_accvgpr_write_b32 a71, 0
v_accvgpr_write_b32 a72, 0
v_accvgpr_write_b32 a73, 0
v_accvgpr_write_b32 a74, 0
v_accvgpr_write_b32 a75, 0
v_accvgpr_write_b32 a76, 0
v_accvgpr_write_b32 a77, 0
v_accvgpr_write_b32 a78, 0
v_accvgpr_write_b32 a79, 0
v_accvgpr_write_b32 a80, 0
v_accvgpr_write_b32 a81, 0
v_accvgpr_write_b32 a82, 0
v_accvgpr_write_b32 a83, 0
v_accvgpr_write_b32 a84, 0
v_accvgpr_write_b32 a85, 0
v_accvgpr_write_b32 a86, 0
v_accvgpr_write_b32 a87, 0
v_accvgpr_write_b32 a88, 0
v_accvgpr_write_b32 a89, 0
v_accvgpr_write_b32 a90, 0
v_accvgpr_write_b32 a91, 0
v_accvgpr_write_b32 a92, 0
v_accvgpr_write_b32 a93, 0
v_accvgpr_write_b32 a94, 0
v_accvgpr_write_b32 a95, 0
v_accvgpr_write_b32 a96, 0
v_accvgpr_write_b32 a97, 0
v_accvgpr_write_b32 a98, 0
v_accvgpr_write_b32 a99, 0
v_accvgpr_write_b32 a100, 0
v_accvgpr_write_b32 a101, 0
v_accvgpr_write_b32 a102, 0
v_accvgpr_write_b32 a103, 0
v_accvgpr_write_b32 a104, 0
v_accvgpr_write_b32 a105, 0
v_accvgpr_write_b32 a106, 0
v_accvgpr_write_b32 a107, 0
v_accvgpr_write_b32 a108, 0
v_accvgpr_write_b32 a109, 0
v_accvgpr_write_b32 a110, 0
v_accvgpr_write_b32 a111, 0
v_accvgpr_write_b32 a112, 0
v_accvgpr_write_b32 a113, 0
v_accvgpr_write_b32 a114, 0
v_accvgpr_write_b32 a115, 0
v_accvgpr_write_b32 a116, 0
v_accvgpr_write_b32 a117, 0
v_accvgpr_write_b32 a118, 0
v_accvgpr_write_b32 a119, 0
v_accvgpr_write_b32 a120, 0
v_accvgpr_write_b32 a121, 0
v_accvgpr_write_b32 a122, 0
v_accvgpr_write_b32 a123, 0
v_accvgpr_write_b32 a124, 0
v_accvgpr_write_b32 a125, 0
v_accvgpr_write_b32 a126, 0
v_accvgpr_write_b32 a127, 0
s_mov_b32 s90, s88
s_mov_b32 s91, 0
L_sl2_%=:
s_add_u32 s92, s90, 4096
buffer_load_dwordx4 v[112:115], v236, s[16:19], s90 offen offset:0
buffer_load_dwordx4 v[116:119], v236, s[16:19], s90 offen offset:64
buffer_load_dwordx4 v[120:123], v236, s[16:19], s90 offen offset:128
buffer_load_dwordx4 v[124:127], v236, s[16:19], s90 offen offset:192
buffer_load_dwordx4 v[128:131], v236, s[16:19], s92 offen offset:0
buffer_load_dwordx4 v[132:135], v236, s[16:19], s92 offen offset:64
buffer_load_dwordx4 v[136:139], v236, s[16:19], s92 offen offset:128
buffer_load_dwordx4 v[140:143], v236, s[16:19], s92 offen offset:192
s_waitcnt vmcnt(0)
v_mfma_f32_16x16x32_bf16 v[64:67], v[112:115], v[0:3], v[160:163]
v_mfma_f32_16x16x32_bf16 v[72:75], v[112:115], v[16:19], v[164:167]
v_mfma_f32_16x16x32_bf16 v[80:83], v[112:115], v[32:35], v[168:171]
v_mfma_f32_16x16x32_bf16 v[88:91], v[112:115], v[48:51], v[172:175]
v_mfma_f32_16x16x32_bf16 v[64:67], v[116:119], v[4:7], v[64:67]
v_mfma_f32_16x16x32_bf16 v[72:75], v[116:119], v[20:23], v[72:75]
v_mfma_f32_16x16x32_bf16 v[80:83], v[116:119], v[36:39], v[80:83]
v_mfma_f32_16x16x32_bf16 v[88:91], v[116:119], v[52:55], v[88:91]
v_mfma_f32_16x16x32_bf16 v[64:67], v[120:123], v[8:11], v[64:67]
v_mfma_f32_16x16x32_bf16 v[72:75], v[120:123], v[24:27], v[72:75]
v_mfma_f32_16x16x32_bf16 v[80:83], v[120:123], v[40:43], v[80:83]
v_mfma_f32_16x16x32_bf16 v[88:91], v[120:123], v[56:59], v[88:91]
v_mfma_f32_16x16x32_bf16 v[64:67], v[124:127], v[12:15], v[64:67]
v_mfma_f32_16x16x32_bf16 v[72:75], v[124:127], v[28:31], v[72:75]
v_mfma_f32_16x16x32_bf16 v[80:83], v[124:127], v[44:47], v[80:83]
v_mfma_f32_16x16x32_bf16 v[88:91], v[124:127], v[60:63], v[88:91]
v_mfma_f32_16x16x32_bf16 v[68:71], v[128:131], v[0:3], v[160:163]
v_mfma_f32_16x16x32_bf16 v[76:79], v[128:131], v[16:19], v[164:167]
v_mfma_f32_16x16x32_bf16 v[84:87], v[128:131], v[32:35], v[168:171]
v_mfma_f32_16x16x32_bf16 v[92:95], v[128:131], v[48:51], v[172:175]
v_mfma_f32_16x16x32_bf16 v[68:71], v[132:135], v[4:7], v[68:71]
v_mfma_f32_16x16x32_bf16 v[76:79], v[132:135], v[20:23], v[76:79]
v_mfma_f32_16x16x32_bf16 v[84:87], v[132:135], v[36:39], v[84:87]
v_mfma_f32_16x16x32_bf16 v[92:95], v[132:135], v[52:55], v[92:95]
v_mfma_f32_16x16x32_bf16 v[68:71], v[136:139], v[8:11], v[68:71]
v_mfma_f32_16x16x32_bf16 v[76:79], v[136:139], v[24:27], v[76:79]
v_mfma_f32_16x16x32_bf16 v[84:87], v[136:139], v[40:43], v[84:87]
v_mfma_f32_16x16x32_bf16 v[92:95], v[136:139], v[56:59], v[92:95]
v_mfma_f32_16x16x32_bf16 v[68:71], v[140:143], v[12:15], v[68:71]
v_mfma_f32_16x16x32_bf16 v[76:79], v[140:143], v[28:31], v[76:79]
v_mfma_f32_16x16x32_bf16 v[84:87], v[140:143], v[44:47], v[84:87]
v_mfma_f32_16x16x32_bf16 v[92:95], v[140:143], v[60:63], v[92:95]
s_nop 15
v_exp_f32 v64, v64
v_exp_f32 v65, v65
v_exp_f32 v66, v66
v_exp_f32 v67, v67
v_exp_f32 v68, v68
v_exp_f32 v69, v69
v_exp_f32 v70, v70
v_exp_f32 v71, v71
v_exp_f32 v72, v72
v_exp_f32 v73, v73
v_exp_f32 v74, v74
v_exp_f32 v75, v75
v_exp_f32 v76, v76
v_exp_f32 v77, v77
v_exp_f32 v78, v78
v_exp_f32 v79, v79
v_exp_f32 v80, v80
v_exp_f32 v81, v81
v_exp_f32 v82, v82
v_exp_f32 v83, v83
v_exp_f32 v84, v84
v_exp_f32 v85, v85
v_exp_f32 v86, v86
v_exp_f32 v87, v87
v_exp_f32 v88, v88
v_exp_f32 v89, v89
v_exp_f32 v90, v90
v_exp_f32 v91, v91
v_exp_f32 v92, v92
v_exp_f32 v93, v93
v_exp_f32 v94, v94
v_exp_f32 v95, v95
s_nop 1
v_cvt_pk_bf16_f32 v96, v64, v65
v_cvt_pk_bf16_f32 v97, v66, v67
v_cvt_pk_bf16_f32 v98, v68, v69
v_cvt_pk_bf16_f32 v99, v70, v71
v_cvt_pk_bf16_f32 v100, v72, v73
v_cvt_pk_bf16_f32 v101, v74, v75
v_cvt_pk_bf16_f32 v102, v76, v77
v_cvt_pk_bf16_f32 v103, v78, v79
v_cvt_pk_bf16_f32 v104, v80, v81
v_cvt_pk_bf16_f32 v105, v82, v83
v_cvt_pk_bf16_f32 v106, v84, v85
v_cvt_pk_bf16_f32 v107, v86, v87
v_cvt_pk_bf16_f32 v108, v88, v89
v_cvt_pk_bf16_f32 v109, v90, v91
v_cvt_pk_bf16_f32 v110, v92, v93
v_cvt_pk_bf16_f32 v111, v94, v95
s_nop 3
v_mfma_f32_16x16x32_bf16 v[176:179], v[232:235], v[96:99], v[176:179]
v_mfma_f32_16x16x32_bf16 v[180:183], v[232:235], v[100:103], v[180:183]
v_mfma_f32_16x16x32_bf16 v[184:187], v[232:235], v[104:107], v[184:187]
v_mfma_f32_16x16x32_bf16 v[188:191], v[232:235], v[108:111], v[188:191]
buffer_load_ushort v144, v237, s[20:23], s90 offen offset:0
buffer_load_ushort v145, v237, s[20:23], s90 offen offset:256
buffer_load_ushort v146, v237, s[20:23], s90 offen offset:512
buffer_load_ushort v147, v237, s[20:23], s90 offen offset:768
buffer_load_ushort v148, v237, s[20:23], s92 offen offset:0
buffer_load_ushort v149, v237, s[20:23], s92 offen offset:256
buffer_load_ushort v150, v237, s[20:23], s92 offen offset:512
buffer_load_ushort v151, v237, s[20:23], s92 offen offset:768
s_waitcnt vmcnt(0)
v_lshl_or_b32 v152, v145, 16, v144
v_lshl_or_b32 v153, v147, 16, v146
v_lshl_or_b32 v154, v149, 16, v148
v_lshl_or_b32 v155, v151, 16, v150
s_nop 1
v_mfma_f32_16x16x32_bf16 a[0:3], v[152:155], v[96:99], a[0:3]
v_mfma_f32_16x16x32_bf16 a[4:7], v[152:155], v[100:103], a[4:7]
v_mfma_f32_16x16x32_bf16 a[8:11], v[152:155], v[104:107], a[8:11]
v_mfma_f32_16x16x32_bf16 a[12:15], v[152:155], v[108:111], a[12:15]
s_nop 7
buffer_load_ushort v144, v237, s[20:23], s90 offen offset:32
buffer_load_ushort v145, v237, s[20:23], s90 offen offset:288
buffer_load_ushort v146, v237, s[20:23], s90 offen offset:544
buffer_load_ushort v147, v237, s[20:23], s90 offen offset:800
buffer_load_ushort v148, v237, s[20:23], s92 offen offset:32
buffer_load_ushort v149, v237, s[20:23], s92 offen offset:288
buffer_load_ushort v150, v237, s[20:23], s92 offen offset:544
buffer_load_ushort v151, v237, s[20:23], s92 offen offset:800
s_waitcnt vmcnt(0)
v_lshl_or_b32 v152, v145, 16, v144
v_lshl_or_b32 v153, v147, 16, v146
v_lshl_or_b32 v154, v149, 16, v148
v_lshl_or_b32 v155, v151, 16, v150
s_nop 1
v_mfma_f32_16x16x32_bf16 a[16:19], v[152:155], v[96:99], a[16:19]
v_mfma_f32_16x16x32_bf16 a[20:23], v[152:155], v[100:103], a[20:23]
v_mfma_f32_16x16x32_bf16 a[24:27], v[152:155], v[104:107], a[24:27]
v_mfma_f32_16x16x32_bf16 a[28:31], v[152:155], v[108:111], a[28:31]
s_nop 7
buffer_load_ushort v144, v237, s[20:23], s90 offen offset:64
buffer_load_ushort v145, v237, s[20:23], s90 offen offset:320
buffer_load_ushort v146, v237, s[20:23], s90 offen offset:576
buffer_load_ushort v147, v237, s[20:23], s90 offen offset:832
buffer_load_ushort v148, v237, s[20:23], s92 offen offset:64
buffer_load_ushort v149, v237, s[20:23], s92 offen offset:320
buffer_load_ushort v150, v237, s[20:23], s92 offen offset:576
buffer_load_ushort v151, v237, s[20:23], s92 offen offset:832
s_waitcnt vmcnt(0)
v_lshl_or_b32 v152, v145, 16, v144
v_lshl_or_b32 v153, v147, 16, v146
v_lshl_or_b32 v154, v149, 16, v148
v_lshl_or_b32 v155, v151, 16, v150
s_nop 1
v_mfma_f32_16x16x32_bf16 a[32:35], v[152:155], v[96:99], a[32:35]
v_mfma_f32_16x16x32_bf16 a[36:39], v[152:155], v[100:103], a[36:39]
v_mfma_f32_16x16x32_bf16 a[40:43], v[152:155], v[104:107], a[40:43]
v_mfma_f32_16x16x32_bf16 a[44:47], v[152:155], v[108:111], a[44:47]
s_nop 7
buffer_load_ushort v144, v237, s[20:23], s90 offen offset:96
buffer_load_ushort v145, v237, s[20:23], s90 offen offset:352
buffer_load_ushort v146, v237, s[20:23], s90 offen offset:608
buffer_load_ushort v147, v237, s[20:23], s90 offen offset:864
buffer_load_ushort v148, v237, s[20:23], s92 offen offset:96
buffer_load_ushort v149, v237, s[20:23], s92 offen offset:352
buffer_load_ushort v150, v237, s[20:23], s92 offen offset:608
buffer_load_ushort v151, v237, s[20:23], s92 offen offset:864
s_waitcnt vmcnt(0)
v_lshl_or_b32 v152, v145, 16, v144
v_lshl_or_b32 v153, v147, 16, v146
v_lshl_or_b32 v154, v149, 16, v148
v_lshl_or_b32 v155, v151, 16, v150
s_nop 1
v_mfma_f32_16x16x32_bf16 a[48:51], v[152:155], v[96:99], a[48:51]
v_mfma_f32_16x16x32_bf16 a[52:55], v[152:155], v[100:103], a[52:55]
v_mfma_f32_16x16x32_bf16 a[56:59], v[152:155], v[104:107], a[56:59]
v_mfma_f32_16x16x32_bf16 a[60:63], v[152:155], v[108:111], a[60:63]
s_nop 7
buffer_load_ushort v144, v237, s[20:23], s90 offen offset:128
buffer_load_ushort v145, v237, s[20:23], s90 offen offset:384
buffer_load_ushort v146, v237, s[20:23], s90 offen offset:640
buffer_load_ushort v147, v237, s[20:23], s90 offen offset:896
buffer_load_ushort v148, v237, s[20:23], s92 offen offset:128
buffer_load_ushort v149, v237, s[20:23], s92 offen offset:384
buffer_load_ushort v150, v237, s[20:23], s92 offen offset:640
buffer_load_ushort v151, v237, s[20:23], s92 offen offset:896
s_waitcnt vmcnt(0)
v_lshl_or_b32 v152, v145, 16, v144
v_lshl_or_b32 v153, v147, 16, v146
v_lshl_or_b32 v154, v149, 16, v148
v_lshl_or_b32 v155, v151, 16, v150
s_nop 1
v_mfma_f32_16x16x32_bf16 a[64:67], v[152:155], v[96:99], a[64:67]
v_mfma_f32_16x16x32_bf16 a[68:71], v[152:155], v[100:103], a[68:71]
v_mfma_f32_16x16x32_bf16 a[72:75], v[152:155], v[104:107], a[72:75]
v_mfma_f32_16x16x32_bf16 a[76:79], v[152:155], v[108:111], a[76:79]
s_nop 7
buffer_load_ushort v144, v237, s[20:23], s90 offen offset:160
buffer_load_ushort v145, v237, s[20:23], s90 offen offset:416
buffer_load_ushort v146, v237, s[20:23], s90 offen offset:672
buffer_load_ushort v147, v237, s[20:23], s90 offen offset:928
buffer_load_ushort v148, v237, s[20:23], s92 offen offset:160
buffer_load_ushort v149, v237, s[20:23], s92 offen offset:416
buffer_load_ushort v150, v237, s[20:23], s92 offen offset:672
buffer_load_ushort v151, v237, s[20:23], s92 offen offset:928
s_waitcnt vmcnt(0)
v_lshl_or_b32 v152, v145, 16, v144
v_lshl_or_b32 v153, v147, 16, v146
v_lshl_or_b32 v154, v149, 16, v148
v_lshl_or_b32 v155, v151, 16, v150
s_nop 1
v_mfma_f32_16x16x32_bf16 a[80:83], v[152:155], v[96:99], a[80:83]
v_mfma_f32_16x16x32_bf16 a[84:87], v[152:155], v[100:103], a[84:87]
v_mfma_f32_16x16x32_bf16 a[88:91], v[152:155], v[104:107], a[88:91]
v_mfma_f32_16x16x32_bf16 a[92:95], v[152:155], v[108:111], a[92:95]
s_nop 7
buffer_load_ushort v144, v237, s[20:23], s90 offen offset:192
buffer_load_ushort v145, v237, s[20:23], s90 offen offset:448
buffer_load_ushort v146, v237, s[20:23], s90 offen offset:704
buffer_load_ushort v147, v237, s[20:23], s90 offen offset:960
buffer_load_ushort v148, v237, s[20:23], s92 offen offset:192
buffer_load_ushort v149, v237, s[20:23], s92 offen offset:448
buffer_load_ushort v150, v237, s[20:23], s92 offen offset:704
buffer_load_ushort v151, v237, s[20:23], s92 offen offset:960
s_waitcnt vmcnt(0)
v_lshl_or_b32 v152, v145, 16, v144
v_lshl_or_b32 v153, v147, 16, v146
v_lshl_or_b32 v154, v149, 16, v148
v_lshl_or_b32 v155, v151, 16, v150
s_nop 1
v_mfma_f32_16x16x32_bf16 a[96:99], v[152:155], v[96:99], a[96:99]
v_mfma_f32_16x16x32_bf16 a[100:103], v[152:155], v[100:103], a[100:103]
v_mfma_f32_16x16x32_bf16 a[104:107], v[152:155], v[104:107], a[104:107]
v_mfma_f32_16x16x32_bf16 a[108:111], v[152:155], v[108:111], a[108:111]
s_nop 7
buffer_load_ushort v144, v237, s[20:23], s90 offen offset:224
buffer_load_ushort v145, v237, s[20:23], s90 offen offset:480
buffer_load_ushort v146, v237, s[20:23], s90 offen offset:736
buffer_load_ushort v147, v237, s[20:23], s90 offen offset:992
buffer_load_ushort v148, v237, s[20:23], s92 offen offset:224
buffer_load_ushort v149, v237, s[20:23], s92 offen offset:480
buffer_load_ushort v150, v237, s[20:23], s92 offen offset:736
buffer_load_ushort v151, v237, s[20:23], s92 offen offset:992
s_waitcnt vmcnt(0)
v_lshl_or_b32 v152, v145, 16, v144
v_lshl_or_b32 v153, v147, 16, v146
v_lshl_or_b32 v154, v149, 16, v148
v_lshl_or_b32 v155, v151, 16, v150
s_nop 1
v_mfma_f32_16x16x32_bf16 a[112:115], v[152:155], v[96:99], a[112:115]
v_mfma_f32_16x16x32_bf16 a[116:119], v[152:155], v[100:103], a[116:119]
v_mfma_f32_16x16x32_bf16 a[120:123], v[152:155], v[104:107], a[120:123]
v_mfma_f32_16x16x32_bf16 a[124:127], v[152:155], v[108:111], a[124:127]
s_nop 7
s_add_u32 s90, s90, 8192
s_add_u32 s91, s91, 1
s_cmp_lt_u32 s91, 16
s_cbranch_scc1 L_sl2_%=
s_nop 15
v_rcp_f32 v222, v176
v_rcp_f32 v223, v180
v_rcp_f32 v224, v184
v_rcp_f32 v225, v188
s_nop 3
s_cmp_eq_u32 s89, 0
s_cbranch_scc1 L_ret0_%=
s_branch L_ret1_%=
L_end_%=:
)ASM"
    :: "s"(__builtin_amdgcn_kernarg_segment_ptr()), "s"((unsigned)blockIdx.x), "s"(w), "s"(ldsb)
    : "v0","v1","v2","v3","v4","v5","v6","v7","v8","v9","v10","v11","v12","v13","v14","v15","v16","v17","v18","v19","v20","v21","v22","v23","v24","v25","v26","v27","v28","v29","v30","v31","v32","v33","v34","v35","v36","v37","v38","v39","v40","v41","v42","v43","v44","v45","v46","v47","v48","v49","v50","v51","v52","v53","v54","v55","v56","v57","v58","v59","v60","v61","v62","v63","v64","v65","v66","v67","v68","v69","v70","v71","v72","v73","v74","v75","v76","v77","v78","v79","v80","v81","v82","v83","v84","v85","v86","v87","v88","v89","v90","v91","v92","v93","v94","v95","v96","v97","v98","v99","v100","v101","v102","v103","v104","v105","v106","v107","v108","v109","v110","v111","v112","v113","v114","v115","v116","v117","v118","v119","v120","v121","v122","v123","v124","v125","v126","v127","v128","v129","v130","v131","v132","v133","v134","v135","v136","v137","v138","v139","v140","v141","v142","v143","v144","v145","v146","v147","v148","v149","v150","v151","v152","v153","v154","v155","v156","v157","v158","v159","v160","v161","v162","v163","v164","v165","v166","v167","v168","v169","v170","v171","v172","v173","v174","v175","v176","v177","v178","v179","v180","v181","v182","v183","v184","v185","v186","v187","v188","v189","v190","v191","v192","v193","v194","v195","v196","v197","v198","v199","v200","v201","v202","v203","v204","v205","v206","v207","v208","v209","v210","v211","v212","v213","v214","v215","v216","v217","v218","v219","v220","v221","v222","v223","v224","v225","v226","v227","v228","v229","v230","v231","v232","v233","v234","v235","v236","v237","v238","v239","v240","v241","v242","v243","v244","v245","v246","v247","v248","v249","v250","v251","v252","v253","v254","v255","a0","a1","a2","a3","a4","a5","a6","a7","a8","a9","a10","a11","a12","a13","a14","a15","a16","a17","a18","a19","a20","a21","a22","a23","a24","a25","a26","a27","a28","a29","a30","a31","a32","a33","a34","a35","a36","a37","a38","a39","a40","a41","a42","a43","a44","a45","a46","a47","a48","a49","a50","a51","a52","a53","a54","a55","a56","a57","a58","a59","a60","a61","a62","a63","a64","a65","a66","a67","a68","a69","a70","a71","a72","a73","a74","a75","a76","a77","a78","a79","a80","a81","a82","a83","a84","a85","a86","a87","a88","a89","a90","a91","a92","a93","a94","a95","a96","a97","a98","a99","a100","a101","a102","a103","a104","a105","a106","a107","a108","a109","a110","a111","a112","a113","a114","a115","a116","a117","a118","a119","a120","a121","a122","a123","a124","a125","a126","a127","a128","a129","a130","a131","a132","a133","a134","a135","a136","a137","a138","a139","a140","a141","a142","a143","a144","a145","a146","a147","a148","a149","a150","a151","a152","a153","a154","a155","a156","a157","a158","a159","a160","a161","a162","a163","a164","a165","a166","a167","a168","a169","a170","a171","a172","a173","a174","a175","a176","a177","a178","a179","a180","a181","a182","a183","a184","a185","a186","a187","a188","a189","a190","a191","a192","a193","a194","a195","a196","a197","a198","a199","a200","a201","a202","a203","a204","a205","a206","a207","a208","a209","a210","a211","a212","a213","a214","a215","a216","a217","a218","a219","a220","a221","a222","a223","a224","a225","a226","a227","a228","a229","a230","a231","a232","a233","a234","a235","a236","a237","a238","a239","a240","a241","a242","a243","a244","a245","a246","a247","a248","a249","a250","a251","a252","a253","a254","a255","s8","s9","s10","s11","s12","s13","s14","s15","s16","s17","s18","s19","s20","s21","s22","s23","s24","s25","s26","s27","s28","s29","s30","s31","s36","s37","s38","s39","s40","s41","s42","s43","s44","s45","s46","s47","s48","s49","s50","s51","s52","s53","s54","s55","s56","s57","s58","s59","s60","s61","s62","s63","s64","s65","s66","s67","s68","s69","s70","s71","s72","s73","s74","s75","s76","s77","s78","s79","s80","s81","s82","s83","s84","s85","s86","s87","s88","s89","s90","s91","s92","s93","s94","s95","s96","s97","s98","s99","vcc","scc","memory");
}

extern "C" void kernel_launch(void* const* d_in, const int* in_sizes, int n_in, void* d_out, int out_size, void* d_ws, size_t ws_size, hipStream_t stream) {
  (void)hipFuncSetAttribute((const void*)gqa_block_fused, hipFuncAttributeMaxDynamicSharedMemorySize, 163840);
  hipLaunchKernelGGL(gqa_block_fused, dim3(256), dim3(256), 163840, stream,
                     (const unsigned short*)d_in[0], (const unsigned short*)d_in[1], (const unsigned short*)d_in[2],
                     (const unsigned short*)d_in[3], (const unsigned short*)d_in[4], (unsigned short*)d_out, (char*)d_ws);
}
```
